```python
import torch

def _softmax(x, axis=-1):
    return torch.softmax(x, dim=axis)
def _gelu(x):
    return torch.nn.functional.gelu(x, approximate="tanh")
def _silu(x):
    return torch.nn.functional.silu(x)
def _sigmoid(x):
    return torch.sigmoid(x)
def _layernorm(x, axis=-1, eps=1e-5):
    m = x.mean(dim=axis, keepdim=True)
    v = x.var(dim=axis, keepdim=True, unbiased=False)
    return (x - m) / torch.sqrt(v + eps)
def _scatter_add(tgt, idx, src, axis=0):
    return tgt.clone().index_add_(axis, idx, src)
def _segment_sum(src, seg, n):
    out = torch.zeros((n,) + src.shape[1:], dtype=src.dtype)
    return out.index_add_(0, seg, src)
def _pad(x, pw):
    flat = tuple(v for pair in reversed(pw) for v in pair)
    return torch.nn.functional.pad(x, flat)

kernel_name = 'concat_heads__mha_block__ssm_conv_s4d__prompt'

def setup_inputs():

    _w = (lambda a: (lambda t: t.to(torch.bfloat16) if t.is_floating_point() else t)(torch.as_tensor(a)))
    def _A_setup():
        B, S, D, H = 1, 2048, 448, 16
        return {
            "x": _w((lambda *s: torch.randn(*s, dtype=torch.bfloat16) * 0.2)(B, S, D)),
            "wqkv": _w((lambda *s: torch.randn(*s, dtype=torch.bfloat16) * 0.2)(D, 3 * D)),
            "wo": _w((lambda *s: torch.randn(*s, dtype=torch.bfloat16) * 0.05)(D, D)),
        }
    _A_in = _A_setup()

    def _B_setup():
        Bsz, L, D, N = 1, 2048, 448, 256
        return {
            "u": _w((lambda *s: torch.randn(*s, dtype=torch.bfloat16) * 0.05)(Bsz, L, D)),
            "log_A_real": _w((lambda *s: torch.randn(*s, dtype=torch.bfloat16) * 6.4)(D, N) * 0.5),
            "A_imag": _w((lambda *s: torch.randn(*s, dtype=torch.bfloat16) * 0.1)(D, N) * 3.14),
            "C_real": _w((lambda *s: torch.randn(*s, dtype=torch.bfloat16) * 0.4)(D, N) / N ** 0.5),
            "C_imag": _w((lambda *s: torch.randn(*s, dtype=torch.bfloat16) * 0.4)(D, N) / N ** 0.5),
            "log_dt": _w((lambda *s: torch.randn(*s, dtype=torch.bfloat16) * 0.1)(D) * 0.5 - 4.0),
            "D_skip": _w((lambda *s: torch.randn(*s, dtype=torch.bfloat16) * 0.1)(D)),
        }
    _B_in = _B_setup()

    return {
        "A_x": _A_in["x"],
        "A_wqkv": _A_in["wqkv"],
        "A_wo": _A_in["wo"],
        "B_u": _B_in["u"],
        "B_log_A_real": _B_in["log_A_real"],
        "B_A_imag": _B_in["A_imag"],
        "B_C_real": _B_in["C_real"],
        "B_C_imag": _B_in["C_imag"],
        "B_log_dt": _B_in["log_dt"],
        "B_D_skip": _B_in["D_skip"],
    }

def reference(A_x, A_wqkv, A_wo, B_u, B_log_A_real, B_A_imag, B_C_real, B_C_imag, B_log_dt, B_D_skip):
    def _A_ref(x, wqkv, wo):
        B, S, D = x.shape
        H = 16
        Dh = D // H
        qkv = (lambda x, *d: x.permute(*d))((x @ wqkv).reshape(B, S, 3, H, Dh), 2, 0, 3, 1, 4)
        q, k, v = qkv[0], qkv[1], qkv[2]
        scores = torch.einsum("bhqd,bhkd->bhqk", q, k) * (Dh ** -0.5)
        p = _softmax(scores, axis=-1)
        o = torch.einsum("bhqk,bhkd->bhqd", p, v)
        return (lambda x, *d: x.permute(*d))(o, 0, 2, 1, 3).reshape(B, S, D) @ wo

    def _B_ref(u, log_A_real, A_imag, C_real, C_imag, log_dt, D_skip):
        L = u.shape[1]
        dt = torch.exp(log_dt)
        A = -torch.exp(log_A_real) + 1j * A_imag
        C = (C_real + 1j * C_imag) * dt[:, None]
        pos = (lambda x, dt: x.to(dt))(torch.arange(L), log_dt.dtype)
        P = torch.exp((A * dt[:, None])[:, :, None] * pos[None, None, :])
        K = 2.0 * torch.einsum("dn,dnl->dl", C, P).real
        L2 = 2 * L
        u_t = (lambda x, *d: x.permute(*d))(u, 0, 2, 1)
        Kf = (lambda x, *a, **k: torch.fft.rfft(x.float(), *a, **k))(K, L2)
        Uf = (lambda x, *a, **k: torch.fft.rfft(x.float(), *a, **k))(u_t, L2)
        y = (lambda x, *a, **k: torch.fft.irfft(x, *a, **k).to(torch.bfloat16))(Uf * Kf[None, :, :], L2)[:, :, :L]
        y = (lambda x, *d: x.permute(*d))(y, 0, 2, 1)
        return y + u * D_skip[None, None, :]

    a = _A_ref(A_x, A_wqkv, A_wo)
    b = _B_ref(B_u, B_log_A_real, B_A_imag, B_C_real, B_C_imag, B_log_dt, B_D_skip)
    return torch.concatenate([a, b], axis=-1)
```

```cpp
#include <hip/hip_runtime.h>
#include <hip/hip_bf16.h>
typedef __hip_bfloat16 bf16;
__device__ inline float b2f(bf16 x){ return __bfloat162float(x); }
__device__ inline float rb(float x){ return __bfloat162float(__float2bfloat16(x)); }
constexpr int S=2048,D=448,H=16,DH=28,N=256;
__global__ void k_qkv(const bf16* x,const bf16* w,float* qkv){
  int c=blockIdx.x*blockDim.x+threadIdx.x; int r=blockIdx.y; if(c>=3*D) return;
  float a=0; for(int k=0;k<D;k++) a+=b2f(x[r*D+k])*b2f(w[k*3*D+c]);
  qkv[r*3*D+c]=rb(a);
}
__global__ void k_attn(const float* qkv,float* o){
  int q=blockIdx.x, h=blockIdx.y, t=threadIdx.x;
  __shared__ float p[S]; __shared__ float red[256]; __shared__ float qs[DH];
  if(t<DH) qs[t]=qkv[q*3*D+h*DH+t]; __syncthreads();
  float m=-1e30f;
  for(int k=t;k<S;k+=256){ float a=0; for(int d=0;d<DH;d++) a+=qs[d]*qkv[k*3*D+D+h*DH+d]; a*=0.18898223650461362f; p[k]=a; m=fmaxf(m,a);}
  red[t]=m; __syncthreads(); for(int s=128;s>0;s>>=1){ if(t<s) red[t]=fmaxf(red[t],red[t+s]); __syncthreads(); } m=red[0]; __syncthreads();
  float l=0; for(int k=t;k<S;k+=256){ float e=__expf(p[k]-m); p[k]=e; l+=e; }
  red[t]=l; __syncthreads(); for(int s=128;s>0;s>>=1){ if(t<s) red[t]+=red[t+s]; __syncthreads(); } l=red[0]; __syncthreads();
  int d=t%32, g=t/32; float a=0; if(d<DH) for(int k=g;k<S;k+=8) a+=p[k]*qkv[k*3*D+2*D+h*DH+d];
  red[t]=a; __syncthreads();
  if(t<DH){ float s=0; for(int j=0;j<8;j++) s+=red[j*32+t]; o[q*D+h*DH+t]=rb(s/l); }
}
__global__ void k_out(const float* o,const bf16* wo,bf16* out){
  int c=blockIdx.x*blockDim.x+threadIdx.x; int r=blockIdx.y; if(c>=D) return;
  float a=0; for(int k=0;k<D;k++) a+=o[r*D+k]*b2f(wo[k*D+c]);
  out[r*2*D+c]=__float2bfloat16(a);
}
__global__ void k_kgen(const bf16* lar,const bf16* aim,const bf16* cr,const bf16* ci,const bf16* ldt,float* K){
  int d=blockIdx.y; int l=blockIdx.x*blockDim.x+threadIdx.x;
  float dt=rb(expf(b2f(ldt[d]))); float pos=rb((float)l); float a=0;
  for(int n=0;n<N;n++){ float ar=-rb(expf(b2f(lar[d*N+n])))*dt, ai=b2f(aim[d*N+n])*dt; float e=expf(ar*pos), s,c; sincosf(ai*pos,&s,&c);
    a+=e*(b2f(cr[d*N+n])*dt*c-b2f(ci[d*N+n])*dt*s); }
  K[d*S+l]=2*a;
}
__global__ void k_conv(const float* K,const bf16* u,const bf16* dsk,bf16* out){
  int d=blockIdx.x*blockDim.x+threadIdx.x; int l=blockIdx.y; if(d>=D) return;
  float a=0; for(int j=0;j<=l;j++) a+=K[d*S+j]*b2f(u[(l-j)*D+d]);
  float y=rb(a); float sk=rb(b2f(u[l*D+d])*b2f(dsk[d]));
  out[l*2*D+D+d]=__float2bfloat16(y+sk);
}
extern "C" void kernel_launch(void* const* d_in,const int* in_sizes,int n_in,void* d_out,int out_size,void* d_ws,size_t ws_size,hipStream_t stream){
  const bf16* x=(const bf16*)d_in[0]; const bf16* wqkv=(const bf16*)d_in[1]; const bf16* wo=(const bf16*)d_in[2]; const bf16* u=(const bf16*)d_in[3];
  float* qkv=(float*)d_ws; float* o=qkv+S*3*D; float* K=o+S*D; bf16* out=(bf16*)d_out;
  k_qkv<<<dim3(6,S),256,0,stream>>>(x,wqkv,qkv);
  k_attn<<<dim3(S,H),256,0,stream>>>(qkv,o);
  k_out<<<dim3(2,S),256,0,stream>>>(o,wo,out);
  k_kgen<<<dim3(8,D),256,0,stream>>>((const bf16*)d_in[4],(const bf16*)d_in[5],(const bf16*)d_in[6],(const bf16*)d_in[7],(const bf16*)d_in[8],K);
  k_conv<<<dim3(2,S),256,0,stream>>>(K,u,(const bf16*)d_in[9],out);
}
```

```cpp
#include <hip/hip_runtime.h>
#include <hip/hip_bf16.h>
#include <stdint.h>

typedef short bf16x8 __attribute__((ext_vector_type(8)));
typedef _Float16 f16x8 __attribute__((ext_vector_type(8)));
typedef _Float16 f16x2 __attribute__((ext_vector_type(2)));
typedef __bf16 bf16x2 __attribute__((ext_vector_type(2)));
typedef short s16x4 __attribute__((ext_vector_type(4)));
typedef float f32x16 __attribute__((ext_vector_type(16)));
typedef float f32x4 __attribute__((ext_vector_type(4)));
typedef float f32x2 __attribute__((ext_vector_type(2)));
typedef unsigned u32x4 __attribute__((ext_vector_type(4)));
typedef unsigned u32x2 __attribute__((ext_vector_type(2)));
#define LAS __attribute__((address_space(3)))
#define DI __device__ __forceinline__
#define MFMA_BF(a, b, c) __builtin_amdgcn_mfma_f32_32x32x16_bf16((a), (b), (c), 0, 0, 0)
#define MFMA_F16(a, b, c) __builtin_amdgcn_mfma_f32_32x32x16_f16((a), (b), (c), 0, 0, 0)
#define SBAR() __builtin_amdgcn_sched_barrier(0)

namespace k {
constexpr int S = 2048, D = 448, NH = 16, DH = 28, NS = 256;
constexpr size_t WS_Q = 0;
constexpr size_t WS_KV = WS_Q + (2u << 20);
constexpr size_t WS_O = WS_KV + (4u << 20);
constexpr size_t WS_WO = WS_O + (2u << 20);
constexpr size_t WS_UT = WS_WO + (512u << 10);
constexpr size_t WS_KR = WS_UT + (2u << 20);
constexpr size_t WS_BT = WS_KR + (2u << 20);

DI unsigned pk_bf16(float a, float b) { f32x2 v = {a, b}; return __builtin_bit_cast(unsigned, __builtin_convertvector(v, bf16x2)); }
DI unsigned pk_f16(float a, float b) { f32x2 v = {a, b}; return __builtin_bit_cast(unsigned, __builtin_convertvector(v, f16x2)); }
DI float bf_lo(unsigned w) { return __uint_as_float(w << 16); }
DI float bf_hi(unsigned w) { return __uint_as_float(w & 0xffff0000u); }
DI float rbf(float x) { return bf_lo(pk_bf16(x, x)); }
DI void glds16(const void* sbase, unsigned voff, unsigned lds_base) {
    unsigned sv;
    asm volatile("s_mov_b32 %0, m0\n\ts_mov_b32 m0, %3\n\ts_nop 0\n\tglobal_load_lds_dwordx4 %1, %2\n\ts_mov_b32 m0, %0"
                 : "=&s"(sv) : "v"(voff), "s"(sbase), "s"(lds_base) : "memory");
}
DI s16x4 trread(LAS const char* p) { return __builtin_bit_cast(s16x4, __builtin_amdgcn_ds_read_tr16_b64_v4i16((LAS s16x4*)p)); }
DI bf16x8 cat8(s16x4 a, s16x4 b) { return __builtin_shufflevector(a, b, 0, 1, 2, 3, 4, 5, 6, 7); }
DI f32x2 cmul(f32x2 a, f32x2 b) {
    f32x2 t, r;
    asm("v_pk_mul_f32 %0, %1, %2 op_sel:[0,0] op_sel_hi:[0,1]" : "=v"(t) : "v"(a), "v"(b));
    asm("v_pk_fma_f32 %0, %1, %2, %3 op_sel:[1,1,0] op_sel_hi:[1,0,1] neg_lo:[1,0,0]" : "=v"(r) : "v"(a), "v"(b), "v"(t));
    return r;
}
DI f32x2 anchor(float a2, float rev, float s) {
    const float mag = __builtin_amdgcn_exp2f(a2 * s);
    float r = rev * s; r -= __builtin_rintf(r);
    f32x2 z = {mag * __builtin_amdgcn_cosf(r), mag * __builtin_amdgcn_sinf(r)};
    return z;
}
DI void transpose16(LAS const char* lds, int lane, u32x4& o0, u32x4& o1) {
    LAS const char* p = lds + ((lane & 15) >> 2) * 128 + (lane >> 4) * 32 + (lane & 3) * 8;
    const s16x4 a = trread(p), b = trread(p + 512), c = trread(p + 1024), d = trread(p + 1536);
    o0 = __builtin_bit_cast(u32x4, cat8(a, b)); o1 = __builtin_bit_cast(u32x4, cat8(c, d));
}

constexpr int WROW = 192, TROW = 272, TABW = 53 * TROW;
constexpr int L1_W = 0, L1_TAB = D * WROW, L1_T1 = L1_TAB + 4 * TABW, L1_KT = L1_T1 + 8192, L1_BYTES = L1_KT + 2 * 1312;

__global__ __launch_bounds__(256, 1) void k1(const uint16_t* __restrict__ x, const uint16_t* __restrict__ wqkv, const uint16_t* __restrict__ wo,
                                            const uint16_t* __restrict__ u, const uint16_t* __restrict__ lar, const uint16_t* __restrict__ aim,
                                            const uint16_t* __restrict__ cre, const uint16_t* __restrict__ cim, const uint16_t* __restrict__ ldt,
                                            char* __restrict__ ws) {
    extern __shared__ __attribute__((aligned(16))) char smem[];
    LAS char* lds = (LAS char*)smem;
    const unsigned lds0 = (unsigned)(uintptr_t)lds;
    const int b = blockIdx.x, tid = threadIdx.x, lane = tid & 63, r32 = lane & 31, hi = lane >> 5;
    const int w = __builtin_amdgcn_readfirstlane(tid >> 6);
    const int xcd = b & 7, bi = b >> 3;
    const int head = 8 * (xcd & 1) + (bi & 7), grp = 4 * (xcd >> 1) + (bi >> 3);
    const bool bwork = b < 224;
    const int ch = 2 * b + (w >> 1);
    unsigned pl = 0, pa = 0, pr = 0, pi = 0;
    if (bwork) {
        const unsigned off = (unsigned)(ch * NS + 128 * (w & 1) + 2 * lane) * 2;
        asm volatile("global_load_dword %0, %4, %5\n\tglobal_load_dword %1, %4, %6\n\tglobal_load_dword %2, %4, %7\n\tglobal_load_dword %3, %4, %8"
                     : "=&v"(pl), "=&v"(pa), "=&v"(pr), "=&v"(pi) : "v"(off), "s"(lar), "s"(aim), "s"(cre), "s"(cim) : "memory");
        const int lt = b / 7, dt_ = b % 7;
        const unsigned uo = (unsigned)((64 * lt + 16 * w + (lane >> 3)) * D + 64 * dt_ + (lane & 7) * 8) * 2;
        glds16(u, uo, lds0 + L1_T1 + w * 2048);
        glds16(u, uo + 8 * D * 2, lds0 + L1_T1 + w * 2048 + 1024);
    }
    const int wsh = head == 15 ? 4 : 0;
    {
        unsigned wo3[3];
#pragma unroll
        for (int i = 0; i < 3; ++i) { const int p = i * 64 + lane, kr = p / 12, q = p % 12;
            wo3[i] = (unsigned)(kr * 3 * D + (q >> 2) * D + (q & 3) * 8) * 2; }
        const uint16_t* wb = wqkv + (size_t)(112 * w) * 3 * D + DH * head - wsh;
#pragma unroll
        for (int st = 0; st < 7; ++st)
#pragma unroll
            for (int i = 0; i < 3; ++i) glds16(wb + (size_t)st * 16 * 3 * D, wo3[i], lds0 + L1_W + (112 * w + 16 * st) * WROW + i * 1024);
    }
    bf16x8 xf[28];
    {
        const uint16_t* xp = x + (size_t)(128 * grp + 32 * w + r32) * D + 8 * hi;
#pragma unroll
        for (int ks = 0; ks < 28; ++ks) xf[ks] = *(const bf16x8*)(xp + 16 * ks);
    }
    if (bwork) {
        asm volatile("s_waitcnt vmcnt(49)" : "+v"(pl), "+v"(pa), "+v"(pr), "+v"(pi) :: "memory");
        { u32x4 o0, o1; transpose16(lds + L1_T1 + w * 2048, lane, o0, o1);
          const int lt = b / 7, dt_ = b % 7;
          u32x4* dst = (u32x4*)(ws + WS_UT + ((size_t)(64 * dt_ + lane) * S + 64 * lt + 16 * w) * 2);
          dst[0] = o0; dst[1] = o1; }
        const unsigned lw = ((const unsigned*)ldt)[b];
        const float dt = rbf(expf((w >> 1) ? bf_hi(lw) : bf_lo(lw)));
        float cm = fmaxf(fmaxf(fabsf(bf_lo(pr)), fabsf(bf_hi(pr))), fmaxf(fabsf(bf_lo(pi)), fabsf(bf_hi(pi))));
#pragma unroll
        for (int o = 32; o >= 1; o >>= 1) cm = fmaxf(cm, __shfl_xor(cm, o));
        unsigned ce = __float_as_uint(cm) & 0x7f800000u; ce = ce < 0x01000000u ? 0x01000000u : (ce > 0x7e800000u ? 0x7e800000u : ce);
        const float cscale = __uint_as_float(0x7f000000u - ce);
        const float oscale = 2.0f * dt * __uint_as_float(ce);
        LAS char* tab = lds + L1_TAB + w * TABW;
        LAS unsigned* tw = (LAS unsigned*)(tab + lane * 4);
        LAS const char* trd = tab + r32 * TROW + hi * 16;
        f32x16 dacc = {};
#pragma unroll
        for (int c = 0; c < 2; ++c) {
            const float vl = c ? bf_hi(pl) : bf_lo(pl), va = c ? bf_hi(pa) : bf_lo(pa), vr = c ? bf_hi(pr) : bf_lo(pr), vi = c ? bf_hi(pi) : bf_lo(pi);
            const float a2 = (-rbf(expf(vl)) * dt) * 1.4426950408889634f, rev = (va * dt) * 0.15915494309189535f;
            const f32x2 z1 = anchor(a2, rev, 1.f), z8 = anchor(a2, rev, 8.f), z256 = anchor(a2, rev, 256.f);
#define PUTX(row, v) tw[(row) * (TROW / 4)] = pk_f16((v)[0], -(v)[1])
#define PUTY(col, v) tw[(21 + (col)) * (TROW / 4)] = pk_f16((v)[0], (v)[1])
            f32x2 X[8]; X[0] = (f32x2){vr * cscale, vi * cscale};
            const f32x2 z16 = cmul(z8, z8);
            f32x2 ye = {1.f, 0.f}, yo = z8;
            PUTY(0, ye); PUTY(1, yo); PUTX(0, X[0]);
            ye = z16; PUTY(2, ye);
#pragma unroll
            for (int i = 1; i < 16; ++i) {
                yo = cmul(yo, z16); PUTY(2 * i + 1, yo);
                if (i > 1) { ye = cmul(ye, z16); PUTY(2 * i, ye); }
                if (i < 8) { X[i] = cmul(X[i - 1], z1); PUTX(i, X[i]); }
                if (i == 8) { X[0] = cmul(X[0], z256); PUTX(8, X[0]); X[2] = cmul(X[2], z256); PUTX(9, X[2]); }
                if (i == 9) { X[4] = cmul(X[4], z256); PUTX(10, X[4]); X[6] = cmul(X[6], z256); PUTX(11, X[6]); }
                if (i == 10) { X[0] = cmul(X[0], z256); PUTX(12, X[0]); X[4] = cmul(X[4], z256); PUTX(13, X[4]); }
                if (i == 11) { X[0] = cmul(X[0], z256); PUTX(14, X[0]); X[4] = cmul(X[4], z256); PUTX(15, X[4]); }
                if (i >= 12) { X[0] = cmul(X[0], z256); PUTX(4 + i, X[0]); }
                if (i == 15) { X[0] = cmul(X[0], z256); PUTX(20, X[0]); }
            }
#pragma unroll
            for (int kk = 0; kk < 8; ++kk) {
                const f16x8 fa = *(LAS const f16x8*)(trd + kk * 32), fb = *(LAS const f16x8*)(trd + 21 * TROW + kk * 32);
                dacc = MFMA_F16(fa, fb, dacc);
            }
        }
        {
            LAS float* pt = (LAS float*)tab;
            *(LAS f32x4*)(pt + 8 * r32 + 4 * hi) = (f32x4){dacc[0] * oscale, dacc[1] * oscale, dacc[2] * oscale, dacc[3] * oscale};
            *(LAS f32x2*)(pt + (hi ? 384 + 2 * r32 : 256 + 4 * r32)) = (f32x2){dacc[4] * oscale, dacc[5] * oscale};
            *(LAS f32x2*)(pt + (hi ? 448 + 2 * r32 : 258 + 4 * r32)) = (f32x2){dacc[6] * oscale, dacc[7] * oscale};
            pt[hi ? 640 + r32 : 512 + r32] = dacc[8] * oscale;
            pt[hi ? 672 + r32 : 544 + r32] = dacc[9] * oscale;
            pt[hi ? 672 + r32 : 576 + r32] = dacc[10] * oscale;
            pt[hi ? 672 + r32 : 608 + r32] = dacc[11] * oscale;
        }
        asm volatile("s_waitcnt vmcnt(28) lgkmcnt(0)\n\ts_barrier" ::: "memory");
#pragma unroll
        for (int c = 0; c < 2; ++c) if (tid < 161) {
            const f32x4 p0 = *(LAS const f32x4*)(lds + L1_TAB + (2 * c) * TABW + tid * 16), p1 = *(LAS const f32x4*)(lds + L1_TAB + (2 * c + 1) * TABW + tid * 16);
            *(LAS u32x2*)(lds + L1_KT + c * 1312 + tid * 8) = (u32x2){pk_bf16(p0[0] + p1[0], p0[1] + p1[1]), pk_bf16(p0[2] + p1[2], p0[3] + p1[3])};
        }
        asm volatile("s_waitcnt lgkmcnt(0)\n\ts_barrier" ::: "memory");
        {
            const int jb = 2047 - 8 * tid, sh = jb >= 1024 ? 3 : (jb >= 512 ? 2 : (jb >= 256 ? 1 : 0));
            const int rnd = sh ? (1 << (sh - 1)) - 1 : 0, odd = sh ? 1 : 0;
            int ix[8];
#pragma unroll
            for (int e = 0; e < 8; ++e) { const int j = jb - e; ix[e] = 128 * sh + ((j + rnd + ((j >> sh) & odd)) >> sh); }
#pragma unroll
            for (int c = 0; c < 2; ++c) {
                LAS const uint16_t* kt = (LAS const uint16_t*)(lds + L1_KT + c * 1312);
                u32x4 o;
#pragma unroll
                for (int e = 0; e < 4; ++e) o[e] = (unsigned)kt[ix[2 * e]] | ((unsigned)kt[ix[2 * e + 1]] << 16);
                *(u32x4*)(ws + WS_KR + (size_t)(2 * b + c) * S * 2 + tid * 16) = o;
            }
        }
    } else {
        const int kap = b - 224, hh = kap >> 1, s = kap & 1;
        for (int ct = w; ct < 14; ct += 4) {
            u32x4 o;
#pragma unroll
            for (int e = 0; e < 4; ++e) {
                unsigned v2[2];
#pragma unroll
                for (int t = 0; t < 2; ++t) { const int j = 2 * e + t, d = 16 * s + 8 * (j >> 2) + 4 * hi + (j & 3);
                    v2[t] = d < DH ? wo[(size_t)(DH * hh + d) * D + 32 * ct + r32] : 0; }
                o[e] = v2[0] | (v2[1] << 16);
            }
            *(u32x4*)(ws + WS_WO + ((size_t)(ct * 32 + kap) * 64 + lane) * 16) = o;
        }
        asm volatile("s_waitcnt vmcnt(0)\n\ts_barrier" ::: "memory");
    }
    f32x16 aq = {}, ak = {}, av = {};
    {
        LAS const char* wp = lds + L1_W + (8 * hi + ((lane & 15) >> 2)) * WROW + ((lane >> 4) & 1) * 32 + (lane & 3) * 8 + wsh * 2;
#pragma unroll
        for (int ks = 0; ks < 28; ++ks) {
            LAS const char* p = wp + ks * 16 * WROW;
            const bf16x8 fq = cat8(trread(p), trread(p + 4 * WROW));
            const bf16x8 fk = cat8(trread(p + 64), trread(p + 4 * WROW + 64));
            const bf16x8 fv = cat8(trread(p + 128), trread(p + 4 * WROW + 128));
            aq = MFMA_BF(fq, xf[ks], aq); ak = MFMA_BF(fk, xf[ks], ak); av = MFMA_BF(xf[ks], fv, av);
        }
    }
    {
        const float cq = 0.18898223650461362f * 1.4426950408889634f;
        const int tile = 4 * grp + w;
        u32x4 q0, q1, k0, k1_, v0, v1;
#pragma unroll
        for (int i = 0; i < 4; ++i) {
            q0[i] = pk_bf16(aq[2 * i] * cq, aq[2 * i + 1] * cq); q1[i] = pk_bf16(aq[8 + 2 * i] * cq, aq[9 + 2 * i] * cq);
            k0[i] = pk_bf16(ak[2 * i], ak[2 * i + 1]); k1_[i] = pk_bf16(ak[8 + 2 * i], ak[9 + 2 * i]);
            v0[i] = pk_bf16(av[2 * i], av[2 * i + 1]); v1[i] = pk_bf16(av[8 + 2 * i], av[9 + 2 * i]);
            if (r32 >= DH) { v0[i] = v1[i] = (r32 == DH ? 0x3f803f80u : 0u); }
        }
        if (hi) { q1[2] = 0; q1[3] = 0; k1_[2] = 0x3f803f80u; k1_[3] = 0x00003f80u; }
        u32x4* qd = (u32x4*)(ws + WS_Q + (size_t)(head * 64 + tile) * 2048) + lane;
        qd[0] = q0; qd[64] = q1;
        u32x4* kd = (u32x4*)(ws + WS_KV + (size_t)(head * 64 + tile) * 4096) + lane;
        kd[0] = k0; kd[64] = k1_; kd[128] = v0; kd[192] = v1;
    }
}

constexpr int CPY = 4160, L2_U = 36864, L2_BYTES = 49152;

DI void attn_block(int b, LAS char* lds, char* __restrict__ ws) {
    const unsigned lds0 = (unsigned)(uintptr_t)lds;
    const int tid = threadIdx.x, lane = tid & 63, hi = lane >> 5;
    const int w = __builtin_amdgcn_readfirstlane(tid >> 6);
    const int head = 2 * (b & 7) + ((b >> 3) & 1), grp = b >> 4, tile = 4 * grp + w;
    const char* kvh = ws + WS_KV + (size_t)head * 64 * 4096;
    const unsigned voff = lane * 16;
    const u32x4* qs = (const u32x4*)(ws + WS_Q + (size_t)(head * 64 + tile) * 2048) + lane;
    const bf16x8 q0 = __builtin_bit_cast(bf16x8, qs[0]);
    const u32x4 q1raw = qs[64];
    LAS const char* lp = lds + lane * 16;
    f32x16 o;
    float mtrue = 0.f;
#define LOADCH(j, slot) do { const char* sb_ = kvh + (size_t)(((grp + (j)) & 15) * 4 + w) * 4096; \
        _Pragma("unroll") for (int f_ = 0; f_ < 4; ++f_) glds16(sb_, voff + f_ * 1024, lds0 + (slot) + w * 4096 + f_ * 1024); } while (0)
#define FR(p, off) (*(LAS const bf16x8*)((p) + (off)))
    for (int attempt = 0;; ++attempt) {
        LOADCH(0, 0); LOADCH(1, 16384); LOADCH(2, 32768);
        asm volatile("s_waitcnt vmcnt(8)\n\ts_barrier" ::: "memory");
        bf16x8 k0 = FR(lp, 0), k1_ = FR(lp, 1024);
        const f32x16 zero = {};
        float m;
        if (attempt == 0) {
            f32x16 s = MFMA_BF(k0, q0, zero); s = MFMA_BF(k1_, __builtin_bit_cast(bf16x8, q1raw), s);
            m = s[0];
#pragma unroll
            for (int i = 1; i < 16; ++i) m = fmaxf(m, s[i]);
            m = fmaxf(m, __shfl_xor(m, 32));
        } else m = mtrue;
        u32x4 q1w = q1raw;
        { const float m1 = rbf(m), m2 = rbf(m - m1), m3 = m - m1 - m2;
          if (hi) { q1w[2] = pk_bf16(-m1, -m2); q1w[3] = pk_bf16(-m3, 0.f); } }
        const bf16x8 q1 = __builtin_bit_cast(bf16x8, q1w);
        f32x16 sa = MFMA_BF(k0, q0, zero); sa = MFMA_BF(k1_, q1, sa);
        f32x16 sb;
        k0 = FR(lp, 4096); k1_ = FR(lp, 4096 + 1024);
        bf16x8 v0 = FR(lp, 2048), v1 = {};
        u32x4 p0, p1 = {};
        o = zero;
        int cur = 0, nxt = 16384;
#define EX(x) __builtin_amdgcn_exp2f(x)
#define STEP(SC, SN, PK, OK, PV1, OV1, PV0, OV0) do { \
        SN = MFMA_BF(k0, q0, zero); k0 = FR(PK, OK); \
        p0[0] = pk_bf16(EX(SC[0]), EX(SC[1])); p0[1] = pk_bf16(EX(SC[2]), EX(SC[3])); SBAR(); \
        o = MFMA_BF(v1, __builtin_bit_cast(bf16x8, p1), o); v1 = FR(PV1, OV1); \
        p0[2] = pk_bf16(EX(SC[4]), EX(SC[5])); p0[3] = pk_bf16(EX(SC[6]), EX(SC[7])); SBAR(); \
        SN = MFMA_BF(k1_, q1, SN); k1_ = FR(PK, OK + 1024); \
        p1[0] = pk_bf16(EX(SC[8]), EX(SC[9])); p1[1] = pk_bf16(EX(SC[10]), EX(SC[11])); SBAR(); \
        o = MFMA_BF(v0, __builtin_bit_cast(bf16x8, p0), o); v0 = FR(PV0, OV0); \
        p1[2] = pk_bf16(EX(SC[12]), EX(SC[13])); p1[3] = pk_bf16(EX(SC[14]), EX(SC[15])); SBAR(); } while (0)
        for (int c = 0; c < 16; ++c) {
            LAS const char* pc = lp + cur; LAS const char* pn = lp + nxt;
            STEP(sa, sb, pc, 8192, pc, 3072, pc, 4096 + 2048);
            STEP(sb, sa, pc, 12288, pc, 4096 + 3072, pc, 8192 + 2048);
            if (c == 0) asm volatile("s_waitcnt vmcnt(4)\n\ts_barrier" ::: "memory");
            else {
                asm volatile("s_waitcnt vmcnt(0)\n\ts_barrier" ::: "memory");
                if (c < 14) { const int prv = 49152 - cur - nxt; LOADCH(c + 2, prv); }
            }
            STEP(sa, sb, pn, 0, pc, 8192 + 3072, pc, 12288 + 2048);
            STEP(sb, sa, pn, 4096, pc, 12288 + 3072, pn, 2048);
            cur = nxt; nxt = nxt == 32768 ? 0 : nxt + 16384;
        }
        o = MFMA_BF(v1, __builtin_bit_cast(bf16x8, p1), o);
        if (attempt) break;
        bool bad = false;
#pragma unroll
        for (int i = 0; i < 16; ++i) bad |= !(fabsf(o[i]) < 1e30f);
        if (!__syncthreads_or(bad)) break;
        float mm = -INFINITY;
        for (int t = 0; t < 64; ++t) {
            const u32x4* kp = (const u32x4*)(kvh + (size_t)t * 4096) + lane;
            f32x16 s = MFMA_BF(__builtin_bit_cast(bf16x8, kp[0]), q0, zero); s = MFMA_BF(__builtin_bit_cast(bf16x8, kp[64]), __builtin_bit_cast(bf16x8, q1raw), s);
#pragma unroll
            for (int i = 0; i < 16; ++i) mm = fmaxf(mm, s[i]);
        }
        mtrue = fmaxf(mm, __shfl_xor(mm, 32));
    }
    const float l = __shfl(o[12], (lane & 31) + 32);
    const float rl = 1.0f / l;
    u32x4 o0, o1;
#pragma unroll
    for (int i = 0; i < 4; ++i) { o0[i] = pk_bf16(o[2 * i] * rl, o[2 * i + 1] * rl); o1[i] = pk_bf16(o[8 + 2 * i] * rl, o[9 + 2 * i] * rl); }
    if (hi) { o1[2] = 0; o1[3] = 0; }
    u32x4* od = (u32x4*)(ws + WS_O + (size_t)(tile * 16 + head) * 2048) + lane;
    od[0] = o0; od[64] = o1;
}

DI void conv_block(int ch, LAS char* lds, char* __restrict__ ws, const uint16_t* __restrict__ dskip) {
    const int tid = threadIdx.x, lane = tid & 63, r32 = lane & 31, hi = lane >> 5;
    const int w = __builtin_amdgcn_readfirstlane(tid >> 6);
    const u32x4* kr = (const u32x4*)(ws + WS_KR + (size_t)ch * S * 2);
    const u32x4 c0 = kr[tid];
    u32x4 c1 = kr[tid < 255 ? tid + 1 : 255];
    const u32x4 uu = ((const u32x4*)(ws + WS_UT + (size_t)ch * S * 2))[tid];
    const float dsk = bf_lo(dskip[ch]);
    const u32x4 z4 = {};
    if (tid < 160) *(LAS u32x4*)(lds + L2_U + tid * 16) = z4;
    if (tid < 32) *(LAS u32x4*)(lds + (tid >> 2) * CPY + (256 + (tid & 3)) * 16) = z4;
    if (tid == 255) c1 = z4;
    *(LAS u32x4*)(lds + L2_U + (32 + (tid >> 2)) * 80 + (tid & 3) * 16) = uu;
    {
        const unsigned W[8] = {c0[0], c0[1], c0[2], c0[3], c1[0], c1[1], c1[2], c1[3]};
        unsigned V[7];
#pragma unroll
        for (int i = 0; i < 7; ++i) V[i] = __builtin_amdgcn_alignbit(W[i + 1], W[i], 16);
#pragma unroll
        for (int e = 0; e < 4; ++e) {
            *(LAS u32x4*)(lds + (2 * e) * CPY + tid * 16) = (u32x4){W[e], W[e + 1], W[e + 2], W[e + 3]};
            *(LAS u32x4*)(lds + (2 * e + 1) * CPY + tid * 16) = (u32x4){V[e], V[e + 1], V[e + 2], V[e + 3]};
        }
    }
    __syncthreads();
    f32x16 a0 = {}, a1 = {};
    {
        LAS const char* pa = lds + (7 - (r32 & 7)) * CPY + (255 - (r32 >> 3) + hi) * 16 - 64 * (8 * w + 7) - 2048;
        LAS const char* pb = lds + L2_U + (32 + r32 - 8 * w - 7) * 80 + 16 * hi;
#pragma unroll
        for (int mm = 0; mm < 8; ++mm)
#pragma unroll
            for (int s = 0; s < 2; ++s) {
                const bf16x8 ta = FR(pa, 64 * (7 - mm) + 2048 + 32 * s), tb = FR(pa, 64 * (7 - mm) + 32 * s);
                const bf16x8 b0 = FR(pb, 80 * (7 - mm) + 32 * s), b1 = FR(pb, 80 * (7 - mm) + 32 * s + 2560);
                a0 = MFMA_BF(ta, b0, a0); a1 = MFMA_BF(ta, b1, a1); a1 = MFMA_BF(tb, b0, a1);
            }
    }
    __syncthreads();
    {
        LAS char* pp = lds + w * 9216 + r32 * 144 + hi * 16;
#pragma unroll
        for (int g = 0; g < 4; ++g) {
            *(LAS f32x4*)(pp + g * 32) = (f32x4){a0[4 * g], a0[4 * g + 1], a0[4 * g + 2], a0[4 * g + 3]};
            *(LAS f32x4*)(pp + 32 * 144 + g * 32) = (f32x4){a1[4 * g], a1[4 * g + 1], a1[4 * g + 2], a1[4 * g + 3]};
        }
    }
    __syncthreads();
    {
        LAS const char* pp = lds + (tid >> 2) * 144 + (tid & 3) * 32;
        f32x4 y0 = *(LAS const f32x4*)pp, y1 = *(LAS const f32x4*)(pp + 16);
#pragma unroll
        for (int ww = 1; ww < 4; ++ww) { y0 += *(LAS const f32x4*)(pp + ww * 9216); y1 += *(LAS const f32x4*)(pp + ww * 9216 + 16); }
        const float y[8] = {y0[0], y0[1], y0[2], y0[3], y1[0], y1[1], y1[2], y1[3]};
        u32x4 o;
#pragma unroll
        for (int e = 0; e < 4; ++e) {
            const unsigned yb = pk_bf16(y[2 * e], y[2 * e + 1]), sk = pk_bf16(bf_lo(uu[e]) * dsk, bf_hi(uu[e]) * dsk);
            o[e] = pk_bf16(bf_lo(yb) + bf_lo(sk), bf_hi(yb) + bf_hi(sk));
        }
        ((u32x4*)(ws + WS_BT + (size_t)ch * S * 2))[tid] = o;
    }
}

__global__ __launch_bounds__(256, 3) void k2(char* __restrict__ ws, const uint16_t* __restrict__ dskip) {
    extern __shared__ __attribute__((aligned(16))) char smem[];
    if (blockIdx.x < 256) attn_block(blockIdx.x, (LAS char*)smem, ws);
    else conv_block(blockIdx.x - 256, (LAS char*)smem, ws, dskip);
}

constexpr int L3_T = 131072, L3_BYTES = L3_T + 8192;
__global__ __launch_bounds__(256, 1) void k3(const char* __restrict__ ws, uint16_t* __restrict__ out) {
    extern __shared__ __attribute__((aligned(16))) char smem[];
    LAS char* lds = (LAS char*)smem;
    const unsigned lds0 = (unsigned)(uintptr_t)lds;
    const int b = blockIdx.x, tid = threadIdx.x, lane = tid & 63, r32 = lane & 31, hi = lane >> 5;
    const int w = __builtin_amdgcn_readfirstlane(tid >> 6);
    const int rp = b / 7, cp = b % 7;
    {
        const char* src = w < 2 ? ws + WS_O + (size_t)(2 * rp + w) * 32768 : ws + WS_WO + (size_t)(2 * cp + w - 2) * 32768;
#pragma unroll
        for (int i = 0; i < 32; ++i) glds16(src + i * 1024, lane * 16, lds0 + w * 32768 + i * 1024);
        const unsigned bo = (unsigned)((64 * cp + 16 * w + (lane >> 3)) * S + 64 * rp + (lane & 7) * 8) * 2;
        glds16(ws + WS_BT, bo, lds0 + L3_T + w * 2048);
        glds16(ws + WS_BT, bo + 8 * S * 2, lds0 + L3_T + w * 2048 + 1024);
    }
    f32x16 acc = {};
    LAS const char* po = lds + (w >> 1) * 32768 + lane * 16;
    LAS const char* pw = lds + (2 + (w & 1)) * 32768 + lane * 16;
#define K3STG(j, N) do { asm volatile("s_waitcnt vmcnt(" #N ")\n\ts_barrier" ::: "memory"); \
        _Pragma("unroll") for (int i = 8 * (j); i < 8 * (j) + 8; ++i) acc = MFMA_BF(FR(pw, i * 1024), FR(po, i * 1024), acc); } while (0)
    K3STG(0, 26); K3STG(1, 18); K3STG(2, 10); K3STG(3, 2);
    {
        uint16_t* op = out + (size_t)(64 * rp + 32 * (w >> 1) + r32) * 2 * D + 64 * cp + 32 * (w & 1) + 4 * hi;
#pragma unroll
        for (int g = 0; g < 4; ++g) *(u32x2*)(op + 8 * g) = (u32x2){pk_bf16(acc[4 * g], acc[4 * g + 1]), pk_bf16(acc[4 * g + 2], acc[4 * g + 3])};
    }
    asm volatile("s_waitcnt vmcnt(4)" ::: "memory");
    {
        u32x4 o0, o1; transpose16(lds + L3_T + w * 2048, lane, o0, o1);
        u32x4* dst = (u32x4*)(out + (size_t)(64 * rp + lane) * 2 * D + D + 64 * cp + 16 * w);
        dst[0] = o0; dst[1] = o1;
    }
}
}

extern "C" void kernel_launch(void* const* d_in, const int* in_sizes, int n_in, void* d_out, int out_size, void* d_ws, size_t ws_size, hipStream_t stream) {
    using namespace k;
    (void)hipFuncSetAttribute((const void*)k1, hipFuncAttributeMaxDynamicSharedMemorySize, L1_BYTES);
    (void)hipFuncSetAttribute((const void*)k3, hipFuncAttributeMaxDynamicSharedMemorySize, L3_BYTES);
    const uint16_t* const* in = (const uint16_t* const*)d_in;
    k1<<<256, 256, L1_BYTES, stream>>>(in[0], in[1], in[2], in[3], in[4], in[5], in[6], in[7], in[8], (char*)d_ws);
    k2<<<256 + 448, 256, L2_BYTES, stream>>>((char*)d_ws, in[9]);
    k3<<<224, 256, L3_BYTES, stream>>>((const char*)d_ws, (uint16_t*)d_out);
}
```

```cpp
#include <hip/hip_runtime.h>
#include <hip/hip_bf16.h>
#include <stdint.h>

typedef short bf16x8 __attribute__((ext_vector_type(8)));
typedef _Float16 f16x8 __attribute__((ext_vector_type(8)));
typedef _Float16 f16x2 __attribute__((ext_vector_type(2)));
typedef __bf16 bf16x2 __attribute__((ext_vector_type(2)));
typedef short s16x4 __attribute__((ext_vector_type(4)));
typedef float f32x16 __attribute__((ext_vector_type(16)));
typedef float f32x4 __attribute__((ext_vector_type(4)));
typedef float f32x2 __attribute__((ext_vector_type(2)));
typedef unsigned u32x4 __attribute__((ext_vector_type(4)));
typedef unsigned u32x2 __attribute__((ext_vector_type(2)));
typedef unsigned long long u64;
#define LAS __attribute__((address_space(3)))
#define GAS __attribute__((address_space(1)))
#define DI __device__ __forceinline__
#define MFMA_BF(a, b, c) __builtin_amdgcn_mfma_f32_32x32x16_bf16((a), (b), (c), 0, 0, 0)
#define MFMA16(a, b, c) __builtin_amdgcn_mfma_f32_16x16x32_bf16((a), (b), (c), 0, 0, 0)
#define MFMA_F16(a, b, c) __builtin_amdgcn_mfma_f32_32x32x16_f16((a), (b), (c), 0, 0, 0)
#define SBAR() __builtin_amdgcn_sched_barrier(0)
#define CFENCE() asm volatile("" ::: "memory")
#define PIN(x) asm volatile("" : "+v"(x))
#define BC8(x) __builtin_bit_cast(bf16x8, (x))

#define PRIO_A1 0
#define PRIO_A2 0
#define PRIO_B 1
#define WOSL 2
#define PRIO_B2 1
#define RHO ((2 * (bi & 3) + (bi >> 2)) % 7)
#define KVSL 2
#define OSL 2
#define WOAUX 16
#define NCONV 4
#define KVLD glds16c
#define NOPF
namespace k {
#define STAMP(i)
constexpr int S = 2048, D = 448, DH = 28, NS = 256;
constexpr unsigned FS = 128;
constexpr unsigned FL_KV = FS, FL_O = FL_KV + 1024 * FS, FL_UT = FL_O + 1024 * FS, FL_BT = FL_UT + 896 * FS, FL_WO = FL_BT + 896 * FS;
constexpr unsigned WS_KV = 512u << 10;
constexpr unsigned WS_O = WS_KV + (4u << 20);
constexpr unsigned WS_WO = WS_O + (2u << 20);
constexpr unsigned WS_UT = WS_WO + (448u << 10);
constexpr unsigned WS_BT = WS_UT + D * S * 2;
constexpr unsigned WS_END = WS_BT + D * S * 2;
constexpr int STG = 28672, LB = 86016, LQ = 0, LX = 8192;
constexpr int TROW = 272, TABW = 53 * TROW, CBW = 2 * TABW, CPY = 4160, EXO = 4 * CPY;
constexpr int LU = LB + 2 * CBW, UIMG = 7680, LKT = LU + 2 * UIMG, KTW = 1312, LSY = LKT + 2 * KTW, LDS_BYTES = LSY + 256;
static_assert(LDS_BYTES <= 163840 && (LSY & 15) == 0, "lds");
enum { SY_TAG = 0, SY_F = 2, SY_P = 10, SY_WO = 18, SY_BG = 19, SY_Q = 20, SY_X = 21, SY_G = 22, SY_B2 = 23, SY_T1 = 24 };
constexpr int SPIN = 1 << 18;

struct IdxTab { uint16_t v[256][12]; };
constexpr IdxTab make_idx() {
    IdxTab t = {};
    for (int y = 0; y < 256; ++y) for (int e = 0; e < 12; ++e) {
        const int j = 2047 - 8 * y - e; int ix = 644;
        if (j >= 0) { const int sh = j >= 1024 ? 3 : (j >= 512 ? 2 : (j >= 256 ? 1 : 0));
            ix = sh ? 128 * sh + ((j + (1 << (sh - 1)) - 1 + ((j >> sh) & 1)) >> sh) : j; }
        t.v[y][e] = (uint16_t)(2 * ix);
    }
    return t;
}
__device__ const IdxTab g_idx __attribute__((aligned(16))) = make_idx();

DI unsigned pk_bf16(float a, float b) { f32x2 v = {a, b}; return __builtin_bit_cast(unsigned, __builtin_convertvector(v, bf16x2)); }
DI unsigned pk_f16(f32x2 v) { return __builtin_bit_cast(unsigned, __builtin_convertvector(v, f16x2)); }
DI float bf_lo(unsigned w) { return __uint_as_float(w << 16); }
DI float bf_hi(unsigned w) { return __uint_as_float(w & 0xffff0000u); }
DI float rbf(float x) { return bf_lo(pk_bf16(x, x)); }
DI void glds16(const void* sbase, unsigned voff, unsigned lds_base) {
    unsigned sv;
    asm volatile("s_mov_b32 %0, m0\n\ts_mov_b32 m0, %3\n\ts_nop 0\n\tglobal_load_lds_dwordx4 %1, %2\n\ts_mov_b32 m0, %0"
                 : "=&s"(sv) : "v"(voff), "s"(sbase), "s"(lds_base) : "memory");
}
DI void glds16c(const void* sbase, unsigned voff, unsigned lds_base) {
    unsigned sv;
    asm volatile("s_mov_b32 %0, m0\n\ts_mov_b32 m0, %3\n\ts_nop 0\n\tglobal_load_lds_dwordx4 %1, %2 sc1\n\ts_mov_b32 m0, %0"
                 : "=&s"(sv) : "v"(voff), "s"(sbase), "s"(lds_base) : "memory");
}
#define GL1(V, O, C) "global_load_lds_dwordx4 " V ", %1 offset:" #O C "\n\t"
DI void glds4(const void* sbase, unsigned v0, unsigned v1, unsigned v2, unsigned v3, unsigned lds_base) {
    unsigned sv;
    asm volatile("s_mov_b32 %0, m0\n\ts_mov_b32 m0, %2\n\ts_nop 0\n\t" GL1("%3", 0, "") GL1("%4", 1024, "") GL1("%5", 2048, "") GL1("%6", 3072, "") "s_mov_b32 m0, %0"
                 : "=&s"(sv) : "s"(sbase), "s"(lds_base), "v"(v0), "v"(v1), "v"(v2), "v"(v3) : "memory");
}
DI void glds3(const void* sbase, unsigned v0, unsigned v1, unsigned v2, unsigned lds_base) {
    unsigned sv;
    asm volatile("s_mov_b32 %0, m0\n\ts_mov_b32 m0, %2\n\ts_nop 0\n\t" GL1("%3", 0, "") GL1("%4", 1024, "") GL1("%5", 2048, "") "s_mov_b32 m0, %0"
                 : "=&s"(sv) : "s"(sbase), "s"(lds_base), "v"(v0), "v"(v1), "v"(v2) : "memory");
}
template <int OFF> DI void glds1(const void* sbase, unsigned v0, unsigned lds_base) {
    unsigned sv;
    asm volatile("s_mov_b32 %0, m0\n\ts_mov_b32 m0, %2\n\ts_nop 0\n\tglobal_load_lds_dwordx4 %3, %1 offset:%4\n\ts_mov_b32 m0, %0" : "=&s"(sv) : "s"(sbase), "s"(lds_base), "v"(v0), "i"(OFF) : "memory");
}
DI void glds2(const void* sbase, unsigned v0, unsigned v1, unsigned lds_base) {
    unsigned sv;
    asm volatile("s_mov_b32 %0, m0\n\ts_mov_b32 m0, %2\n\ts_nop 0\n\t" GL1("%3", 0, "") GL1("%4", 1024, "") "s_mov_b32 m0, %0"
                 : "=&s"(sv) : "s"(sbase), "s"(lds_base), "v"(v0), "v"(v1) : "memory");
}
DI void glds4c(const void* sbase, unsigned v0, unsigned lds_base) {
    unsigned sv;
    asm volatile("s_mov_b32 %0, m0\n\ts_mov_b32 m0, %2\n\ts_nop 0\n\t" GL1("%3", 0, " sc1") GL1("%3", 1024, " sc1") GL1("%3", 2048, " sc1") GL1("%3", 3072, " sc1") "s_mov_b32 m0, %0"
                 : "=&s"(sv) : "s"(sbase), "s"(lds_base), "v"(v0) : "memory");
}
DI void glds2c(const void* sbase, unsigned v0, unsigned v1, unsigned lds_base) {
    unsigned sv;
    asm volatile("s_mov_b32 %0, m0\n\ts_mov_b32 m0, %2\n\ts_nop 0\n\t" GL1("%3", 0, " sc1") GL1("%4", 1024, " sc1") "s_mov_b32 m0, %0"
                 : "=&s"(sv) : "s"(sbase), "s"(lds_base), "v"(v0), "v"(v1) : "memory");
}
DI float wave_max(float v) {
#define DPPMAX(ctrl, rm) v = fmaxf(v, __uint_as_float(__builtin_amdgcn_update_dpp(0, __float_as_uint(v), ctrl, rm, 0xf, true)))
    DPPMAX(0x111, 0xf); DPPMAX(0x112, 0xf); DPPMAX(0x114, 0xf); DPPMAX(0x118, 0xf); DPPMAX(0x142, 0xa); DPPMAX(0x143, 0xc);
    return __uint_as_float(__builtin_amdgcn_readlane(__float_as_uint(v), 63));
}
DI s16x4 trread(LAS const char* p) { return __builtin_bit_cast(s16x4, __builtin_amdgcn_ds_read_tr16_b64_v4i16((LAS s16x4*)p)); }
DI bf16x8 cat8(s16x4 a, s16x4 b) { return __builtin_shufflevector(a, b, 0, 1, 2, 3, 4, 5, 6, 7); }
DI f32x2 cmul(f32x2 a, f32x2 b, f32x2 bs) { return __builtin_elementwise_fma(a.yy, bs, a.xx * b); }
DI f32x2 rot(f32x2 b) { return (f32x2){-b.y, b.x}; }
DI f32x2 anchor(float a2, float rev, float s) {
    const float mag = __builtin_amdgcn_exp2f(a2 * s);
    float r = rev * s; r -= __builtin_rintf(r);
    return (f32x2){mag * __builtin_amdgcn_cosf(r), mag * __builtin_amdgcn_sinf(r)};
}
DI void transpose16(LAS const char* lds, int lane, u32x4& o0, u32x4& o1) {
    LAS const char* p = lds + ((lane & 15) >> 2) * 128 + (lane >> 4) * 32 + (lane & 3) * 8;
    const s16x4 a = trread(p), b = trread(p + 512), c = trread(p + 1024), d = trread(p + 1536);
    o0 = __builtin_bit_cast(u32x4, cat8(a, b)); o1 = __builtin_bit_cast(u32x4, cat8(c, d));
}
DI void arrive(LAS unsigned* c, int) {
    asm volatile("s_mov_b64 exec, 1\n\tds_add_u32 %0, %1\n\ts_mov_b64 exec, -1" :: "v"((unsigned)(uintptr_t)c), "v"(1u) : "memory");
}
DI void await(LAS unsigned* c, unsigned n) {
    unsigned t, i = SPIN;
    asm volatile("1:\n\tds_read_b32 %0, %2\n\ts_waitcnt lgkmcnt(0)\n\tv_cmp_gt_u32 vcc, %3, %0\n\ts_cbranch_vccz 2f\n\ts_sleep 1\n\ts_sub_u32 %1, %1, 1\n\ts_cbranch_scc0 1b\n2:"
                 : "=&v"(t), "+s"(i) : "v"((unsigned)(uintptr_t)c), "s"(n) : "vcc", "scc", "memory");
}
DI u64 get_tag(LAS unsigned* sy) {
    u64 t = 0;
#pragma unroll 1
    for (int i = 0; i < SPIN; ++i) {
        const u64 v = __hip_atomic_load((LAS u64*)sy, __ATOMIC_RELAXED, __HIP_MEMORY_SCOPE_WORKGROUP);
        t = ((u64)(unsigned)__builtin_amdgcn_readfirstlane((unsigned)(v >> 32)) << 32) | (unsigned)__builtin_amdgcn_readfirstlane((unsigned)v);
        if (t) break;
        __builtin_amdgcn_s_sleep(1);
    }
    return t;
}
DI void drain() { asm volatile("s_waitcnt vmcnt(0)" ::: "memory"); }
DI void put_flag(char* ws, unsigned off, u64 tag, int lane) { if (lane == 0) __hip_atomic_store((GAS u64*)(ws + off), tag, __ATOMIC_RELAXED, __HIP_MEMORY_SCOPE_AGENT); }
template <int SLP> DI void wait_flags(char* ws, unsigned off, u64 tag, bool pend) {
    GAS u64* p = (GAS u64*)(ws + off);
    CFENCE();
#pragma unroll 1
    for (int i = 0; i < SPIN; ++i) {
        if (pend) pend = __hip_atomic_load(p, __ATOMIC_RELAXED, __HIP_MEMORY_SCOPE_AGENT) != tag;
        if (!__any(pend)) break;
        __builtin_amdgcn_s_sleep(SLP);
    }
    CFENCE();
}
DI void await_slow(LAS unsigned* c, unsigned n) {
#pragma unroll 1
    for (int i = 0; i < SPIN && __hip_atomic_load(c, __ATOMIC_RELAXED, __HIP_MEMORY_SCOPE_WORKGROUP) < n; ++i) __builtin_amdgcn_s_sleep(4);
    CFENCE();
}
#define BST(v, off) __builtin_amdgcn_raw_buffer_store_b128((v), rs, (off), 0, 16)
#define BLD(off) __builtin_amdgcn_raw_buffer_load_b128(rs, (off), 0, 16)
#define FR(p, off) (*(LAS const bf16x8*)((p) + (off)))
DI u32x4 widen(unsigned a0, unsigned a1, unsigned b0, unsigned b1) {
    const auto r0 = __builtin_amdgcn_permlane32_swap(a0, b0, false, false), r1 = __builtin_amdgcn_permlane32_swap(a1, b1, false, false);
    return (u32x4){r0[0], r1[0], r0[1], r1[1]};
}

#define BAR() asm volatile("s_waitcnt lgkmcnt(0)\n\ts_barrier" ::: "memory")
#define EX(x) __builtin_amdgcn_exp2f(x)
DI float swapmax(float m) { const auto r = __builtin_amdgcn_permlane32_swap(__float_as_uint(m), __float_as_uint(m), false, false); return fmaxf(__uint_as_float(r[0]), __uint_as_float(r[1])); }
DI u32x4 put_ref(u32x4 q1, float m, int hi) {
    const float m1 = rbf(m), m2 = rbf(m - m1), m3 = m - m1 - m2;
    if (hi) { q1[2] = pk_bf16(-m1, -m2); q1[3] = pk_bf16(-m3, 0.f); }
    return q1;
}
DI u64 mix(u64 e) { u64 z = e + 0x9E3779B97F4A7C15ull; z = (z ^ (z >> 30)) * 0xBF58476D1CE4E5B9ull; z = (z ^ (z >> 27)) * 0x94D049BB133111EBull; return (z ^ (z >> 31)) | 1ull; }

DI void role_a(const uint16_t* __restrict__ x, const uint16_t* __restrict__ wqkv, char* __restrict__ ws, LAS char* lds, int w, int lane) {
    const int head = 4 * (blockIdx.x & 3) + ((blockIdx.x >> 3) & 3), grp = 8 * ((blockIdx.x >> 2) & 1) + (blockIdx.x >> 5);
    const unsigned lds0 = (unsigned)(uintptr_t)lds;
    LAS unsigned* sy = (LAS unsigned*)(lds + LSY);
    const __amdgpu_buffer_rsrc_t rs = __builtin_amdgcn_make_buffer_rsrc(ws, 0, WS_END, 0x00020000);
    const int bi = blockIdx.x >> 3, r32 = lane & 31, hi = lane >> 5;
    const int wsh = (head & 7) == 7 ? 4 : 0, rho = RHO;
    __builtin_amdgcn_s_setprio(PRIO_A1);
    const unsigned wv = (unsigned)((lane >> 2) * 3 * D + (lane & 3) * 8) * 2;
    const unsigned xv0 = (unsigned)((lane >> 3) * D + (((lane & 7) ^ (lane >> 4)) * 8)) * 2, xv1 = (unsigned)((lane >> 3) * D + (((lane & 7) ^ (4 + (lane >> 4))) * 8)) * 2;
    const unsigned xv[4] = {xv0, xv1 + 16 * D - 1024, xv0 + 32 * D - 2048, xv1 + 48 * D - 3072};
    const unsigned wvv[3] = {wv + 2048, wv + 2 * D + 1024, wv + 4 * D};
    const char* wb = (const char*)(wqkv + DH * head - wsh + (size_t)(16 * w) * 3 * D) - 2048;
    const char* xb = (const char*)(x + (size_t)(128 * grp + 32 * w) * D);
#define LOADX(sp, sl) glds4(xb + (sp) * 128, xv[0], xv[1], xv[2], xv[3], lds0 + (sl) + w * 4096)
#define LOADW(sp, sl) glds3(wb + (size_t)(sp) * 64 * 3 * D * 2, wvv[0], wvv[1], wvv[2], lds0 + (sl) + 16384 + w * 3072)
    int sp = rho;
    LOADW(sp, 0); LOADX(sp, 0); sp = sp == 6 ? 0 : sp + 1;
    asm volatile("s_barrier" ::: "memory");
    LOADW(sp, STG); LOADX(sp, STG); sp = sp == 6 ? 0 : sp + 1;
    STAMP(1);
    const int xs = (r32 >> 1) & 7;
    LAS const char* xp[4];
#pragma unroll
    for (int ks = 0; ks < 4; ++ks) xp[ks] = lds + w * 4096 + r32 * 128 + (((2 * ks + hi) ^ xs) * 16);
    LAS const char* wp = lds + 16384 + (8 * hi + ((lane & 15) >> 2)) * 64 + ((lane >> 4) & 1) * 32 + (lane & 3) * 8 + wsh * 2;
    f32x16 aq = {}, ak = {}, av = {};
    int so = 0;
    bf16x8 xf[4]; s16x4 fr[4][6];
#define RX(ks, o) xf[ks] = FR(xp[ks], o)
#define RW(ks, j, o) fr[ks][j] = trread(wp + (o) + (ks) * 3072 + ((((j) >> 1) + 1) % 3) * 1024 + ((j) & 1) * 256)
#define MK(ks) ak = MFMA_BF(cat8(fr[ks][0], fr[ks][1]), xf[ks], ak); SBAR()
#define MV(ks) av = MFMA_BF(xf[ks], cat8(fr[ks][2], fr[ks][3]), av); SBAR()
#define MQ(ks) aq = MFMA_BF(cat8(fr[ks][4], fr[ks][5]), xf[ks], aq); SBAR()
#define QSTEP(ks, L0, L1) do { \
            MK(ks); RX(ks + 1, so); RW(ks + 1, 0, so); RW(ks + 1, 1, so); SBAR(); \
            MV(ks); RW(ks + 1, 2, so); RW(ks + 1, 3, so); SBAR(); if (LD) { L0; } \
            MQ(ks); RW(ks + 1, 4, so); RW(ks + 1, 5, so); SBAR(); if (LD) { L1; } } while (0)
#define STAGE(LD_, NXT, VM) do { constexpr bool LD = LD_; \
        const char* wg = wb + (size_t)sp * 64 * 3 * D * 2; const char* xg = xb + sp * 128; const unsigned lf = lds0 + sf; \
        QSTEP(0, glds2(wg, wvv[0], wvv[1], lf + 16384 + w * 3072), glds1<2048>(wg, wvv[2], lf + 16384 + w * 3072)); \
        QSTEP(1, glds2(xg, xv[0], xv[1], lf + w * 4096), (glds1<2048>(xg, xv[2], lf + w * 4096))); \
        QSTEP(2, (glds1<3072>(xg, xv[3], lf + w * 4096)), ); \
        asm volatile("s_waitcnt vmcnt(" #VM ")" ::: "memory"); \
        arrive(fc, lane); SBAR(); \
        MK(3); await(fc, 4); ++fc; \
        sf = so; so = so == 2 * STG ? 0 : so + STG; sp = sp == 6 ? 0 : sp + 1; \
        if (NXT) { RX(0, so); RW(0, 0, so); RW(0, 1, so); RW(0, 2, so); } SBAR(); \
        MV(3); if (NXT) { RW(0, 3, so); RW(0, 4, so); RW(0, 5, so); } SBAR(); \
        MQ(3); } while (0)
    LAS unsigned* fc = sy + SY_F;
    int sf = 2 * STG;
    asm volatile("s_waitcnt vmcnt(7)" ::: "memory");
    arrive(fc, lane); await(fc, 4); ++fc;
    STAMP(16);
    RX(0, 0); RW(0, 0, 0); RW(0, 1, 0); RW(0, 2, 0); RW(0, 3, 0); RW(0, 4, 0); RW(0, 5, 0); SBAR();
#pragma unroll 1
    for (int st = 0; st < 5; ++st) STAGE(true, true, 7);
    const u64 tag = get_tag(sy);
    STAGE(false, true, 0);
    STAGE(false, false, 0);
    STAMP(3);
    const float cq = 0.18898223650461362f * 1.4426950408889634f;
    u32x4 q0, q1, k0, k1, v0, v1;
#pragma unroll
    for (int i = 0; i < 4; ++i) {
        k0[i] = pk_bf16(ak[2 * i], ak[2 * i + 1]); k1[i] = pk_bf16(ak[8 + 2 * i], ak[9 + 2 * i]);
        v0[i] = pk_bf16(av[2 * i], av[2 * i + 1]); v1[i] = pk_bf16(av[8 + 2 * i], av[9 + 2 * i]);
        if (r32 >= DH) { v0[i] = v1[i] = (r32 == DH ? 0x3f803f80u : 0u); }
    }
    if (hi) { k1[2] = 0x3f803f80u; k1[3] = 0x00003f80u; }
    const unsigned tb = WS_KV + (unsigned)(head * 64 + 4 * grp + w) * 4096 + lane * 16;
    BST(k0, tb); BST(k1, tb + 1024); BST(v0, tb + 2048); BST(v1, tb + 3072);
#pragma unroll
    for (int i = 0; i < 4; ++i) { q0[i] = pk_bf16(aq[2 * i] * cq, aq[2 * i + 1] * cq); q1[i] = pk_bf16(aq[8 + 2 * i] * cq, aq[9 + 2 * i] * cq); }
    if (hi) { q1[2] = 0; q1[3] = 0; }
    {
        const f32x16 zero = {};
        f32x16 s = MFMA_BF(BC8(k0), BC8(q0), zero); s = MFMA_BF(BC8(k1), BC8(q1), s);
        float m = s[0];
#pragma unroll
        for (int i = 1; i < 16; ++i) m = fmaxf(m, s[i]);
        q1 = put_ref(q1, swapmax(m), hi);
    }
    LAS u32x4* qo = (LAS u32x4*)(lds + LQ + w * 2048) + lane;
    qo[0] = q0; qo[64] = q1;
    arrive(sy + SY_Q, lane);
    drain();
    put_flag(ws, FL_KV + (unsigned)((head * 4 + w) * 16 + grp) * FS, tag, lane);
    STAMP(4);
    await(sy + SY_Q, 4);
    __builtin_amdgcn_s_setprio(PRIO_A2);
    const unsigned voff = lane * 16;
    const f32x16 zero = {};
    bf16x8 Q[4][2];
#pragma unroll
    for (int i = 0; i < 4; ++i) { LAS const char* p = lds + LQ + ((w + i) & 3) * 2048 + voff; Q[i][0] = FR(p, 0); Q[i][1] = FR(p, 1024); }
    u32x4 kv[3][4] = {{k0, k1, v0, v1}, {}, {}};
    f32x16 O[4] = {}, sa, sb;
    u32x4 p0, p1;
#define GRP(SC, i, P, j) do { float e0 = EX(SC[i]); SBAR(); float e1 = EX(SC[i + 1]); SBAR(); float e2 = EX(SC[i + 2]); SBAR(); float e3 = EX(SC[i + 3]); SBAR(); \
        P[j] = pk_bf16(e0, e1); SBAR(); P[j + 1] = pk_bf16(e2, e3); PIN(P); SBAR(); } while (0)
#define SLOT(SC, SN, cb, i, QK, PV1, LD) do { constexpr int nb_ = (i) < 3 ? (cb) : ((cb) + 1) % 3, pb_ = (i) > 0 ? (cb) : ((cb) + 2) % 3; \
        if (QK) SN = MFMA_BF(BC8(kv[nb_][0]), Q[((i) + 1) & 3][0], zero); SBAR(); GRP(SC, 0, p0, 0); \
        if (PV1) O[((i) + 3) & 3] = MFMA_BF(BC8(kv[pb_][3]), BC8(p1), O[((i) + 3) & 3]); SBAR(); \
        if (LD) { kv[((cb) + 2) % 3][i] = __builtin_amdgcn_raw_buffer_load_b128(rs, voff + (i) * 1024, kvo, 16); SBAR(); } GRP(SC, 4, p0, 2); \
        if (QK) SN = MFMA_BF(BC8(kv[nb_][1]), Q[((i) + 1) & 3][1], SN); SBAR(); GRP(SC, 8, p1, 0); \
        O[i] = MFMA_BF(BC8(kv[cb][2]), BC8(p0), O[i]); SBAR(); GRP(SC, 12, p1, 2); } while (0)
    const unsigned kvb = WS_KV + (unsigned)(head * 64 + w) * 4096;
    unsigned kvo = 0;
    sa = MFMA_BF(BC8(k0), Q[0][0], zero); sa = MFMA_BF(BC8(k1), Q[0][1], sa);
    SLOT(sa, sb, 0, 0, true, false, false); SLOT(sb, sa, 0, 1, true, true, false); SLOT(sa, sb, 0, 2, true, true, false); SLOT(sb, sa, 0, 3, false, true, false);
    STAMP(5);
    wait_flags<KVSL>(ws, FL_KV + (unsigned)((head * 4 + w) * 16 + lane) * FS, tag, lane < 16 && lane != grp);
    arrive(sy + SY_G, lane);
    STAMP(6);
#pragma unroll
    for (int j = 1; j < 3; ++j)
#pragma unroll
        for (int f = 0; f < 4; ++f) kv[j][f] = __builtin_amdgcn_raw_buffer_load_b128(rs, voff + f * 1024, kvb + ((grp + j) & 15) * 16384, 16);
    sa = MFMA_BF(BC8(kv[1][0]), Q[0][0], zero); sa = MFMA_BF(BC8(kv[1][1]), Q[0][1], sa);
    STAMP(7);
    int nx = grp + 3;
#define TILE(cb) do { kvo = kvb + (nx & 15) * 16384; ++nx; SBAR(); \
        SLOT(sa, sb, cb, 0, true, true, true); SLOT(sb, sa, cb, 1, true, true, true); SLOT(sa, sb, cb, 2, true, true, true); SLOT(sb, sa, cb, 3, true, true, true); } while (0)
#pragma unroll 1
    for (int it = 0; it < 5; ++it) { TILE(1); TILE(2); TILE(0); }
    O[3] = MFMA_BF(BC8(kv[0][3]), BC8(p1), O[3]);
    STAMP(8);
    f32x16 o = O[0];
    {
#pragma unroll
        for (int i = 1; i < 4; ++i) {
            LAS char* ex = lds + LX + (((w + i) & 3) * 3 + i - 1) * 4096 + voff;
#pragma unroll
            for (int g = 0; g < 4; ++g) *(LAS f32x4*)(ex + g * 1024) = (f32x4){O[i][4 * g], O[i][4 * g + 1], O[i][4 * g + 2], O[i][4 * g + 3]};
        }
        arrive(sy + SY_X, lane); await(sy + SY_X, 4);
        LAS const char* er = lds + LX + w * 3 * 4096 + voff;
#pragma unroll
        for (int i = 0; i < 3; ++i)
#pragma unroll
            for (int g = 0; g < 4; ++g) { const f32x4 r = *(LAS const f32x4*)(er + i * 4096 + g * 1024);
#pragma unroll
                for (int e = 0; e < 4; ++e) o[4 * g + e] += r[e]; }
        f32x2 chk = {0.f, 0.f};
#pragma unroll
        for (int i = 0; i < 8; ++i) chk = __builtin_elementwise_fma((f32x2){o[2 * i], o[2 * i + 1]}, (f32x2){0.f, 0.f}, chk);
        if (__builtin_expect(__any(!(chk[0] + chk[1] == 0.f) || !(o[12] < 1e30f)), 0)) {
            const unsigned hb = WS_KV + (unsigned)head * 64 * 4096 + voff;
            u32x4 qr = __builtin_bit_cast(u32x4, Q[0][1]); if (hi) { qr[2] = 0; qr[3] = 0; }
            float mm = -INFINITY;
            wait_flags<4>(ws, FL_KV + (unsigned)(head * 64 + lane) * FS, tag, true);
#pragma unroll 1
            for (int t = 0; t < 64; ++t) {
                f32x16 s = MFMA_BF(BC8(BLD(hb + t * 4096)), Q[0][0], zero); s = MFMA_BF(BC8(BLD(hb + t * 4096 + 1024)), BC8(qr), s);
#pragma unroll
                for (int i = 0; i < 16; ++i) mm = fmaxf(mm, s[i]);
            }
            qr = put_ref(qr, swapmax(mm), hi);
            o = zero;
#pragma unroll 1
            for (int t = 0; t < 64; ++t) {
                f32x16 s = MFMA_BF(BC8(BLD(hb + t * 4096)), Q[0][0], zero); s = MFMA_BF(BC8(BLD(hb + t * 4096 + 1024)), BC8(qr), s);
#pragma unroll
                for (int i = 0; i < 4; ++i) { p0[i] = pk_bf16(EX(s[2 * i]), EX(s[2 * i + 1])); p1[i] = pk_bf16(EX(s[8 + 2 * i]), EX(s[9 + 2 * i])); }
                o = MFMA_BF(BC8(BLD(hb + t * 4096 + 2048)), BC8(p0), o); o = MFMA_BF(BC8(BLD(hb + t * 4096 + 3072)), BC8(p1), o);
            }
        }
    }
    {
        const auto r = __builtin_amdgcn_permlane32_swap(__float_as_uint(o[12]), __float_as_uint(o[12]), false, false);
        const float rl = __builtin_amdgcn_rcpf(__uint_as_float(r[1]));
        u32x4 o0, o1;
#pragma unroll
        for (int i = 0; i < 4; ++i) { o0[i] = pk_bf16(o[2 * i] * rl, o[2 * i + 1] * rl); o1[i] = pk_bf16(o[8 + 2 * i] * rl, o[9 + 2 * i] * rl); }
        if (hi) { o1[2] = 0; o1[3] = 0; }
        const unsigned fi = (unsigned)((4 * grp + w) * 16 + head);
        BST(o0, WS_O + fi * 2048 + voff); BST(o1, WS_O + fi * 2048 + 1024 + voff);
        STAMP(9);
        drain();
        put_flag(ws, FL_O + fi * FS, tag, lane);
    }
    STAMP(10);
}

DI void role_b(const uint16_t* __restrict__ x, const uint16_t* __restrict__ wqkv, const uint16_t* __restrict__ wo, const uint16_t* __restrict__ u, const uint16_t* __restrict__ lar, const uint16_t* __restrict__ aim,
               const uint16_t* __restrict__ cre, const uint16_t* __restrict__ cim, const uint16_t* __restrict__ ldt, const uint16_t* __restrict__ dskip,
               uint16_t* __restrict__ out, char* __restrict__ ws, LAS char* lds, int w, int lane) {
    u64 tag;
    const unsigned lds0 = (unsigned)(uintptr_t)lds;
    LAS unsigned* sy = (LAS unsigned*)(lds + LSY);
    const __amdgpu_buffer_rsrc_t rs = __builtin_amdgcn_make_buffer_rsrc(ws, 0, WS_END, 0x00020000);
    const int b = blockIdx.x, xcd = b & 7, bi = b >> 3, r32 = lane & 31, hi = lane >> 5;
    __builtin_amdgcn_s_setprio(PRIO_B);
    if (w == 0) sy[lane] = 0;
#define KEEPPF()
    if (xcd < 7) {
        const int cc = w >> 1, hh = w & 1, pair = 32 * xcd + bi, ch = 2 * pair + cc;
        LAS char* cb = lds + LB + cc * CBW;
        LAS char* tab = cb + hh * TABW;
        const unsigned uo = (unsigned)((64 * bi + 16 * w + (lane >> 3)) * D + 64 * xcd + (lane & 7) * 8) * 2;
        glds2(u, uo, uo + 8 * D * 2 - 1024, lds0 + LU + w * 2048);
        const unsigned po = (unsigned)(ch * NS + 128 * hh) / 2 + lane;
        const unsigned pl = ((const unsigned*)lar)[po], pa = ((const unsigned*)aim)[po], pr = ((const unsigned*)cre)[po], pi = ((const unsigned*)cim)[po];
        const u64 e0 = __hip_atomic_load((GAS u64*)ws, __ATOMIC_RELAXED, __HIP_MEMORY_SCOPE_AGENT);
        u32x2 gi[2][3];
#pragma unroll
        for (int ps = 0; ps < 2; ++ps)
#pragma unroll
            for (int i = 0; i < 3; ++i) gi[ps][i] = ((const u32x2*)g_idx.v[128 * hh + 64 * ps + lane])[i];
        const unsigned lw = ((const unsigned*)ldt)[pair], dw = ((const unsigned*)dskip)[pair];
        asm volatile("s_waitcnt lgkmcnt(0)\n\ts_barrier" ::: "memory");
        drain(); KEEPPF();
        STAMP(1);
        arrive(sy + SY_WO, lane); await(sy + SY_WO, 4);
        {
            LAS const char* p = lds + LU + (16 * (lane >> 4) + ((lane & 15) >> 2)) * 128 + w * 32 + (lane & 3) * 8;
            const bf16x8 t0 = cat8(trread(p), trread(p + 512)), t1 = cat8(trread(p + 1024), trread(p + 1536));
            arrive(sy + SY_T1, lane);
            LAS char* sc = tab + (lane & 15) * 144 + (lane >> 4) * 32;
            *(LAS bf16x8*)sc = t0; *(LAS bf16x8*)(sc + 16) = t1;
            LAS const char* sr = tab + (lane >> 3) * 144 + (lane & 7) * 16;
            const u32x4 o0 = *(LAS const u32x4*)sr, o1 = *(LAS const u32x4*)(sr + 8 * 144);
            const unsigned to = WS_UT + (unsigned)((64 * xcd + 16 * w + (lane >> 3)) * S + 64 * bi) * 2 + (lane & 7) * 16;
            BST(o0, to); BST(o1, to + 8 * S * 2);
        }
        if (w == 0 && lane == 0) __hip_atomic_store((LAS u64*)sy, mix(e0), __ATOMIC_RELAXED, __HIP_MEMORY_SCOPE_WORKGROUP);
        const float dt = rbf(expf(cc ? bf_hi(lw) : bf_lo(lw)));
        float cm = fmaxf(fmaxf(fabsf(bf_lo(pr)), fabsf(bf_hi(pr))), fmaxf(fabsf(bf_lo(pi)), fabsf(bf_hi(pi))));
        cm = wave_max(cm);
        unsigned ce = __float_as_uint(cm) & 0x7f800000u; ce = ce < 0x01000000u ? 0x01000000u : (ce > 0x7e800000u ? 0x7e800000u : ce);
        const float cscale = __uint_as_float(0x7f000000u - ce), oscale = 2.0f * dt * __uint_as_float(ce);
        LAS unsigned* tw = (LAS unsigned*)(tab + lane * 4);
        LAS const char* trd = tab + r32 * TROW + hi * 16;
        f32x16 dacc = {};
        STAMP(13);
        f32x2 z1, z8, z256, x0;
#define ANCH(vl, va, vr, vi) do { const float a2 = (-rbf(expf(vl)) * dt) * 1.4426950408889634f, rev = ((va) * dt) * 0.15915494309189535f; \
            z1 = anchor(a2, -rev, 1.f); z8 = anchor(a2, rev, 8.f); z256 = anchor(a2, -rev, 256.f); x0 = (f32x2){(vr) * cscale, -(vi) * cscale}; } while (0)
#define PUTX(row, v) tw[(row) * (TROW / 4)] = pk_f16(v)
#define PUTY(col, v) tw[(21 + (col)) * (TROW / 4)] = pk_f16(v)
#define CHAIN() do { \
            const f32x2 z1s = rot(z1), zs = rot(z256), z8s = rot(z8); \
            f32x2 y[4], X[8]; y[1] = z8; y[2] = cmul(z8, z8, z8s); X[0] = x0; X[1] = cmul(x0, z1, z1s); \
            y[3] = cmul(y[2], z8, z8s); const f32x2 z32 = cmul(y[2], y[2], rot(y[2])), z32s = rot(z32); X[2] = cmul(X[1], z1, z1s); y[0] = z32; \
            tw[21 * (TROW / 4)] = 0x3c00u; PUTY(1, y[1]); PUTY(2, y[2]); PUTY(3, y[3]); PUTY(4, y[0]); PUTX(0, X[0]); PUTX(1, X[1]); PUTX(2, X[2]); \
            f32x2 a = cmul(x0, z256, zs), b2 = cmul(X[2], z256, zs), c4; PUTX(8, a); PUTX(9, b2); SBAR(); \
            _Pragma("unroll") for (int i = 1; i < 8; ++i) { \
                f32x2 t0, t1, t2, t3, t4, t5, t6; \
                if (i > 1) t0 = y[0].xx * z32; t1 = y[1].xx * z32; t2 = y[2].xx * z32; t3 = y[3].xx * z32; \
                if (i < 6) t4 = X[i + 1].xx * z1; t5 = a.xx * z256; \
                if (i == 3) t6 = X[4].xx * z256; if (i == 4 || i == 5) t6 = c4.xx * z256; if (i == 6) t6 = X[6].xx * z256; SBAR(); \
                if (i > 1) y[0] = __builtin_elementwise_fma(y[0].yy, z32s, t0); y[1] = __builtin_elementwise_fma(y[1].yy, z32s, t1); \
                y[2] = __builtin_elementwise_fma(y[2].yy, z32s, t2); y[3] = __builtin_elementwise_fma(y[3].yy, z32s, t3); \
                if (i < 6) X[i + 2] = __builtin_elementwise_fma(X[i + 1].yy, z1s, t4); a = __builtin_elementwise_fma(a.yy, zs, t5); \
                if (i == 3) c4 = __builtin_elementwise_fma(X[4].yy, zs, t6); if (i == 4 || i == 5) c4 = __builtin_elementwise_fma(c4.yy, zs, t6); \
                if (i == 6) c4 = __builtin_elementwise_fma(X[6].yy, zs, t6); SBAR(); \
                if (i > 1) PUTY(4 * i, y[0]); PUTY(4 * i + 1, y[1]); PUTY(4 * i + 2, y[2]); PUTY(4 * i + 3, y[3]); \
                if (i < 6) PUTX(i + 2, X[i + 2]); PUTX(i == 1 ? 12 : (i == 2 ? 14 : 13 + i), a); \
                if (i == 3) PUTX(10, c4); if (i == 4) PUTX(13, c4); if (i == 5) PUTX(15, c4); if (i == 6) PUTX(11, c4); SBAR(); \
            } } while (0)
        f16x8 fa[8], fb[8];
#define RDT() do { _Pragma("unroll") for (int kk = 0; kk < 8; ++kk) { fa[kk] = *(LAS const f16x8*)(trd + kk * 32); fb[kk] = *(LAS const f16x8*)(trd + 21 * TROW + kk * 32); } SBAR(); } while (0)
#define MMT() do { _Pragma("unroll") for (int kk = 0; kk < 8; ++kk) dacc = MFMA_F16(fa[kk], fb[kk], dacc); } while (0)
        ANCH(bf_lo(pl), bf_lo(pa), bf_lo(pr), bf_lo(pi));
        PIN(z1); PIN(z8); PIN(z256); SBAR();
        tag = get_tag(sy); drain(); put_flag(ws, FL_UT + (unsigned)(xcd * 128 + w * 32 + bi) * FS, tag, lane);
        CHAIN(); RDT();
        ANCH(bf_hi(pl), bf_hi(pa), bf_hi(pr), bf_hi(pi));
        PIN(z1); PIN(z8); PIN(z256); SBAR(); MMT(); SBAR();
        GAS u64* utf = (GAS u64*)(ws + FL_UT + (unsigned)(xcd * 128 + (bi >> 3) * 32 + r32) * FS);
        const u64 utv = __hip_atomic_load(utf, __ATOMIC_RELAXED, __HIP_MEMORY_SCOPE_AGENT); SBAR();
        CHAIN(); RDT();
        if (__any(utv != tag)) wait_flags<4>(ws, FL_UT + (unsigned)(xcd * 128 + (bi >> 3) * 32 + r32) * FS, tag, !hi);
        CFENCE();
        const unsigned ub = WS_UT + (unsigned)(ch * S + 1024 * hh) * 2 + lane * 16;
        const u32x4 u0 = BLD(ub), u1 = BLD(ub + 1024); SBAR();
        MMT();
        STAMP(2);
        {
            LAS float* pt = (LAS float*)tab;
            *(LAS f32x4*)(pt + 8 * r32 + 4 * hi) = (f32x4){dacc[0] * oscale, dacc[1] * oscale, dacc[2] * oscale, dacc[3] * oscale};
            *(LAS f32x2*)(pt + (hi ? 384 + 2 * r32 : 256 + 4 * r32)) = (f32x2){dacc[4] * oscale, dacc[5] * oscale};
            *(LAS f32x2*)(pt + (hi ? 448 + 2 * r32 : 258 + 4 * r32)) = (f32x2){dacc[6] * oscale, dacc[7] * oscale};
            pt[hi ? 640 + r32 : 512 + r32] = dacc[8] * oscale;
            pt[hi ? 672 + r32 : 544 + r32] = dacc[9] * oscale;
            pt[hi ? 672 + r32 : 576 + r32] = dacc[10] * oscale;
            pt[hi ? 672 + r32 : 608 + r32] = dacc[11] * oscale;
        }
        LAS unsigned* ps_ = sy + SY_P + 4 * cc;
        arrive(ps_ + 0, lane);
        LAS char* ui = lds + LU + cc * UIMG;
        LAS char* kt = lds + LKT + cc * KTW;
        await(sy + SY_T1, 4); await(ps_ + 0, 2);
#pragma unroll
        for (int ps = 0; ps < 2; ++ps) {
            const int v = 81 * hh + 64 * ps + lane;
            if (ps == 0 || lane < 17) {
                const f32x4 p0 = *(LAS const f32x4*)(cb + v * 16), p1 = *(LAS const f32x4*)(cb + TABW + v * 16);
                u32x2 r = {pk_bf16(p0[0] + p1[0], p0[1] + p1[1]), pk_bf16(p0[2] + p1[2], p0[3] + p1[3])};
                if (v == 161) r = (u32x2){0u, 0u};
                *(LAS u32x2*)(kt + v * 8) = r;
            }
        }
        const u32x4 z4 = {};
        *(LAS u32x4*)(ui + (64 * hh + lane) * 16) = z4;
        if (lane < 16) *(LAS u32x4*)(ui + (128 + 16 * hh + lane) * 16) = z4;
        arrive(ps_ + 1, lane); await(ps_ + 1, 2);
        STAMP(3);
        __builtin_amdgcn_s_setprio(PRIO_B2);
#pragma unroll
        for (int ps = 0; ps < 2; ++ps) {
            unsigned W[6];
#pragma unroll
            for (int i = 0; i < 6; ++i) { const unsigned gv = gi[ps][i >> 1][i & 1];
                W[i] = *(LAS const uint16_t*)(kt + (gv & 0xffffu));
                if (i < 5) W[i] |= (unsigned)*(LAS const uint16_t*)(kt + (gv >> 16)) << 16; }
            unsigned V[5];
#pragma unroll
            for (int i = 0; i < 5; ++i) V[i] = __builtin_amdgcn_alignbit(W[i + 1], W[i], 16);
            LAS char* cp = cb + (128 * hh + 64 * ps + lane) * 16;
            *(LAS u32x4*)(cp) = (u32x4){W[0], W[1], W[2], W[3]};
            *(LAS u32x4*)(cp + CPY) = (u32x4){V[0], V[1], V[2], V[3]};
            *(LAS u32x4*)(cp + 2 * CPY) = (u32x4){W[1], W[2], W[3], W[4]};
            *(LAS u32x4*)(cp + 3 * CPY) = (u32x4){V[1], V[2], V[3], V[4]};
        }
        if (lane < 8) *(LAS u32x4*)(cb + (2 * hh + (lane >> 2)) * CPY + (256 + (lane & 3)) * 16) = z4;
        *(LAS u32x4*)(ui + (32 + 32 * hh + (lane >> 2)) * 80 + (lane & 3) * 16) = u0;
        *(LAS u32x4*)(ui + (48 + 32 * hh + (lane >> 2)) * 80 + (lane & 3) * 16) = u1;
        arrive(ps_ + 2, lane); await(ps_ + 2, 2);
        STAMP(4);
        f32x16 a0 = {}, a1 = {};
        {
            const int sb = 7 - (r32 & 7);
            LAS const char* pa = cb + (sb & 3) * CPY + (255 - (r32 >> 3) + hi) * 16 + (sb >> 2) * 8 - 64 * (16 * hh + 3) - 2048;
            LAS const char* pa2 = pa + 8; asm volatile("" : "+v"(pa2));
            LAS const char* pb = ui + (32 + r32 - 16 * hh - 3) * 80 + 16 * hi;
#define TF(off) cat8(*(LAS const s16x4*)(pa + (off)), *(LAS const s16x4*)(pa2 + (off)))
            s16x4 ta[2][2], tb[2][2]; bf16x8 b0[2], b1[2];
#define T4(q, off) (*(LAS const s16x4*)((q) + (off)))
#define CSTEP(i, j) do { const int o_ = 64 * (3 - ((j) >> 1)) + 32 * ((j) & 1), u_ = 80 * (3 - ((j) >> 1)) + 32 * ((j) & 1); \
                a1 = MFMA_BF(cat8(ta[i][0], ta[i][1]), b1[i], a1); SBAR(); ta[1 - i][0] = T4(pa, o_ + 2048); ta[1 - i][1] = T4(pa2, o_ + 2048); SBAR(); \
                a0 = MFMA_BF(cat8(ta[i][0], ta[i][1]), b0[i], a0); SBAR(); b1[1 - i] = FR(pb, u_ + 2560); b0[1 - i] = FR(pb, u_); SBAR(); \
                a1 = MFMA_BF(cat8(tb[i][0], tb[i][1]), b0[i], a1); SBAR(); tb[1 - i][0] = T4(pa, o_); tb[1 - i][1] = T4(pa2, o_); SBAR(); } while (0)
            ta[0][0] = T4(pa, 192 + 2048); ta[0][1] = T4(pa2, 192 + 2048); b1[0] = FR(pb, 240 + 2560); b0[0] = FR(pb, 240); tb[0][0] = T4(pa, 192); tb[0][1] = T4(pa2, 192);
#pragma unroll 1
            for (int m4 = 0; m4 < NCONV; ++m4) {
                CSTEP(0, 1); CSTEP(1, 2); CSTEP(0, 3); CSTEP(1, 4); CSTEP(0, 5); CSTEP(1, 6); CSTEP(0, 7);
                if (m4 < 3) { pa -= 256; pa2 -= 256; pb -= 320; }
                CSTEP(1, 0);
            }
        }
        STAMP(5);
        {
            LAS char* ex = cb + EXO + r32 * 144 + hi * 16;
            const f32x16 snd = hh ? a0 : a1; f32x16 y = hh ? a1 : a0;
#pragma unroll
            for (int g = 0; g < 4; ++g) *(LAS f32x4*)(ex + hh * 4608 + g * 32) = (f32x4){snd[4 * g], snd[4 * g + 1], snd[4 * g + 2], snd[4 * g + 3]};
            arrive(ps_ + 3, lane); await(ps_ + 3, 2);
            const float dsk = cc ? bf_hi(dw) : bf_lo(dw);
            unsigned r[8];
#pragma unroll
            for (int g = 0; g < 4; ++g) {
                const f32x4 p = *(LAS const f32x4*)(ex + (1 - hh) * 4608 + g * 32);
                const u32x2 uu = *(LAS const u32x2*)(ui + (32 + 32 * hh + r32) * 80 + 16 * g + 8 * hi);
#pragma unroll
                for (int e = 0; e < 2; ++e) {
                    const unsigned yb = pk_bf16(y[4 * g + 2 * e] + p[2 * e], y[4 * g + 2 * e + 1] + p[2 * e + 1]), sk = pk_bf16(bf_lo(uu[e]) * dsk, bf_hi(uu[e]) * dsk);
                    r[2 * g + e] = pk_bf16(bf_lo(yb) + bf_lo(sk), bf_hi(yb) + bf_hi(sk));
                }
            }
            LAS char* sc = cb + hh * 2048;
#pragma unroll
            for (int g = 0; g < 4; ++g) *(LAS u32x2*)(sc + 64 * r32 + 16 * g + 8 * hi) = (u32x2){r[2 * g], r[2 * g + 1]};
            const u32x4 o0 = *(LAS const u32x4*)(sc + lane * 16), o1 = *(LAS const u32x4*)(sc + 1024 + lane * 16);
            const unsigned bo = WS_BT + (unsigned)(ch * S + 1024 * hh) * 2 + lane * 16;
            BST(o0, bo); BST(o1, bo + 1024);
        }
        STAMP(6);
    } else {
        {
            const unsigned nb = (bi & 1) ? 12 * 2 * D : 16 * 2 * D;
            unsigned v4[4];
#pragma unroll
            for (int i = 0; i < 4; ++i) { const unsigned o = (unsigned)(w * 4096 + i * 1024 + lane * 16); v4[i] = (o < nb ? o : 0u) + 4096 - i * 1024; }
            glds4((const char*)(wo + (size_t)(DH * (bi >> 1) + 16 * (bi & 1)) * D) - 4096, v4[0], v4[1], v4[2], v4[3], lds0 + LB + w * 4096);
        }
        const u64 e0 = __hip_atomic_load((GAS u64*)ws, __ATOMIC_RELAXED, __HIP_MEMORY_SCOPE_AGENT);
        asm volatile("s_waitcnt lgkmcnt(0)\n\ts_barrier" ::: "memory");
        drain(); KEEPPF();
        if (w == 0 && lane == 0) __hip_atomic_store((LAS u64*)sy, mix(e0), __ATOMIC_RELAXED, __HIP_MEMORY_SCOPE_WORKGROUP);
        arrive(sy + SY_WO, lane); await(sy + SY_WO, 4);
        LAS const char* p = lds + LB + (4 * hi + ((lane & 15) >> 2)) * (2 * D) + ((lane >> 4) & 1) * 32 + (lane & 3) * 8;
#pragma unroll
        for (int i = 0; i < 4; ++i) {
            const int ct = w + 4 * i;
            if (ct < 14) {
                u32x4 o = __builtin_bit_cast(u32x4, cat8(trread(p + ct * 64), trread(p + 8 * 2 * D + ct * 64)));
                if (hi && (bi & 1)) { o[2] = 0; o[3] = 0; }
                BST(o, WS_WO + (unsigned)((bi * 14 + ct) * 64 + lane) * 16);
            }
        }
        tag = get_tag(sy);
        drain();
        put_flag(ws, FL_WO + (unsigned)(w * 32 + bi) * FS, tag, lane);
    }
    const int qt = 8 * xcd + (bi >> 2), ct = 4 * (bi & 3) + w;
    const unsigned voff = lane * 16;
    drain();
    if (xcd < 7) put_flag(ws, FL_BT + (unsigned)(xcd * 128 + bi * 4 + w) * FS, tag, lane);
    bf16x8 wf[32];
    await_slow(sy + SY_G, 4);
    wait_flags<8>(ws, FL_WO + (unsigned)(w * 32 + r32) * FS, tag, !hi);
    if (ct < 14) {
#pragma unroll
        for (int i = 0; i < 32; ++i) { wf[i] = BC8(BLD(WS_WO + (unsigned)(i * 14 + ct) * 1024 + voff)); __builtin_amdgcn_s_sleep(WOSL); SBAR(); }
    }
    STAMP(7);
    if (xcd < 7) {
        wait_flags<32>(ws, FL_BT + (unsigned)(xcd * 128 + w * 32 + r32) * FS, tag, !hi);
        const unsigned st = LU + (w >> 1) * UIMG + (w & 1) * 3840;
        const unsigned bo = WS_BT + (unsigned)((64 * xcd + 16 * w + (lane >> 3)) * S + 64 * bi + (lane & 7) * 8) * 2;
        arrive(sy + SY_BG, lane); await(sy + SY_BG, 4);
        glds2c(ws, bo, bo + 8 * S * 2 - 1024, lds0 + st);
        drain();
        u32x4 o0, o1; transpose16(lds + st, lane, o0, o1);
        u32x4* dst = (u32x4*)(out + (size_t)(64 * bi + lane) * 2 * D + D + 64 * xcd + 16 * w);
        dst[0] = o0; dst[1] = o1;
    }
    STAMP(8);
    await_slow(sy + SY_X, 4);
    wait_flags<OSL>(ws, FL_O + (unsigned)(qt * 16 + 4 * w + lane) * FS, tag, lane < 4);
    STAMP(9);
    {
        u32x4 of[8];
#pragma unroll
        for (int i = 0; i < 8; ++i) of[i] = BLD(WS_O + (unsigned)(qt * 32 + 8 * w + i) * 1024 + voff);
#pragma unroll
        for (int i = 0; i < 8; ++i) *(LAS u32x4*)(lds + LB + (8 * w + i) * 1024 + voff) = of[i];
        STAMP(10);
        arrive(sy + SY_B2, lane); await(sy + SY_B2, 4);
    }
    STAMP(11);
    if (ct < 14) {
        f32x16 acc = {};
        LAS const char* po = lds + LB + voff;
        bf16x8 of[4];
#pragma unroll
        for (int i = 0; i < 3; ++i) of[i] = FR(po, i * 1024);
#pragma unroll
        for (int i = 0; i < 32; ++i) { if (i < 29) of[(i + 3) & 3] = FR(po, (i + 3) * 1024); SBAR(); acc = MFMA_BF(wf[i], of[i & 3], acc); SBAR(); }
        u32x4* op = (u32x4*)(out + (size_t)(32 * qt + r32) * 2 * D + 32 * ct + 8 * hi);
        op[0] = widen(pk_bf16(acc[0], acc[1]), pk_bf16(acc[2], acc[3]), pk_bf16(acc[4], acc[5]), pk_bf16(acc[6], acc[7]));
        op[2] = widen(pk_bf16(acc[8], acc[9]), pk_bf16(acc[10], acc[11]), pk_bf16(acc[12], acc[13]), pk_bf16(acc[14], acc[15]));
    }
    if (b == 0 && w == 0) put_flag(ws, 0, tag, lane);
    STAMP(12);
}

__global__ __launch_bounds__(512, 2) void fused(const uint16_t* __restrict__ x, const uint16_t* __restrict__ wqkv, const uint16_t* __restrict__ wo,
                                               const uint16_t* __restrict__ u, const uint16_t* __restrict__ lar, const uint16_t* __restrict__ aim,
                                               const uint16_t* __restrict__ cre, const uint16_t* __restrict__ cim, const uint16_t* __restrict__ ldt,
                                               const uint16_t* __restrict__ dskip, uint16_t* __restrict__ out, char* __restrict__ ws) {
    extern __shared__ __attribute__((aligned(16))) char smem[];
    LAS char* lds = (LAS char*)smem;
    const int lane = threadIdx.x & 63, w = __builtin_amdgcn_readfirstlane(threadIdx.x >> 6);
    STAMP(0);
    if (w < 4) role_a(x, wqkv, ws, lds, w, lane);
    else role_b(x, wqkv, wo, u, lar, aim, cre, cim, ldt, dskip, out, ws, lds, w - 4, lane);
}
}

extern "C" void kernel_launch(void* const* d_in, const int* in_sizes, int n_in, void* d_out, int out_size, void* d_ws, size_t ws_size, hipStream_t stream) {
    using namespace k;
    (void)hipFuncSetAttribute((const void*)fused, hipFuncAttributeMaxDynamicSharedMemorySize, LDS_BYTES);
    const uint16_t* const* in = (const uint16_t* const*)d_in;
    fused<<<256, 512, LDS_BYTES, stream>>>(in[0], in[1], in[2], in[3], in[4], in[5], in[6], in[7], in[8], in[9], (uint16_t*)d_out, (char*)d_ws);
}
```

```cpp
#define HA 3
#define HB 2
#define HT 1500
#include <hip/hip_runtime.h>
#include <hip/hip_bf16.h>
#include <stdint.h>

typedef short bf16x8 __attribute__((ext_vector_type(8)));
typedef _Float16 f16x8 __attribute__((ext_vector_type(8)));
typedef _Float16 f16x2 __attribute__((ext_vector_type(2)));
typedef __bf16 bf16x2 __attribute__((ext_vector_type(2)));
typedef short s16x4 __attribute__((ext_vector_type(4)));
typedef float f32x16 __attribute__((ext_vector_type(16)));
typedef float f32x4 __attribute__((ext_vector_type(4)));
typedef float f32x2 __attribute__((ext_vector_type(2)));
typedef unsigned u32x4 __attribute__((ext_vector_type(4)));
typedef unsigned u32x2 __attribute__((ext_vector_type(2)));
typedef unsigned long long u64;
#define LAS __attribute__((address_space(3)))
#define GAS __attribute__((address_space(1)))
#define DI __device__ __forceinline__
#define MFMA_BF(a, b, c) __builtin_amdgcn_mfma_f32_32x32x16_bf16((a), (b), (c), 0, 0, 0)
#define MFMA16(a, b, c) __builtin_amdgcn_mfma_f32_16x16x32_bf16((a), (b), (c), 0, 0, 0)
#define MFMA_F16(a, b, c) __builtin_amdgcn_mfma_f32_32x32x16_f16((a), (b), (c), 0, 0, 0)
#define SBAR() __builtin_amdgcn_sched_barrier(0)
#define CFENCE() asm volatile("" ::: "memory")
#define PIN(x) asm volatile("" : "+v"(x))
#define BC8(x) __builtin_bit_cast(bf16x8, (x))

#define PRIO_A1 0
#define PRIO_A2 0
#define PRIO_B 1
#define WOSL 2
#define PRIO_B2 1
#define RHO ((2 * (bi & 3) + (bi >> 2)) % 7)
#define KVSL 2
#define OSL 2
#define WOAUX 16
#define NCONV 4
#define KVLD glds16c
#define NOPF
namespace k {
#define STAMP(i) do { if ((i) == ((threadIdx.x >> 8) ? HB : HA)) { while (__builtin_amdgcn_s_memrealtime() - t_start < HT) __builtin_amdgcn_s_sleep(2); } } while (0)
constexpr int S = 2048, D = 448, DH = 28, NS = 256;
constexpr unsigned FS = 128;
constexpr unsigned FL_KV = FS, FL_O = FL_KV + 1024 * FS, FL_UT = FL_O + 1024 * FS, FL_BT = FL_UT + 896 * FS, FL_WO = FL_BT + 896 * FS;
constexpr unsigned WS_KV = 512u << 10;
constexpr unsigned WS_O = WS_KV + (4u << 20);
constexpr unsigned WS_WO = WS_O + (2u << 20);
constexpr unsigned WS_UT = WS_WO + (448u << 10);
constexpr unsigned WS_BT = WS_UT + D * S * 2;
constexpr unsigned WS_END = WS_BT + D * S * 2;
constexpr int STG = 28672, LB = 86016, LQ = 0, LX = 8192;
constexpr int TROW = 272, TABW = 53 * TROW, CBW = 2 * TABW, CPY = 4160, EXO = 4 * CPY;
constexpr int LU = LB + 2 * CBW, UIMG = 7680, LKT = LU + 2 * UIMG, KTW = 1312, LSY = LKT + 2 * KTW, LDS_BYTES = LSY + 256;
static_assert(LDS_BYTES <= 163840 && (LSY & 15) == 0, "lds");
enum { SY_TAG = 0, SY_F = 2, SY_P = 10, SY_WO = 18, SY_BG = 19, SY_Q = 20, SY_X = 21, SY_G = 22, SY_B2 = 23, SY_T1 = 24 };
constexpr int SPIN = 1 << 18;

struct IdxTab { uint16_t v[256][12]; };
constexpr IdxTab make_idx() {
    IdxTab t = {};
    for (int y = 0; y < 256; ++y) for (int e = 0; e < 12; ++e) {
        const int j = 2047 - 8 * y - e; int ix = 644;
        if (j >= 0) { const int sh = j >= 1024 ? 3 : (j >= 512 ? 2 : (j >= 256 ? 1 : 0));
            ix = sh ? 128 * sh + ((j + (1 << (sh - 1)) - 1 + ((j >> sh) & 1)) >> sh) : j; }
        t.v[y][e] = (uint16_t)(2 * ix);
    }
    return t;
}
__device__ const IdxTab g_idx __attribute__((aligned(16))) = make_idx();

DI unsigned pk_bf16(float a, float b) { f32x2 v = {a, b}; return __builtin_bit_cast(unsigned, __builtin_convertvector(v, bf16x2)); }
DI unsigned pk_f16(f32x2 v) { return __builtin_bit_cast(unsigned, __builtin_convertvector(v, f16x2)); }
DI float bf_lo(unsigned w) { return __uint_as_float(w << 16); }
DI float bf_hi(unsigned w) { return __uint_as_float(w & 0xffff0000u); }
DI float rbf(float x) { return bf_lo(pk_bf16(x, x)); }
DI void glds16(const void* sbase, unsigned voff, unsigned lds_base) {
    unsigned sv;
    asm volatile("s_mov_b32 %0, m0\n\ts_mov_b32 m0, %3\n\ts_nop 0\n\tglobal_load_lds_dwordx4 %1, %2\n\ts_mov_b32 m0, %0"
                 : "=&s"(sv) : "v"(voff), "s"(sbase), "s"(lds_base) : "memory");
}
DI void glds16c(const void* sbase, unsigned voff, unsigned lds_base) {
    unsigned sv;
    asm volatile("s_mov_b32 %0, m0\n\ts_mov_b32 m0, %3\n\ts_nop 0\n\tglobal_load_lds_dwordx4 %1, %2 sc1\n\ts_mov_b32 m0, %0"
                 : "=&s"(sv) : "v"(voff), "s"(sbase), "s"(lds_base) : "memory");
}
#define GL1(V, O, C) "global_load_lds_dwordx4 " V ", %1 offset:" #O C "\n\t"
DI void glds4(const void* sbase, unsigned v0, unsigned v1, unsigned v2, unsigned v3, unsigned lds_base) {
    unsigned sv;
    asm volatile("s_mov_b32 %0, m0\n\ts_mov_b32 m0, %2\n\ts_nop 0\n\t" GL1("%3", 0, "") GL1("%4", 1024, "") GL1("%5", 2048, "") GL1("%6", 3072, "") "s_mov_b32 m0, %0"
                 : "=&s"(sv) : "s"(sbase), "s"(lds_base), "v"(v0), "v"(v1), "v"(v2), "v"(v3) : "memory");
}
DI void glds3(const void* sbase, unsigned v0, unsigned v1, unsigned v2, unsigned lds_base) {
    unsigned sv;
    asm volatile("s_mov_b32 %0, m0\n\ts_mov_b32 m0, %2\n\ts_nop 0\n\t" GL1("%3", 0, "") GL1("%4", 1024, "") GL1("%5", 2048, "") "s_mov_b32 m0, %0"
                 : "=&s"(sv) : "s"(sbase), "s"(lds_base), "v"(v0), "v"(v1), "v"(v2) : "memory");
}
template <int OFF> DI void glds1(const void* sbase, unsigned v0, unsigned lds_base) {
    unsigned sv;
    asm volatile("s_mov_b32 %0, m0\n\ts_mov_b32 m0, %2\n\ts_nop 0\n\tglobal_load_lds_dwordx4 %3, %1 offset:%4\n\ts_mov_b32 m0, %0" : "=&s"(sv) : "s"(sbase), "s"(lds_base), "v"(v0), "i"(OFF) : "memory");
}
DI void glds2(const void* sbase, unsigned v0, unsigned v1, unsigned lds_base) {
    unsigned sv;
    asm volatile("s_mov_b32 %0, m0\n\ts_mov_b32 m0, %2\n\ts_nop 0\n\t" GL1("%3", 0, "") GL1("%4", 1024, "") "s_mov_b32 m0, %0"
                 : "=&s"(sv) : "s"(sbase), "s"(lds_base), "v"(v0), "v"(v1) : "memory");
}
DI void glds4c(const void* sbase, unsigned v0, unsigned lds_base) {
    unsigned sv;
    asm volatile("s_mov_b32 %0, m0\n\ts_mov_b32 m0, %2\n\ts_nop 0\n\t" GL1("%3", 0, " sc1") GL1("%3", 1024, " sc1") GL1("%3", 2048, " sc1") GL1("%3", 3072, " sc1") "s_mov_b32 m0, %0"
                 : "=&s"(sv) : "s"(sbase), "s"(lds_base), "v"(v0) : "memory");
}
DI void glds2c(const void* sbase, unsigned v0, unsigned v1, unsigned lds_base) {
    unsigned sv;
    asm volatile("s_mov_b32 %0, m0\n\ts_mov_b32 m0, %2\n\ts_nop 0\n\t" GL1("%3", 0, " sc1") GL1("%4", 1024, " sc1") "s_mov_b32 m0, %0"
                 : "=&s"(sv) : "s"(sbase), "s"(lds_base), "v"(v0), "v"(v1) : "memory");
}
DI float wave_max(float v) {
#define DPPMAX(ctrl, rm) v = fmaxf(v, __uint_as_float(__builtin_amdgcn_update_dpp(0, __float_as_uint(v), ctrl, rm, 0xf, true)))
    DPPMAX(0x111, 0xf); DPPMAX(0x112, 0xf); DPPMAX(0x114, 0xf); DPPMAX(0x118, 0xf); DPPMAX(0x142, 0xa); DPPMAX(0x143, 0xc);
    return __uint_as_float(__builtin_amdgcn_readlane(__float_as_uint(v), 63));
}
DI s16x4 trread(LAS const char* p) { return __builtin_bit_cast(s16x4, __builtin_amdgcn_ds_read_tr16_b64_v4i16((LAS s16x4*)p)); }
DI bf16x8 cat8(s16x4 a, s16x4 b) { return __builtin_shufflevector(a, b, 0, 1, 2, 3, 4, 5, 6, 7); }
DI f32x2 cmul(f32x2 a, f32x2 b, f32x2 bs) { return __builtin_elementwise_fma(a.yy, bs, a.xx * b); }
DI f32x2 rot(f32x2 b) { return (f32x2){-b.y, b.x}; }
DI f32x2 anchor(float a2, float rev, float s) {
    const float mag = __builtin_amdgcn_exp2f(a2 * s);
    float r = rev * s; r -= __builtin_rintf(r);
    return (f32x2){mag * __builtin_amdgcn_cosf(r), mag * __builtin_amdgcn_sinf(r)};
}
DI void transpose16(LAS const char* lds, int lane, u32x4& o0, u32x4& o1) {
    LAS const char* p = lds + ((lane & 15) >> 2) * 128 + (lane >> 4) * 32 + (lane & 3) * 8;
    const s16x4 a = trread(p), b = trread(p + 512), c = trread(p + 1024), d = trread(p + 1536);
    o0 = __builtin_bit_cast(u32x4, cat8(a, b)); o1 = __builtin_bit_cast(u32x4, cat8(c, d));
}
DI void arrive(LAS unsigned* c, int) {
    asm volatile("s_mov_b64 exec, 1\n\tds_add_u32 %0, %1\n\ts_mov_b64 exec, -1" :: "v"((unsigned)(uintptr_t)c), "v"(1u) : "memory");
}
DI void await(LAS unsigned* c, unsigned n) {
    unsigned t, i = SPIN;
    asm volatile("1:\n\tds_read_b32 %0, %2\n\ts_waitcnt lgkmcnt(0)\n\tv_cmp_gt_u32 vcc, %3, %0\n\ts_cbranch_vccz 2f\n\ts_sleep 1\n\ts_sub_u32 %1, %1, 1\n\ts_cbranch_scc0 1b\n2:"
                 : "=&v"(t), "+s"(i) : "v"((unsigned)(uintptr_t)c), "s"(n) : "vcc", "scc", "memory");
}
DI u64 get_tag(LAS unsigned* sy) {
    u64 t = 0;
#pragma unroll 1
    for (int i = 0; i < SPIN; ++i) {
        const u64 v = __hip_atomic_load((LAS u64*)sy, __ATOMIC_RELAXED, __HIP_MEMORY_SCOPE_WORKGROUP);
        t = ((u64)(unsigned)__builtin_amdgcn_readfirstlane((unsigned)(v >> 32)) << 32) | (unsigned)__builtin_amdgcn_readfirstlane((unsigned)v);
        if (t) break;
        __builtin_amdgcn_s_sleep(1);
    }
    return t;
}
DI void drain() { asm volatile("s_waitcnt vmcnt(0)" ::: "memory"); }
DI void put_flag(char* ws, unsigned off, u64 tag, int lane) { if (lane == 0) __hip_atomic_store((GAS u64*)(ws + off), tag, __ATOMIC_RELAXED, __HIP_MEMORY_SCOPE_AGENT); }
template <int SLP> DI void wait_flags(char* ws, unsigned off, u64 tag, bool pend) {
    GAS u64* p = (GAS u64*)(ws + off);
    CFENCE();
#pragma unroll 1
    for (int i = 0; i < SPIN; ++i) {
        if (pend) pend = __hip_atomic_load(p, __ATOMIC_RELAXED, __HIP_MEMORY_SCOPE_AGENT) != tag;
        if (!__any(pend)) break;
        __builtin_amdgcn_s_sleep(SLP);
    }
    CFENCE();
}
DI void await_slow(LAS unsigned* c, unsigned n) {
#pragma unroll 1
    for (int i = 0; i < SPIN && __hip_atomic_load(c, __ATOMIC_RELAXED, __HIP_MEMORY_SCOPE_WORKGROUP) < n; ++i) __builtin_amdgcn_s_sleep(4);
    CFENCE();
}
#define BST(v, off) __builtin_amdgcn_raw_buffer_store_b128((v), rs, (off), 0, 16)
#define BLD(off) __builtin_amdgcn_raw_buffer_load_b128(rs, (off), 0, 16)
#define FR(p, off) (*(LAS const bf16x8*)((p) + (off)))
DI u32x4 widen(unsigned a0, unsigned a1, unsigned b0, unsigned b1) {
    const auto r0 = __builtin_amdgcn_permlane32_swap(a0, b0, false, false), r1 = __builtin_amdgcn_permlane32_swap(a1, b1, false, false);
    return (u32x4){r0[0], r1[0], r0[1], r1[1]};
}

#define BAR() asm volatile("s_waitcnt lgkmcnt(0)\n\ts_barrier" ::: "memory")
#define EX(x) __builtin_amdgcn_exp2f(x)
DI float swapmax(float m) { const auto r = __builtin_amdgcn_permlane32_swap(__float_as_uint(m), __float_as_uint(m), false, false); return fmaxf(__uint_as_float(r[0]), __uint_as_float(r[1])); }
DI u32x4 put_ref(u32x4 q1, float m, int hi) {
    const float m1 = rbf(m), m2 = rbf(m - m1), m3 = m - m1 - m2;
    if (hi) { q1[2] = pk_bf16(-m1, -m2); q1[3] = pk_bf16(-m3, 0.f); }
    return q1;
}
DI u64 mix(u64 e) { u64 z = e + 0x9E3779B97F4A7C15ull; z = (z ^ (z >> 30)) * 0xBF58476D1CE4E5B9ull; z = (z ^ (z >> 27)) * 0x94D049BB133111EBull; return (z ^ (z >> 31)) | 1ull; }

DI void role_a(const uint16_t* __restrict__ x, const uint16_t* __restrict__ wqkv, char* __restrict__ ws, LAS char* lds, int w, int lane, u64 t_start) {
    const int head = 4 * (blockIdx.x & 3) + ((blockIdx.x >> 3) & 3), grp = 8 * ((blockIdx.x >> 2) & 1) + (blockIdx.x >> 5);
    const unsigned lds0 = (unsigned)(uintptr_t)lds;
    LAS unsigned* sy = (LAS unsigned*)(lds + LSY);
    const __amdgpu_buffer_rsrc_t rs = __builtin_amdgcn_make_buffer_rsrc(ws, 0, WS_END, 0x00020000);
    const int bi = blockIdx.x >> 3, r32 = lane & 31, hi = lane >> 5;
    const int wsh = (head & 7) == 7 ? 4 : 0, rho = RHO;
    __builtin_amdgcn_s_setprio(PRIO_A1);
    const unsigned wv = (unsigned)((lane >> 2) * 3 * D + (lane & 3) * 8) * 2;
    const unsigned xv0 = (unsigned)((lane >> 3) * D + (((lane & 7) ^ (lane >> 4)) * 8)) * 2, xv1 = (unsigned)((lane >> 3) * D + (((lane & 7) ^ (4 + (lane >> 4))) * 8)) * 2;
    const unsigned xv[4] = {xv0, xv1 + 16 * D - 1024, xv0 + 32 * D - 2048, xv1 + 48 * D - 3072};
    const unsigned wvv[3] = {wv + 2048, wv + 2 * D + 1024, wv + 4 * D};
    const char* wb = (const char*)(wqkv + DH * head - wsh + (size_t)(16 * w) * 3 * D) - 2048;
    const char* xb = (const char*)(x + (size_t)(128 * grp + 32 * w) * D);
#define LOADX(sp, sl) glds4(xb + (sp) * 128, xv[0], xv[1], xv[2], xv[3], lds0 + (sl) + w * 4096)
#define LOADW(sp, sl) glds3(wb + (size_t)(sp) * 64 * 3 * D * 2, wvv[0], wvv[1], wvv[2], lds0 + (sl) + 16384 + w * 3072)
    int sp = rho;
    LOADW(sp, 0); LOADX(sp, 0); sp = sp == 6 ? 0 : sp + 1;
    asm volatile("s_barrier" ::: "memory");
    LOADW(sp, STG); LOADX(sp, STG); sp = sp == 6 ? 0 : sp + 1;
    STAMP(1);
    const int xs = (r32 >> 1) & 7;
    LAS const char* xp[4];
#pragma unroll
    for (int ks = 0; ks < 4; ++ks) xp[ks] = lds + w * 4096 + r32 * 128 + (((2 * ks + hi) ^ xs) * 16);
    LAS const char* wp = lds + 16384 + (8 * hi + ((lane & 15) >> 2)) * 64 + ((lane >> 4) & 1) * 32 + (lane & 3) * 8 + wsh * 2;
    f32x16 aq = {}, ak = {}, av = {};
    int so = 0;
    bf16x8 xf[4]; s16x4 fr[4][6];
#define RX(ks, o) xf[ks] = FR(xp[ks], o)
#define RW(ks, j, o) fr[ks][j] = trread(wp + (o) + (ks) * 3072 + ((((j) >> 1) + 1) % 3) * 1024 + ((j) & 1) * 256)
#define MK(ks) ak = MFMA_BF(cat8(fr[ks][0], fr[ks][1]), xf[ks], ak); SBAR()
#define MV(ks) av = MFMA_BF(xf[ks], cat8(fr[ks][2], fr[ks][3]), av); SBAR()
#define MQ(ks) aq = MFMA_BF(cat8(fr[ks][4], fr[ks][5]), xf[ks], aq); SBAR()
#define QSTEP(ks, L0, L1) do { \
            MK(ks); RX(ks + 1, so); RW(ks + 1, 0, so); RW(ks + 1, 1, so); SBAR(); \
            MV(ks); RW(ks + 1, 2, so); RW(ks + 1, 3, so); SBAR(); if (LD) { L0; } \
            MQ(ks); RW(ks + 1, 4, so); RW(ks + 1, 5, so); SBAR(); if (LD) { L1; } } while (0)
#define STAGE(LD_, NXT, VM) do { constexpr bool LD = LD_; \
        const char* wg = wb + (size_t)sp * 64 * 3 * D * 2; const char* xg = xb + sp * 128; const unsigned lf = lds0 + sf; \
        QSTEP(0, glds2(wg, wvv[0], wvv[1], lf + 16384 + w * 3072), glds1<2048>(wg, wvv[2], lf + 16384 + w * 3072)); \
        QSTEP(1, glds2(xg, xv[0], xv[1], lf + w * 4096), (glds1<2048>(xg, xv[2], lf + w * 4096))); \
        QSTEP(2, (glds1<3072>(xg, xv[3], lf + w * 4096)), ); \
        asm volatile("s_waitcnt vmcnt(" #VM ")" ::: "memory"); \
        arrive(fc, lane); SBAR(); \
        MK(3); await(fc, 4); ++fc; \
        sf = so; so = so == 2 * STG ? 0 : so + STG; sp = sp == 6 ? 0 : sp + 1; \
        if (NXT) { RX(0, so); RW(0, 0, so); RW(0, 1, so); RW(0, 2, so); } SBAR(); \
        MV(3); if (NXT) { RW(0, 3, so); RW(0, 4, so); RW(0, 5, so); } SBAR(); \
        MQ(3); } while (0)
    LAS unsigned* fc = sy + SY_F;
    int sf = 2 * STG;
    asm volatile("s_waitcnt vmcnt(7)" ::: "memory");
    arrive(fc, lane); await(fc, 4); ++fc;
    STAMP(16);
    RX(0, 0); RW(0, 0, 0); RW(0, 1, 0); RW(0, 2, 0); RW(0, 3, 0); RW(0, 4, 0); RW(0, 5, 0); SBAR();
#pragma unroll 1
    for (int st = 0; st < 5; ++st) STAGE(true, true, 7);
    const u64 tag = get_tag(sy);
    STAGE(false, true, 0);
    STAGE(false, false, 0);
    STAMP(3);
    const float cq = 0.18898223650461362f * 1.4426950408889634f;
    u32x4 q0, q1, k0, k1, v0, v1;
#pragma unroll
    for (int i = 0; i < 4; ++i) {
        k0[i] = pk_bf16(ak[2 * i], ak[2 * i + 1]); k1[i] = pk_bf16(ak[8 + 2 * i], ak[9 + 2 * i]);
        v0[i] = pk_bf16(av[2 * i], av[2 * i + 1]); v1[i] = pk_bf16(av[8 + 2 * i], av[9 + 2 * i]);
        if (r32 >= DH) { v0[i] = v1[i] = (r32 == DH ? 0x3f803f80u : 0u); }
    }
    if (hi) { k1[2] = 0x3f803f80u; k1[3] = 0x00003f80u; }
    const unsigned tb = WS_KV + (unsigned)(head * 64 + 4 * grp + w) * 4096 + lane * 16;
    BST(k0, tb); BST(k1, tb + 1024); BST(v0, tb + 2048); BST(v1, tb + 3072);
#pragma unroll
    for (int i = 0; i < 4; ++i) { q0[i] = pk_bf16(aq[2 * i] * cq, aq[2 * i + 1] * cq); q1[i] = pk_bf16(aq[8 + 2 * i] * cq, aq[9 + 2 * i] * cq); }
    if (hi) { q1[2] = 0; q1[3] = 0; }
    {
        const f32x16 zero = {};
        f32x16 s = MFMA_BF(BC8(k0), BC8(q0), zero); s = MFMA_BF(BC8(k1), BC8(q1), s);
        float m = s[0];
#pragma unroll
        for (int i = 1; i < 16; ++i) m = fmaxf(m, s[i]);
        q1 = put_ref(q1, swapmax(m), hi);
    }
    LAS u32x4* qo = (LAS u32x4*)(lds + LQ + w * 2048) + lane;
    qo[0] = q0; qo[64] = q1;
    arrive(sy + SY_Q, lane);
    drain();
    put_flag(ws, FL_KV + (unsigned)((head * 4 + w) * 16 + grp) * FS, tag, lane);
    STAMP(4);
    await(sy + SY_Q, 4);
    __builtin_amdgcn_s_setprio(PRIO_A2);
    const unsigned voff = lane * 16;
    const f32x16 zero = {};
    bf16x8 Q[4][2];
#pragma unroll
    for (int i = 0; i < 4; ++i) { LAS const char* p = lds + LQ + ((w + i) & 3) * 2048 + voff; Q[i][0] = FR(p, 0); Q[i][1] = FR(p, 1024); }
    u32x4 kv[3][4] = {{k0, k1, v0, v1}, {}, {}};
    f32x16 O[4] = {}, sa, sb;
    u32x4 p0, p1;
#define GRP(SC, i, P, j) do { float e0 = EX(SC[i]); SBAR(); float e1 = EX(SC[i + 1]); SBAR(); float e2 = EX(SC[i + 2]); SBAR(); float e3 = EX(SC[i + 3]); SBAR(); \
        P[j] = pk_bf16(e0, e1); SBAR(); P[j + 1] = pk_bf16(e2, e3); PIN(P); SBAR(); } while (0)
#define SLOT(SC, SN, cb, i, QK, PV1, LD) do { constexpr int nb_ = (i) < 3 ? (cb) : ((cb) + 1) % 3, pb_ = (i) > 0 ? (cb) : ((cb) + 2) % 3; \
        if (QK) SN = MFMA_BF(BC8(kv[nb_][0]), Q[((i) + 1) & 3][0], zero); SBAR(); GRP(SC, 0, p0, 0); \
        if (PV1) O[((i) + 3) & 3] = MFMA_BF(BC8(kv[pb_][3]), BC8(p1), O[((i) + 3) & 3]); SBAR(); \
        if (LD) { kv[((cb) + 2) % 3][i] = __builtin_amdgcn_raw_buffer_load_b128(rs, voff + (i) * 1024, kvo, 16); SBAR(); } GRP(SC, 4, p0, 2); \
        if (QK) SN = MFMA_BF(BC8(kv[nb_][1]), Q[((i) + 1) & 3][1], SN); SBAR(); GRP(SC, 8, p1, 0); \
        O[i] = MFMA_BF(BC8(kv[cb][2]), BC8(p0), O[i]); SBAR(); GRP(SC, 12, p1, 2); } while (0)
    const unsigned kvb = WS_KV + (unsigned)(head * 64 + w) * 4096;
    unsigned kvo = 0;
    sa = MFMA_BF(BC8(k0), Q[0][0], zero); sa = MFMA_BF(BC8(k1), Q[0][1], sa);
    SLOT(sa, sb, 0, 0, true, false, false); SLOT(sb, sa, 0, 1, true, true, false); SLOT(sa, sb, 0, 2, true, true, false); SLOT(sb, sa, 0, 3, false, true, false);
    STAMP(5);
    wait_flags<KVSL>(ws, FL_KV + (unsigned)((head * 4 + w) * 16 + lane) * FS, tag, lane < 16 && lane != grp);
    arrive(sy + SY_G, lane);
    STAMP(6);
#pragma unroll
    for (int j = 1; j < 3; ++j)
#pragma unroll
        for (int f = 0; f < 4; ++f) kv[j][f] = __builtin_amdgcn_raw_buffer_load_b128(rs, voff + f * 1024, kvb + ((grp + j) & 15) * 16384, 16);
    sa = MFMA_BF(BC8(kv[1][0]), Q[0][0], zero); sa = MFMA_BF(BC8(kv[1][1]), Q[0][1], sa);
    STAMP(7);
    int nx = grp + 3;
#define TILE(cb) do { kvo = kvb + (nx & 15) * 16384; ++nx; SBAR(); \
        SLOT(sa, sb, cb, 0, true, true, true); SLOT(sb, sa, cb, 1, true, true, true); SLOT(sa, sb, cb, 2, true, true, true); SLOT(sb, sa, cb, 3, true, true, true); } while (0)
#pragma unroll 1
    for (int it = 0; it < 5; ++it) { TILE(1); TILE(2); TILE(0); }
    O[3] = MFMA_BF(BC8(kv[0][3]), BC8(p1), O[3]);
    STAMP(8);
    f32x16 o = O[0];
    {
#pragma unroll
        for (int i = 1; i < 4; ++i) {
            LAS char* ex = lds + LX + (((w + i) & 3) * 3 + i - 1) * 4096 + voff;
#pragma unroll
            for (int g = 0; g < 4; ++g) *(LAS f32x4*)(ex + g * 1024) = (f32x4){O[i][4 * g], O[i][4 * g + 1], O[i][4 * g + 2], O[i][4 * g + 3]};
        }
        arrive(sy + SY_X, lane); await(sy + SY_X, 4);
        LAS const char* er = lds + LX + w * 3 * 4096 + voff;
#pragma unroll
        for (int i = 0; i < 3; ++i)
#pragma unroll
            for (int g = 0; g < 4; ++g) { const f32x4 r = *(LAS const f32x4*)(er + i * 4096 + g * 1024);
#pragma unroll
                for (int e = 0; e < 4; ++e) o[4 * g + e] += r[e]; }
        f32x2 chk = {0.f, 0.f};
#pragma unroll
        for (int i = 0; i < 8; ++i) chk = __builtin_elementwise_fma((f32x2){o[2 * i], o[2 * i + 1]}, (f32x2){0.f, 0.f}, chk);
        if (__builtin_expect(__any(!(chk[0] + chk[1] == 0.f) || !(o[12] < 1e30f)), 0)) {
            const unsigned hb = WS_KV + (unsigned)head * 64 * 4096 + voff;
            u32x4 qr = __builtin_bit_cast(u32x4, Q[0][1]); if (hi) { qr[2] = 0; qr[3] = 0; }
            float mm = -INFINITY;
            wait_flags<4>(ws, FL_KV + (unsigned)(head * 64 + lane) * FS, tag, true);
#pragma unroll 1
            for (int t = 0; t < 64; ++t) {
                f32x16 s = MFMA_BF(BC8(BLD(hb + t * 4096)), Q[0][0], zero); s = MFMA_BF(BC8(BLD(hb + t * 4096 + 1024)), BC8(qr), s);
#pragma unroll
                for (int i = 0; i < 16; ++i) mm = fmaxf(mm, s[i]);
            }
            qr = put_ref(qr, swapmax(mm), hi);
            o = zero;
#pragma unroll 1
            for (int t = 0; t < 64; ++t) {
                f32x16 s = MFMA_BF(BC8(BLD(hb + t * 4096)), Q[0][0], zero); s = MFMA_BF(BC8(BLD(hb + t * 4096 + 1024)), BC8(qr), s);
#pragma unroll
                for (int i = 0; i < 4; ++i) { p0[i] = pk_bf16(EX(s[2 * i]), EX(s[2 * i + 1])); p1[i] = pk_bf16(EX(s[8 + 2 * i]), EX(s[9 + 2 * i])); }
                o = MFMA_BF(BC8(BLD(hb + t * 4096 + 2048)), BC8(p0), o); o = MFMA_BF(BC8(BLD(hb + t * 4096 + 3072)), BC8(p1), o);
            }
        }
    }
    {
        const auto r = __builtin_amdgcn_permlane32_swap(__float_as_uint(o[12]), __float_as_uint(o[12]), false, false);
        const float rl = __builtin_amdgcn_rcpf(__uint_as_float(r[1]));
        u32x4 o0, o1;
#pragma unroll
        for (int i = 0; i < 4; ++i) { o0[i] = pk_bf16(o[2 * i] * rl, o[2 * i + 1] * rl); o1[i] = pk_bf16(o[8 + 2 * i] * rl, o[9 + 2 * i] * rl); }
        if (hi) { o1[2] = 0; o1[3] = 0; }
        const unsigned fi = (unsigned)((4 * grp + w) * 16 + head);
        BST(o0, WS_O + fi * 2048 + voff); BST(o1, WS_O + fi * 2048 + 1024 + voff);
        STAMP(9);
        drain();
        put_flag(ws, FL_O + fi * FS, tag, lane);
    }
    STAMP(10);
}

DI void role_b(const uint16_t* __restrict__ x, const uint16_t* __restrict__ wqkv, const uint16_t* __restrict__ wo, const uint16_t* __restrict__ u, const uint16_t* __restrict__ lar, const uint16_t* __restrict__ aim,
               const uint16_t* __restrict__ cre, const uint16_t* __restrict__ cim, const uint16_t* __restrict__ ldt, const uint16_t* __restrict__ dskip,
               uint16_t* __restrict__ out, char* __restrict__ ws, LAS char* lds, int w, int lane, u64 t_start) {
    u64 tag;
    const unsigned lds0 = (unsigned)(uintptr_t)lds;
    LAS unsigned* sy = (LAS unsigned*)(lds + LSY);
    const __amdgpu_buffer_rsrc_t rs = __builtin_amdgcn_make_buffer_rsrc(ws, 0, WS_END, 0x00020000);
    const int b = blockIdx.x, xcd = b & 7, bi = b >> 3, r32 = lane & 31, hi = lane >> 5;
    __builtin_amdgcn_s_setprio(PRIO_B);
    if (w == 0) sy[lane] = 0;
#define KEEPPF()
    if (xcd < 7) {
        const int cc = w >> 1, hh = w & 1, pair = 32 * xcd + bi, ch = 2 * pair + cc;
        LAS char* cb = lds + LB + cc * CBW;
        LAS char* tab = cb + hh * TABW;
        const unsigned uo = (unsigned)((64 * bi + 16 * w + (lane >> 3)) * D + 64 * xcd + (lane & 7) * 8) * 2;
        glds2(u, uo, uo + 8 * D * 2 - 1024, lds0 + LU + w * 2048);
        const unsigned po = (unsigned)(ch * NS + 128 * hh) / 2 + lane;
        const unsigned pl = ((const unsigned*)lar)[po], pa = ((const unsigned*)aim)[po], pr = ((const unsigned*)cre)[po], pi = ((const unsigned*)cim)[po];
        const u64 e0 = __hip_atomic_load((GAS u64*)ws, __ATOMIC_RELAXED, __HIP_MEMORY_SCOPE_AGENT);
        u32x2 gi[2][3];
#pragma unroll
        for (int ps = 0; ps < 2; ++ps)
#pragma unroll
            for (int i = 0; i < 3; ++i) gi[ps][i] = ((const u32x2*)g_idx.v[128 * hh + 64 * ps + lane])[i];
        const unsigned lw = ((const unsigned*)ldt)[pair], dw = ((const unsigned*)dskip)[pair];
        asm volatile("s_waitcnt lgkmcnt(0)\n\ts_barrier" ::: "memory");
        drain(); KEEPPF();
        STAMP(1);
        arrive(sy + SY_WO, lane); await(sy + SY_WO, 4);
        {
            LAS const char* p = lds + LU + (16 * (lane >> 4) + ((lane & 15) >> 2)) * 128 + w * 32 + (lane & 3) * 8;
            const bf16x8 t0 = cat8(trread(p), trread(p + 512)), t1 = cat8(trread(p + 1024), trread(p + 1536));
            arrive(sy + SY_T1, lane);
            LAS char* sc = tab + (lane & 15) * 144 + (lane >> 4) * 32;
            *(LAS bf16x8*)sc = t0; *(LAS bf16x8*)(sc + 16) = t1;
            LAS const char* sr = tab + (lane >> 3) * 144 + (lane & 7) * 16;
            const u32x4 o0 = *(LAS const u32x4*)sr, o1 = *(LAS const u32x4*)(sr + 8 * 144);
            const unsigned to = WS_UT + (unsigned)((64 * xcd + 16 * w + (lane >> 3)) * S + 64 * bi) * 2 + (lane & 7) * 16;
            BST(o0, to); BST(o1, to + 8 * S * 2);
        }
        if (w == 0 && lane == 0) __hip_atomic_store((LAS u64*)sy, mix(e0), __ATOMIC_RELAXED, __HIP_MEMORY_SCOPE_WORKGROUP);
        const float dt = rbf(expf(cc ? bf_hi(lw) : bf_lo(lw)));
        float cm = fmaxf(fmaxf(fabsf(bf_lo(pr)), fabsf(bf_hi(pr))), fmaxf(fabsf(bf_lo(pi)), fabsf(bf_hi(pi))));
        cm = wave_max(cm);
        unsigned ce = __float_as_uint(cm) & 0x7f800000u; ce = ce < 0x01000000u ? 0x01000000u : (ce > 0x7e800000u ? 0x7e800000u : ce);
        const float cscale = __uint_as_float(0x7f000000u - ce), oscale = 2.0f * dt * __uint_as_float(ce);
        LAS unsigned* tw = (LAS unsigned*)(tab + lane * 4);
        LAS const char* trd = tab + r32 * TROW + hi * 16;
        f32x16 dacc = {};
        STAMP(13);
        f32x2 z1, z8, z256, x0;
#define ANCH(vl, va, vr, vi) do { const float a2 = (-rbf(expf(vl)) * dt) * 1.4426950408889634f, rev = ((va) * dt) * 0.15915494309189535f; \
            z1 = anchor(a2, -rev, 1.f); z8 = anchor(a2, rev, 8.f); z256 = anchor(a2, -rev, 256.f); x0 = (f32x2){(vr) * cscale, -(vi) * cscale}; } while (0)
#define PUTX(row, v) tw[(row) * (TROW / 4)] = pk_f16(v)
#define PUTY(col, v) tw[(21 + (col)) * (TROW / 4)] = pk_f16(v)
#define CHAIN() do { \
            const f32x2 z1s = rot(z1), zs = rot(z256), z8s = rot(z8); \
            f32x2 y[4], X[8]; y[1] = z8; y[2] = cmul(z8, z8, z8s); X[0] = x0; X[1] = cmul(x0, z1, z1s); \
            y[3] = cmul(y[2], z8, z8s); const f32x2 z32 = cmul(y[2], y[2], rot(y[2])), z32s = rot(z32); X[2] = cmul(X[1], z1, z1s); y[0] = z32; \
            tw[21 * (TROW / 4)] = 0x3c00u; PUTY(1, y[1]); PUTY(2, y[2]); PUTY(3, y[3]); PUTY(4, y[0]); PUTX(0, X[0]); PUTX(1, X[1]); PUTX(2, X[2]); \
            f32x2 a = cmul(x0, z256, zs), b2 = cmul(X[2], z256, zs), c4; PUTX(8, a); PUTX(9, b2); SBAR(); \
            _Pragma("unroll") for (int i = 1; i < 8; ++i) { \
                f32x2 t0, t1, t2, t3, t4, t5, t6; \
                if (i > 1) t0 = y[0].xx * z32; t1 = y[1].xx * z32; t2 = y[2].xx * z32; t3 = y[3].xx * z32; \
                if (i < 6) t4 = X[i + 1].xx * z1; t5 = a.xx * z256; \
                if (i == 3) t6 = X[4].xx * z256; if (i == 4 || i == 5) t6 = c4.xx * z256; if (i == 6) t6 = X[6].xx * z256; SBAR(); \
                if (i > 1) y[0] = __builtin_elementwise_fma(y[0].yy, z32s, t0); y[1] = __builtin_elementwise_fma(y[1].yy, z32s, t1); \
                y[2] = __builtin_elementwise_fma(y[2].yy, z32s, t2); y[3] = __builtin_elementwise_fma(y[3].yy, z32s, t3); \
                if (i < 6) X[i + 2] = __builtin_elementwise_fma(X[i + 1].yy, z1s, t4); a = __builtin_elementwise_fma(a.yy, zs, t5); \
                if (i == 3) c4 = __builtin_elementwise_fma(X[4].yy, zs, t6); if (i == 4 || i == 5) c4 = __builtin_elementwise_fma(c4.yy, zs, t6); \
                if (i == 6) c4 = __builtin_elementwise_fma(X[6].yy, zs, t6); SBAR(); \
                if (i > 1) PUTY(4 * i, y[0]); PUTY(4 * i + 1, y[1]); PUTY(4 * i + 2, y[2]); PUTY(4 * i + 3, y[3]); \
                if (i < 6) PUTX(i + 2, X[i + 2]); PUTX(i == 1 ? 12 : (i == 2 ? 14 : 13 + i), a); \
                if (i == 3) PUTX(10, c4); if (i == 4) PUTX(13, c4); if (i == 5) PUTX(15, c4); if (i == 6) PUTX(11, c4); SBAR(); \
            } } while (0)
        f16x8 fa[8], fb[8];
#define RDT() do { _Pragma("unroll") for (int kk = 0; kk < 8; ++kk) { fa[kk] = *(LAS const f16x8*)(trd + kk * 32); fb[kk] = *(LAS const f16x8*)(trd + 21 * TROW + kk * 32); } SBAR(); } while (0)
#define MMT() do { _Pragma("unroll") for (int kk = 0; kk < 8; ++kk) dacc = MFMA_F16(fa[kk], fb[kk], dacc); } while (0)
        ANCH(bf_lo(pl), bf_lo(pa), bf_lo(pr), bf_lo(pi));
        PIN(z1); PIN(z8); PIN(z256); SBAR();
        tag = get_tag(sy); drain(); put_flag(ws, FL_UT + (unsigned)(xcd * 128 + w * 32 + bi) * FS, tag, lane);
        CHAIN(); RDT();
        ANCH(bf_hi(pl), bf_hi(pa), bf_hi(pr), bf_hi(pi));
        PIN(z1); PIN(z8); PIN(z256); SBAR(); MMT(); SBAR();
        GAS u64* utf = (GAS u64*)(ws + FL_UT + (unsigned)(xcd * 128 + (bi >> 3) * 32 + r32) * FS);
        const u64 utv = __hip_atomic_load(utf, __ATOMIC_RELAXED, __HIP_MEMORY_SCOPE_AGENT); SBAR();
        CHAIN(); RDT();
        if (__any(utv != tag)) wait_flags<4>(ws, FL_UT + (unsigned)(xcd * 128 + (bi >> 3) * 32 + r32) * FS, tag, !hi);
        CFENCE();
        const unsigned ub = WS_UT + (unsigned)(ch * S + 1024 * hh) * 2 + lane * 16;
        const u32x4 u0 = BLD(ub), u1 = BLD(ub + 1024); SBAR();
        MMT();
        STAMP(2);
        {
            LAS float* pt = (LAS float*)tab;
            *(LAS f32x4*)(pt + 8 * r32 + 4 * hi) = (f32x4){dacc[0] * oscale, dacc[1] * oscale, dacc[2] * oscale, dacc[3] * oscale};
            *(LAS f32x2*)(pt + (hi ? 384 + 2 * r32 : 256 + 4 * r32)) = (f32x2){dacc[4] * oscale, dacc[5] * oscale};
            *(LAS f32x2*)(pt + (hi ? 448 + 2 * r32 : 258 + 4 * r32)) = (f32x2){dacc[6] * oscale, dacc[7] * oscale};
            pt[hi ? 640 + r32 : 512 + r32] = dacc[8] * oscale;
            pt[hi ? 672 + r32 : 544 + r32] = dacc[9] * oscale;
            pt[hi ? 672 + r32 : 576 + r32] = dacc[10] * oscale;
            pt[hi ? 672 + r32 : 608 + r32] = dacc[11] * oscale;
        }
        LAS unsigned* ps_ = sy + SY_P + 4 * cc;
        arrive(ps_ + 0, lane);
        LAS char* ui = lds + LU + cc * UIMG;
        LAS char* kt = lds + LKT + cc * KTW;
        await(sy + SY_T1, 4); await(ps_ + 0, 2);
#pragma unroll
        for (int ps = 0; ps < 2; ++ps) {
            const int v = 81 * hh + 64 * ps + lane;
            if (ps == 0 || lane < 17) {
                const f32x4 p0 = *(LAS const f32x4*)(cb + v * 16), p1 = *(LAS const f32x4*)(cb + TABW + v * 16);
                u32x2 r = {pk_bf16(p0[0] + p1[0], p0[1] + p1[1]), pk_bf16(p0[2] + p1[2], p0[3] + p1[3])};
                if (v == 161) r = (u32x2){0u, 0u};
                *(LAS u32x2*)(kt + v * 8) = r;
            }
        }
        const u32x4 z4 = {};
        *(LAS u32x4*)(ui + (64 * hh + lane) * 16) = z4;
        if (lane < 16) *(LAS u32x4*)(ui + (128 + 16 * hh + lane) * 16) = z4;
        arrive(ps_ + 1, lane); await(ps_ + 1, 2);
        STAMP(3);
        __builtin_amdgcn_s_setprio(PRIO_B2);
#pragma unroll
        for (int ps = 0; ps < 2; ++ps) {
            unsigned W[6];
#pragma unroll
            for (int i = 0; i < 6; ++i) { const unsigned gv = gi[ps][i >> 1][i & 1];
                W[i] = *(LAS const uint16_t*)(kt + (gv & 0xffffu));
                if (i < 5) W[i] |= (unsigned)*(LAS const uint16_t*)(kt + (gv >> 16)) << 16; }
            unsigned V[5];
#pragma unroll
            for (int i = 0; i < 5; ++i) V[i] = __builtin_amdgcn_alignbit(W[i + 1], W[i], 16);
            LAS char* cp = cb + (128 * hh + 64 * ps + lane) * 16;
            *(LAS u32x4*)(cp) = (u32x4){W[0], W[1], W[2], W[3]};
            *(LAS u32x4*)(cp + CPY) = (u32x4){V[0], V[1], V[2], V[3]};
            *(LAS u32x4*)(cp + 2 * CPY) = (u32x4){W[1], W[2], W[3], W[4]};
            *(LAS u32x4*)(cp + 3 * CPY) = (u32x4){V[1], V[2], V[3], V[4]};
        }
        if (lane < 8) *(LAS u32x4*)(cb + (2 * hh + (lane >> 2)) * CPY + (256 + (lane & 3)) * 16) = z4;
        *(LAS u32x4*)(ui + (32 + 32 * hh + (lane >> 2)) * 80 + (lane & 3) * 16) = u0;
        *(LAS u32x4*)(ui + (48 + 32 * hh + (lane >> 2)) * 80 + (lane & 3) * 16) = u1;
        arrive(ps_ + 2, lane); await(ps_ + 2, 2);
        STAMP(4);
        f32x16 a0 = {}, a1 = {};
        {
            const int sb = 7 - (r32 & 7);
            LAS const char* pa = cb + (sb & 3) * CPY + (255 - (r32 >> 3) + hi) * 16 + (sb >> 2) * 8 - 64 * (16 * hh + 3) - 2048;
            LAS const char* pa2 = pa + 8; asm volatile("" : "+v"(pa2));
            LAS const char* pb = ui + (32 + r32 - 16 * hh - 3) * 80 + 16 * hi;
#define TF(off) cat8(*(LAS const s16x4*)(pa + (off)), *(LAS const s16x4*)(pa2 + (off)))
            s16x4 ta[2][2], tb[2][2]; bf16x8 b0[2], b1[2];
#define T4(q, off) (*(LAS const s16x4*)((q) + (off)))
#define CSTEP(i, j) do { const int o_ = 64 * (3 - ((j) >> 1)) + 32 * ((j) & 1), u_ = 80 * (3 - ((j) >> 1)) + 32 * ((j) & 1); \
                a1 = MFMA_BF(cat8(ta[i][0], ta[i][1]), b1[i], a1); SBAR(); ta[1 - i][0] = T4(pa, o_ + 2048); ta[1 - i][1] = T4(pa2, o_ + 2048); SBAR(); \
                a0 = MFMA_BF(cat8(ta[i][0], ta[i][1]), b0[i], a0); SBAR(); b1[1 - i] = FR(pb, u_ + 2560); b0[1 - i] = FR(pb, u_); SBAR(); \
                a1 = MFMA_BF(cat8(tb[i][0], tb[i][1]), b0[i], a1); SBAR(); tb[1 - i][0] = T4(pa, o_); tb[1 - i][1] = T4(pa2, o_); SBAR(); } while (0)
            ta[0][0] = T4(pa, 192 + 2048); ta[0][1] = T4(pa2, 192 + 2048); b1[0] = FR(pb, 240 + 2560); b0[0] = FR(pb, 240); tb[0][0] = T4(pa, 192); tb[0][1] = T4(pa2, 192);
#pragma unroll 1
            for (int m4 = 0; m4 < NCONV; ++m4) {
                CSTEP(0, 1); CSTEP(1, 2); CSTEP(0, 3); CSTEP(1, 4); CSTEP(0, 5); CSTEP(1, 6); CSTEP(0, 7);
                if (m4 < 3) { pa -= 256; pa2 -= 256; pb -= 320; }
                CSTEP(1, 0);
            }
        }
        STAMP(5);
        {
            LAS char* ex = cb + EXO + r32 * 144 + hi * 16;
            const f32x16 snd = hh ? a0 : a1; f32x16 y = hh ? a1 : a0;
#pragma unroll
            for (int g = 0; g < 4; ++g) *(LAS f32x4*)(ex + hh * 4608 + g * 32) = (f32x4){snd[4 * g], snd[4 * g + 1], snd[4 * g + 2], snd[4 * g + 3]};
            arrive(ps_ + 3, lane); await(ps_ + 3, 2);
            const float dsk = cc ? bf_hi(dw) : bf_lo(dw);
            unsigned r[8];
#pragma unroll
            for (int g = 0; g < 4; ++g) {
                const f32x4 p = *(LAS const f32x4*)(ex + (1 - hh) * 4608 + g * 32);
                const u32x2 uu = *(LAS const u32x2*)(ui + (32 + 32 * hh + r32) * 80 + 16 * g + 8 * hi);
#pragma unroll
                for (int e = 0; e < 2; ++e) {
                    const unsigned yb = pk_bf16(y[4 * g + 2 * e] + p[2 * e], y[4 * g + 2 * e + 1] + p[2 * e + 1]), sk = pk_bf16(bf_lo(uu[e]) * dsk, bf_hi(uu[e]) * dsk);
                    r[2 * g + e] = pk_bf16(bf_lo(yb) + bf_lo(sk), bf_hi(yb) + bf_hi(sk));
                }
            }
            LAS char* sc = cb + hh * 2048;
#pragma unroll
            for (int g = 0; g < 4; ++g) *(LAS u32x2*)(sc + 64 * r32 + 16 * g + 8 * hi) = (u32x2){r[2 * g], r[2 * g + 1]};
            const u32x4 o0 = *(LAS const u32x4*)(sc + lane * 16), o1 = *(LAS const u32x4*)(sc + 1024 + lane * 16);
            const unsigned bo = WS_BT + (unsigned)(ch * S + 1024 * hh) * 2 + lane * 16;
            BST(o0, bo); BST(o1, bo + 1024);
        }
        STAMP(6);
    } else {
        {
            const unsigned nb = (bi & 1) ? 12 * 2 * D : 16 * 2 * D;
            unsigned v4[4];
#pragma unroll
            for (int i = 0; i < 4; ++i) { const unsigned o = (unsigned)(w * 4096 + i * 1024 + lane * 16); v4[i] = (o < nb ? o : 0u) + 4096 - i * 1024; }
            glds4((const char*)(wo + (size_t)(DH * (bi >> 1) + 16 * (bi & 1)) * D) - 4096, v4[0], v4[1], v4[2], v4[3], lds0 + LB + w * 4096);
        }
        const u64 e0 = __hip_atomic_load((GAS u64*)ws, __ATOMIC_RELAXED, __HIP_MEMORY_SCOPE_AGENT);
        asm volatile("s_waitcnt lgkmcnt(0)\n\ts_barrier" ::: "memory");
        drain(); KEEPPF();
        if (w == 0 && lane == 0) __hip_atomic_store((LAS u64*)sy, mix(e0), __ATOMIC_RELAXED, __HIP_MEMORY_SCOPE_WORKGROUP);
        arrive(sy + SY_WO, lane); await(sy + SY_WO, 4);
        LAS const char* p = lds + LB + (4 * hi + ((lane & 15) >> 2)) * (2 * D) + ((lane >> 4) & 1) * 32 + (lane & 3) * 8;
#pragma unroll
        for (int i = 0; i < 4; ++i) {
            const int ct = w + 4 * i;
            if (ct < 14) {
                u32x4 o = __builtin_bit_cast(u32x4, cat8(trread(p + ct * 64), trread(p + 8 * 2 * D + ct * 64)));
                if (hi && (bi & 1)) { o[2] = 0; o[3] = 0; }
                BST(o, WS_WO + (unsigned)((bi * 14 + ct) * 64 + lane) * 16);
            }
        }
        tag = get_tag(sy);
        drain();
        put_flag(ws, FL_WO + (unsigned)(w * 32 + bi) * FS, tag, lane);
    }
    const int qt = 8 * xcd + (bi >> 2), ct = 4 * (bi & 3) + w;
    const unsigned voff = lane * 16;
    drain();
    if (xcd < 7) put_flag(ws, FL_BT + (unsigned)(xcd * 128 + bi * 4 + w) * FS, tag, lane);
    bf16x8 wf[32];
    await_slow(sy + SY_G, 4);
    wait_flags<8>(ws, FL_WO + (unsigned)(w * 32 + r32) * FS, tag, !hi);
    if (ct < 14) {
#pragma unroll
        for (int i = 0; i < 32; ++i) { wf[i] = BC8(BLD(WS_WO + (unsigned)(i * 14 + ct) * 1024 + voff)); __builtin_amdgcn_s_sleep(WOSL); SBAR(); }
    }
    STAMP(7);
    if (xcd < 7) {
        wait_flags<32>(ws, FL_BT + (unsigned)(xcd * 128 + w * 32 + r32) * FS, tag, !hi);
        const unsigned st = LU + (w >> 1) * UIMG + (w & 1) * 3840;
        const unsigned bo = WS_BT + (unsigned)((64 * xcd + 16 * w + (lane >> 3)) * S + 64 * bi + (lane & 7) * 8) * 2;
        arrive(sy + SY_BG, lane); await(sy + SY_BG, 4);
        glds2c(ws, bo, bo + 8 * S * 2 - 1024, lds0 + st);
        drain();
        u32x4 o0, o1; transpose16(lds + st, lane, o0, o1);
        u32x4* dst = (u32x4*)(out + (size_t)(64 * bi + lane) * 2 * D + D + 64 * xcd + 16 * w);
        dst[0] = o0; dst[1] = o1;
    }
    STAMP(8);
    await_slow(sy + SY_X, 4);
    wait_flags<OSL>(ws, FL_O + (unsigned)(qt * 16 + 4 * w + lane) * FS, tag, lane < 4);
    STAMP(9);
    {
        u32x4 of[8];
#pragma unroll
        for (int i = 0; i < 8; ++i) of[i] = BLD(WS_O + (unsigned)(qt * 32 + 8 * w + i) * 1024 + voff);
#pragma unroll
        for (int i = 0; i < 8; ++i) *(LAS u32x4*)(lds + LB + (8 * w + i) * 1024 + voff) = of[i];
        STAMP(10);
        arrive(sy + SY_B2, lane); await(sy + SY_B2, 4);
    }
    STAMP(11);
    if (ct < 14) {
        f32x16 acc = {};
        LAS const char* po = lds + LB + voff;
        bf16x8 of[4];
#pragma unroll
        for (int i = 0; i < 3; ++i) of[i] = FR(po, i * 1024);
#pragma unroll
        for (int i = 0; i < 32; ++i) { if (i < 29) of[(i + 3) & 3] = FR(po, (i + 3) * 1024); SBAR(); acc = MFMA_BF(wf[i], of[i & 3], acc); SBAR(); }
        u32x4* op = (u32x4*)(out + (size_t)(32 * qt + r32) * 2 * D + 32 * ct + 8 * hi);
        op[0] = widen(pk_bf16(acc[0], acc[1]), pk_bf16(acc[2], acc[3]), pk_bf16(acc[4], acc[5]), pk_bf16(acc[6], acc[7]));
        op[2] = widen(pk_bf16(acc[8], acc[9]), pk_bf16(acc[10], acc[11]), pk_bf16(acc[12], acc[13]), pk_bf16(acc[14], acc[15]));
    }
    if (b == 0 && w == 0) put_flag(ws, 0, tag, lane);
    STAMP(12);
}

__global__ __launch_bounds__(512, 2) void fused(const uint16_t* __restrict__ x, const uint16_t* __restrict__ wqkv, const uint16_t* __restrict__ wo,
                                               const uint16_t* __restrict__ u, const uint16_t* __restrict__ lar, const uint16_t* __restrict__ aim,
                                               const uint16_t* __restrict__ cre, const uint16_t* __restrict__ cim, const uint16_t* __restrict__ ldt,
                                               const uint16_t* __restrict__ dskip, uint16_t* __restrict__ out, char* __restrict__ ws) {
    extern __shared__ __attribute__((aligned(16))) char smem[];
    LAS char* lds = (LAS char*)smem;
    const int lane = threadIdx.x & 63, w = __builtin_amdgcn_readfirstlane(threadIdx.x >> 6);
    const u64 t_start = __builtin_amdgcn_s_memrealtime();
    if (w < 4) role_a(x, wqkv, ws, lds, w, lane, t_start);
    else role_b(x, wqkv, wo, u, lar, aim, cre, cim, ldt, dskip, out, ws, lds, w - 4, lane, t_start);
}
}

extern "C" void kernel_launch(void* const* d_in, const int* in_sizes, int n_in, void* d_out, int out_size, void* d_ws, size_t ws_size, hipStream_t stream) {
    using namespace k;
    (void)hipFuncSetAttribute((const void*)fused, hipFuncAttributeMaxDynamicSharedMemorySize, LDS_BYTES);
    const uint16_t* const* in = (const uint16_t* const*)d_in;
    fused<<<256, 512, LDS_BYTES, stream>>>(in[0], in[1], in[2], in[3], in[4], in[5], in[6], in[7], in[8], in[9], (uint16_t*)d_out, (char*)d_ws);
}
```

```cpp
#define HA -1
#define HB 12
#define HT 4000
#include <hip/hip_runtime.h>
#include <hip/hip_bf16.h>
#include <stdint.h>

typedef short bf16x8 __attribute__((ext_vector_type(8)));
typedef _Float16 f16x8 __attribute__((ext_vector_type(8)));
typedef _Float16 f16x2 __attribute__((ext_vector_type(2)));
typedef __bf16 bf16x2 __attribute__((ext_vector_type(2)));
typedef short s16x4 __attribute__((ext_vector_type(4)));
typedef float f32x16 __attribute__((ext_vector_type(16)));
typedef float f32x4 __attribute__((ext_vector_type(4)));
typedef float f32x2 __attribute__((ext_vector_type(2)));
typedef unsigned u32x4 __attribute__((ext_vector_type(4)));
typedef unsigned u32x2 __attribute__((ext_vector_type(2)));
typedef unsigned long long u64;
#define LAS __attribute__((address_space(3)))
#define GAS __attribute__((address_space(1)))
#define DI __device__ __forceinline__
#define MFMA_BF(a, b, c) __builtin_amdgcn_mfma_f32_32x32x16_bf16((a), (b), (c), 0, 0, 0)
#define MFMA16(a, b, c) __builtin_amdgcn_mfma_f32_16x16x32_bf16((a), (b), (c), 0, 0, 0)
#define MFMA_F16(a, b, c) __builtin_amdgcn_mfma_f32_32x32x16_f16((a), (b), (c), 0, 0, 0)
#define SBAR() __builtin_amdgcn_sched_barrier(0)
#define CFENCE() asm volatile("" ::: "memory")
#define PIN(x) asm volatile("" : "+v"(x))
#define BC8(x) __builtin_bit_cast(bf16x8, (x))

#define PRIO_A1 0
#define PRIO_A2 0
#define PRIO_B 1
#define WOSL 2
#define PRIO_B2 1
#define RHO ((2 * (bi & 3) + (bi >> 2)) % 7)
#define KVSL 2
#define OSL 2
#define WOAUX 16
#define NCONV 4
#define KVLD glds16c
#define NOPF
namespace k {
#define STAMP(i) do { if ((i) == ((threadIdx.x >> 8) ? HB : HA)) { while (__builtin_amdgcn_s_memrealtime() - t_start < HT) __builtin_amdgcn_s_sleep(2); } } while (0)
constexpr int S = 2048, D = 448, DH = 28, NS = 256;
constexpr unsigned FS = 128;
constexpr unsigned FL_KV = FS, FL_O = FL_KV + 1024 * FS, FL_UT = FL_O + 1024 * FS, FL_BT = FL_UT + 896 * FS, FL_WO = FL_BT + 896 * FS;
constexpr unsigned WS_KV = 512u << 10;
constexpr unsigned WS_O = WS_KV + (4u << 20);
constexpr unsigned WS_WO = WS_O + (2u << 20);
constexpr unsigned WS_UT = WS_WO + (448u << 10);
constexpr unsigned WS_BT = WS_UT + D * S * 2;
constexpr unsigned WS_END = WS_BT + D * S * 2;
constexpr int STG = 28672, LB = 86016, LQ = 0, LX = 8192;
constexpr int TROW = 272, TABW = 53 * TROW, CBW = 2 * TABW, CPY = 4160, EXO = 4 * CPY;
constexpr int LU = LB + 2 * CBW, UIMG = 7680, LKT = LU + 2 * UIMG, KTW = 1312, LSY = LKT + 2 * KTW, LDS_BYTES = LSY + 256;
static_assert(LDS_BYTES <= 163840 && (LSY & 15) == 0, "lds");
enum { SY_TAG = 0, SY_F = 2, SY_P = 10, SY_WO = 18, SY_BG = 19, SY_Q = 20, SY_X = 21, SY_G = 22, SY_B2 = 23, SY_T1 = 24 };
constexpr int SPIN = 1 << 18;

struct IdxTab { uint16_t v[256][12]; };
constexpr IdxTab make_idx() {
    IdxTab t = {};
    for (int y = 0; y < 256; ++y) for (int e = 0; e < 12; ++e) {
        const int j = 2047 - 8 * y - e; int ix = 644;
        if (j >= 0) { const int sh = j >= 1024 ? 3 : (j >= 512 ? 2 : (j >= 256 ? 1 : 0));
            ix = sh ? 128 * sh + ((j + (1 << (sh - 1)) - 1 + ((j >> sh) & 1)) >> sh) : j; }
        t.v[y][e] = (uint16_t)(2 * ix);
    }
    return t;
}
__device__ const IdxTab g_idx __attribute__((aligned(16))) = make_idx();

DI unsigned pk_bf16(float a, float b) { f32x2 v = {a, b}; return __builtin_bit_cast(unsigned, __builtin_convertvector(v, bf16x2)); }
DI unsigned pk_f16(f32x2 v) { return __builtin_bit_cast(unsigned, __builtin_convertvector(v, f16x2)); }
DI float bf_lo(unsigned w) { return __uint_as_float(w << 16); }
DI float bf_hi(unsigned w) { return __uint_as_float(w & 0xffff0000u); }
DI float rbf(float x) { return bf_lo(pk_bf16(x, x)); }
DI void glds16(const void* sbase, unsigned voff, unsigned lds_base) {
    unsigned sv;
    asm volatile("s_mov_b32 %0, m0\n\ts_mov_b32 m0, %3\n\ts_nop 0\n\tglobal_load_lds_dwordx4 %1, %2\n\ts_mov_b32 m0, %0"
                 : "=&s"(sv) : "v"(voff), "s"(sbase), "s"(lds_base) : "memory");
}
DI void glds16c(const void* sbase, unsigned voff, unsigned lds_base) {
    unsigned sv;
    asm volatile("s_mov_b32 %0, m0\n\ts_mov_b32 m0, %3\n\ts_nop 0\n\tglobal_load_lds_dwordx4 %1, %2 sc1\n\ts_mov_b32 m0, %0"
                 : "=&s"(sv) : "v"(voff), "s"(sbase), "s"(lds_base) : "memory");
}
#define GL1(V, O, C) "global_load_lds_dwordx4 " V ", %1 offset:" #O C "\n\t"
DI void glds4(const void* sbase, unsigned v0, unsigned v1, unsigned v2, unsigned v3, unsigned lds_base) {
    unsigned sv;
    asm volatile("s_mov_b32 %0, m0\n\ts_mov_b32 m0, %2\n\ts_nop 0\n\t" GL1("%3", 0, "") GL1("%4", 1024, "") GL1("%5", 2048, "") GL1("%6", 3072, "") "s_mov_b32 m0, %0"
                 : "=&s"(sv) : "s"(sbase), "s"(lds_base), "v"(v0), "v"(v1), "v"(v2), "v"(v3) : "memory");
}
DI void glds3(const void* sbase, unsigned v0, unsigned v1, unsigned v2, unsigned lds_base) {
    unsigned sv;
    asm volatile("s_mov_b32 %0, m0\n\ts_mov_b32 m0, %2\n\ts_nop 0\n\t" GL1("%3", 0, "") GL1("%4", 1024, "") GL1("%5", 2048, "") "s_mov_b32 m0, %0"
                 : "=&s"(sv) : "s"(sbase), "s"(lds_base), "v"(v0), "v"(v1), "v"(v2) : "memory");
}
template <int OFF> DI void glds1(const void* sbase, unsigned v0, unsigned lds_base) {
    unsigned sv;
    asm volatile("s_mov_b32 %0, m0\n\ts_mov_b32 m0, %2\n\ts_nop 0\n\tglobal_load_lds_dwordx4 %3, %1 offset:%4\n\ts_mov_b32 m0, %0" : "=&s"(sv) : "s"(sbase), "s"(lds_base), "v"(v0), "i"(OFF) : "memory");
}
DI void glds2(const void* sbase, unsigned v0, unsigned v1, unsigned lds_base) {
    unsigned sv;
    asm volatile("s_mov_b32 %0, m0\n\ts_mov_b32 m0, %2\n\ts_nop 0\n\t" GL1("%3", 0, "") GL1("%4", 1024, "") "s_mov_b32 m0, %0"
                 : "=&s"(sv) : "s"(sbase), "s"(lds_base), "v"(v0), "v"(v1) : "memory");
}
DI void glds4c(const void* sbase, unsigned v0, unsigned lds_base) {
    unsigned sv;
    asm volatile("s_mov_b32 %0, m0\n\ts_mov_b32 m0, %2\n\ts_nop 0\n\t" GL1("%3", 0, " sc1") GL1("%3", 1024, " sc1") GL1("%3", 2048, " sc1") GL1("%3", 3072, " sc1") "s_mov_b32 m0, %0"
                 : "=&s"(sv) : "s"(sbase), "s"(lds_base), "v"(v0) : "memory");
}
DI void glds2c(const void* sbase, unsigned v0, unsigned v1, unsigned lds_base) {
    unsigned sv;
    asm volatile("s_mov_b32 %0, m0\n\ts_mov_b32 m0, %2\n\ts_nop 0\n\t" GL1("%3", 0, " sc1") GL1("%4", 1024, " sc1") "s_mov_b32 m0, %0"
                 : "=&s"(sv) : "s"(sbase), "s"(lds_base), "v"(v0), "v"(v1) : "memory");
}
DI float wave_max(float v) {
#define DPPMAX(ctrl, rm) v = fmaxf(v, __uint_as_float(__builtin_amdgcn_update_dpp(0, __float_as_uint(v), ctrl, rm, 0xf, true)))
    DPPMAX(0x111, 0xf); DPPMAX(0x112, 0xf); DPPMAX(0x114, 0xf); DPPMAX(0x118, 0xf); DPPMAX(0x142, 0xa); DPPMAX(0x143, 0xc);
    return __uint_as_float(__builtin_amdgcn_readlane(__float_as_uint(v), 63));
}
DI s16x4 trread(LAS const char* p) { return __builtin_bit_cast(s16x4, __builtin_amdgcn_ds_read_tr16_b64_v4i16((LAS s16x4*)p)); }
DI bf16x8 cat8(s16x4 a, s16x4 b) { return __builtin_shufflevector(a, b, 0, 1, 2, 3, 4, 5, 6, 7); }
DI f32x2 cmul(f32x2 a, f32x2 b, f32x2 bs) { return __builtin_elementwise_fma(a.yy, bs, a.xx * b); }
DI f32x2 rot(f32x2 b) { return (f32x2){-b.y, b.x}; }
DI f32x2 anchor(float a2, float rev, float s) {
    const float mag = __builtin_amdgcn_exp2f(a2 * s);
    float r = rev * s; r -= __builtin_rintf(r);
    return (f32x2){mag * __builtin_amdgcn_cosf(r), mag * __builtin_amdgcn_sinf(r)};
}
DI void transpose16(LAS const char* lds, int lane, u32x4& o0, u32x4& o1) {
    LAS const char* p = lds + ((lane & 15) >> 2) * 128 + (lane >> 4) * 32 + (lane & 3) * 8;
    const s16x4 a = trread(p), b = trread(p + 512), c = trread(p + 1024), d = trread(p + 1536);
    o0 = __builtin_bit_cast(u32x4, cat8(a, b)); o1 = __builtin_bit_cast(u32x4, cat8(c, d));
}
DI void arrive(LAS unsigned* c, int) {
    asm volatile("s_mov_b64 exec, 1\n\tds_add_u32 %0, %1\n\ts_mov_b64 exec, -1" :: "v"((unsigned)(uintptr_t)c), "v"(1u) : "memory");
}
DI void await(LAS unsigned* c, unsigned n) {
    unsigned t, i = SPIN;
    asm volatile("1:\n\tds_read_b32 %0, %2\n\ts_waitcnt lgkmcnt(0)\n\tv_cmp_gt_u32 vcc, %3, %0\n\ts_cbranch_vccz 2f\n\ts_sleep 1\n\ts_sub_u32 %1, %1, 1\n\ts_cbranch_scc0 1b\n2:"
                 : "=&v"(t), "+s"(i) : "v"((unsigned)(uintptr_t)c), "s"(n) : "vcc", "scc", "memory");
}
DI u64 get_tag(LAS unsigned* sy) {
    u64 t = 0;
#pragma unroll 1
    for (int i = 0; i < SPIN; ++i) {
        const u64 v = __hip_atomic_load((LAS u64*)sy, __ATOMIC_RELAXED, __HIP_MEMORY_SCOPE_WORKGROUP);
        t = ((u64)(unsigned)__builtin_amdgcn_readfirstlane((unsigned)(v >> 32)) << 32) | (unsigned)__builtin_amdgcn_readfirstlane((unsigned)v);
        if (t) break;
        __builtin_amdgcn_s_sleep(1);
    }
    return t;
}
DI void drain() { asm volatile("s_waitcnt vmcnt(0)" ::: "memory"); }
DI void put_flag(char* ws, unsigned off, u64 tag, int lane) { if (lane == 0) __hip_atomic_store((GAS u64*)(ws + off), tag, __ATOMIC_RELAXED, __HIP_MEMORY_SCOPE_AGENT); }
template <int SLP> DI void wait_flags(char* ws, unsigned off, u64 tag, bool pend) {
    GAS u64* p = (GAS u64*)(ws + off);
    CFENCE();
#pragma unroll 1
    for (int i = 0; i < SPIN; ++i) {
        if (pend) pend = __hip_atomic_load(p, __ATOMIC_RELAXED, __HIP_MEMORY_SCOPE_AGENT) != tag;
        if (!__any(pend)) break;
        __builtin_amdgcn_s_sleep(SLP);
    }
    CFENCE();
}
DI void await_slow(LAS unsigned* c, unsigned n) {
#pragma unroll 1
    for (int i = 0; i < SPIN && __hip_atomic_load(c, __ATOMIC_RELAXED, __HIP_MEMORY_SCOPE_WORKGROUP) < n; ++i) __builtin_amdgcn_s_sleep(4);
    CFENCE();
}
#define BST(v, off) __builtin_amdgcn_raw_buffer_store_b128((v), rs, (off), 0, 16)
#define BLD(off) __builtin_amdgcn_raw_buffer_load_b128(rs, (off), 0, 16)
#define FR(p, off) (*(LAS const bf16x8*)((p) + (off)))
DI u32x4 widen(unsigned a0, unsigned a1, unsigned b0, unsigned b1) {
    const auto r0 = __builtin_amdgcn_permlane32_swap(a0, b0, false, false), r1 = __builtin_amdgcn_permlane32_swap(a1, b1, false, false);
    return (u32x4){r0[0], r1[0], r0[1], r1[1]};
}

#define BAR() asm volatile("s_waitcnt lgkmcnt(0)\n\ts_barrier" ::: "memory")
#define EX(x) __builtin_amdgcn_exp2f(x)
DI float swapmax(float m) { const auto r = __builtin_amdgcn_permlane32_swap(__float_as_uint(m), __float_as_uint(m), false, false); return fmaxf(__uint_as_float(r[0]), __uint_as_float(r[1])); }
DI u32x4 put_ref(u32x4 q1, float m, int hi) {
    const float m1 = rbf(m), m2 = rbf(m - m1), m3 = m - m1 - m2;
    if (hi) { q1[2] = pk_bf16(-m1, -m2); q1[3] = pk_bf16(-m3, 0.f); }
    return q1;
}
DI u64 mix(u64 e) { u64 z = e + 0x9E3779B97F4A7C15ull; z = (z ^ (z >> 30)) * 0xBF58476D1CE4E5B9ull; z = (z ^ (z >> 27)) * 0x94D049BB133111EBull; return (z ^ (z >> 31)) | 1ull; }

DI void role_a(const uint16_t* __restrict__ x, const uint16_t* __restrict__ wqkv, char* __restrict__ ws, LAS char* lds, int w, int lane, u64 t_start) {
    const int head = 4 * (blockIdx.x & 3) + ((blockIdx.x >> 3) & 3), grp = 8 * ((blockIdx.x >> 2) & 1) + (blockIdx.x >> 5);
    const unsigned lds0 = (unsigned)(uintptr_t)lds;
    LAS unsigned* sy = (LAS unsigned*)(lds + LSY);
    const __amdgpu_buffer_rsrc_t rs = __builtin_amdgcn_make_buffer_rsrc(ws, 0, WS_END, 0x00020000);
    const int bi = blockIdx.x >> 3, r32 = lane & 31, hi = lane >> 5;
    const int wsh = (head & 7) == 7 ? 4 : 0, rho = RHO;
    __builtin_amdgcn_s_setprio(PRIO_A1);
    const unsigned wv = (unsigned)((lane >> 2) * 3 * D + (lane & 3) * 8) * 2;
    const unsigned xv0 = (unsigned)((lane >> 3) * D + (((lane & 7) ^ (lane >> 4)) * 8)) * 2, xv1 = (unsigned)((lane >> 3) * D + (((lane & 7) ^ (4 + (lane >> 4))) * 8)) * 2;
    const unsigned xv[4] = {xv0, xv1 + 16 * D - 1024, xv0 + 32 * D - 2048, xv1 + 48 * D - 3072};
    const unsigned wvv[3] = {wv + 2048, wv + 2 * D + 1024, wv + 4 * D};
    const char* wb = (const char*)(wqkv + DH * head - wsh + (size_t)(16 * w) * 3 * D) - 2048;
    const char* xb = (const char*)(x + (size_t)(128 * grp + 32 * w) * D);
#define LOADX(sp, sl) glds4(xb + (sp) * 128, xv[0], xv[1], xv[2], xv[3], lds0 + (sl) + w * 4096)
#define LOADW(sp, sl) glds3(wb + (size_t)(sp) * 64 * 3 * D * 2, wvv[0], wvv[1], wvv[2], lds0 + (sl) + 16384 + w * 3072)
    int sp = rho;
    LOADW(sp, 0); LOADX(sp, 0); sp = sp == 6 ? 0 : sp + 1;
    asm volatile("s_barrier" ::: "memory");
    LOADW(sp, STG); LOADX(sp, STG); sp = sp == 6 ? 0 : sp + 1;
    STAMP(1);
    const int xs = (r32 >> 1) & 7;
    LAS const char* xp[4];
#pragma unroll
    for (int ks = 0; ks < 4; ++ks) xp[ks] = lds + w * 4096 + r32 * 128 + (((2 * ks + hi) ^ xs) * 16);
    LAS const char* wp = lds + 16384 + (8 * hi + ((lane & 15) >> 2)) * 64 + ((lane >> 4) & 1) * 32 + (lane & 3) * 8 + wsh * 2;
    f32x16 aq = {}, ak = {}, av = {};
    int so = 0;
    bf16x8 xf[4]; s16x4 fr[4][6];
#define RX(ks, o) xf[ks] = FR(xp[ks], o)
#define RW(ks, j, o) fr[ks][j] = trread(wp + (o) + (ks) * 3072 + ((((j) >> 1) + 1) % 3) * 1024 + ((j) & 1) * 256)
#define MK(ks) ak = MFMA_BF(cat8(fr[ks][0], fr[ks][1]), xf[ks], ak); SBAR()
#define MV(ks) av = MFMA_BF(xf[ks], cat8(fr[ks][2], fr[ks][3]), av); SBAR()
#define MQ(ks) aq = MFMA_BF(cat8(fr[ks][4], fr[ks][5]), xf[ks], aq); SBAR()
#define QSTEP(ks, L0, L1) do { \
            MK(ks); RX(ks + 1, so); RW(ks + 1, 0, so); RW(ks + 1, 1, so); SBAR(); \
            MV(ks); RW(ks + 1, 2, so); RW(ks + 1, 3, so); SBAR(); if (LD) { L0; } \
            MQ(ks); RW(ks + 1, 4, so); RW(ks + 1, 5, so); SBAR(); if (LD) { L1; } } while (0)
#define STAGE(LD_, NXT, VM) do { constexpr bool LD = LD_; \
        const char* wg = wb + (size_t)sp * 64 * 3 * D * 2; const char* xg = xb + sp * 128; const unsigned lf = lds0 + sf; \
        QSTEP(0, glds2(wg, wvv[0], wvv[1], lf + 16384 + w * 3072), glds1<2048>(wg, wvv[2], lf + 16384 + w * 3072)); \
        QSTEP(1, glds2(xg, xv[0], xv[1], lf + w * 4096), (glds1<2048>(xg, xv[2], lf + w * 4096))); \
        QSTEP(2, (glds1<3072>(xg, xv[3], lf + w * 4096)), ); \
        asm volatile("s_waitcnt vmcnt(" #VM ")" ::: "memory"); \
        arrive(fc, lane); SBAR(); \
        MK(3); await(fc, 4); ++fc; \
        sf = so; so = so == 2 * STG ? 0 : so + STG; sp = sp == 6 ? 0 : sp + 1; \
        if (NXT) { RX(0, so); RW(0, 0, so); RW(0, 1, so); RW(0, 2, so); } SBAR(); \
        MV(3); if (NXT) { RW(0, 3, so); RW(0, 4, so); RW(0, 5, so); } SBAR(); \
        MQ(3); } while (0)
    LAS unsigned* fc = sy + SY_F;
    int sf = 2 * STG;
    asm volatile("s_waitcnt vmcnt(7)" ::: "memory");
    arrive(fc, lane); await(fc, 4); ++fc;
    STAMP(16);
    RX(0, 0); RW(0, 0, 0); RW(0, 1, 0); RW(0, 2, 0); RW(0, 3, 0); RW(0, 4, 0); RW(0, 5, 0); SBAR();
#pragma unroll 1
    for (int st = 0; st < 5; ++st) STAGE(true, true, 7);
    const u64 tag = get_tag(sy);
    STAGE(false, true, 0);
    STAGE(false, false, 0);
    STAMP(3);
    const float cq = 0.18898223650461362f * 1.4426950408889634f;
    u32x4 q0, q1, k0, k1, v0, v1;
#pragma unroll
    for (int i = 0; i < 4; ++i) {
        k0[i] = pk_bf16(ak[2 * i], ak[2 * i + 1]); k1[i] = pk_bf16(ak[8 + 2 * i], ak[9 + 2 * i]);
        v0[i] = pk_bf16(av[2 * i], av[2 * i + 1]); v1[i] = pk_bf16(av[8 + 2 * i], av[9 + 2 * i]);
        if (r32 >= DH) { v0[i] = v1[i] = (r32 == DH ? 0x3f803f80u : 0u); }
    }
    if (hi) { k1[2] = 0x3f803f80u; k1[3] = 0x00003f80u; }
    const unsigned tb = WS_KV + (unsigned)(head * 64 + 4 * grp + w) * 4096 + lane * 16;
    BST(k0, tb); BST(k1, tb + 1024); BST(v0, tb + 2048); BST(v1, tb + 3072);
#pragma unroll
    for (int i = 0; i < 4; ++i) { q0[i] = pk_bf16(aq[2 * i] * cq, aq[2 * i + 1] * cq); q1[i] = pk_bf16(aq[8 + 2 * i] * cq, aq[9 + 2 * i] * cq); }
    if (hi) { q1[2] = 0; q1[3] = 0; }
    {
        const f32x16 zero = {};
        f32x16 s = MFMA_BF(BC8(k0), BC8(q0), zero); s = MFMA_BF(BC8(k1), BC8(q1), s);
        float m = s[0];
#pragma unroll
        for (int i = 1; i < 16; ++i) m = fmaxf(m, s[i]);
        q1 = put_ref(q1, swapmax(m), hi);
    }
    LAS u32x4* qo = (LAS u32x4*)(lds + LQ + w * 2048) + lane;
    qo[0] = q0; qo[64] = q1;
    arrive(sy + SY_Q, lane);
    drain();
    put_flag(ws, FL_KV + (unsigned)((head * 4 + w) * 16 + grp) * FS, tag, lane);
    STAMP(4);
    await(sy + SY_Q, 4);
    __builtin_amdgcn_s_setprio(PRIO_A2);
    const unsigned voff = lane * 16;
    const f32x16 zero = {};
    bf16x8 Q[4][2];
#pragma unroll
    for (int i = 0; i < 4; ++i) { LAS const char* p = lds + LQ + ((w + i) & 3) * 2048 + voff; Q[i][0] = FR(p, 0); Q[i][1] = FR(p, 1024); }
    u32x4 kv[3][4] = {{k0, k1, v0, v1}, {}, {}};
    f32x16 O[4] = {}, sa, sb;
    u32x4 p0, p1;
#define GRP(SC, i, P, j) do { float e0 = EX(SC[i]); SBAR(); float e1 = EX(SC[i + 1]); SBAR(); float e2 = EX(SC[i + 2]); SBAR(); float e3 = EX(SC[i + 3]); SBAR(); \
        P[j] = pk_bf16(e0, e1); SBAR(); P[j + 1] = pk_bf16(e2, e3); PIN(P); SBAR(); } while (0)
#define SLOT(SC, SN, cb, i, QK, PV1, LD) do { constexpr int nb_ = (i) < 3 ? (cb) : ((cb) + 1) % 3, pb_ = (i) > 0 ? (cb) : ((cb) + 2) % 3; \
        if (QK) SN = MFMA_BF(BC8(kv[nb_][0]), Q[((i) + 1) & 3][0], zero); SBAR(); GRP(SC, 0, p0, 0); \
        if (PV1) O[((i) + 3) & 3] = MFMA_BF(BC8(kv[pb_][3]), BC8(p1), O[((i) + 3) & 3]); SBAR(); \
        if (LD) { kv[((cb) + 2) % 3][i] = __builtin_amdgcn_raw_buffer_load_b128(rs, voff + (i) * 1024, kvo, 16); SBAR(); } GRP(SC, 4, p0, 2); \
        if (QK) SN = MFMA_BF(BC8(kv[nb_][1]), Q[((i) + 1) & 3][1], SN); SBAR(); GRP(SC, 8, p1, 0); \
        O[i] = MFMA_BF(BC8(kv[cb][2]), BC8(p0), O[i]); SBAR(); GRP(SC, 12, p1, 2); } while (0)
    const unsigned kvb = WS_KV + (unsigned)(head * 64 + w) * 4096;
    unsigned kvo = 0;
    sa = MFMA_BF(BC8(k0), Q[0][0], zero); sa = MFMA_BF(BC8(k1), Q[0][1], sa);
    SLOT(sa, sb, 0, 0, true, false, false); SLOT(sb, sa, 0, 1, true, true, false); SLOT(sa, sb, 0, 2, true, true, false); SLOT(sb, sa, 0, 3, false, true, false);
    STAMP(5);
    wait_flags<KVSL>(ws, FL_KV + (unsigned)((head * 4 + w) * 16 + lane) * FS, tag, lane < 16 && lane != grp);
    arrive(sy + SY_G, lane);
    STAMP(6);
#pragma unroll
    for (int j = 1; j < 3; ++j)
#pragma unroll
        for (int f = 0; f < 4; ++f) kv[j][f] = __builtin_amdgcn_raw_buffer_load_b128(rs, voff + f * 1024, kvb + ((grp + j) & 15) * 16384, 16);
    sa = MFMA_BF(BC8(kv[1][0]), Q[0][0], zero); sa = MFMA_BF(BC8(kv[1][1]), Q[0][1], sa);
    STAMP(7);
    int nx = grp + 3;
#define TILE(cb) do { kvo = kvb + (nx & 15) * 16384; ++nx; SBAR(); \
        SLOT(sa, sb, cb, 0, true, true, true); SLOT(sb, sa, cb, 1, true, true, true); SLOT(sa, sb, cb, 2, true, true, true); SLOT(sb, sa, cb, 3, true, true, true); } while (0)
#pragma unroll 1
    for (int it = 0; it < 5; ++it) { TILE(1); TILE(2); TILE(0); }
    O[3] = MFMA_BF(BC8(kv[0][3]), BC8(p1), O[3]);
    STAMP(8);
    f32x16 o = O[0];
    {
#pragma unroll
        for (int i = 1; i < 4; ++i) {
            LAS char* ex = lds + LX + (((w + i) & 3) * 3 + i - 1) * 4096 + voff;
#pragma unroll
            for (int g = 0; g < 4; ++g) *(LAS f32x4*)(ex + g * 1024) = (f32x4){O[i][4 * g], O[i][4 * g + 1], O[i][4 * g + 2], O[i][4 * g + 3]};
        }
        arrive(sy + SY_X, lane); await(sy + SY_X, 4);
        LAS const char* er = lds + LX + w * 3 * 4096 + voff;
#pragma unroll
        for (int i = 0; i < 3; ++i)
#pragma unroll
            for (int g = 0; g < 4; ++g) { const f32x4 r = *(LAS const f32x4*)(er + i * 4096 + g * 1024);
#pragma unroll
                for (int e = 0; e < 4; ++e) o[4 * g + e] += r[e]; }
        f32x2 chk = {0.f, 0.f};
#pragma unroll
        for (int i = 0; i < 8; ++i) chk = __builtin_elementwise_fma((f32x2){o[2 * i], o[2 * i + 1]}, (f32x2){0.f, 0.f}, chk);
        if (__builtin_expect(__any(!(chk[0] + chk[1] == 0.f) || !(o[12] < 1e30f)), 0)) {
            const unsigned hb = WS_KV + (unsigned)head * 64 * 4096 + voff;
            u32x4 qr = __builtin_bit_cast(u32x4, Q[0][1]); if (hi) { qr[2] = 0; qr[3] = 0; }
            float mm = -INFINITY;
            wait_flags<4>(ws, FL_KV + (unsigned)(head * 64 + lane) * FS, tag, true);
#pragma unroll 1
            for (int t = 0; t < 64; ++t) {
                f32x16 s = MFMA_BF(BC8(BLD(hb + t * 4096)), Q[0][0], zero); s = MFMA_BF(BC8(BLD(hb + t * 4096 + 1024)), BC8(qr), s);
#pragma unroll
                for (int i = 0; i < 16; ++i) mm = fmaxf(mm, s[i]);
            }
            qr = put_ref(qr, swapmax(mm), hi);
            o = zero;
#pragma unroll 1
            for (int t = 0; t < 64; ++t) {
                f32x16 s = MFMA_BF(BC8(BLD(hb + t * 4096)), Q[0][0], zero); s = MFMA_BF(BC8(BLD(hb + t * 4096 + 1024)), BC8(qr), s);
#pragma unroll
                for (int i = 0; i < 4; ++i) { p0[i] = pk_bf16(EX(s[2 * i]), EX(s[2 * i + 1])); p1[i] = pk_bf16(EX(s[8 + 2 * i]), EX(s[9 + 2 * i])); }
                o = MFMA_BF(BC8(BLD(hb + t * 4096 + 2048)), BC8(p0), o); o = MFMA_BF(BC8(BLD(hb + t * 4096 + 3072)), BC8(p1), o);
            }
        }
    }
    {
        const auto r = __builtin_amdgcn_permlane32_swap(__float_as_uint(o[12]), __float_as_uint(o[12]), false, false);
        const float rl = __builtin_amdgcn_rcpf(__uint_as_float(r[1]));
        u32x4 o0, o1;
#pragma unroll
        for (int i = 0; i < 4; ++i) { o0[i] = pk_bf16(o[2 * i] * rl, o[2 * i + 1] * rl); o1[i] = pk_bf16(o[8 + 2 * i] * rl, o[9 + 2 * i] * rl); }
        if (hi) { o1[2] = 0; o1[3] = 0; }
        const unsigned fi = (unsigned)((4 * grp + w) * 16 + head);
        BST(o0, WS_O + fi * 2048 + voff); BST(o1, WS_O + fi * 2048 + 1024 + voff);
        STAMP(9);
        drain();
        put_flag(ws, FL_O + fi * FS, tag, lane);
    }
    STAMP(10);
}

DI void role_b(const uint16_t* __restrict__ x, const uint16_t* __restrict__ wqkv, const uint16_t* __restrict__ wo, const uint16_t* __restrict__ u, const uint16_t* __restrict__ lar, const uint16_t* __restrict__ aim,
               const uint16_t* __restrict__ cre, const uint16_t* __restrict__ cim, const uint16_t* __restrict__ ldt, const uint16_t* __restrict__ dskip,
               uint16_t* __restrict__ out, char* __restrict__ ws, LAS char* lds, int w, int lane, u64 t_start) {
    u64 tag;
    const unsigned lds0 = (unsigned)(uintptr_t)lds;
    LAS unsigned* sy = (LAS unsigned*)(lds + LSY);
    const __amdgpu_buffer_rsrc_t rs = __builtin_amdgcn_make_buffer_rsrc(ws, 0, WS_END, 0x00020000);
    const int b = blockIdx.x, xcd = b & 7, bi = b >> 3, r32 = lane & 31, hi = lane >> 5;
    __builtin_amdgcn_s_setprio(PRIO_B);
    if (w == 0) sy[lane] = 0;
#define KEEPPF()
    if (xcd < 7) {
        const int cc = w >> 1, hh = w & 1, pair = 32 * xcd + bi, ch = 2 * pair + cc;
        LAS char* cb = lds + LB + cc * CBW;
        LAS char* tab = cb + hh * TABW;
        const unsigned uo = (unsigned)((64 * bi + 16 * w + (lane >> 3)) * D + 64 * xcd + (lane & 7) * 8) * 2;
        glds2(u, uo, uo + 8 * D * 2 - 1024, lds0 + LU + w * 2048);
        const unsigned po = (unsigned)(ch * NS + 128 * hh) / 2 + lane;
        const unsigned pl = ((const unsigned*)lar)[po], pa = ((const unsigned*)aim)[po], pr = ((const unsigned*)cre)[po], pi = ((const unsigned*)cim)[po];
        const u64 e0 = __hip_atomic_load((GAS u64*)ws, __ATOMIC_RELAXED, __HIP_MEMORY_SCOPE_AGENT);
        u32x2 gi[2][3];
#pragma unroll
        for (int ps = 0; ps < 2; ++ps)
#pragma unroll
            for (int i = 0; i < 3; ++i) gi[ps][i] = ((const u32x2*)g_idx.v[128 * hh + 64 * ps + lane])[i];
        const unsigned lw = ((const unsigned*)ldt)[pair], dw = ((const unsigned*)dskip)[pair];
        asm volatile("s_waitcnt lgkmcnt(0)\n\ts_barrier" ::: "memory");
        drain(); KEEPPF();
        STAMP(1);
        arrive(sy + SY_WO, lane); await(sy + SY_WO, 4);
        {
            LAS const char* p = lds + LU + (16 * (lane >> 4) + ((lane & 15) >> 2)) * 128 + w * 32 + (lane & 3) * 8;
            const bf16x8 t0 = cat8(trread(p), trread(p + 512)), t1 = cat8(trread(p + 1024), trread(p + 1536));
            arrive(sy + SY_T1, lane);
            LAS char* sc = tab + (lane & 15) * 144 + (lane >> 4) * 32;
            *(LAS bf16x8*)sc = t0; *(LAS bf16x8*)(sc + 16) = t1;
            LAS const char* sr = tab + (lane >> 3) * 144 + (lane & 7) * 16;
            const u32x4 o0 = *(LAS const u32x4*)sr, o1 = *(LAS const u32x4*)(sr + 8 * 144);
            const unsigned to = WS_UT + (unsigned)((64 * xcd + 16 * w + (lane >> 3)) * S + 64 * bi) * 2 + (lane & 7) * 16;
            BST(o0, to); BST(o1, to + 8 * S * 2);
        }
        if (w == 0 && lane == 0) __hip_atomic_store((LAS u64*)sy, mix(e0), __ATOMIC_RELAXED, __HIP_MEMORY_SCOPE_WORKGROUP);
        const float dt = rbf(expf(cc ? bf_hi(lw) : bf_lo(lw)));
        float cm = fmaxf(fmaxf(fabsf(bf_lo(pr)), fabsf(bf_hi(pr))), fmaxf(fabsf(bf_lo(pi)), fabsf(bf_hi(pi))));
        cm = wave_max(cm);
        unsigned ce = __float_as_uint(cm) & 0x7f800000u; ce = ce < 0x01000000u ? 0x01000000u : (ce > 0x7e800000u ? 0x7e800000u : ce);
        const float cscale = __uint_as_float(0x7f000000u - ce), oscale = 2.0f * dt * __uint_as_float(ce);
        LAS unsigned* tw = (LAS unsigned*)(tab + lane * 4);
        LAS const char* trd = tab + r32 * TROW + hi * 16;
        f32x16 dacc = {};
        STAMP(13);
        f32x2 z1, z8, z256, x0;
#define ANCH(vl, va, vr, vi) do { const float a2 = (-rbf(expf(vl)) * dt) * 1.4426950408889634f, rev = ((va) * dt) * 0.15915494309189535f; \
            z1 = anchor(a2, -rev, 1.f); z8 = anchor(a2, rev, 8.f); z256 = anchor(a2, -rev, 256.f); x0 = (f32x2){(vr) * cscale, -(vi) * cscale}; } while (0)
#define PUTX(row, v) tw[(row) * (TROW / 4)] = pk_f16(v)
#define PUTY(col, v) tw[(21 + (col)) * (TROW / 4)] = pk_f16(v)
#define CHAIN() do { \
            const f32x2 z1s = rot(z1), zs = rot(z256), z8s = rot(z8); \
            f32x2 y[4], X[8]; y[1] = z8; y[2] = cmul(z8, z8, z8s); X[0] = x0; X[1] = cmul(x0, z1, z1s); \
            y[3] = cmul(y[2], z8, z8s); const f32x2 z32 = cmul(y[2], y[2], rot(y[2])), z32s = rot(z32); X[2] = cmul(X[1], z1, z1s); y[0] = z32; \
            tw[21 * (TROW / 4)] = 0x3c00u; PUTY(1, y[1]); PUTY(2, y[2]); PUTY(3, y[3]); PUTY(4, y[0]); PUTX(0, X[0]); PUTX(1, X[1]); PUTX(2, X[2]); \
            f32x2 a = cmul(x0, z256, zs), b2 = cmul(X[2], z256, zs), c4; PUTX(8, a); PUTX(9, b2); SBAR(); \
            _Pragma("unroll") for (int i = 1; i < 8; ++i) { \
                f32x2 t0, t1, t2, t3, t4, t5, t6; \
                if (i > 1) t0 = y[0].xx * z32; t1 = y[1].xx * z32; t2 = y[2].xx * z32; t3 = y[3].xx * z32; \
                if (i < 6) t4 = X[i + 1].xx * z1; t5 = a.xx * z256; \
                if (i == 3) t6 = X[4].xx * z256; if (i == 4 || i == 5) t6 = c4.xx * z256; if (i == 6) t6 = X[6].xx * z256; SBAR(); \
                if (i > 1) y[0] = __builtin_elementwise_fma(y[0].yy, z32s, t0); y[1] = __builtin_elementwise_fma(y[1].yy, z32s, t1); \
                y[2] = __builtin_elementwise_fma(y[2].yy, z32s, t2); y[3] = __builtin_elementwise_fma(y[3].yy, z32s, t3); \
                if (i < 6) X[i + 2] = __builtin_elementwise_fma(X[i + 1].yy, z1s, t4); a = __builtin_elementwise_fma(a.yy, zs, t5); \
                if (i == 3) c4 = __builtin_elementwise_fma(X[4].yy, zs, t6); if (i == 4 || i == 5) c4 = __builtin_elementwise_fma(c4.yy, zs, t6); \
                if (i == 6) c4 = __builtin_elementwise_fma(X[6].yy, zs, t6); SBAR(); \
                if (i > 1) PUTY(4 * i, y[0]); PUTY(4 * i + 1, y[1]); PUTY(4 * i + 2, y[2]); PUTY(4 * i + 3, y[3]); \
                if (i < 6) PUTX(i + 2, X[i + 2]); PUTX(i == 1 ? 12 : (i == 2 ? 14 : 13 + i), a); \
                if (i == 3) PUTX(10, c4); if (i == 4) PUTX(13, c4); if (i == 5) PUTX(15, c4); if (i == 6) PUTX(11, c4); SBAR(); \
            } } while (0)
        f16x8 fa[8], fb[8];
#define RDT() do { _Pragma("unroll") for (int kk = 0; kk < 8; ++kk) { fa[kk] = *(LAS const f16x8*)(trd + kk * 32); fb[kk] = *(LAS const f16x8*)(trd + 21 * TROW + kk * 32); } SBAR(); } while (0)
#define MMT() do { _Pragma("unroll") for (int kk = 0; kk < 8; ++kk) dacc = MFMA_F16(fa[kk], fb[kk], dacc); } while (0)
        ANCH(bf_lo(pl), bf_lo(pa), bf_lo(pr), bf_lo(pi));
        PIN(z1); PIN(z8); PIN(z256); SBAR();
        tag = get_tag(sy); drain(); put_flag(ws, FL_UT + (unsigned)(xcd * 128 + w * 32 + bi) * FS, tag, lane);
        CHAIN(); RDT();
        ANCH(bf_hi(pl), bf_hi(pa), bf_hi(pr), bf_hi(pi));
        PIN(z1); PIN(z8); PIN(z256); SBAR(); MMT(); SBAR();
        GAS u64* utf = (GAS u64*)(ws + FL_UT + (unsigned)(xcd * 128 + (bi >> 3) * 32 + r32) * FS);
        const u64 utv = __hip_atomic_load(utf, __ATOMIC_RELAXED, __HIP_MEMORY_SCOPE_AGENT); SBAR();
        CHAIN(); RDT();
        if (__any(utv != tag)) wait_flags<4>(ws, FL_UT + (unsigned)(xcd * 128 + (bi >> 3) * 32 + r32) * FS, tag, !hi);
        CFENCE();
        const unsigned ub = WS_UT + (unsigned)(ch * S + 1024 * hh) * 2 + lane * 16;
        const u32x4 u0 = BLD(ub), u1 = BLD(ub + 1024); SBAR();
        MMT();
        STAMP(2);
        {
            LAS float* pt = (LAS float*)tab;
            *(LAS f32x4*)(pt + 8 * r32 + 4 * hi) = (f32x4){dacc[0] * oscale, dacc[1] * oscale, dacc[2] * oscale, dacc[3] * oscale};
            *(LAS f32x2*)(pt + (hi ? 384 + 2 * r32 : 256 + 4 * r32)) = (f32x2){dacc[4] * oscale, dacc[5] * oscale};
            *(LAS f32x2*)(pt + (hi ? 448 + 2 * r32 : 258 + 4 * r32)) = (f32x2){dacc[6] * oscale, dacc[7] * oscale};
            pt[hi ? 640 + r32 : 512 + r32] = dacc[8] * oscale;
            pt[hi ? 672 + r32 : 544 + r32] = dacc[9] * oscale;
            pt[hi ? 672 + r32 : 576 + r32] = dacc[10] * oscale;
            pt[hi ? 672 + r32 : 608 + r32] = dacc[11] * oscale;
        }
        LAS unsigned* ps_ = sy + SY_P + 4 * cc;
        arrive(ps_ + 0, lane);
        LAS char* ui = lds + LU + cc * UIMG;
        LAS char* kt = lds + LKT + cc * KTW;
        await(sy + SY_T1, 4); await(ps_ + 0, 2);
#pragma unroll
        for (int ps = 0; ps < 2; ++ps) {
            const int v = 81 * hh + 64 * ps + lane;
            if (ps == 0 || lane < 17) {
                const f32x4 p0 = *(LAS const f32x4*)(cb + v * 16), p1 = *(LAS const f32x4*)(cb + TABW + v * 16);
                u32x2 r = {pk_bf16(p0[0] + p1[0], p0[1] + p1[1]), pk_bf16(p0[2] + p1[2], p0[3] + p1[3])};
                if (v == 161) r = (u32x2){0u, 0u};
                *(LAS u32x2*)(kt + v * 8) = r;
            }
        }
        const u32x4 z4 = {};
        *(LAS u32x4*)(ui + (64 * hh + lane) * 16) = z4;
        if (lane < 16) *(LAS u32x4*)(ui + (128 + 16 * hh + lane) * 16) = z4;
        arrive(ps_ + 1, lane); await(ps_ + 1, 2);
        STAMP(3);
        __builtin_amdgcn_s_setprio(PRIO_B2);
#pragma unroll
        for (int ps = 0; ps < 2; ++ps) {
            unsigned W[6];
#pragma unroll
            for (int i = 0; i < 6; ++i) { const unsigned gv = gi[ps][i >> 1][i & 1];
                W[i] = *(LAS const uint16_t*)(kt + (gv & 0xffffu));
                if (i < 5) W[i] |= (unsigned)*(LAS const uint16_t*)(kt + (gv >> 16)) << 16; }
            unsigned V[5];
#pragma unroll
            for (int i = 0; i < 5; ++i) V[i] = __builtin_amdgcn_alignbit(W[i + 1], W[i], 16);
            LAS char* cp = cb + (128 * hh + 64 * ps + lane) * 16;
            *(LAS u32x4*)(cp) = (u32x4){W[0], W[1], W[2], W[3]};
            *(LAS u32x4*)(cp + CPY) = (u32x4){V[0], V[1], V[2], V[3]};
            *(LAS u32x4*)(cp + 2 * CPY) = (u32x4){W[1], W[2], W[3], W[4]};
            *(LAS u32x4*)(cp + 3 * CPY) = (u32x4){V[1], V[2], V[3], V[4]};
        }
        if (lane < 8) *(LAS u32x4*)(cb + (2 * hh + (lane >> 2)) * CPY + (256 + (lane & 3)) * 16) = z4;
        *(LAS u32x4*)(ui + (32 + 32 * hh + (lane >> 2)) * 80 + (lane & 3) * 16) = u0;
        *(LAS u32x4*)(ui + (48 + 32 * hh + (lane >> 2)) * 80 + (lane & 3) * 16) = u1;
        arrive(ps_ + 2, lane); await(ps_ + 2, 2);
        STAMP(4);
        f32x16 a0 = {}, a1 = {};
        {
            const int sb = 7 - (r32 & 7);
            LAS const char* pa = cb + (sb & 3) * CPY + (255 - (r32 >> 3) + hi) * 16 + (sb >> 2) * 8 - 64 * (16 * hh + 3) - 2048;
            LAS const char* pa2 = pa + 8; asm volatile("" : "+v"(pa2));
            LAS const char* pb = ui + (32 + r32 - 16 * hh - 3) * 80 + 16 * hi;
#define TF(off) cat8(*(LAS const s16x4*)(pa + (off)), *(LAS const s16x4*)(pa2 + (off)))
            s16x4 ta[2][2], tb[2][2]; bf16x8 b0[2], b1[2];
#define T4(q, off) (*(LAS const s16x4*)((q) + (off)))
#define CSTEP(i, j) do { const int o_ = 64 * (3 - ((j) >> 1)) + 32 * ((j) & 1), u_ = 80 * (3 - ((j) >> 1)) + 32 * ((j) & 1); \
                a1 = MFMA_BF(cat8(ta[i][0], ta[i][1]), b1[i], a1); SBAR(); ta[1 - i][0] = T4(pa, o_ + 2048); ta[1 - i][1] = T4(pa2, o_ + 2048); SBAR(); \
                a0 = MFMA_BF(cat8(ta[i][0], ta[i][1]), b0[i], a0); SBAR(); b1[1 - i] = FR(pb, u_ + 2560); b0[1 - i] = FR(pb, u_); SBAR(); \
                a1 = MFMA_BF(cat8(tb[i][0], tb[i][1]), b0[i], a1); SBAR(); tb[1 - i][0] = T4(pa, o_); tb[1 - i][1] = T4(pa2, o_); SBAR(); } while (0)
            ta[0][0] = T4(pa, 192 + 2048); ta[0][1] = T4(pa2, 192 + 2048); b1[0] = FR(pb, 240 + 2560); b0[0] = FR(pb, 240); tb[0][0] = T4(pa, 192); tb[0][1] = T4(pa2, 192);
#pragma unroll 1
            for (int m4 = 0; m4 < NCONV; ++m4) {
                CSTEP(0, 1); CSTEP(1, 2); CSTEP(0, 3); CSTEP(1, 4); CSTEP(0, 5); CSTEP(1, 6); CSTEP(0, 7);
                if (m4 < 3) { pa -= 256; pa2 -= 256; pb -= 320; }
                CSTEP(1, 0);
            }
        }
        STAMP(5);
        {
            LAS char* ex = cb + EXO + r32 * 144 + hi * 16;
            const f32x16 snd = hh ? a0 : a1; f32x16 y = hh ? a1 : a0;
#pragma unroll
            for (int g = 0; g < 4; ++g) *(LAS f32x4*)(ex + hh * 4608 + g * 32) = (f32x4){snd[4 * g], snd[4 * g + 1], snd[4 * g + 2], snd[4 * g + 3]};
            arrive(ps_ + 3, lane); await(ps_ + 3, 2);
            const float dsk = cc ? bf_hi(dw) : bf_lo(dw);
            unsigned r[8];
#pragma unroll
            for (int g = 0; g < 4; ++g) {
                const f32x4 p = *(LAS const f32x4*)(ex + (1 - hh) * 4608 + g * 32);
                const u32x2 uu = *(LAS const u32x2*)(ui + (32 + 32 * hh + r32) * 80 + 16 * g + 8 * hi);
#pragma unroll
                for (int e = 0; e < 2; ++e) {
                    const unsigned yb = pk_bf16(y[4 * g + 2 * e] + p[2 * e], y[4 * g + 2 * e + 1] + p[2 * e + 1]), sk = pk_bf16(bf_lo(uu[e]) * dsk, bf_hi(uu[e]) * dsk);
                    r[2 * g + e] = pk_bf16(bf_lo(yb) + bf_lo(sk), bf_hi(yb) + bf_hi(sk));
                }
            }
            LAS char* sc = cb + hh * 2048;
#pragma unroll
            for (int g = 0; g < 4; ++g) *(LAS u32x2*)(sc + 64 * r32 + 16 * g + 8 * hi) = (u32x2){r[2 * g], r[2 * g + 1]};
            const u32x4 o0 = *(LAS const u32x4*)(sc + lane * 16), o1 = *(LAS const u32x4*)(sc + 1024 + lane * 16);
            const unsigned bo = WS_BT + (unsigned)(ch * S + 1024 * hh) * 2 + lane * 16;
            BST(o0, bo); BST(o1, bo + 1024);
        }
        STAMP(6);
    } else {
        {
            const unsigned nb = (bi & 1) ? 12 * 2 * D : 16 * 2 * D;
            unsigned v4[4];
#pragma unroll
            for (int i = 0; i < 4; ++i) { const unsigned o = (unsigned)(w * 4096 + i * 1024 + lane * 16); v4[i] = (o < nb ? o : 0u) + 4096 - i * 1024; }
            glds4((const char*)(wo + (size_t)(DH * (bi >> 1) + 16 * (bi & 1)) * D) - 4096, v4[0], v4[1], v4[2], v4[3], lds0 + LB + w * 4096);
        }
        const u64 e0 = __hip_atomic_load((GAS u64*)ws, __ATOMIC_RELAXED, __HIP_MEMORY_SCOPE_AGENT);
        asm volatile("s_waitcnt lgkmcnt(0)\n\ts_barrier" ::: "memory");
        drain(); KEEPPF();
        if (w == 0 && lane == 0) __hip_atomic_store((LAS u64*)sy, mix(e0), __ATOMIC_RELAXED, __HIP_MEMORY_SCOPE_WORKGROUP);
        arrive(sy + SY_WO, lane); await(sy + SY_WO, 4);
        LAS const char* p = lds + LB + (4 * hi + ((lane & 15) >> 2)) * (2 * D) + ((lane >> 4) & 1) * 32 + (lane & 3) * 8;
#pragma unroll
        for (int i = 0; i < 4; ++i) {
            const int ct = w + 4 * i;
            if (ct < 14) {
                u32x4 o = __builtin_bit_cast(u32x4, cat8(trread(p + ct * 64), trread(p + 8 * 2 * D + ct * 64)));
                if (hi && (bi & 1)) { o[2] = 0; o[3] = 0; }
                BST(o, WS_WO + (unsigned)((bi * 14 + ct) * 64 + lane) * 16);
            }
        }
        tag = get_tag(sy);
        drain();
        put_flag(ws, FL_WO + (unsigned)(w * 32 + bi) * FS, tag, lane);
    }
    const int qt = 8 * xcd + (bi >> 2), ct = 4 * (bi & 3) + w;
    const unsigned voff = lane * 16;
    drain();
    if (xcd < 7) put_flag(ws, FL_BT + (unsigned)(xcd * 128 + bi * 4 + w) * FS, tag, lane);
    bf16x8 wf[32];
    await_slow(sy + SY_G, 4);
    wait_flags<8>(ws, FL_WO + (unsigned)(w * 32 + r32) * FS, tag, !hi);
    if (ct < 14) {
#pragma unroll
        for (int i = 0; i < 32; ++i) { wf[i] = BC8(BLD(WS_WO + (unsigned)(i * 14 + ct) * 1024 + voff)); __builtin_amdgcn_s_sleep(WOSL); SBAR(); }
    }
    STAMP(7);
    if (xcd < 7) {
        wait_flags<32>(ws, FL_BT + (unsigned)(xcd * 128 + w * 32 + r32) * FS, tag, !hi);
        const unsigned st = LU + (w >> 1) * UIMG + (w & 1) * 3840;
        const unsigned bo = WS_BT + (unsigned)((64 * xcd + 16 * w + (lane >> 3)) * S + 64 * bi + (lane & 7) * 8) * 2;
        arrive(sy + SY_BG, lane); await(sy + SY_BG, 4);
        glds2c(ws, bo, bo + 8 * S * 2 - 1024, lds0 + st);
        drain();
        u32x4 o0, o1; transpose16(lds + st, lane, o0, o1);
        u32x4* dst = (u32x4*)(out + (size_t)(64 * bi + lane) * 2 * D + D + 64 * xcd + 16 * w);
        asm volatile("global_store_dwordx4 %0, %1, off sc1\n\tglobal_store_dwordx4 %0, %2, off offset:16 sc1\n\ts_nop 1" :: "v"(dst), "v"(o0), "v"(o1) : "memory");
    }
    STAMP(8);
    await_slow(sy + SY_X, 4);
    wait_flags<OSL>(ws, FL_O + (unsigned)(qt * 16 + 4 * w + lane) * FS, tag, lane < 4);
    STAMP(9);
    {
        u32x4 of[8];
#pragma unroll
        for (int i = 0; i < 8; ++i) of[i] = BLD(WS_O + (unsigned)(qt * 32 + 8 * w + i) * 1024 + voff);
#pragma unroll
        for (int i = 0; i < 8; ++i) *(LAS u32x4*)(lds + LB + (8 * w + i) * 1024 + voff) = of[i];
        STAMP(10);
        arrive(sy + SY_B2, lane); await(sy + SY_B2, 4);
    }
    STAMP(11);
    if (ct < 14) {
        f32x16 acc = {};
        LAS const char* po = lds + LB + voff;
        bf16x8 of[4];
#pragma unroll
        for (int i = 0; i < 3; ++i) of[i] = FR(po, i * 1024);
#pragma unroll
        for (int i = 0; i < 32; ++i) { if (i < 29) of[(i + 3) & 3] = FR(po, (i + 3) * 1024); SBAR(); acc = MFMA_BF(wf[i], of[i & 3], acc); SBAR(); }
        u32x4* op = (u32x4*)(out + (size_t)(32 * qt + r32) * 2 * D + 32 * ct + 8 * hi);
        { const u32x4 t_ = widen(pk_bf16(acc[0], acc[1]), pk_bf16(acc[2], acc[3]), pk_bf16(acc[4], acc[5]), pk_bf16(acc[6], acc[7])); asm volatile("global_store_dwordx4 %0, %1, off sc1\n\ts_nop 1" :: "v"(op), "v"(t_) : "memory"); }
        { const u32x4 t_ = widen(pk_bf16(acc[8], acc[9]), pk_bf16(acc[10], acc[11]), pk_bf16(acc[12], acc[13]), pk_bf16(acc[14], acc[15])); asm volatile("global_store_dwordx4 %0, %1, off offset:32 sc1\n\ts_nop 1" :: "v"(op), "v"(t_) : "memory"); }
    }
    if (b == 0 && w == 0) put_flag(ws, 0, tag, lane);
    STAMP(12);
}

__global__ __launch_bounds__(512, 2) void fused(const uint16_t* __restrict__ x, const uint16_t* __restrict__ wqkv, const uint16_t* __restrict__ wo,
                                               const uint16_t* __restrict__ u, const uint16_t* __restrict__ lar, const uint16_t* __restrict__ aim,
                                               const uint16_t* __restrict__ cre, const uint16_t* __restrict__ cim, const uint16_t* __restrict__ ldt,
                                               const uint16_t* __restrict__ dskip, uint16_t* __restrict__ out, char* __restrict__ ws) {
    extern __shared__ __attribute__((aligned(16))) char smem[];
    LAS char* lds = (LAS char*)smem;
    const int lane = threadIdx.x & 63, w = __builtin_amdgcn_readfirstlane(threadIdx.x >> 6);
    const u64 t_start = *(const u64*)(ws + (32u << 20));
    if (w < 4) role_a(x, wqkv, ws, lds, w, lane, t_start);
    else role_b(x, wqkv, wo, u, lar, aim, cre, cim, ldt, dskip, out, ws, lds, w - 4, lane, t_start);
}
}

__global__ void dlead(char* ws) { if (threadIdx.x == 0) *(u64*)(ws + (32u << 20)) = __builtin_amdgcn_s_memrealtime(); }
extern "C" void kernel_launch(void* const* d_in, const int* in_sizes, int n_in, void* d_out, int out_size, void* d_ws, size_t ws_size, hipStream_t stream) {
    using namespace k;
    (void)hipFuncSetAttribute((const void*)fused, hipFuncAttributeMaxDynamicSharedMemorySize, LDS_BYTES);
    const uint16_t* const* in = (const uint16_t* const*)d_in;
    dlead<<<1, 64, 0, stream>>>((char*)d_ws);
    fused<<<256, 512, LDS_BYTES, stream>>>(in[0], in[1], in[2], in[3], in[4], in[5], in[6], in[7], in[8], in[9], (uint16_t*)d_out, (char*)d_ws);
}
```

```cpp
#define PRIO_A1 1
#define PRIO_B2 2
#include <hip/hip_runtime.h>
#include <hip/hip_bf16.h>
#include <stdint.h>

typedef short bf16x8 __attribute__((ext_vector_type(8)));
typedef _Float16 f16x8 __attribute__((ext_vector_type(8)));
typedef _Float16 f16x2 __attribute__((ext_vector_type(2)));
typedef __bf16 bf16x2 __attribute__((ext_vector_type(2)));
typedef short s16x4 __attribute__((ext_vector_type(4)));
typedef float f32x16 __attribute__((ext_vector_type(16)));
typedef float f32x4 __attribute__((ext_vector_type(4)));
typedef float f32x2 __attribute__((ext_vector_type(2)));
typedef unsigned u32x4 __attribute__((ext_vector_type(4)));
typedef unsigned u32x2 __attribute__((ext_vector_type(2)));
typedef unsigned long long u64;
#define LAS __attribute__((address_space(3)))
#define GAS __attribute__((address_space(1)))
#define DI __device__ __forceinline__
#define MFMA_BF(a, b, c) __builtin_amdgcn_mfma_f32_32x32x16_bf16((a), (b), (c), 0, 0, 0)
#define MFMA_F16(a, b, c) __builtin_amdgcn_mfma_f32_32x32x16_f16((a), (b), (c), 0, 0, 0)
#define SBAR() __builtin_amdgcn_sched_barrier(0)
#define CFENCE() asm volatile("" ::: "memory")
#define PIN(x) asm volatile("" : "+v"(x))
#define BC8(x) __builtin_bit_cast(bf16x8, (x))
#define EX(x) __builtin_amdgcn_exp2f(x)
#define PRIO_B 0
#define ROT 1
#define WOSL 2

namespace k {
#define STAMP(i)
constexpr int S = 2048, D = 448, DH = 28, NS = 256;
constexpr unsigned FS = 128;
constexpr unsigned FL_KV = FS, FL_O = FL_KV + 1024 * FS, FL_UT = FL_O + 1024 * FS, FL_BT = FL_UT + 896 * FS, FL_WO = FL_BT + 896 * FS;
constexpr unsigned WS_KV = 512u << 10;
constexpr unsigned WS_O = WS_KV + (4u << 20);
constexpr unsigned WS_WO = WS_O + (2u << 20);
constexpr unsigned WS_UT = WS_WO + (448u << 10);
constexpr unsigned WS_BT = WS_UT + D * S * 2;
constexpr unsigned WS_END = WS_BT + D * S * 2;
constexpr int WSL = 12288, LXR = 4 * WSL, XSL = 16384, LB = LXR + 3 * XSL;
constexpr int LQ = 0, LX = 8192;
constexpr int TROW = 272, TABW = 53 * TROW, CBW = 2 * TABW, CPY = 4160, UOF = 4 * CPY, KOF = CBW - 1312;
constexpr int LSY = LB + 2 * CBW, LDS_BYTES = LSY + 256;
static_assert(LDS_BYTES <= 163840 && (LSY & 15) == 0 && UOF + 7680 <= KOF, "lds");
enum { SY_TAG = 0, SY_F = 2, SY_P = 10, SY_WO = 20, SY_T1 = 21, SY_BG = 22, SY_B2 = 23, SY_Q = 24, SY_X = 25, SY_G = 26, SY_T2 = 27 };
constexpr int SPIN = 1 << 18;

struct IdxTab { uint16_t v[256][12]; };
constexpr IdxTab make_idx() {
    IdxTab t = {};
    for (int y = 0; y < 256; ++y) for (int e = 0; e < 12; ++e) {
        const int j = 2047 - 8 * y - e; int ix = 644;
        if (j >= 0) { const int sh = j >= 1024 ? 3 : (j >= 512 ? 2 : (j >= 256 ? 1 : 0));
            ix = sh ? 128 * sh + ((j + (1 << (sh - 1)) - 1 + ((j >> sh) & 1)) >> sh) : j; }
        t.v[y][e] = (uint16_t)(2 * ix);
    }
    return t;
}
__device__ const IdxTab g_idx __attribute__((aligned(16))) = make_idx();

DI unsigned pk_bf16(float a, float b) { f32x2 v = {a, b}; return __builtin_bit_cast(unsigned, __builtin_convertvector(v, bf16x2)); }
DI unsigned pk_f16(f32x2 v) { return __builtin_bit_cast(unsigned, __builtin_convertvector(v, f16x2)); }
DI float bf_lo(unsigned w) { return __uint_as_float(w << 16); }
DI float bf_hi(unsigned w) { return __uint_as_float(w & 0xffff0000u); }
DI float rbf(float x) { return bf_lo(pk_bf16(x, x)); }
#define GL0 "s_mov_b32 %0, m0\n\ts_mov_b32 m0, %2\n\ts_nop 0\n\t"
#define GL1(V, O, C) "global_load_lds_dwordx4 " V ", %1 offset:" #O C "\n\t"
#define GLE "s_mov_b32 m0, %0"
DI void glds4(const void* sb, unsigned v0, unsigned v1, unsigned v2, unsigned v3, unsigned lb) { unsigned sv;
    asm volatile(GL0 GL1("%3", 0, "") GL1("%4", 1024, "") GL1("%5", 2048, "") GL1("%6", 3072, "") GLE : "=&s"(sv) : "s"(sb), "s"(lb), "v"(v0), "v"(v1), "v"(v2), "v"(v3) : "memory"); }
DI void glds3(const void* sb, unsigned v0, unsigned v1, unsigned v2, unsigned lb) { unsigned sv;
    asm volatile(GL0 GL1("%3", 0, "") GL1("%4", 1024, "") GL1("%5", 2048, "") GLE : "=&s"(sv) : "s"(sb), "s"(lb), "v"(v0), "v"(v1), "v"(v2) : "memory"); }
DI void glds2(const void* sb, unsigned v0, unsigned v1, unsigned lb) { unsigned sv;
    asm volatile(GL0 GL1("%3", 0, "") GL1("%4", 1024, "") GLE : "=&s"(sv) : "s"(sb), "s"(lb), "v"(v0), "v"(v1) : "memory"); }
DI void glds2c(const void* sb, unsigned v0, unsigned v1, unsigned lb) { unsigned sv;
    asm volatile(GL0 GL1("%3", 0, " sc1") GL1("%4", 1024, " sc1") GLE : "=&s"(sv) : "s"(sb), "s"(lb), "v"(v0), "v"(v1) : "memory"); }
DI float wave_max(float v) {
#define DPPMAX(ctrl, rm) v = fmaxf(v, __uint_as_float(__builtin_amdgcn_update_dpp(0, __float_as_uint(v), ctrl, rm, 0xf, true)))
    DPPMAX(0x111, 0xf); DPPMAX(0x112, 0xf); DPPMAX(0x114, 0xf); DPPMAX(0x118, 0xf); DPPMAX(0x142, 0xa); DPPMAX(0x143, 0xc);
    return __uint_as_float(__builtin_amdgcn_readlane(__float_as_uint(v), 63));
}
DI s16x4 trread(LAS const char* p) { return __builtin_bit_cast(s16x4, __builtin_amdgcn_ds_read_tr16_b64_v4i16((LAS s16x4*)p)); }
DI bf16x8 cat8(s16x4 a, s16x4 b) { return __builtin_shufflevector(a, b, 0, 1, 2, 3, 4, 5, 6, 7); }
DI f32x2 pfma(f32x2 a, f32x2 b, f32x2 c) { return __builtin_elementwise_fma(a, b, c); }
DI f32x2 cmul(f32x2 a, f32x2 b, f32x2 bs) { return pfma(a.yy, bs, a.xx * b); }
DI f32x2 rot(f32x2 b) { return (f32x2){-b.y, b.x}; }
DI f32x2 anchor(float a2, float rev, float s) {
    const float mag = EX(a2 * s);
    float r = rev * s; r -= __builtin_rintf(r);
    return (f32x2){mag * __builtin_amdgcn_cosf(r), mag * __builtin_amdgcn_sinf(r)};
}
DI void arrive(LAS unsigned* c) { asm volatile("s_mov_b64 exec, 1\n\tds_add_u32 %0, %1\n\ts_mov_b64 exec, -1" :: "v"((unsigned)(uintptr_t)c), "v"(1u) : "memory"); }
DI void await(LAS unsigned* c, unsigned n) {
    unsigned t, i = SPIN;
    asm volatile("1:\n\tds_read_b32 %0, %2\n\ts_waitcnt lgkmcnt(0)\n\tv_cmp_gt_u32 vcc, %3, %0\n\ts_cbranch_vccz 2f\n\ts_sleep 1\n\ts_sub_u32 %1, %1, 1\n\ts_cbranch_scc0 1b\n2:"
                 : "=&v"(t), "+s"(i) : "v"((unsigned)(uintptr_t)c), "s"(n) : "vcc", "scc", "memory");
}
DI void await_slow(LAS unsigned* c, unsigned n) {
    unsigned t, i = SPIN;
    asm volatile("1:\n\tds_read_b32 %0, %2\n\ts_waitcnt lgkmcnt(0)\n\tv_cmp_gt_u32 vcc, %3, %0\n\ts_cbranch_vccz 2f\n\ts_sleep 3\n\ts_sub_u32 %1, %1, 1\n\ts_cbranch_scc0 1b\n2:"
                 : "=&v"(t), "+s"(i) : "v"((unsigned)(uintptr_t)c), "s"(n) : "vcc", "scc", "memory");
}
DI u64 get_tag(LAS unsigned* sy) {
    await(sy, 1);
    const u64 v = __hip_atomic_load((LAS u64*)sy, __ATOMIC_RELAXED, __HIP_MEMORY_SCOPE_WORKGROUP);
    return ((u64)(unsigned)__builtin_amdgcn_readfirstlane((unsigned)(v >> 32)) << 32) | (unsigned)__builtin_amdgcn_readfirstlane((unsigned)v);
}
DI void drain() { asm volatile("s_waitcnt vmcnt(0)" ::: "memory"); }
DI void put_flag(char* ws, unsigned off, u64 tag, int lane) { if (lane == 0) __hip_atomic_store((GAS u64*)(ws + off), tag, __ATOMIC_RELAXED, __HIP_MEMORY_SCOPE_AGENT); }
template <int SLP> DI void wait_flags(char* ws, unsigned off, u64 tag, bool pend) {
    GAS u64* p = (GAS u64*)(ws + off);
    CFENCE();
#pragma unroll 1
    for (int i = 0; i < SPIN; ++i) {
        if (pend) pend = __hip_atomic_load(p, __ATOMIC_RELAXED, __HIP_MEMORY_SCOPE_AGENT) != tag;
        if (!__any(pend)) break;
        __builtin_amdgcn_s_sleep(SLP);
    }
    CFENCE();
}
#define BST(v, off) __builtin_amdgcn_raw_buffer_store_b128((v), rs, (off), 0, 16)
#define BLD(off) __builtin_amdgcn_raw_buffer_load_b128(rs, (off), 0, 16)
#define FR(p, off) (*(LAS const bf16x8*)((p) + (off)))
DI float swapmax(float m) { const auto r = __builtin_amdgcn_permlane32_swap(__float_as_uint(m), __float_as_uint(m), false, false); return fmaxf(__uint_as_float(r[0]), __uint_as_float(r[1])); }
DI u32x4 put_ref(u32x4 q1, float m, int hi) {
    const float m1 = rbf(m), m2 = rbf(m - m1), m3 = m - m1 - m2;
    if (hi) { q1[2] = pk_bf16(-m1, -m2); q1[3] = pk_bf16(-m3, 0.f); }
    return q1;
}
DI u64 mix(u64 e) { u64 z = e + 0x9E3779B97F4A7C15ull; z = (z ^ (z >> 30)) * 0xBF58476D1CE4E5B9ull; z = (z ^ (z >> 27)) * 0x94D049BB133111EBull; return (z ^ (z >> 31)) | 1ull; }
DI void transpose_tile(LAS const char* stage, int pst, LAS char* scratch, LAS unsigned* done, int w, int lane, u32x4& o0, u32x4& o1) {
    LAS const char* p = stage + (lane >> 4) * pst + ((lane & 15) >> 2) * 128 + w * 32 + (lane & 3) * 8;
    const bf16x8 t0 = cat8(trread(p), trread(p + 512)), t1 = cat8(trread(p + 1024), trread(p + 1536));
    if (done) arrive(done);
    LAS char* sc = scratch + (lane & 15) * 144 + (lane >> 4) * 32;
    *(LAS bf16x8*)sc = t0; *(LAS bf16x8*)(sc + 16) = t1;
    LAS const char* sr = scratch + (lane >> 3) * 144 + (lane & 7) * 16;
    o0 = *(LAS const u32x4*)sr; o1 = *(LAS const u32x4*)(sr + 8 * 144);
}

DI void role_a(const uint16_t* __restrict__ x, const uint16_t* __restrict__ wqkv, char* __restrict__ ws, LAS char* lds, int w, int lane) {
    const int head = 4 * (blockIdx.x & 3) + ((blockIdx.x >> 3) & 3), grp = 8 * ((blockIdx.x >> 2) & 1) + (blockIdx.x >> 5);
    const unsigned lds0 = (unsigned)(uintptr_t)lds;
    const int r32 = lane & 31, hi = lane >> 5;
    const int wsh = (head & 7) == 7 ? 4 : 0;
    const unsigned wv = (unsigned)((lane >> 2) * 3 * D + (lane & 3) * 8) * 2;
    const char* wb = (const char*)(wqkv + DH * head - wsh + (16 * w) * 3 * D) - 2048;
    const unsigned xv0 = (unsigned)((lane >> 3) * D + (((lane & 7) ^ (lane >> 4)) * 8)) * 2, xv1 = xv0 ^ 64;
    const char* xb = (const char*)(x + (128 * grp + 32 * w) * D);
    const int rho = ROT ? ((blockIdx.x >> 3) + ((blockIdx.x >> 1) & 3)) % 7 : 0;
#define SP(st) ((st) + rho >= 7 ? (st) + rho - 7 : (st) + rho)
#define LOADW(st) glds3(wb + SP(st) * (64 * 3 * D * 2), wv + 2048, wv + 2 * D + 1024, wv + 4 * D, lds0 + ((st) & 3) * WSL + w * 3072)
#define LOADX(st) glds4(xb + SP(st) * 128, xv0, xv1 + 16 * D - 1024, xv0 + 32 * D - 2048, xv1 + 48 * D - 3072, lds0 + LXR + ((st) % 3) * XSL + w * 4096)
    LOADW(0); LOADX(0);
    asm volatile("s_barrier" ::: "memory");
    LOADW(1); LOADX(1); LOADW(2); LOADX(2);
    __builtin_amdgcn_s_setprio(PRIO_A1);
    LAS unsigned* sy = (LAS unsigned*)(lds + LSY);
    const __amdgpu_buffer_rsrc_t rs = __builtin_amdgcn_make_buffer_rsrc(ws, 0, WS_END, 0x00020000);
    const int xs = (r32 >> 1) & 7;
    LAS const char* xp[4];
#pragma unroll
    for (int ks = 0; ks < 4; ++ks) xp[ks] = lds + LXR + w * 4096 + r32 * 128 + (((2 * ks + hi) ^ xs) * 16);
    LAS const char* wp = lds + (8 * hi + ((lane & 15) >> 2)) * 64 + ((lane >> 4) & 1) * 32 + (lane & 3) * 8 + wsh * 2;
    f32x16 aq = {}, ak = {}, av = {};
    bf16x8 xf[2][4]; s16x4 fr[4][6];
#define RX(st, ks) xf[(st) & 1][ks] = FR(xp[ks], ((st) % 3) * XSL)
#define RW(st, ks, j) fr[ks][j] = trread(wp + ((st) & 3) * WSL + (ks) * 3072 + ((((j) >> 1) + 1) % 3) * 1024 + ((j) & 1) * 256)
#define MK(st, ks) ak = MFMA_BF(cat8(fr[ks][0], fr[ks][1]), xf[(st) & 1][ks], ak); SBAR()
#define MV(st, ks) av = MFMA_BF(xf[(st) & 1][ks], cat8(fr[ks][2], fr[ks][3]), av); SBAR()
#define MQ(st, ks) aq = MFMA_BF(cat8(fr[ks][4], fr[ks][5]), xf[(st) & 1][ks], aq); SBAR()
#define QSTEP(st, ks) MK(st, ks); RW(st, ks + 1, 0); RW(st, ks + 1, 1); SBAR(); MV(st, ks); RW(st, ks + 1, 2); RW(st, ks + 1, 3); SBAR(); MQ(st, ks); RW(st, ks + 1, 4); RW(st, ks + 1, 5); SBAR()
    u64 tag = 0;
    asm volatile("s_waitcnt vmcnt(14)" ::: "memory");
    arrive(sy + SY_F); await(sy + SY_F, 4);
    STAMP(16);
    RX(0, 0); RW(0, 0, 0); RW(0, 0, 1); RW(0, 0, 2); RW(0, 0, 3); RW(0, 0, 4); RW(0, 0, 5); RX(0, 1); RX(0, 2); RX(0, 3); SBAR();
#pragma unroll
    for (int st = 0; st < 7; ++st) {
        QSTEP(st, 0); if (st < 4) LOADW(st + 3);
        QSTEP(st, 1); if (st < 4) LOADX(st + 3);
        QSTEP(st, 2);
        if (st < 4) asm volatile("s_waitcnt vmcnt(14)" ::: "memory"); else if (st == 4) asm volatile("s_waitcnt vmcnt(7)" ::: "memory"); else if (st == 5) drain();
        arrive(sy + SY_F + 1 + st); SBAR();
        MK(st, 3); await(sy + SY_F + 1 + st, 4);
        if (st < 6) { RX(st + 1, 0); RW(st + 1, 0, 0); RW(st + 1, 0, 1); RW(st + 1, 0, 2); RW(st + 1, 0, 3); } SBAR();
        MV(st, 3); if (st < 6) { RW(st + 1, 0, 4); RW(st + 1, 0, 5); RX(st + 1, 1); } SBAR();
        MQ(st, 3); if (st < 6) { RX(st + 1, 2); RX(st + 1, 3); } SBAR();
        if (st == 2) tag = get_tag(sy);
    }
    STAMP(3);
    const float cq = 0.18898223650461362f * 1.4426950408889634f;
    const unsigned voff = lane * 16;
    u32x4 q0, q1, k0, k1, v0, v1;
    const unsigned tb = WS_KV + (unsigned)(head * 64 + 4 * grp + w) * 4096 + voff;
#pragma unroll
    for (int i = 0; i < 4; ++i) { k0[i] = pk_bf16(ak[2 * i], ak[2 * i + 1]); k1[i] = pk_bf16(ak[8 + 2 * i], ak[9 + 2 * i]); }
    if (hi) { k1[2] = 0x3f803f80u; k1[3] = 0x00003f80u; }
    BST(k0, tb); BST(k1, tb + 1024);
#pragma unroll
    for (int i = 0; i < 4; ++i) {
        v0[i] = pk_bf16(av[2 * i], av[2 * i + 1]); v1[i] = pk_bf16(av[8 + 2 * i], av[9 + 2 * i]);
        if (r32 >= DH) { v0[i] = v1[i] = (r32 == DH ? 0x3f803f80u : 0u); }
    }
    BST(v0, tb + 2048); BST(v1, tb + 3072);
#pragma unroll
    for (int i = 0; i < 4; ++i) { q0[i] = pk_bf16(aq[2 * i] * cq, aq[2 * i + 1] * cq); q1[i] = pk_bf16(aq[8 + 2 * i] * cq, aq[9 + 2 * i] * cq); }
    if (hi) { q1[2] = 0; q1[3] = 0; }
    const f32x16 zero = {};
    {
        f32x16 s = MFMA_BF(BC8(k0), BC8(q0), zero); s = MFMA_BF(BC8(k1), BC8(q1), s);
        float m = s[0];
#pragma unroll
        for (int i = 1; i < 16; ++i) m = fmaxf(m, s[i]);
        q1 = put_ref(q1, swapmax(m), hi);
    }
    { LAS u32x4* qo = (LAS u32x4*)(lds + LQ + w * 2048) + lane; qo[0] = q0; qo[64] = q1; }
    arrive(sy + SY_Q); await(sy + SY_Q, 4);
    __builtin_amdgcn_s_setprio(0);
    bf16x8 Q[4][2];
    Q[0][0] = BC8(q0); Q[0][1] = BC8(q1);
#pragma unroll
    for (int i = 1; i < 4; ++i) { LAS const char* p = lds + LQ + ((w + i) & 3) * 2048 + voff; Q[i][0] = FR(p, 0); Q[i][1] = FR(p, 1024); }
    u32x4 kv[3][4] = {{k0, k1, v0, v1}, {}, {}};
    f32x16 O[4] = {}, sa, sb;
    u32x4 p0, p1;
#define GRP(SC, i, P, j) do { float e0 = EX(SC[i]); SBAR(); float e1 = EX(SC[i + 1]); SBAR(); float e2 = EX(SC[i + 2]); SBAR(); float e3 = EX(SC[i + 3]); SBAR(); \
        P[j] = pk_bf16(e0, e1); SBAR(); P[j + 1] = pk_bf16(e2, e3); PIN(P); SBAR(); } while (0)
#define SLOT(SC, SN, cb, i, QK, PV1, LD) do { constexpr int nb_ = (i) < 3 ? (cb) : ((cb) + 1) % 3, pb_ = (i) > 0 ? (cb) : ((cb) + 2) % 3; \
        if (QK) SN = MFMA_BF(BC8(kv[nb_][0]), Q[((i) + 1) & 3][0], zero); SBAR(); GRP(SC, 0, p0, 0); \
        if (PV1) O[((i) + 3) & 3] = MFMA_BF(BC8(kv[pb_][3]), BC8(p1), O[((i) + 3) & 3]); SBAR(); \
        if (LD) { kv[((cb) + 2) % 3][i] = __builtin_amdgcn_raw_buffer_load_b128(rs, voff + (i) * 1024, kvo, 16); SBAR(); } GRP(SC, 4, p0, 2); \
        if (QK) SN = MFMA_BF(BC8(kv[nb_][1]), Q[((i) + 1) & 3][1], SN); SBAR(); GRP(SC, 8, p1, 0); \
        O[i] = MFMA_BF(BC8(kv[cb][2]), BC8(p0), O[i]); SBAR(); GRP(SC, 12, p1, 2); } while (0)
    const unsigned kvb = WS_KV + (unsigned)(head * 64 + w) * 4096;
    unsigned kvo = 0;
    sa = MFMA_BF(BC8(k0), Q[0][0], zero); sa = MFMA_BF(BC8(k1), Q[0][1], sa);
    SLOT(sa, sb, 0, 0, true, false, false); SLOT(sb, sa, 0, 1, true, true, false);
    drain();
    put_flag(ws, FL_KV + (unsigned)((head * 4 + w) * 16 + grp) * FS, tag, lane);
    STAMP(4);
    SLOT(sa, sb, 0, 2, true, true, false); SLOT(sb, sa, 0, 3, false, true, false);
    STAMP(5);
    wait_flags<2>(ws, FL_KV + (unsigned)((head * 4 + w) * 16 + lane) * FS, tag, lane < 16 && lane != grp);
    STAMP(6);
#pragma unroll
    for (int j = 1; j < 3; ++j)
#pragma unroll
        for (int f = 0; f < 4; ++f) kv[j][f] = __builtin_amdgcn_raw_buffer_load_b128(rs, voff + f * 1024, kvb + ((grp + j) & 15) * 16384, 16);
    arrive(sy + SY_G);
    sa = MFMA_BF(BC8(kv[1][0]), Q[0][0], zero); sa = MFMA_BF(BC8(kv[1][1]), Q[0][1], sa);
    STAMP(7);
    int nx = grp + 3;
#define TILE(cb) do { kvo = kvb + (nx & 15) * 16384; ++nx; SBAR(); \
        SLOT(sa, sb, cb, 0, true, true, true); SLOT(sb, sa, cb, 1, true, true, true); SLOT(sa, sb, cb, 2, true, true, true); SLOT(sb, sa, cb, 3, true, true, true); } while (0)
#pragma unroll 1
    for (int it = 0; it < 5; ++it) { TILE(1); TILE(2); TILE(0); }
    O[3] = MFMA_BF(BC8(kv[0][3]), BC8(p1), O[3]);
    STAMP(8);
    f32x16 o = O[0];
    {
#pragma unroll
        for (int i = 1; i < 4; ++i) {
            LAS char* ex = lds + LX + (((w + i) & 3) * 3 + i - 1) * 4096 + voff;
#pragma unroll
            for (int g = 0; g < 4; ++g) *(LAS f32x4*)(ex + g * 1024) = (f32x4){O[i][4 * g], O[i][4 * g + 1], O[i][4 * g + 2], O[i][4 * g + 3]};
        }
        arrive(sy + SY_X); await(sy + SY_X, 4);
        LAS const char* er = lds + LX + w * 3 * 4096 + voff;
#pragma unroll
        for (int i = 0; i < 3; ++i)
#pragma unroll
            for (int g = 0; g < 4; ++g) { const f32x4 r = *(LAS const f32x4*)(er + i * 4096 + g * 1024);
#pragma unroll
                for (int e = 0; e < 4; ++e) o[4 * g + e] += r[e]; }
        f32x2 chk = {0.f, 0.f};
#pragma unroll
        for (int i = 0; i < 8; ++i) chk = pfma((f32x2){o[2 * i], o[2 * i + 1]}, (f32x2){0.f, 0.f}, chk);
        if (__builtin_expect(__any(!(chk[0] + chk[1] == 0.f) || !(o[12] < 1e30f)), 0)) {
            const unsigned hb = WS_KV + (unsigned)head * 64 * 4096 + voff;
            u32x4 qr = __builtin_bit_cast(u32x4, Q[0][1]); if (hi) { qr[2] = 0; qr[3] = 0; }
            float mm = -INFINITY;
            wait_flags<4>(ws, FL_KV + (unsigned)(head * 64 + lane) * FS, tag, true);
#pragma unroll 1
            for (int t = 0; t < 64; ++t) {
                f32x16 s = MFMA_BF(BC8(BLD(hb + t * 4096)), Q[0][0], zero); s = MFMA_BF(BC8(BLD(hb + t * 4096 + 1024)), BC8(qr), s);
#pragma unroll
                for (int i = 0; i < 16; ++i) mm = fmaxf(mm, s[i]);
            }
            qr = put_ref(qr, swapmax(mm), hi);
            o = zero;
#pragma unroll 1
            for (int t = 0; t < 64; ++t) {
                f32x16 s = MFMA_BF(BC8(BLD(hb + t * 4096)), Q[0][0], zero); s = MFMA_BF(BC8(BLD(hb + t * 4096 + 1024)), BC8(qr), s);
#pragma unroll
                for (int i = 0; i < 4; ++i) { p0[i] = pk_bf16(EX(s[2 * i]), EX(s[2 * i + 1])); p1[i] = pk_bf16(EX(s[8 + 2 * i]), EX(s[9 + 2 * i])); }
                o = MFMA_BF(BC8(BLD(hb + t * 4096 + 2048)), BC8(p0), o); o = MFMA_BF(BC8(BLD(hb + t * 4096 + 3072)), BC8(p1), o);
            }
        }
    }
    {
        const auto r = __builtin_amdgcn_permlane32_swap(__float_as_uint(o[12]), __float_as_uint(o[12]), false, false);
        const float rl = __builtin_amdgcn_rcpf(__uint_as_float(r[1]));
        u32x4 o0, o1;
#pragma unroll
        for (int i = 0; i < 4; ++i) { o0[i] = pk_bf16(o[2 * i] * rl, o[2 * i + 1] * rl); o1[i] = pk_bf16(o[8 + 2 * i] * rl, o[9 + 2 * i] * rl); }
        if (hi) { o1[2] = 0; o1[3] = 0; }
        const unsigned fi = (unsigned)((4 * grp + w) * 16 + head);
        BST(o0, WS_O + fi * 2048 + voff); BST(o1, WS_O + fi * 2048 + 1024 + voff);
        STAMP(9);
        drain();
        put_flag(ws, FL_O + fi * FS, tag, lane);
    }
    STAMP(10);
}

DI void role_b(const uint16_t* __restrict__ wo, const uint16_t* __restrict__ u, const uint16_t* __restrict__ lar, const uint16_t* __restrict__ aim,
               const uint16_t* __restrict__ cre, const uint16_t* __restrict__ cim, const uint16_t* __restrict__ ldt, const uint16_t* __restrict__ dskip,
               uint16_t* __restrict__ out, char* __restrict__ ws, LAS char* lds, int w, int lane) {
    u64 tag;
    const unsigned lds0 = (unsigned)(uintptr_t)lds;
    LAS unsigned* sy = (LAS unsigned*)(lds + LSY);
    const __amdgpu_buffer_rsrc_t rs = __builtin_amdgcn_make_buffer_rsrc(ws, 0, WS_END, 0x00020000);
    const int b = blockIdx.x, xcd = b & 7, bi = b >> 3, r32 = lane & 31, hi = lane >> 5;
    const unsigned voff = lane * 16;
    LAS char* tab = lds + LB + w * TABW;
    if (w == 0) sy[lane] = 0;
    if (xcd < 7) {
        const int cc = w >> 1, hh = w & 1, pair = 32 * xcd + bi, ch = 2 * pair + cc;
        LAS char* cb = lds + LB + cc * CBW;
        const unsigned po = (unsigned)(ch * NS + 128 * hh) / 2 + lane;
        const unsigned pl = ((const unsigned*)lar)[po], pa = ((const unsigned*)aim)[po], pr = ((const unsigned*)cre)[po], pi = ((const unsigned*)cim)[po];
        const unsigned uo = (unsigned)((64 * bi + 16 * w + (lane >> 3)) * D + 64 * xcd + (lane & 7) * 8) * 2;
        glds2(u, uo, uo + 8 * D * 2 - 1024, lds0 + LB + w * TABW + 4096);
        const u64 e0 = __hip_atomic_load((GAS u64*)ws, __ATOMIC_RELAXED, __HIP_MEMORY_SCOPE_AGENT);
        u32x2 gi[2][3];
#pragma unroll
        for (int ps = 0; ps < 2; ++ps)
#pragma unroll
            for (int i = 0; i < 3; ++i) gi[ps][i] = ((const u32x2*)g_idx.v[128 * hh + 64 * ps + lane])[i];
        const unsigned lw = ((const unsigned*)ldt)[pair], dw = ((const unsigned*)dskip)[pair];
        asm volatile("s_waitcnt lgkmcnt(0)\n\ts_barrier" ::: "memory");
        __builtin_amdgcn_s_setprio(PRIO_B);
        const float dt = rbf(expf(cc ? bf_hi(lw) : bf_lo(lw)));
        drain();
        STAMP(1);
        arrive(sy + SY_WO);
        if (w == 0 && lane == 0) __hip_atomic_store((LAS u64*)sy, mix(e0), __ATOMIC_RELAXED, __HIP_MEMORY_SCOPE_WORKGROUP);
        await(sy + SY_WO, 4);
        {
            u32x4 o0, o1; transpose_tile(lds + LB + 4096, TABW, tab, sy + SY_T1, w, lane, o0, o1);
            const unsigned to = WS_UT + (unsigned)((64 * xcd + 16 * w + (lane >> 3)) * S + 64 * bi) * 2 + (lane & 7) * 16;
            __builtin_amdgcn_s_setprio(3); BST(o0, to); BST(o1, to + 8 * S * 2); __builtin_amdgcn_s_setprio(PRIO_B);
        }
        const float cm = wave_max(fmaxf(fmaxf(fabsf(bf_lo(pr)), fabsf(bf_hi(pr))), fmaxf(fabsf(bf_lo(pi)), fabsf(bf_hi(pi)))));
        unsigned ce = __float_as_uint(cm) & 0x7f800000u; ce = ce < 0x01000000u ? 0x01000000u : (ce > 0x7e800000u ? 0x7e800000u : ce);
        const float cscale = __uint_as_float(0x7f000000u - ce), oscale = 2.0f * dt * __uint_as_float(ce);
        LAS unsigned* tw = (LAS unsigned*)(tab + lane * 4);
        LAS const char* trd = tab + r32 * TROW + hi * 16;
        f32x16 dacc = {};
        f32x2 z1, z8, z256, x0;
#define ANCH(vl, va, vr, vi) do { const float a2 = (-rbf(expf(vl)) * dt) * 1.4426950408889634f, rev = ((va) * dt) * 0.15915494309189535f; \
            z1 = anchor(a2, -rev, 1.f); z8 = anchor(a2, rev, 8.f); z256 = anchor(a2, -rev, 256.f); x0 = (f32x2){(vr) * cscale, -(vi) * cscale}; \
            PIN(z1); PIN(z8); PIN(z256); SBAR(); } while (0)
#define TW(row) tw[(row) * (TROW / 4)]
#define CHAIN() do { \
            const f32x2 z1s = rot(z1), zs = rot(z256), z8s = rot(z8); \
            f32x2 y[4], X[8]; y[1] = z8; y[2] = cmul(z8, z8, z8s); X[0] = x0; X[1] = cmul(x0, z1, z1s); f32x2 a = cmul(x0, z256, zs); \
            y[3] = cmul(y[2], z8, z8s); const f32x2 z32 = cmul(y[2], y[2], rot(y[2])), z32s = rot(z32); X[2] = cmul(X[1], z1, z1s); y[0] = z32; \
            f32x2 c4 = cmul(X[2], z256, zs); \
            unsigned c[2][7], d0 = pk_f16(X[0]), d1 = pk_f16(X[1]), d2 = pk_f16(y[1]); \
            c[0][0] = pk_f16(y[0]); c[0][1] = 0x3c00u; c[0][2] = pk_f16(y[2]); c[0][3] = pk_f16(y[3]); c[0][4] = pk_f16(X[2]); c[0][5] = pk_f16(a); c[0][6] = pk_f16(c4); \
            TW(0) = d0; TW(1) = d1; TW(22) = d2; SBAR(); \
            _Pragma("unroll") for (int i = 1; i < 9; ++i) { \
                f32x2 t0, t1, t2, t3, t4, t5, t6; const int j = i - 1; unsigned* pc = c[j & 1]; unsigned* nc = c[i & 1]; \
                if (i < 8) { if (i > 1) t0 = y[0].xx * z32; t1 = y[1].xx * z32; t2 = y[2].xx * z32; t3 = y[3].xx * z32; \
                    if (i < 6) t4 = X[i + 1].xx * z1; t5 = a.xx * z256; \
                    if (i == 3) t6 = X[4].xx * z256; if (i == 4 || i == 5) t6 = c4.xx * z256; if (i == 6) t6 = X[6].xx * z256; } SBAR(); \
                if (j != 1) TW(21 + (j ? 4 * j : 4)) = pc[0]; TW(21 + (j ? 4 * j + 1 : 0)) = pc[1]; TW(23 + 4 * j) = pc[2]; TW(24 + 4 * j) = pc[3]; \
                if (j < 6) TW(j + 2) = pc[4]; TW(j == 0 ? 8 : (j == 1 ? 12 : (j == 2 ? 14 : 13 + j))) = pc[5]; \
                if (j == 0) TW(9) = pc[6]; if (j == 3) TW(10) = pc[6]; if (j == 4) TW(13) = pc[6]; if (j == 5) TW(15) = pc[6]; if (j == 6) TW(11) = pc[6]; SBAR(); \
                if (i < 8) { if (i > 1) y[0] = pfma(y[0].yy, z32s, t0); y[1] = pfma(y[1].yy, z32s, t1); y[2] = pfma(y[2].yy, z32s, t2); y[3] = pfma(y[3].yy, z32s, t3); \
                    if (i < 6) X[i + 2] = pfma(X[i + 1].yy, z1s, t4); a = pfma(a.yy, zs, t5); \
                    if (i == 3) c4 = pfma(X[4].yy, zs, t6); if (i == 4 || i == 5) c4 = pfma(c4.yy, zs, t6); if (i == 6) c4 = pfma(X[6].yy, zs, t6); SBAR(); \
                    if (i > 1) nc[0] = pk_f16(y[0]); nc[1] = pk_f16(y[1]); nc[2] = pk_f16(y[2]); nc[3] = pk_f16(y[3]); \
                    if (i < 6) nc[4] = pk_f16(X[i + 2]); nc[5] = pk_f16(a); if (i >= 3 && i <= 6) nc[6] = pk_f16(c4); \
                    asm volatile("" :: "v"(pc[0]), "v"(pc[1]), "v"(pc[2]), "v"(pc[3]), "v"(pc[4]), "v"(pc[5]), "v"(pc[6])); SBAR(); } \
            } } while (0)
        f16x8 fa[8], fb[8];
#define RDT() do { _Pragma("unroll") for (int kk = 0; kk < 8; ++kk) { fa[kk] = *(LAS const f16x8*)(trd + kk * 32); fb[kk] = *(LAS const f16x8*)(trd + 21 * TROW + kk * 32); } SBAR(); } while (0)
#define MMT() do { _Pragma("unroll") for (int kk = 0; kk < 8; ++kk) dacc = MFMA_F16(fa[kk], fb[kk], dacc); } while (0)
        ANCH(bf_lo(pl), bf_lo(pa), bf_lo(pr), bf_lo(pi));
        tag = get_tag(sy); await(sy + SY_T1, 4);
        STAMP(13);
        CHAIN(); RDT();
        drain(); __builtin_amdgcn_s_setprio(3); put_flag(ws, FL_UT + (unsigned)(xcd * 128 + w * 32 + bi) * FS, tag, lane); __builtin_amdgcn_s_setprio(PRIO_B);
        STAMP(14);
        ANCH(bf_hi(pl), bf_hi(pa), bf_hi(pr), bf_hi(pi));
        MMT(); SBAR();
        CHAIN(); RDT();
        const unsigned utf = FL_UT + (unsigned)(xcd * 128 + (bi >> 3) * 32 + r32) * FS;
        const u64 utv = __hip_atomic_load((GAS u64*)(ws + utf), __ATOMIC_RELAXED, __HIP_MEMORY_SCOPE_AGENT); SBAR();
        MMT();
        STAMP(2);
        {
            LAS float* pt = (LAS float*)tab;
            *(LAS f32x4*)(pt + 8 * r32 + 4 * hi) = (f32x4){dacc[0] * oscale, dacc[1] * oscale, dacc[2] * oscale, dacc[3] * oscale};
            *(LAS f32x2*)(pt + (hi ? 384 + 2 * r32 : 256 + 4 * r32)) = (f32x2){dacc[4] * oscale, dacc[5] * oscale};
            *(LAS f32x2*)(pt + (hi ? 448 + 2 * r32 : 258 + 4 * r32)) = (f32x2){dacc[6] * oscale, dacc[7] * oscale};
            pt[hi ? 640 + r32 : 512 + r32] = dacc[8] * oscale;
            pt[hi ? 672 + r32 : 544 + r32] = dacc[9] * oscale;
            pt[hi ? 672 + r32 : 576 + r32] = dacc[10] * oscale;
            pt[hi ? 672 + r32 : 608 + r32] = dacc[11] * oscale;
        }
        LAS unsigned* ps_ = sy + SY_P + 5 * cc;
        arrive(ps_ + 0);
        LAS char* ui = cb + UOF;
        LAS char* kt = cb + KOF;
        await(ps_ + 0, 2);
#pragma unroll
        for (int ps = 0; ps < 2; ++ps) {
            const int v = 81 * hh + 64 * ps + lane;
            if (ps == 0 || lane < 17) {
                const f32x4 p0 = *(LAS const f32x4*)(cb + v * 16), p1 = *(LAS const f32x4*)(cb + TABW + v * 16);
                u32x2 r = {pk_bf16(p0[0] + p1[0], p0[1] + p1[1]), pk_bf16(p0[2] + p1[2], p0[3] + p1[3])};
                if (v == 161) r = (u32x2){0u, 0u};
                *(LAS u32x2*)(kt + v * 8) = r;
            }
        }
        arrive(ps_ + 1);
        if (__any(utv != tag)) wait_flags<4>(ws, utf, tag, !hi);
        CFENCE();
        const unsigned ub = WS_UT + (unsigned)(ch * S + 1024 * hh) * 2 + voff;
        const u32x4 u0 = BLD(ub), u1 = BLD(ub + 1024); SBAR();
        await(ps_ + 1, 2);
        STAMP(3);
        __builtin_amdgcn_s_setprio(PRIO_B2);
        const u32x4 z4 = {};
        {
            unsigned W[2][6];
#pragma unroll
            for (int ps = 0; ps < 2; ++ps)
#pragma unroll
                for (int i = 0; i < 6; ++i) { const unsigned gv = gi[ps][i >> 1][i & 1];
                    W[ps][i] = *(LAS const uint16_t*)(kt + (gv & 0xffffu));
                    if (i < 5) W[ps][i] |= (unsigned)*(LAS const uint16_t*)(kt + (gv >> 16)) << 16; }
            *(LAS u32x4*)(ui + (64 * hh + lane) * 16) = z4;
            if (lane < 16) *(LAS u32x4*)(ui + (128 + 16 * hh + lane) * 16) = z4;
            if (lane < 8) *(LAS u32x4*)(cb + (2 * hh + (lane >> 2)) * CPY + (256 + (lane & 3)) * 16) = z4;
#pragma unroll
            for (int ps = 0; ps < 2; ++ps) {
                unsigned V[5];
#pragma unroll
                for (int i = 0; i < 5; ++i) V[i] = __builtin_amdgcn_alignbit(W[ps][i + 1], W[ps][i], 16);
                LAS char* cp = cb + (128 * hh + 64 * ps + lane) * 16;
                *(LAS u32x4*)(cp) = (u32x4){W[ps][0], W[ps][1], W[ps][2], W[ps][3]};
                *(LAS u32x4*)(cp + CPY) = (u32x4){V[0], V[1], V[2], V[3]};
                *(LAS u32x4*)(cp + 2 * CPY) = (u32x4){W[ps][1], W[ps][2], W[ps][3], W[ps][4]};
                *(LAS u32x4*)(cp + 3 * CPY) = (u32x4){V[1], V[2], V[3], V[4]};
            }
        }
        *(LAS u32x4*)(ui + (32 + 32 * hh + (lane >> 2)) * 80 + (lane & 3) * 16) = u0;
        *(LAS u32x4*)(ui + (48 + 32 * hh + (lane >> 2)) * 80 + (lane & 3) * 16) = u1;
        arrive(ps_ + 2); await(ps_ + 2, 2);
        STAMP(4);
        f32x16 a0 = {}, a1 = {};
        {
            const int sb = 7 - (r32 & 7);
            LAS const char* pa = cb + (sb & 3) * CPY + (255 - (r32 >> 3) + hi) * 16 + (sb >> 2) * 8 - 64 * (16 * hh + 3) - 2048;
            LAS const char* pa2 = pa + 8; asm volatile("" : "+v"(pa2));
            LAS const char* pb = ui + (32 + r32 - 16 * hh - 3) * 80 + 16 * hi;
            s16x4 ta[2][2], tb[2][2]; bf16x8 b0[2], b1[2];
#define T4(q, off) (*(LAS const s16x4*)((q) + (off)))
#define CSTEP(i, j, RD) do { const int o_ = 64 * (3 - ((j) >> 1)) + 32 * ((j) & 1), u_ = 80 * (3 - ((j) >> 1)) + 32 * ((j) & 1); \
                a1 = MFMA_BF(cat8(ta[i][0], ta[i][1]), b1[i], a1); SBAR(); if (RD) { ta[1 - i][0] = T4(pa, o_ + 2048); ta[1 - i][1] = T4(pa2, o_ + 2048); } SBAR(); \
                a0 = MFMA_BF(cat8(ta[i][0], ta[i][1]), b0[i], a0); SBAR(); if (RD) { b1[1 - i] = FR(pb, u_ + 2560); b0[1 - i] = FR(pb, u_); } SBAR(); \
                a1 = MFMA_BF(cat8(tb[i][0], tb[i][1]), b0[i], a1); SBAR(); if (RD) { tb[1 - i][0] = T4(pa, o_); tb[1 - i][1] = T4(pa2, o_); } SBAR(); } while (0)
            ta[0][0] = T4(pa, 192 + 2048); ta[0][1] = T4(pa2, 192 + 2048); b1[0] = FR(pb, 240 + 2560); b0[0] = FR(pb, 240); tb[0][0] = T4(pa, 192); tb[0][1] = T4(pa2, 192);
#pragma unroll 1
            for (int m4 = 0; m4 < 3; ++m4) {
                CSTEP(0, 1, 1); CSTEP(1, 2, 1); CSTEP(0, 3, 1); CSTEP(1, 4, 1); CSTEP(0, 5, 1); CSTEP(1, 6, 1); CSTEP(0, 7, 1);
                pa -= 256; pa2 -= 256; pb -= 320;
                CSTEP(1, 0, 1);
            }
            CSTEP(0, 1, 1); CSTEP(1, 2, 1); CSTEP(0, 3, 1); CSTEP(1, 4, 1); CSTEP(0, 5, 1); CSTEP(1, 6, 1); CSTEP(0, 7, 1);
            arrive(ps_ + 3);
            CSTEP(1, 0, 0);
        }
        STAMP(5);
        {
            u32x2 uu[4];
#pragma unroll
            for (int g = 0; g < 4; ++g) uu[g] = *(LAS const u32x2*)(ui + (32 + 32 * hh + r32) * 80 + 16 * g + 8 * hi);
            LAS char* ex = cb + r32 * 144 + hi * 16;
            const f32x16 snd = hh ? a0 : a1; const f32x16 y = hh ? a1 : a0;
            await(ps_ + 3, 2);
#pragma unroll
            for (int g = 0; g < 4; ++g) *(LAS f32x4*)(ex + hh * 4608 + g * 32) = (f32x4){snd[4 * g], snd[4 * g + 1], snd[4 * g + 2], snd[4 * g + 3]};
            arrive(ps_ + 4);
            const float dsk = cc ? bf_hi(dw) : bf_lo(dw);
            unsigned sk[8];
#pragma unroll
            for (int g = 0; g < 4; ++g)
#pragma unroll
                for (int e = 0; e < 2; ++e) sk[2 * g + e] = pk_bf16(bf_lo(uu[g][e]) * dsk, bf_hi(uu[g][e]) * dsk);
            await(ps_ + 4, 2);
            LAS char* sc = cb + 9216 + hh * 2048;
#pragma unroll
            for (int g = 0; g < 4; ++g) {
                const f32x4 p = *(LAS const f32x4*)(ex + (1 - hh) * 4608 + g * 32);
                u32x2 r;
#pragma unroll
                for (int e = 0; e < 2; ++e) {
                    const unsigned yb = pk_bf16(y[4 * g + 2 * e] + p[2 * e], y[4 * g + 2 * e + 1] + p[2 * e + 1]);
                    r[e] = pk_bf16(bf_lo(yb) + bf_lo(sk[2 * g + e]), bf_hi(yb) + bf_hi(sk[2 * g + e]));
                }
                *(LAS u32x2*)(sc + 64 * r32 + 16 * g + 8 * hi) = r;
            }
            const u32x4 o0 = *(LAS const u32x4*)(sc + voff), o1 = *(LAS const u32x4*)(sc + 1024 + voff);
            const unsigned bo = WS_BT + (unsigned)(ch * S + 1024 * hh) * 2 + voff;
            BST(o0, bo); BST(o1, bo + 1024);
        }
        arrive(sy + SY_BG);
        STAMP(6);
        drain();
        put_flag(ws, FL_BT + (unsigned)(xcd * 128 + bi * 4 + w) * FS, tag, lane);
    } else {
        {
            const unsigned nb = (bi & 1) ? 12 * 2 * D : 16 * 2 * D;
            unsigned v4[4];
#pragma unroll
            for (int i = 0; i < 4; ++i) { const unsigned o = (unsigned)(w * 4096 + i * 1024) + voff; v4[i] = (o < nb ? o : 0u) + 4096 - i * 1024; }
            glds4((const char*)(wo + (DH * (bi >> 1) + 16 * (bi & 1)) * D) - 4096, v4[0], v4[1], v4[2], v4[3], lds0 + LB + w * 4096);
        }
        const u64 e0 = __hip_atomic_load((GAS u64*)ws, __ATOMIC_RELAXED, __HIP_MEMORY_SCOPE_AGENT);
        asm volatile("s_waitcnt lgkmcnt(0)\n\ts_barrier" ::: "memory");
        drain();
        if (w == 0 && lane == 0) __hip_atomic_store((LAS u64*)sy, mix(e0), __ATOMIC_RELAXED, __HIP_MEMORY_SCOPE_WORKGROUP);
        arrive(sy + SY_WO); await(sy + SY_WO, 4);
        LAS const char* p = lds + LB + (4 * hi + ((lane & 15) >> 2)) * (2 * D) + ((lane >> 4) & 1) * 32 + (lane & 3) * 8;
#pragma unroll
        for (int i = 0; i < 4; ++i) {
            const int ct = w + 4 * i;
            if (ct < 14) {
                u32x4 o = __builtin_bit_cast(u32x4, cat8(trread(p + ct * 64), trread(p + 8 * 2 * D + ct * 64)));
                if (hi && (bi & 1)) { o[2] = 0; o[3] = 0; }
                BST(o, WS_WO + (unsigned)(bi * 14 + ct) * 1024 + voff);
            }
        }
        tag = get_tag(sy);
        drain();
        put_flag(ws, FL_WO + (unsigned)(w * 32 + bi) * FS, tag, lane);
        arrive(sy + SY_BG);
    }
    const int qt = 8 * xcd + (bi >> 2), ct = 4 * (bi & 3) + w;
    bf16x8 wf[32];
    await_slow(sy + SY_G, 4);
    wait_flags<8>(ws, FL_WO + (unsigned)(w * 32 + r32) * FS, tag, !hi);
    if (ct < 14) {
#pragma unroll
        for (int i = 0; i < 32; ++i) { wf[i] = BC8(__builtin_amdgcn_raw_buffer_load_b128(rs, voff, WS_WO + (unsigned)(((8 * w + i) & 31) * 14 + ct) * 1024, 16)); __builtin_amdgcn_s_sleep(WOSL); SBAR(); }
    }
    STAMP(7);
    const __amdgpu_buffer_rsrc_t ro = __builtin_amdgcn_make_buffer_rsrc(out, 0, S * 2 * D * 2, 0x00020000);
    if (xcd < 7) {
        wait_flags<32>(ws, FL_BT + (unsigned)(xcd * 128 + w * 32 + r32) * FS, tag, !hi);
        await(sy + SY_BG, 4);
        const unsigned bo = WS_BT + (unsigned)((64 * xcd + 16 * w + (lane >> 3)) * S + 64 * bi + (lane & 7) * 8) * 2;
        glds2c(ws, bo, bo + 8 * S * 2 - 1024, lds0 + LB + 32768 + w * 2048);
        drain();
        arrive(sy + SY_T2); await(sy + SY_T2, 4);
        u32x4 o0, o1; transpose_tile(lds + LB + 32768, 2048, lds + LB + 40960 + w * 2304, nullptr, w, lane, o0, o1);
        const unsigned oo = (unsigned)((64 * bi + 16 * w + (lane >> 3)) * 2 * D + D + 64 * xcd) * 2 + (lane & 7) * 16;
        __builtin_amdgcn_raw_buffer_store_b128(o0, ro, oo, 0, 16); __builtin_amdgcn_raw_buffer_store_b128(o1, ro, oo + 8 * 2 * D * 2, 0, 16);
    } else await(sy + SY_BG, 4);
    STAMP(8);
    await_slow(sy + SY_X, 4);
    wait_flags<2>(ws, FL_O + (unsigned)(qt * 16 + 4 * w + lane) * FS, tag, lane < 4);
    STAMP(9);
    u32x4 of[8];
#pragma unroll
    for (int i = 0; i < 8; ++i) of[i] = BLD(WS_O + (unsigned)(qt * 32 + 8 * w + i) * 1024 + voff);
#pragma unroll
    for (int i = 0; i < 8; ++i) *(LAS u32x4*)(lds + LB + (8 * w + i) * 1024 + voff) = of[i];
    arrive(sy + SY_B2);
    STAMP(10);
    f32x16 acc = {};
    if (ct < 14) {
#pragma unroll
        for (int i = 0; i < 8; ++i) acc = MFMA_BF(wf[i], BC8(of[i]), acc);
    }
    SBAR();
    await(sy + SY_B2, 4);
    STAMP(11);
    if (ct < 14) {
        LAS const char* po[3];
#pragma unroll
        for (int j = 0; j < 3; ++j) po[j] = lds + LB + ((w + 1 + j) & 3) * 8192 + voff;
        bf16x8 og[4];
#pragma unroll
        for (int i = 0; i < 3; ++i) og[i] = FR(po[0], i * 1024);
#pragma unroll
        for (int i = 0; i < 24; ++i) { if (i < 21) og[(i + 3) & 3] = FR(po[(i + 3) >> 3], ((i + 3) & 7) * 1024); SBAR(); acc = MFMA_BF(wf[8 + i], og[i & 3], acc); SBAR(); }
        LAS char* sc = lds + LB + 40960 + w * 2304;
#pragma unroll
        for (int g = 0; g < 4; ++g) *(LAS u32x2*)(sc + r32 * 72 + 16 * g + 8 * hi) = (u32x2){pk_bf16(acc[4 * g], acc[4 * g + 1]), pk_bf16(acc[4 * g + 2], acc[4 * g + 3])};
        LAS const char* sr = sc + (lane >> 2) * 72 + (lane & 3) * 16;
        const u32x2 a0 = *(LAS const u32x2*)sr, a1 = *(LAS const u32x2*)(sr + 8), b0 = *(LAS const u32x2*)(sr + 16 * 72), b1 = *(LAS const u32x2*)(sr + 16 * 72 + 8);
        const unsigned oo = (unsigned)((32 * qt + (lane >> 2)) * 2 * D + 32 * ct) * 2 + (lane & 3) * 16;
        __builtin_amdgcn_raw_buffer_store_b128((u32x4){a0[0], a0[1], a1[0], a1[1]}, ro, oo, 0, 16);
        __builtin_amdgcn_raw_buffer_store_b128((u32x4){b0[0], b0[1], b1[0], b1[1]}, ro, oo + 16 * 2 * D * 2, 0, 16);
    }
    if (b == 0 && w == 0) put_flag(ws, 0, tag, lane);
    STAMP(12);
}

__global__ __launch_bounds__(512, 2) void fused(const uint16_t* __restrict__ x, const uint16_t* __restrict__ wqkv, const uint16_t* __restrict__ wo,
                                               const uint16_t* __restrict__ u, const uint16_t* __restrict__ lar, const uint16_t* __restrict__ aim,
                                               const uint16_t* __restrict__ cre, const uint16_t* __restrict__ cim, const uint16_t* __restrict__ ldt,
                                               const uint16_t* __restrict__ dskip, uint16_t* __restrict__ out, char* __restrict__ ws) {
    extern __shared__ __attribute__((aligned(16))) char smem[];
    LAS char* lds = (LAS char*)smem;
    const int lane = threadIdx.x & 63, w = __builtin_amdgcn_readfirstlane(threadIdx.x >> 6);
    STAMP(0);
    if (w < 4) role_a(x, wqkv, ws, lds, w, lane);
    else role_b(wo, u, lar, aim, cre, cim, ldt, dskip, out, ws, lds, w - 4, lane);
}
}

extern "C" void kernel_launch(void* const* d_in, const int* in_sizes, int n_in, void* d_out, int out_size, void* d_ws, size_t ws_size, hipStream_t stream) {
    using namespace k;
    (void)hipFuncSetAttribute((const void*)fused, hipFuncAttributeMaxDynamicSharedMemorySize, LDS_BYTES);
    const uint16_t* const* in = (const uint16_t* const*)d_in;
    fused<<<256, 512, LDS_BYTES, stream>>>(in[0], in[1], in[2], in[3], in[4], in[5], in[6], in[7], in[8], in[9], (uint16_t*)d_out, (char*)d_ws);
}
```

```cpp
#define HA 99
#define HB -1
#include <hip/hip_runtime.h>
#include <hip/hip_bf16.h>
#include <stdint.h>

typedef short bf16x8 __attribute__((ext_vector_type(8)));
typedef _Float16 f16x8 __attribute__((ext_vector_type(8)));
typedef _Float16 f16x2 __attribute__((ext_vector_type(2)));
typedef __bf16 bf16x2 __attribute__((ext_vector_type(2)));
typedef short s16x4 __attribute__((ext_vector_type(4)));
typedef float f32x16 __attribute__((ext_vector_type(16)));
typedef float f32x4 __attribute__((ext_vector_type(4)));
typedef float f32x2 __attribute__((ext_vector_type(2)));
typedef unsigned u32x4 __attribute__((ext_vector_type(4)));
typedef unsigned u32x2 __attribute__((ext_vector_type(2)));
typedef unsigned long long u64;
#define LAS __attribute__((address_space(3)))
#define GAS __attribute__((address_space(1)))
#define DI __device__ __forceinline__
#define MFMA_BF(a, b, c) __builtin_amdgcn_mfma_f32_32x32x16_bf16((a), (b), (c), 0, 0, 0)
#define MFMA_F16(a, b, c) __builtin_amdgcn_mfma_f32_32x32x16_f16((a), (b), (c), 0, 0, 0)
#define SBAR() __builtin_amdgcn_sched_barrier(0)
#define CFENCE() asm volatile("" ::: "memory")
#define PIN(x) asm volatile("" : "+v"(x))
#define BC8(x) __builtin_bit_cast(bf16x8, (x))
#define EX(x) __builtin_amdgcn_exp2f(x)
#define PRIO_A1 0
#define PRIO_B 0
#define PRIO_B2 1
#define ROT 1
#define WOSL 2

namespace k {
#define STAMP(i) do { if ((i) == ((threadIdx.x >> 8) ? HB : HA) && (threadIdx.x & 255) == 0) ((LAS volatile unsigned*)(lds + LSY))[40] = (unsigned)(__builtin_amdgcn_s_memrealtime() - t_start); \
    if ((i) == 12 && (threadIdx.x >> 8)) { const u64 e_ = 2800 + ((LAS volatile unsigned*)(lds + LSY))[40]; while (__builtin_amdgcn_s_memrealtime() - t_start < e_) __builtin_amdgcn_s_sleep(2); } } while (0)
constexpr int S = 2048, D = 448, DH = 28, NS = 256;
constexpr unsigned FS = 128;
constexpr unsigned FL_KV = FS, FL_O = FL_KV + 1024 * FS, FL_UT = FL_O + 1024 * FS, FL_BT = FL_UT + 896 * FS, FL_WO = FL_BT + 896 * FS;
constexpr unsigned WS_KV = 512u << 10;
constexpr unsigned WS_O = WS_KV + (4u << 20);
constexpr unsigned WS_WO = WS_O + (2u << 20);
constexpr unsigned WS_UT = WS_WO + (448u << 10);
constexpr unsigned WS_BT = WS_UT + D * S * 2;
constexpr unsigned WS_END = WS_BT + D * S * 2;
constexpr int WSL = 12288, LXR = 4 * WSL, XSL = 16384, LB = LXR + 3 * XSL;
constexpr int LQ = 0, LX = 8192;
constexpr int TROW = 272, TABW = 53 * TROW, CBW = 2 * TABW, CPY = 4160, UOF = 4 * CPY, KOF = CBW - 1312;
constexpr int LSY = LB + 2 * CBW, LDS_BYTES = LSY + 256;
static_assert(LDS_BYTES <= 163840 && (LSY & 15) == 0 && UOF + 7680 <= KOF, "lds");
enum { SY_TAG = 0, SY_F = 2, SY_P = 10, SY_WO = 20, SY_T1 = 21, SY_BG = 22, SY_B2 = 23, SY_Q = 24, SY_X = 25, SY_G = 26, SY_T2 = 27 };
constexpr int SPIN = 1 << 18;

struct IdxTab { uint16_t v[256][12]; };
constexpr IdxTab make_idx() {
    IdxTab t = {};
    for (int y = 0; y < 256; ++y) for (int e = 0; e < 12; ++e) {
        const int j = 2047 - 8 * y - e; int ix = 644;
        if (j >= 0) { const int sh = j >= 1024 ? 3 : (j >= 512 ? 2 : (j >= 256 ? 1 : 0));
            ix = sh ? 128 * sh + ((j + (1 << (sh - 1)) - 1 + ((j >> sh) & 1)) >> sh) : j; }
        t.v[y][e] = (uint16_t)(2 * ix);
    }
    return t;
}
__device__ const IdxTab g_idx __attribute__((aligned(16))) = make_idx();

DI unsigned pk_bf16(float a, float b) { f32x2 v = {a, b}; return __builtin_bit_cast(unsigned, __builtin_convertvector(v, bf16x2)); }
DI unsigned pk_f16(f32x2 v) { return __builtin_bit_cast(unsigned, __builtin_convertvector(v, f16x2)); }
DI float bf_lo(unsigned w) { return __uint_as_float(w << 16); }
DI float bf_hi(unsigned w) { return __uint_as_float(w & 0xffff0000u); }
DI float rbf(float x) { return bf_lo(pk_bf16(x, x)); }
#define GL0 "s_mov_b32 %0, m0\n\ts_mov_b32 m0, %2\n\ts_nop 0\n\t"
#define GL1(V, O, C) "global_load_lds_dwordx4 " V ", %1 offset:" #O C "\n\t"
#define GLE "s_mov_b32 m0, %0"
DI void glds4(const void* sb, unsigned v0, unsigned v1, unsigned v2, unsigned v3, unsigned lb) { unsigned sv;
    asm volatile(GL0 GL1("%3", 0, "") GL1("%4", 1024, "") GL1("%5", 2048, "") GL1("%6", 3072, "") GLE : "=&s"(sv) : "s"(sb), "s"(lb), "v"(v0), "v"(v1), "v"(v2), "v"(v3) : "memory"); }
DI void glds3(const void* sb, unsigned v0, unsigned v1, unsigned v2, unsigned lb) { unsigned sv;
    asm volatile(GL0 GL1("%3", 0, "") GL1("%4", 1024, "") GL1("%5", 2048, "") GLE : "=&s"(sv) : "s"(sb), "s"(lb), "v"(v0), "v"(v1), "v"(v2) : "memory"); }
DI void glds2(const void* sb, unsigned v0, unsigned v1, unsigned lb) { unsigned sv;
    asm volatile(GL0 GL1("%3", 0, "") GL1("%4", 1024, "") GLE : "=&s"(sv) : "s"(sb), "s"(lb), "v"(v0), "v"(v1) : "memory"); }
DI void glds2c(const void* sb, unsigned v0, unsigned v1, unsigned lb) { unsigned sv;
    asm volatile(GL0 GL1("%3", 0, " sc1") GL1("%4", 1024, " sc1") GLE : "=&s"(sv) : "s"(sb), "s"(lb), "v"(v0), "v"(v1) : "memory"); }
DI float wave_max(float v) {
#define DPPMAX(ctrl, rm) v = fmaxf(v, __uint_as_float(__builtin_amdgcn_update_dpp(0, __float_as_uint(v), ctrl, rm, 0xf, true)))
    DPPMAX(0x111, 0xf); DPPMAX(0x112, 0xf); DPPMAX(0x114, 0xf); DPPMAX(0x118, 0xf); DPPMAX(0x142, 0xa); DPPMAX(0x143, 0xc);
    return __uint_as_float(__builtin_amdgcn_readlane(__float_as_uint(v), 63));
}
DI s16x4 trread(LAS const char* p) { return __builtin_bit_cast(s16x4, __builtin_amdgcn_ds_read_tr16_b64_v4i16((LAS s16x4*)p)); }
DI bf16x8 cat8(s16x4 a, s16x4 b) { return __builtin_shufflevector(a, b, 0, 1, 2, 3, 4, 5, 6, 7); }
DI f32x2 pfma(f32x2 a, f32x2 b, f32x2 c) { return __builtin_elementwise_fma(a, b, c); }
DI f32x2 cmul(f32x2 a, f32x2 b, f32x2 bs) { return pfma(a.yy, bs, a.xx * b); }
DI f32x2 rot(f32x2 b) { return (f32x2){-b.y, b.x}; }
DI f32x2 anchor(float a2, float rev, float s) {
    const float mag = EX(a2 * s);
    float r = rev * s; r -= __builtin_rintf(r);
    return (f32x2){mag * __builtin_amdgcn_cosf(r), mag * __builtin_amdgcn_sinf(r)};
}
DI void arrive(LAS unsigned* c) { asm volatile("s_mov_b64 exec, 1\n\tds_add_u32 %0, %1\n\ts_mov_b64 exec, -1" :: "v"((unsigned)(uintptr_t)c), "v"(1u) : "memory"); }
DI void await(LAS unsigned* c, unsigned n) {
    unsigned t, i = SPIN;
    asm volatile("1:\n\tds_read_b32 %0, %2\n\ts_waitcnt lgkmcnt(0)\n\tv_cmp_gt_u32 vcc, %3, %0\n\ts_cbranch_vccz 2f\n\ts_sleep 1\n\ts_sub_u32 %1, %1, 1\n\ts_cbranch_scc0 1b\n2:"
                 : "=&v"(t), "+s"(i) : "v"((unsigned)(uintptr_t)c), "s"(n) : "vcc", "scc", "memory");
}
DI void await_slow(LAS unsigned* c, unsigned n) {
    unsigned t, i = SPIN;
    asm volatile("1:\n\tds_read_b32 %0, %2\n\ts_waitcnt lgkmcnt(0)\n\tv_cmp_gt_u32 vcc, %3, %0\n\ts_cbranch_vccz 2f\n\ts_sleep 3\n\ts_sub_u32 %1, %1, 1\n\ts_cbranch_scc0 1b\n2:"
                 : "=&v"(t), "+s"(i) : "v"((unsigned)(uintptr_t)c), "s"(n) : "vcc", "scc", "memory");
}
DI u64 get_tag(LAS unsigned* sy) {
    await(sy, 1);
    const u64 v = __hip_atomic_load((LAS u64*)sy, __ATOMIC_RELAXED, __HIP_MEMORY_SCOPE_WORKGROUP);
    return ((u64)(unsigned)__builtin_amdgcn_readfirstlane((unsigned)(v >> 32)) << 32) | (unsigned)__builtin_amdgcn_readfirstlane((unsigned)v);
}
DI void drain() { asm volatile("s_waitcnt vmcnt(0)" ::: "memory"); }
DI void put_flag(char* ws, unsigned off, u64 tag, int lane) { if (lane == 0) __hip_atomic_store((GAS u64*)(ws + off), tag, __ATOMIC_RELAXED, __HIP_MEMORY_SCOPE_AGENT); }
template <int SLP> DI void wait_flags(char* ws, unsigned off, u64 tag, bool pend) {
    GAS u64* p = (GAS u64*)(ws + off);
    CFENCE();
#pragma unroll 1
    for (int i = 0; i < SPIN; ++i) {
        if (pend) pend = __hip_atomic_load(p, __ATOMIC_RELAXED, __HIP_MEMORY_SCOPE_AGENT) != tag;
        if (!__any(pend)) break;
        __builtin_amdgcn_s_sleep(SLP);
    }
    CFENCE();
}
#define BST(v, off) __builtin_amdgcn_raw_buffer_store_b128((v), rs, (off), 0, 16)
#define BLD(off) __builtin_amdgcn_raw_buffer_load_b128(rs, (off), 0, 16)
#define FR(p, off) (*(LAS const bf16x8*)((p) + (off)))
DI float swapmax(float m) { const auto r = __builtin_amdgcn_permlane32_swap(__float_as_uint(m), __float_as_uint(m), false, false); return fmaxf(__uint_as_float(r[0]), __uint_as_float(r[1])); }
DI u32x4 put_ref(u32x4 q1, float m, int hi) {
    const float m1 = rbf(m), m2 = rbf(m - m1), m3 = m - m1 - m2;
    if (hi) { q1[2] = pk_bf16(-m1, -m2); q1[3] = pk_bf16(-m3, 0.f); }
    return q1;
}
DI u64 mix(u64 e) { u64 z = e + 0x9E3779B97F4A7C15ull; z = (z ^ (z >> 30)) * 0xBF58476D1CE4E5B9ull; z = (z ^ (z >> 27)) * 0x94D049BB133111EBull; return (z ^ (z >> 31)) | 1ull; }
DI void transpose_tile(LAS const char* stage, int pst, LAS char* scratch, LAS unsigned* done, int w, int lane, u32x4& o0, u32x4& o1) {
    LAS const char* p = stage + (lane >> 4) * pst + ((lane & 15) >> 2) * 128 + w * 32 + (lane & 3) * 8;
    const bf16x8 t0 = cat8(trread(p), trread(p + 512)), t1 = cat8(trread(p + 1024), trread(p + 1536));
    if (done) arrive(done);
    LAS char* sc = scratch + (lane & 15) * 144 + (lane >> 4) * 32;
    *(LAS bf16x8*)sc = t0; *(LAS bf16x8*)(sc + 16) = t1;
    LAS const char* sr = scratch + (lane >> 3) * 144 + (lane & 7) * 16;
    o0 = *(LAS const u32x4*)sr; o1 = *(LAS const u32x4*)(sr + 8 * 144);
}

DI void role_a(const uint16_t* __restrict__ x, const uint16_t* __restrict__ wqkv, char* __restrict__ ws, LAS char* lds, int w, int lane, u64 t_start) {
    const int head = 4 * (blockIdx.x & 3) + ((blockIdx.x >> 3) & 3), grp = 8 * ((blockIdx.x >> 2) & 1) + (blockIdx.x >> 5);
    const unsigned lds0 = (unsigned)(uintptr_t)lds;
    const int r32 = lane & 31, hi = lane >> 5;
    const int wsh = (head & 7) == 7 ? 4 : 0;
    const unsigned wv = (unsigned)((lane >> 2) * 3 * D + (lane & 3) * 8) * 2;
    const char* wb = (const char*)(wqkv + DH * head - wsh + (16 * w) * 3 * D) - 2048;
    const unsigned xv0 = (unsigned)((lane >> 3) * D + (((lane & 7) ^ (lane >> 4)) * 8)) * 2, xv1 = xv0 ^ 64;
    const char* xb = (const char*)(x + (128 * grp + 32 * w) * D);
    const int rho = ROT ? ((blockIdx.x >> 3) + ((blockIdx.x >> 1) & 3)) % 7 : 0;
#define SP(st) ((st) + rho >= 7 ? (st) + rho - 7 : (st) + rho)
#define LOADW(st) glds3(wb + SP(st) * (64 * 3 * D * 2), wv + 2048, wv + 2 * D + 1024, wv + 4 * D, lds0 + ((st) & 3) * WSL + w * 3072)
#define LOADX(st) glds4(xb + SP(st) * 128, xv0, xv1 + 16 * D - 1024, xv0 + 32 * D - 2048, xv1 + 48 * D - 3072, lds0 + LXR + ((st) % 3) * XSL + w * 4096)
    LOADW(0); LOADX(0);
    asm volatile("s_barrier" ::: "memory");
    LOADW(1); LOADX(1); LOADW(2); LOADX(2);
    __builtin_amdgcn_s_setprio(PRIO_A1);
    LAS unsigned* sy = (LAS unsigned*)(lds + LSY);
    const __amdgpu_buffer_rsrc_t rs = __builtin_amdgcn_make_buffer_rsrc(ws, 0, WS_END, 0x00020000);
    const int xs = (r32 >> 1) & 7;
    LAS const char* xp[4];
#pragma unroll
    for (int ks = 0; ks < 4; ++ks) xp[ks] = lds + LXR + w * 4096 + r32 * 128 + (((2 * ks + hi) ^ xs) * 16);
    LAS const char* wp = lds + (8 * hi + ((lane & 15) >> 2)) * 64 + ((lane >> 4) & 1) * 32 + (lane & 3) * 8 + wsh * 2;
    f32x16 aq = {}, ak = {}, av = {};
    bf16x8 xf[2][4]; s16x4 fr[4][6];
#define RX(st, ks) xf[(st) & 1][ks] = FR(xp[ks], ((st) % 3) * XSL)
#define RW(st, ks, j) fr[ks][j] = trread(wp + ((st) & 3) * WSL + (ks) * 3072 + ((((j) >> 1) + 1) % 3) * 1024 + ((j) & 1) * 256)
#define MK(st, ks) ak = MFMA_BF(cat8(fr[ks][0], fr[ks][1]), xf[(st) & 1][ks], ak); SBAR()
#define MV(st, ks) av = MFMA_BF(xf[(st) & 1][ks], cat8(fr[ks][2], fr[ks][3]), av); SBAR()
#define MQ(st, ks) aq = MFMA_BF(cat8(fr[ks][4], fr[ks][5]), xf[(st) & 1][ks], aq); SBAR()
#define QSTEP(st, ks) MK(st, ks); RW(st, ks + 1, 0); RW(st, ks + 1, 1); SBAR(); MV(st, ks); RW(st, ks + 1, 2); RW(st, ks + 1, 3); SBAR(); MQ(st, ks); RW(st, ks + 1, 4); RW(st, ks + 1, 5); SBAR()
    u64 tag = 0;
    asm volatile("s_waitcnt vmcnt(14)" ::: "memory");
    arrive(sy + SY_F); await(sy + SY_F, 4);
    STAMP(16);
    RX(0, 0); RW(0, 0, 0); RW(0, 0, 1); RW(0, 0, 2); RW(0, 0, 3); RW(0, 0, 4); RW(0, 0, 5); RX(0, 1); RX(0, 2); RX(0, 3); SBAR();
#pragma unroll
    for (int st = 0; st < 7; ++st) {
        QSTEP(st, 0); if (st < 4) LOADW(st + 3);
        QSTEP(st, 1); if (st < 4) LOADX(st + 3);
        QSTEP(st, 2);
        if (st < 4) asm volatile("s_waitcnt vmcnt(14)" ::: "memory"); else if (st == 4) asm volatile("s_waitcnt vmcnt(7)" ::: "memory"); else if (st == 5) drain();
        arrive(sy + SY_F + 1 + st); SBAR();
        MK(st, 3); await(sy + SY_F + 1 + st, 4);
        if (st < 6) { RX(st + 1, 0); RW(st + 1, 0, 0); RW(st + 1, 0, 1); RW(st + 1, 0, 2); RW(st + 1, 0, 3); } SBAR();
        MV(st, 3); if (st < 6) { RW(st + 1, 0, 4); RW(st + 1, 0, 5); RX(st + 1, 1); } SBAR();
        MQ(st, 3); if (st < 6) { RX(st + 1, 2); RX(st + 1, 3); } SBAR();
        if (st == 2) tag = get_tag(sy);
    }
    STAMP(3);
    const float cq = 0.18898223650461362f * 1.4426950408889634f;
    const unsigned voff = lane * 16;
    u32x4 q0, q1, k0, k1, v0, v1;
    const unsigned tb = WS_KV + (unsigned)(head * 64 + 4 * grp + w) * 4096 + voff;
#pragma unroll
    for (int i = 0; i < 4; ++i) { k0[i] = pk_bf16(ak[2 * i], ak[2 * i + 1]); k1[i] = pk_bf16(ak[8 + 2 * i], ak[9 + 2 * i]); }
    if (hi) { k1[2] = 0x3f803f80u; k1[3] = 0x00003f80u; }
    BST(k0, tb); BST(k1, tb + 1024);
#pragma unroll
    for (int i = 0; i < 4; ++i) {
        v0[i] = pk_bf16(av[2 * i], av[2 * i + 1]); v1[i] = pk_bf16(av[8 + 2 * i], av[9 + 2 * i]);
        if (r32 >= DH) { v0[i] = v1[i] = (r32 == DH ? 0x3f803f80u : 0u); }
    }
    BST(v0, tb + 2048); BST(v1, tb + 3072);
#pragma unroll
    for (int i = 0; i < 4; ++i) { q0[i] = pk_bf16(aq[2 * i] * cq, aq[2 * i + 1] * cq); q1[i] = pk_bf16(aq[8 + 2 * i] * cq, aq[9 + 2 * i] * cq); }
    if (hi) { q1[2] = 0; q1[3] = 0; }
    const f32x16 zero = {};
    {
        f32x16 s = MFMA_BF(BC8(k0), BC8(q0), zero); s = MFMA_BF(BC8(k1), BC8(q1), s);
        float m = s[0];
#pragma unroll
        for (int i = 1; i < 16; ++i) m = fmaxf(m, s[i]);
        q1 = put_ref(q1, swapmax(m), hi);
    }
    { LAS u32x4* qo = (LAS u32x4*)(lds + LQ + w * 2048) + lane; qo[0] = q0; qo[64] = q1; }
    arrive(sy + SY_Q); await(sy + SY_Q, 4);
    __builtin_amdgcn_s_setprio(0);
    bf16x8 Q[4][2];
    Q[0][0] = BC8(q0); Q[0][1] = BC8(q1);
#pragma unroll
    for (int i = 1; i < 4; ++i) { LAS const char* p = lds + LQ + ((w + i) & 3) * 2048 + voff; Q[i][0] = FR(p, 0); Q[i][1] = FR(p, 1024); }
    u32x4 kv[3][4] = {{k0, k1, v0, v1}, {}, {}};
    f32x16 O[4] = {}, sa, sb;
    u32x4 p0, p1;
#define GRP(SC, i, P, j) do { float e0 = EX(SC[i]); SBAR(); float e1 = EX(SC[i + 1]); SBAR(); float e2 = EX(SC[i + 2]); SBAR(); float e3 = EX(SC[i + 3]); SBAR(); \
        P[j] = pk_bf16(e0, e1); SBAR(); P[j + 1] = pk_bf16(e2, e3); PIN(P); SBAR(); } while (0)
#define SLOT(SC, SN, cb, i, QK, PV1, LD) do { constexpr int nb_ = (i) < 3 ? (cb) : ((cb) + 1) % 3, pb_ = (i) > 0 ? (cb) : ((cb) + 2) % 3; \
        if (QK) SN = MFMA_BF(BC8(kv[nb_][0]), Q[((i) + 1) & 3][0], zero); SBAR(); GRP(SC, 0, p0, 0); \
        if (PV1) O[((i) + 3) & 3] = MFMA_BF(BC8(kv[pb_][3]), BC8(p1), O[((i) + 3) & 3]); SBAR(); \
        if (LD) { kv[((cb) + 2) % 3][i] = __builtin_amdgcn_raw_buffer_load_b128(rs, voff + (i) * 1024, kvo, 16); SBAR(); } GRP(SC, 4, p0, 2); \
        if (QK) SN = MFMA_BF(BC8(kv[nb_][1]), Q[((i) + 1) & 3][1], SN); SBAR(); GRP(SC, 8, p1, 0); \
        O[i] = MFMA_BF(BC8(kv[cb][2]), BC8(p0), O[i]); SBAR(); GRP(SC, 12, p1, 2); } while (0)
    const unsigned kvb = WS_KV + (unsigned)(head * 64 + w) * 4096;
    unsigned kvo = 0;
    sa = MFMA_BF(BC8(k0), Q[0][0], zero); sa = MFMA_BF(BC8(k1), Q[0][1], sa);
    SLOT(sa, sb, 0, 0, true, false, false); SLOT(sb, sa, 0, 1, true, true, false);
    drain();
    put_flag(ws, FL_KV + (unsigned)((head * 4 + w) * 16 + grp) * FS, tag, lane);
    STAMP(4);
    SLOT(sa, sb, 0, 2, true, true, false); SLOT(sb, sa, 0, 3, false, true, false);
    STAMP(5);
    wait_flags<2>(ws, FL_KV + (unsigned)((head * 4 + w) * 16 + lane) * FS, tag, lane < 16 && lane != grp);
    STAMP(6);
#pragma unroll
    for (int j = 1; j < 3; ++j)
#pragma unroll
        for (int f = 0; f < 4; ++f) kv[j][f] = __builtin_amdgcn_raw_buffer_load_b128(rs, voff + f * 1024, kvb + ((grp + j) & 15) * 16384, 16);
    arrive(sy + SY_G);
    sa = MFMA_BF(BC8(kv[1][0]), Q[0][0], zero); sa = MFMA_BF(BC8(kv[1][1]), Q[0][1], sa);
    STAMP(7);
    int nx = grp + 3;
#define TILE(cb) do { kvo = kvb + (nx & 15) * 16384; ++nx; SBAR(); \
        SLOT(sa, sb, cb, 0, true, true, true); SLOT(sb, sa, cb, 1, true, true, true); SLOT(sa, sb, cb, 2, true, true, true); SLOT(sb, sa, cb, 3, true, true, true); } while (0)
#pragma unroll 1
    for (int it = 0; it < 5; ++it) { TILE(1); TILE(2); TILE(0); }
    O[3] = MFMA_BF(BC8(kv[0][3]), BC8(p1), O[3]);
    STAMP(8);
    f32x16 o = O[0];
    {
#pragma unroll
        for (int i = 1; i < 4; ++i) {
            LAS char* ex = lds + LX + (((w + i) & 3) * 3 + i - 1) * 4096 + voff;
#pragma unroll
            for (int g = 0; g < 4; ++g) *(LAS f32x4*)(ex + g * 1024) = (f32x4){O[i][4 * g], O[i][4 * g + 1], O[i][4 * g + 2], O[i][4 * g + 3]};
        }
        arrive(sy + SY_X); await(sy + SY_X, 4);
        LAS const char* er = lds + LX + w * 3 * 4096 + voff;
#pragma unroll
        for (int i = 0; i < 3; ++i)
#pragma unroll
            for (int g = 0; g < 4; ++g) { const f32x4 r = *(LAS const f32x4*)(er + i * 4096 + g * 1024);
#pragma unroll
                for (int e = 0; e < 4; ++e) o[4 * g + e] += r[e]; }
        f32x2 chk = {0.f, 0.f};
#pragma unroll
        for (int i = 0; i < 8; ++i) chk = pfma((f32x2){o[2 * i], o[2 * i + 1]}, (f32x2){0.f, 0.f}, chk);
        if (__builtin_expect(__any(!(chk[0] + chk[1] == 0.f) || !(o[12] < 1e30f)), 0)) {
            const unsigned hb = WS_KV + (unsigned)head * 64 * 4096 + voff;
            u32x4 qr = __builtin_bit_cast(u32x4, Q[0][1]); if (hi) { qr[2] = 0; qr[3] = 0; }
            float mm = -INFINITY;
            wait_flags<4>(ws, FL_KV + (unsigned)(head * 64 + lane) * FS, tag, true);
#pragma unroll 1
            for (int t = 0; t < 64; ++t) {
                f32x16 s = MFMA_BF(BC8(BLD(hb + t * 4096)), Q[0][0], zero); s = MFMA_BF(BC8(BLD(hb + t * 4096 + 1024)), BC8(qr), s);
#pragma unroll
                for (int i = 0; i < 16; ++i) mm = fmaxf(mm, s[i]);
            }
            qr = put_ref(qr, swapmax(mm), hi);
            o = zero;
#pragma unroll 1
            for (int t = 0; t < 64; ++t) {
                f32x16 s = MFMA_BF(BC8(BLD(hb + t * 4096)), Q[0][0], zero); s = MFMA_BF(BC8(BLD(hb + t * 4096 + 1024)), BC8(qr), s);
#pragma unroll
                for (int i = 0; i < 4; ++i) { p0[i] = pk_bf16(EX(s[2 * i]), EX(s[2 * i + 1])); p1[i] = pk_bf16(EX(s[8 + 2 * i]), EX(s[9 + 2 * i])); }
                o = MFMA_BF(BC8(BLD(hb + t * 4096 + 2048)), BC8(p0), o); o = MFMA_BF(BC8(BLD(hb + t * 4096 + 3072)), BC8(p1), o);
            }
        }
    }
    {
        const auto r = __builtin_amdgcn_permlane32_swap(__float_as_uint(o[12]), __float_as_uint(o[12]), false, false);
        const float rl = __builtin_amdgcn_rcpf(__uint_as_float(r[1]));
        u32x4 o0, o1;
#pragma unroll
        for (int i = 0; i < 4; ++i) { o0[i] = pk_bf16(o[2 * i] * rl, o[2 * i + 1] * rl); o1[i] = pk_bf16(o[8 + 2 * i] * rl, o[9 + 2 * i] * rl); }
        if (hi) { o1[2] = 0; o1[3] = 0; }
        const unsigned fi = (unsigned)((4 * grp + w) * 16 + head);
        BST(o0, WS_O + fi * 2048 + voff); BST(o1, WS_O + fi * 2048 + 1024 + voff);
        STAMP(9);
        drain();
        put_flag(ws, FL_O + fi * FS, tag, lane);
    }
    STAMP(10);
}

DI void role_b(const uint16_t* __restrict__ wo, const uint16_t* __restrict__ u, const uint16_t* __restrict__ lar, const uint16_t* __restrict__ aim,
               const uint16_t* __restrict__ cre, const uint16_t* __restrict__ cim, const uint16_t* __restrict__ ldt, const uint16_t* __restrict__ dskip,
               uint16_t* __restrict__ out, char* __restrict__ ws, LAS char* lds, int w, int lane, u64 t_start) {
    u64 tag;
    const unsigned lds0 = (unsigned)(uintptr_t)lds;
    LAS unsigned* sy = (LAS unsigned*)(lds + LSY);
    const __amdgpu_buffer_rsrc_t rs = __builtin_amdgcn_make_buffer_rsrc(ws, 0, WS_END, 0x00020000);
    const int b = blockIdx.x, xcd = b & 7, bi = b >> 3, r32 = lane & 31, hi = lane >> 5;
    const unsigned voff = lane * 16;
    LAS char* tab = lds + LB + w * TABW;
    if (w == 0) sy[lane] = 0;
    if (xcd < 7) {
        const int cc = w >> 1, hh = w & 1, pair = 32 * xcd + bi, ch = 2 * pair + cc;
        LAS char* cb = lds + LB + cc * CBW;
        const unsigned po = (unsigned)(ch * NS + 128 * hh) / 2 + lane;
        const unsigned pl = ((const unsigned*)lar)[po], pa = ((const unsigned*)aim)[po], pr = ((const unsigned*)cre)[po], pi = ((const unsigned*)cim)[po];
        const unsigned uo = (unsigned)((64 * bi + 16 * w + (lane >> 3)) * D + 64 * xcd + (lane & 7) * 8) * 2;
        glds2(u, uo, uo + 8 * D * 2 - 1024, lds0 + LB + w * TABW + 4096);
        const u64 e0 = __hip_atomic_load((GAS u64*)ws, __ATOMIC_RELAXED, __HIP_MEMORY_SCOPE_AGENT);
        u32x2 gi[2][3];
#pragma unroll
        for (int ps = 0; ps < 2; ++ps)
#pragma unroll
            for (int i = 0; i < 3; ++i) gi[ps][i] = ((const u32x2*)g_idx.v[128 * hh + 64 * ps + lane])[i];
        const unsigned lw = ((const unsigned*)ldt)[pair], dw = ((const unsigned*)dskip)[pair];
        asm volatile("s_waitcnt lgkmcnt(0)\n\ts_barrier" ::: "memory");
        __builtin_amdgcn_s_setprio(PRIO_B);
        const float dt = rbf(expf(cc ? bf_hi(lw) : bf_lo(lw)));
        drain();
        STAMP(1);
        arrive(sy + SY_WO);
        if (w == 0 && lane == 0) __hip_atomic_store((LAS u64*)sy, mix(e0), __ATOMIC_RELAXED, __HIP_MEMORY_SCOPE_WORKGROUP);
        await(sy + SY_WO, 4);
        {
            u32x4 o0, o1; transpose_tile(lds + LB + 4096, TABW, tab, sy + SY_T1, w, lane, o0, o1);
            const unsigned to = WS_UT + (unsigned)((64 * xcd + 16 * w + (lane >> 3)) * S + 64 * bi) * 2 + (lane & 7) * 16;
            __builtin_amdgcn_s_setprio(3); BST(o0, to); BST(o1, to + 8 * S * 2); __builtin_amdgcn_s_setprio(PRIO_B);
        }
        const float cm = wave_max(fmaxf(fmaxf(fabsf(bf_lo(pr)), fabsf(bf_hi(pr))), fmaxf(fabsf(bf_lo(pi)), fabsf(bf_hi(pi)))));
        unsigned ce = __float_as_uint(cm) & 0x7f800000u; ce = ce < 0x01000000u ? 0x01000000u : (ce > 0x7e800000u ? 0x7e800000u : ce);
        const float cscale = __uint_as_float(0x7f000000u - ce), oscale = 2.0f * dt * __uint_as_float(ce);
        LAS unsigned* tw = (LAS unsigned*)(tab + lane * 4);
        LAS const char* trd = tab + r32 * TROW + hi * 16;
        f32x16 dacc = {};
        f32x2 z1, z8, z256, x0;
#define ANCH(vl, va, vr, vi) do { const float a2 = (-rbf(expf(vl)) * dt) * 1.4426950408889634f, rev = ((va) * dt) * 0.15915494309189535f; \
            z1 = anchor(a2, -rev, 1.f); z8 = anchor(a2, rev, 8.f); z256 = anchor(a2, -rev, 256.f); x0 = (f32x2){(vr) * cscale, -(vi) * cscale}; \
            PIN(z1); PIN(z8); PIN(z256); SBAR(); } while (0)
#define TW(row) tw[(row) * (TROW / 4)]
#define CHAIN() do { \
            const f32x2 z1s = rot(z1), zs = rot(z256), z8s = rot(z8); \
            f32x2 y[4], X[8]; y[1] = z8; y[2] = cmul(z8, z8, z8s); X[0] = x0; X[1] = cmul(x0, z1, z1s); f32x2 a = cmul(x0, z256, zs); \
            y[3] = cmul(y[2], z8, z8s); const f32x2 z32 = cmul(y[2], y[2], rot(y[2])), z32s = rot(z32); X[2] = cmul(X[1], z1, z1s); y[0] = z32; \
            f32x2 c4 = cmul(X[2], z256, zs); \
            unsigned c[2][7], d0 = pk_f16(X[0]), d1 = pk_f16(X[1]), d2 = pk_f16(y[1]); \
            c[0][0] = pk_f16(y[0]); c[0][1] = 0x3c00u; c[0][2] = pk_f16(y[2]); c[0][3] = pk_f16(y[3]); c[0][4] = pk_f16(X[2]); c[0][5] = pk_f16(a); c[0][6] = pk_f16(c4); \
            TW(0) = d0; TW(1) = d1; TW(22) = d2; SBAR(); \
            _Pragma("unroll") for (int i = 1; i < 9; ++i) { \
                f32x2 t0, t1, t2, t3, t4, t5, t6; const int j = i - 1; unsigned* pc = c[j & 1]; unsigned* nc = c[i & 1]; \
                if (i < 8) { if (i > 1) t0 = y[0].xx * z32; t1 = y[1].xx * z32; t2 = y[2].xx * z32; t3 = y[3].xx * z32; \
                    if (i < 6) t4 = X[i + 1].xx * z1; t5 = a.xx * z256; \
                    if (i == 3) t6 = X[4].xx * z256; if (i == 4 || i == 5) t6 = c4.xx * z256; if (i == 6) t6 = X[6].xx * z256; } SBAR(); \
                if (j != 1) TW(21 + (j ? 4 * j : 4)) = pc[0]; TW(21 + (j ? 4 * j + 1 : 0)) = pc[1]; TW(23 + 4 * j) = pc[2]; TW(24 + 4 * j) = pc[3]; \
                if (j < 6) TW(j + 2) = pc[4]; TW(j == 0 ? 8 : (j == 1 ? 12 : (j == 2 ? 14 : 13 + j))) = pc[5]; \
                if (j == 0) TW(9) = pc[6]; if (j == 3) TW(10) = pc[6]; if (j == 4) TW(13) = pc[6]; if (j == 5) TW(15) = pc[6]; if (j == 6) TW(11) = pc[6]; SBAR(); \
                if (i < 8) { if (i > 1) y[0] = pfma(y[0].yy, z32s, t0); y[1] = pfma(y[1].yy, z32s, t1); y[2] = pfma(y[2].yy, z32s, t2); y[3] = pfma(y[3].yy, z32s, t3); \
                    if (i < 6) X[i + 2] = pfma(X[i + 1].yy, z1s, t4); a = pfma(a.yy, zs, t5); \
                    if (i == 3) c4 = pfma(X[4].yy, zs, t6); if (i == 4 || i == 5) c4 = pfma(c4.yy, zs, t6); if (i == 6) c4 = pfma(X[6].yy, zs, t6); SBAR(); \
                    if (i > 1) nc[0] = pk_f16(y[0]); nc[1] = pk_f16(y[1]); nc[2] = pk_f16(y[2]); nc[3] = pk_f16(y[3]); \
                    if (i < 6) nc[4] = pk_f16(X[i + 2]); nc[5] = pk_f16(a); if (i >= 3 && i <= 6) nc[6] = pk_f16(c4); \
                    asm volatile("" :: "v"(pc[0]), "v"(pc[1]), "v"(pc[2]), "v"(pc[3]), "v"(pc[4]), "v"(pc[5]), "v"(pc[6])); SBAR(); } \
            } } while (0)
        f16x8 fa[8], fb[8];
#define RDT() do { _Pragma("unroll") for (int kk = 0; kk < 8; ++kk) { fa[kk] = *(LAS const f16x8*)(trd + kk * 32); fb[kk] = *(LAS const f16x8*)(trd + 21 * TROW + kk * 32); } SBAR(); } while (0)
#define MMT() do { _Pragma("unroll") for (int kk = 0; kk < 8; ++kk) dacc = MFMA_F16(fa[kk], fb[kk], dacc); } while (0)
        ANCH(bf_lo(pl), bf_lo(pa), bf_lo(pr), bf_lo(pi));
        tag = get_tag(sy); await(sy + SY_T1, 4);
        STAMP(13);
        CHAIN(); RDT();
        drain(); __builtin_amdgcn_s_setprio(3); put_flag(ws, FL_UT + (unsigned)(xcd * 128 + w * 32 + bi) * FS, tag, lane); __builtin_amdgcn_s_setprio(PRIO_B);
        STAMP(14);
        ANCH(bf_hi(pl), bf_hi(pa), bf_hi(pr), bf_hi(pi));
        MMT(); SBAR();
        CHAIN(); RDT();
        const unsigned utf = FL_UT + (unsigned)(xcd * 128 + (bi >> 3) * 32 + r32) * FS;
        const u64 utv = __hip_atomic_load((GAS u64*)(ws + utf), __ATOMIC_RELAXED, __HIP_MEMORY_SCOPE_AGENT); SBAR();
        MMT();
        STAMP(2);
        {
            LAS float* pt = (LAS float*)tab;
            *(LAS f32x4*)(pt + 8 * r32 + 4 * hi) = (f32x4){dacc[0] * oscale, dacc[1] * oscale, dacc[2] * oscale, dacc[3] * oscale};
            *(LAS f32x2*)(pt + (hi ? 384 + 2 * r32 : 256 + 4 * r32)) = (f32x2){dacc[4] * oscale, dacc[5] * oscale};
            *(LAS f32x2*)(pt + (hi ? 448 + 2 * r32 : 258 + 4 * r32)) = (f32x2){dacc[6] * oscale, dacc[7] * oscale};
            pt[hi ? 640 + r32 : 512 + r32] = dacc[8] * oscale;
            pt[hi ? 672 + r32 : 544 + r32] = dacc[9] * oscale;
            pt[hi ? 672 + r32 : 576 + r32] = dacc[10] * oscale;
            pt[hi ? 672 + r32 : 608 + r32] = dacc[11] * oscale;
        }
        LAS unsigned* ps_ = sy + SY_P + 5 * cc;
        arrive(ps_ + 0);
        LAS char* ui = cb + UOF;
        LAS char* kt = cb + KOF;
        await(ps_ + 0, 2);
#pragma unroll
        for (int ps = 0; ps < 2; ++ps) {
            const int v = 81 * hh + 64 * ps + lane;
            if (ps == 0 || lane < 17) {
                const f32x4 p0 = *(LAS const f32x4*)(cb + v * 16), p1 = *(LAS const f32x4*)(cb + TABW + v * 16);
                u32x2 r = {pk_bf16(p0[0] + p1[0], p0[1] + p1[1]), pk_bf16(p0[2] + p1[2], p0[3] + p1[3])};
                if (v == 161) r = (u32x2){0u, 0u};
                *(LAS u32x2*)(kt + v * 8) = r;
            }
        }
        arrive(ps_ + 1);
        if (__any(utv != tag)) wait_flags<4>(ws, utf, tag, !hi);
        CFENCE();
        const unsigned ub = WS_UT + (unsigned)(ch * S + 1024 * hh) * 2 + voff;
        const u32x4 u0 = BLD(ub), u1 = BLD(ub + 1024); SBAR();
        await(ps_ + 1, 2);
        STAMP(3);
        __builtin_amdgcn_s_setprio(PRIO_B2);
        const u32x4 z4 = {};
        {
            unsigned W[2][6];
#pragma unroll
            for (int ps = 0; ps < 2; ++ps)
#pragma unroll
                for (int i = 0; i < 6; ++i) { const unsigned gv = gi[ps][i >> 1][i & 1];
                    W[ps][i] = *(LAS const uint16_t*)(kt + (gv & 0xffffu));
                    if (i < 5) W[ps][i] |= (unsigned)*(LAS const uint16_t*)(kt + (gv >> 16)) << 16; }
            *(LAS u32x4*)(ui + (64 * hh + lane) * 16) = z4;
            if (lane < 16) *(LAS u32x4*)(ui + (128 + 16 * hh + lane) * 16) = z4;
            if (lane < 8) *(LAS u32x4*)(cb + (2 * hh + (lane >> 2)) * CPY + (256 + (lane & 3)) * 16) = z4;
#pragma unroll
            for (int ps = 0; ps < 2; ++ps) {
                unsigned V[5];
#pragma unroll
                for (int i = 0; i < 5; ++i) V[i] = __builtin_amdgcn_alignbit(W[ps][i + 1], W[ps][i], 16);
                LAS char* cp = cb + (128 * hh + 64 * ps + lane) * 16;
                *(LAS u32x4*)(cp) = (u32x4){W[ps][0], W[ps][1], W[ps][2], W[ps][3]};
                *(LAS u32x4*)(cp + CPY) = (u32x4){V[0], V[1], V[2], V[3]};
                *(LAS u32x4*)(cp + 2 * CPY) = (u32x4){W[ps][1], W[ps][2], W[ps][3], W[ps][4]};
                *(LAS u32x4*)(cp + 3 * CPY) = (u32x4){V[1], V[2], V[3], V[4]};
            }
        }
        *(LAS u32x4*)(ui + (32 + 32 * hh + (lane >> 2)) * 80 + (lane & 3) * 16) = u0;
        *(LAS u32x4*)(ui + (48 + 32 * hh + (lane >> 2)) * 80 + (lane & 3) * 16) = u1;
        arrive(ps_ + 2); await(ps_ + 2, 2);
        STAMP(4);
        f32x16 a0 = {}, a1 = {};
        {
            const int sb = 7 - (r32 & 7);
            LAS const char* pa = cb + (sb & 3) * CPY + (255 - (r32 >> 3) + hi) * 16 + (sb >> 2) * 8 - 64 * (16 * hh + 3) - 2048;
            LAS const char* pa2 = pa + 8; asm volatile("" : "+v"(pa2));
            LAS const char* pb = ui + (32 + r32 - 16 * hh - 3) * 80 + 16 * hi;
            s16x4 ta[2][2], tb[2][2]; bf16x8 b0[2], b1[2];
#define T4(q, off) (*(LAS const s16x4*)((q) + (off)))
#define CSTEP(i, j, RD) do { const int o_ = 64 * (3 - ((j) >> 1)) + 32 * ((j) & 1), u_ = 80 * (3 - ((j) >> 1)) + 32 * ((j) & 1); \
                a1 = MFMA_BF(cat8(ta[i][0], ta[i][1]), b1[i], a1); SBAR(); if (RD) { ta[1 - i][0] = T4(pa, o_ + 2048); ta[1 - i][1] = T4(pa2, o_ + 2048); } SBAR(); \
                a0 = MFMA_BF(cat8(ta[i][0], ta[i][1]), b0[i], a0); SBAR(); if (RD) { b1[1 - i] = FR(pb, u_ + 2560); b0[1 - i] = FR(pb, u_); } SBAR(); \
                a1 = MFMA_BF(cat8(tb[i][0], tb[i][1]), b0[i], a1); SBAR(); if (RD) { tb[1 - i][0] = T4(pa, o_); tb[1 - i][1] = T4(pa2, o_); } SBAR(); } while (0)
            ta[0][0] = T4(pa, 192 + 2048); ta[0][1] = T4(pa2, 192 + 2048); b1[0] = FR(pb, 240 + 2560); b0[0] = FR(pb, 240); tb[0][0] = T4(pa, 192); tb[0][1] = T4(pa2, 192);
#pragma unroll 1
            for (int m4 = 0; m4 < 3; ++m4) {
                CSTEP(0, 1, 1); CSTEP(1, 2, 1); CSTEP(0, 3, 1); CSTEP(1, 4, 1); CSTEP(0, 5, 1); CSTEP(1, 6, 1); CSTEP(0, 7, 1);
                pa -= 256; pa2 -= 256; pb -= 320;
                CSTEP(1, 0, 1);
            }
            CSTEP(0, 1, 1); CSTEP(1, 2, 1); CSTEP(0, 3, 1); CSTEP(1, 4, 1); CSTEP(0, 5, 1); CSTEP(1, 6, 1); CSTEP(0, 7, 1);
            arrive(ps_ + 3);
            CSTEP(1, 0, 0);
        }
        STAMP(5);
        {
            u32x2 uu[4];
#pragma unroll
            for (int g = 0; g < 4; ++g) uu[g] = *(LAS const u32x2*)(ui + (32 + 32 * hh + r32) * 80 + 16 * g + 8 * hi);
            LAS char* ex = cb + r32 * 144 + hi * 16;
            const f32x16 snd = hh ? a0 : a1; const f32x16 y = hh ? a1 : a0;
            await(ps_ + 3, 2);
#pragma unroll
            for (int g = 0; g < 4; ++g) *(LAS f32x4*)(ex + hh * 4608 + g * 32) = (f32x4){snd[4 * g], snd[4 * g + 1], snd[4 * g + 2], snd[4 * g + 3]};
            arrive(ps_ + 4);
            const float dsk = cc ? bf_hi(dw) : bf_lo(dw);
            unsigned sk[8];
#pragma unroll
            for (int g = 0; g < 4; ++g)
#pragma unroll
                for (int e = 0; e < 2; ++e) sk[2 * g + e] = pk_bf16(bf_lo(uu[g][e]) * dsk, bf_hi(uu[g][e]) * dsk);
            await(ps_ + 4, 2);
            LAS char* sc = cb + 9216 + hh * 2048;
#pragma unroll
            for (int g = 0; g < 4; ++g) {
                const f32x4 p = *(LAS const f32x4*)(ex + (1 - hh) * 4608 + g * 32);
                u32x2 r;
#pragma unroll
                for (int e = 0; e < 2; ++e) {
                    const unsigned yb = pk_bf16(y[4 * g + 2 * e] + p[2 * e], y[4 * g + 2 * e + 1] + p[2 * e + 1]);
                    r[e] = pk_bf16(bf_lo(yb) + bf_lo(sk[2 * g + e]), bf_hi(yb) + bf_hi(sk[2 * g + e]));
                }
                *(LAS u32x2*)(sc + 64 * r32 + 16 * g + 8 * hi) = r;
            }
            const u32x4 o0 = *(LAS const u32x4*)(sc + voff), o1 = *(LAS const u32x4*)(sc + 1024 + voff);
            const unsigned bo = WS_BT + (unsigned)(ch * S + 1024 * hh) * 2 + voff;
            BST(o0, bo); BST(o1, bo + 1024);
        }
        arrive(sy + SY_BG);
        STAMP(6);
        drain();
        put_flag(ws, FL_BT + (unsigned)(xcd * 128 + bi * 4 + w) * FS, tag, lane);
    } else {
        {
            const unsigned nb = (bi & 1) ? 12 * 2 * D : 16 * 2 * D;
            unsigned v4[4];
#pragma unroll
            for (int i = 0; i < 4; ++i) { const unsigned o = (unsigned)(w * 4096 + i * 1024) + voff; v4[i] = (o < nb ? o : 0u) + 4096 - i * 1024; }
            glds4((const char*)(wo + (DH * (bi >> 1) + 16 * (bi & 1)) * D) - 4096, v4[0], v4[1], v4[2], v4[3], lds0 + LB + w * 4096);
        }
        const u64 e0 = __hip_atomic_load((GAS u64*)ws, __ATOMIC_RELAXED, __HIP_MEMORY_SCOPE_AGENT);
        asm volatile("s_waitcnt lgkmcnt(0)\n\ts_barrier" ::: "memory");
        drain();
        if (w == 0 && lane == 0) __hip_atomic_store((LAS u64*)sy, mix(e0), __ATOMIC_RELAXED, __HIP_MEMORY_SCOPE_WORKGROUP);
        arrive(sy + SY_WO); await(sy + SY_WO, 4);
        LAS const char* p = lds + LB + (4 * hi + ((lane & 15) >> 2)) * (2 * D) + ((lane >> 4) & 1) * 32 + (lane & 3) * 8;
#pragma unroll
        for (int i = 0; i < 4; ++i) {
            const int ct = w + 4 * i;
            if (ct < 14) {
                u32x4 o = __builtin_bit_cast(u32x4, cat8(trread(p + ct * 64), trread(p + 8 * 2 * D + ct * 64)));
                if (hi && (bi & 1)) { o[2] = 0; o[3] = 0; }
                BST(o, WS_WO + (unsigned)(bi * 14 + ct) * 1024 + voff);
            }
        }
        tag = get_tag(sy);
        drain();
        put_flag(ws, FL_WO + (unsigned)(w * 32 + bi) * FS, tag, lane);
        arrive(sy + SY_BG);
    }
    const int qt = 8 * xcd + (bi >> 2), ct = 4 * (bi & 3) + w;
    bf16x8 wf[32];
    await_slow(sy + SY_G, 4);
    wait_flags<8>(ws, FL_WO + (unsigned)(w * 32 + r32) * FS, tag, !hi);
    if (ct < 14) {
#pragma unroll
        for (int i = 0; i < 32; ++i) { wf[i] = BC8(__builtin_amdgcn_raw_buffer_load_b128(rs, voff, WS_WO + (unsigned)(((8 * w + i) & 31) * 14 + ct) * 1024, 16)); __builtin_amdgcn_s_sleep(WOSL); SBAR(); }
    }
    STAMP(7);
    const __amdgpu_buffer_rsrc_t ro = __builtin_amdgcn_make_buffer_rsrc(out, 0, S * 2 * D * 2, 0x00020000);
    if (xcd < 7) {
        wait_flags<32>(ws, FL_BT + (unsigned)(xcd * 128 + w * 32 + r32) * FS, tag, !hi);
        await(sy + SY_BG, 4);
        const unsigned bo = WS_BT + (unsigned)((64 * xcd + 16 * w + (lane >> 3)) * S + 64 * bi + (lane & 7) * 8) * 2;
        glds2c(ws, bo, bo + 8 * S * 2 - 1024, lds0 + LB + 32768 + w * 2048);
        drain();
        arrive(sy + SY_T2); await(sy + SY_T2, 4);
        u32x4 o0, o1; transpose_tile(lds + LB + 32768, 2048, lds + LB + 40960 + w * 2304, nullptr, w, lane, o0, o1);
        const unsigned oo = (unsigned)((64 * bi + 16 * w + (lane >> 3)) * 2 * D + D + 64 * xcd) * 2 + (lane & 7) * 16;
        __builtin_amdgcn_raw_buffer_store_b128(o0, ro, oo, 0, 16); __builtin_amdgcn_raw_buffer_store_b128(o1, ro, oo + 8 * 2 * D * 2, 0, 16);
    } else await(sy + SY_BG, 4);
    STAMP(8);
    await_slow(sy + SY_X, 4);
    wait_flags<2>(ws, FL_O + (unsigned)(qt * 16 + 4 * w + lane) * FS, tag, lane < 4);
    STAMP(9);
    u32x4 of[8];
#pragma unroll
    for (int i = 0; i < 8; ++i) of[i] = BLD(WS_O + (unsigned)(qt * 32 + 8 * w + i) * 1024 + voff);
#pragma unroll
    for (int i = 0; i < 8; ++i) *(LAS u32x4*)(lds + LB + (8 * w + i) * 1024 + voff) = of[i];
    arrive(sy + SY_B2);
    STAMP(10);
    f32x16 acc = {};
    if (ct < 14) {
#pragma unroll
        for (int i = 0; i < 8; ++i) acc = MFMA_BF(wf[i], BC8(of[i]), acc);
    }
    SBAR();
    await(sy + SY_B2, 4);
    STAMP(11);
    if (ct < 14) {
        LAS const char* po[3];
#pragma unroll
        for (int j = 0; j < 3; ++j) po[j] = lds + LB + ((w + 1 + j) & 3) * 8192 + voff;
        bf16x8 og[4];
#pragma unroll
        for (int i = 0; i < 3; ++i) og[i] = FR(po[0], i * 1024);
#pragma unroll
        for (int i = 0; i < 24; ++i) { if (i < 21) og[(i + 3) & 3] = FR(po[(i + 3) >> 3], ((i + 3) & 7) * 1024); SBAR(); acc = MFMA_BF(wf[8 + i], og[i & 3], acc); SBAR(); }
        LAS char* sc = lds + LB + 40960 + w * 2304;
#pragma unroll
        for (int g = 0; g < 4; ++g) *(LAS u32x2*)(sc + r32 * 72 + 16 * g + 8 * hi) = (u32x2){pk_bf16(acc[4 * g], acc[4 * g + 1]), pk_bf16(acc[4 * g + 2], acc[4 * g + 3])};
        LAS const char* sr = sc + (lane >> 2) * 72 + (lane & 3) * 16;
        const u32x2 a0 = *(LAS const u32x2*)sr, a1 = *(LAS const u32x2*)(sr + 8), b0 = *(LAS const u32x2*)(sr + 16 * 72), b1 = *(LAS const u32x2*)(sr + 16 * 72 + 8);
        const unsigned oo = (unsigned)((32 * qt + (lane >> 2)) * 2 * D + 32 * ct) * 2 + (lane & 3) * 16;
        __builtin_amdgcn_raw_buffer_store_b128((u32x4){a0[0], a0[1], a1[0], a1[1]}, ro, oo, 0, 16);
        __builtin_amdgcn_raw_buffer_store_b128((u32x4){b0[0], b0[1], b1[0], b1[1]}, ro, oo + 16 * 2 * D * 2, 0, 16);
    }
    if (b == 0 && w == 0) put_flag(ws, 0, tag, lane);
    STAMP(12);
}

__global__ __launch_bounds__(512, 2) void fused(const uint16_t* __restrict__ x, const uint16_t* __restrict__ wqkv, const uint16_t* __restrict__ wo,
                                               const uint16_t* __restrict__ u, const uint16_t* __restrict__ lar, const uint16_t* __restrict__ aim,
                                               const uint16_t* __restrict__ cre, const uint16_t* __restrict__ cim, const uint16_t* __restrict__ ldt,
                                               const uint16_t* __restrict__ dskip, uint16_t* __restrict__ out, char* __restrict__ ws) {
    extern __shared__ __attribute__((aligned(16))) char smem[];
    LAS char* lds = (LAS char*)smem;
    const int lane = threadIdx.x & 63, w = __builtin_amdgcn_readfirstlane(threadIdx.x >> 6);
    const u64 t_start = __builtin_amdgcn_s_memrealtime();
    if (w < 4) role_a(x, wqkv, ws, lds, w, lane, t_start);
    else role_b(wo, u, lar, aim, cre, cim, ldt, dskip, out, ws, lds, w - 4, lane, t_start);
}
}

extern "C" void kernel_launch(void* const* d_in, const int* in_sizes, int n_in, void* d_out, int out_size, void* d_ws, size_t ws_size, hipStream_t stream) {
    using namespace k;
    (void)hipFuncSetAttribute((const void*)fused, hipFuncAttributeMaxDynamicSharedMemorySize, LDS_BYTES);
    const uint16_t* const* in = (const uint16_t* const*)d_in;
    fused<<<256, 512, LDS_BYTES, stream>>>(in[0], in[1], in[2], in[3], in[4], in[5], in[6], in[7], in[8], in[9], (uint16_t*)d_out, (char*)d_ws);
}
```

```cpp
#include <hip/hip_runtime.h>
#include <hip/hip_bf16.h>
#include <stdint.h>

typedef short bf16x8 __attribute__((ext_vector_type(8)));
typedef _Float16 f16x8 __attribute__((ext_vector_type(8)));
typedef _Float16 f16x2 __attribute__((ext_vector_type(2)));
typedef __bf16 bf16x2 __attribute__((ext_vector_type(2)));
typedef short s16x4 __attribute__((ext_vector_type(4)));
typedef float f32x16 __attribute__((ext_vector_type(16)));
typedef float f32x4 __attribute__((ext_vector_type(4)));
typedef float f32x2 __attribute__((ext_vector_type(2)));
typedef unsigned u32x4 __attribute__((ext_vector_type(4)));
typedef unsigned u32x2 __attribute__((ext_vector_type(2)));
typedef unsigned long long u64;
#define LAS __attribute__((address_space(3)))
#define GAS __attribute__((address_space(1)))
#define DI __device__ __forceinline__
#define MFMA_BF(a, b, c) __builtin_amdgcn_mfma_f32_32x32x16_bf16((a), (b), (c), 0, 0, 0)
#define MFMA_F16(a, b, c) __builtin_amdgcn_mfma_f32_32x32x16_f16((a), (b), (c), 0, 0, 0)
#define SBAR() __builtin_amdgcn_sched_barrier(0)
#define CFENCE() asm volatile("" ::: "memory")
#define PIN(x) asm volatile("" : "+v"(x))
#define BC8(x) __builtin_bit_cast(bf16x8, (x))
#define EX(x) __builtin_amdgcn_exp2f(x)
#define PRIO_A1 0
#define PRIO_B 0
#define PRIO_B2 1
#define ROT 1
#define WOSL 2

namespace k {
#define STAMP(i)
constexpr int S = 2048, D = 448, DH = 28, NS = 256;
constexpr unsigned FS = 128;
constexpr unsigned FL_KV = FS, FL_O = FL_KV + 1024 * FS, FL_UT = FL_O + 1024 * FS, FL_BT = FL_UT + 896 * FS, FL_WO = FL_BT + 896 * FS;
constexpr unsigned WS_KV = 512u << 10;
constexpr unsigned WS_O = WS_KV + (4u << 20);
constexpr unsigned WS_WO = WS_O + (2u << 20);
constexpr unsigned WS_UT = WS_WO + (448u << 10);
constexpr unsigned WS_BT = WS_UT + D * S * 2;
constexpr unsigned WS_END = WS_BT + D * S * 2;
constexpr int WSL = 12288, LXR = 4 * WSL, XSL = 16384, LB = LXR + 3 * XSL;
constexpr int LQ = 0, LX = 8192;
constexpr int TROW = 272, TABW = 53 * TROW, CBW = 2 * TABW, CPY = 4160, UOF = 4 * CPY, KOF = CBW - 1312;
constexpr int LSY = LB + 2 * CBW, LDS_BYTES = LSY + 256;
static_assert(LDS_BYTES <= 163840 && (LSY & 15) == 0 && UOF + 7680 <= KOF, "lds");
enum { SY_TAG = 0, SY_F = 2, SY_P = 10, SY_WO = 20, SY_T1 = 21, SY_BG = 22, SY_B2 = 23, SY_Q = 24, SY_X = 25, SY_G = 26, SY_T2 = 27 };
constexpr int SPIN = 1 << 18;

struct IdxTab { uint16_t v[256][12]; };
constexpr IdxTab make_idx() {
    IdxTab t = {};
    for (int y = 0; y < 256; ++y) for (int e = 0; e < 12; ++e) {
        const int j = 2047 - 8 * y - e; int ix = 644;
        if (j >= 0) { const int sh = j >= 1024 ? 3 : (j >= 512 ? 2 : (j >= 256 ? 1 : 0));
            ix = sh ? 128 * sh + ((j + (1 << (sh - 1)) - 1 + ((j >> sh) & 1)) >> sh) : j; }
        t.v[y][e] = (uint16_t)(2 * ix);
    }
    return t;
}
__device__ const IdxTab g_idx __attribute__((aligned(16))) = make_idx();

DI unsigned pk_bf16(float a, float b) { f32x2 v = {a, b}; return __builtin_bit_cast(unsigned, __builtin_convertvector(v, bf16x2)); }
DI unsigned pk_f16(f32x2 v) { return __builtin_bit_cast(unsigned, __builtin_convertvector(v, f16x2)); }
DI float bf_lo(unsigned w) { return __uint_as_float(w << 16); }
DI float bf_hi(unsigned w) { return __uint_as_float(w & 0xffff0000u); }
DI float rbf(float x) { return bf_lo(pk_bf16(x, x)); }
#define GL0 "s_mov_b32 %0, m0\n\ts_mov_b32 m0, %2\n\ts_nop 0\n\t"
#define GL1(V, O, C) "global_load_lds_dwordx4 " V ", %1 offset:" #O C "\n\t"
#define GLE "s_mov_b32 m0, %0"
DI void glds4(const void* sb, unsigned v0, unsigned v1, unsigned v2, unsigned v3, unsigned lb) { unsigned sv;
    asm volatile(GL0 GL1("%3", 0, "") GL1("%4", 1024, "") GL1("%5", 2048, "") GL1("%6", 3072, "") GLE : "=&s"(sv) : "s"(sb), "s"(lb), "v"(v0), "v"(v1), "v"(v2), "v"(v3) : "memory"); }
DI void glds3(const void* sb, unsigned v0, unsigned v1, unsigned v2, unsigned lb) { unsigned sv;
    asm volatile(GL0 GL1("%3", 0, "") GL1("%4", 1024, "") GL1("%5", 2048, "") GLE : "=&s"(sv) : "s"(sb), "s"(lb), "v"(v0), "v"(v1), "v"(v2) : "memory"); }
DI void glds2(const void* sb, unsigned v0, unsigned v1, unsigned lb) { unsigned sv;
    asm volatile(GL0 GL1("%3", 0, "") GL1("%4", 1024, "") GLE : "=&s"(sv) : "s"(sb), "s"(lb), "v"(v0), "v"(v1) : "memory"); }
DI void glds2c(const void* sb, unsigned v0, unsigned v1, unsigned lb) { unsigned sv;
    asm volatile(GL0 GL1("%3", 0, " sc1") GL1("%4", 1024, " sc1") GLE : "=&s"(sv) : "s"(sb), "s"(lb), "v"(v0), "v"(v1) : "memory"); }
DI float wave_max(float v) {
#define DPPMAX(ctrl, rm) v = fmaxf(v, __uint_as_float(__builtin_amdgcn_update_dpp(0, __float_as_uint(v), ctrl, rm, 0xf, true)))
    DPPMAX(0x111, 0xf); DPPMAX(0x112, 0xf); DPPMAX(0x114, 0xf); DPPMAX(0x118, 0xf); DPPMAX(0x142, 0xa); DPPMAX(0x143, 0xc);
    return __uint_as_float(__builtin_amdgcn_readlane(__float_as_uint(v), 63));
}
DI s16x4 trread(LAS const char* p) { return __builtin_bit_cast(s16x4, __builtin_amdgcn_ds_read_tr16_b64_v4i16((LAS s16x4*)p)); }
DI bf16x8 cat8(s16x4 a, s16x4 b) { return __builtin_shufflevector(a, b, 0, 1, 2, 3, 4, 5, 6, 7); }
DI f32x2 pfma(f32x2 a, f32x2 b, f32x2 c) { return __builtin_elementwise_fma(a, b, c); }
DI f32x2 cmul(f32x2 a, f32x2 b, f32x2 bs) { return pfma(a.yy, bs, a.xx * b); }
DI f32x2 rot(f32x2 b) { return (f32x2){-b.y, b.x}; }
DI f32x2 anchor(float a2, float rev, float s) {
    const float mag = EX(a2 * s);
    float r = rev * s; r -= __builtin_rintf(r);
    return (f32x2){mag * __builtin_amdgcn_cosf(r), mag * __builtin_amdgcn_sinf(r)};
}
DI void arrive(LAS unsigned* c) { asm volatile("s_mov_b64 exec, 1\n\tds_add_u32 %0, %1\n\ts_mov_b64 exec, -1" :: "v"((unsigned)(uintptr_t)c), "v"(1u) : "memory"); }
DI void await(LAS unsigned* c, unsigned n) {
    unsigned t, i = SPIN;
    asm volatile("1:\n\tds_read_b32 %0, %2\n\ts_waitcnt lgkmcnt(0)\n\tv_cmp_gt_u32 vcc, %3, %0\n\ts_cbranch_vccz 2f\n\ts_sleep 1\n\ts_sub_u32 %1, %1, 1\n\ts_cbranch_scc0 1b\n2:"
                 : "=&v"(t), "+s"(i) : "v"((unsigned)(uintptr_t)c), "s"(n) : "vcc", "scc", "memory");
}
DI void await_slow(LAS unsigned* c, unsigned n) {
    unsigned t, i = SPIN;
    asm volatile("1:\n\tds_read_b32 %0, %2\n\ts_waitcnt lgkmcnt(0)\n\tv_cmp_gt_u32 vcc, %3, %0\n\ts_cbranch_vccz 2f\n\ts_sleep 3\n\ts_sub_u32 %1, %1, 1\n\ts_cbranch_scc0 1b\n2:"
                 : "=&v"(t), "+s"(i) : "v"((unsigned)(uintptr_t)c), "s"(n) : "vcc", "scc", "memory");
}
DI u64 get_tag(LAS unsigned* sy) {
    await(sy, 1);
    const u64 v = __hip_atomic_load((LAS u64*)sy, __ATOMIC_RELAXED, __HIP_MEMORY_SCOPE_WORKGROUP);
    return ((u64)(unsigned)__builtin_amdgcn_readfirstlane((unsigned)(v >> 32)) << 32) | (unsigned)__builtin_amdgcn_readfirstlane((unsigned)v);
}
DI void drain() { asm volatile("s_waitcnt vmcnt(0)" ::: "memory"); }
DI void put_flag(char* ws, unsigned off, u64 tag, int lane) { if (lane == 0) __hip_atomic_store((GAS u64*)(ws + off), tag, __ATOMIC_RELAXED, __HIP_MEMORY_SCOPE_AGENT); }
template <int SLP> DI void wait_flags(char* ws, unsigned off, u64 tag, bool pend) {
    GAS u64* p = (GAS u64*)(ws + off);
    CFENCE();
#pragma unroll 1
    for (int i = 0; i < SPIN; ++i) {
        if (pend) pend = __hip_atomic_load(p, __ATOMIC_RELAXED, __HIP_MEMORY_SCOPE_AGENT) != tag;
        if (!__any(pend)) break;
        __builtin_amdgcn_s_sleep(SLP);
    }
    CFENCE();
}
#define BST(v, off) __builtin_amdgcn_raw_buffer_store_b128((v), rs, (off), 0, 16)
#define BLD(off) __builtin_amdgcn_raw_buffer_load_b128(rs, (off), 0, 16)
#define FR(p, off) (*(LAS const bf16x8*)((p) + (off)))
DI float swapmax(float m) { const auto r = __builtin_amdgcn_permlane32_swap(__float_as_uint(m), __float_as_uint(m), false, false); return fmaxf(__uint_as_float(r[0]), __uint_as_float(r[1])); }
DI u32x4 put_ref(u32x4 q1, float m, int hi) {
    const float m1 = rbf(m), m2 = rbf(m - m1), m3 = m - m1 - m2;
    if (hi) { q1[2] = pk_bf16(-m1, -m2); q1[3] = pk_bf16(-m3, 0.f); }
    return q1;
}
DI u64 mix(u64 e) { u64 z = e + 0x9E3779B97F4A7C15ull; z = (z ^ (z >> 30)) * 0xBF58476D1CE4E5B9ull; z = (z ^ (z >> 27)) * 0x94D049BB133111EBull; return (z ^ (z >> 31)) | 1ull; }
DI void transpose_tile(LAS const char* stage, int pst, LAS char* scratch, LAS unsigned* done, int w, int lane, u32x4& o0, u32x4& o1) {
    LAS const char* p = stage + (lane >> 4) * pst + ((lane & 15) >> 2) * 128 + w * 32 + (lane & 3) * 8;
    const bf16x8 t0 = cat8(trread(p), trread(p + 512)), t1 = cat8(trread(p + 1024), trread(p + 1536));
    if (done) arrive(done);
    LAS char* sc = scratch + (lane & 15) * 144 + (lane >> 4) * 32;
    *(LAS bf16x8*)sc = t0; *(LAS bf16x8*)(sc + 16) = t1;
    LAS const char* sr = scratch + (lane >> 3) * 144 + (lane & 7) * 16;
    o0 = *(LAS const u32x4*)sr; o1 = *(LAS const u32x4*)(sr + 8 * 144);
}

DI void role_a(const uint16_t* __restrict__ x, const uint16_t* __restrict__ wqkv, char* __restrict__ ws, LAS char* lds, int w, int lane) {
    const int head = 4 * (blockIdx.x & 3) + ((blockIdx.x >> 3) & 3), grp = 8 * ((blockIdx.x >> 2) & 1) + (blockIdx.x >> 5);
    const unsigned lds0 = (unsigned)(uintptr_t)lds;
    const int r32 = lane & 31, hi = lane >> 5;
    const int wsh = (head & 7) == 7 ? 4 : 0;
    const unsigned wv = (unsigned)((lane >> 2) * 3 * D + (lane & 3) * 8) * 2;
    const char* wb = (const char*)(wqkv + DH * head - wsh + (16 * w) * 3 * D) - 2048;
    const unsigned xv0 = (unsigned)((lane >> 3) * D + (((lane & 7) ^ (lane >> 4)) * 8)) * 2, xv1 = xv0 ^ 64;
    const char* xb = (const char*)(x + (128 * grp + 32 * w) * D);
    const int rho = ROT ? ((blockIdx.x >> 3) + ((blockIdx.x >> 1) & 3)) % 7 : 0;
#define SP(st) ((st) + rho >= 7 ? (st) + rho - 7 : (st) + rho)
#define LOADW(st) glds3(wb + SP(st) * (64 * 3 * D * 2), wv + 2048, wv + 2 * D + 1024, wv + 4 * D, lds0 + ((st) & 3) * WSL + w * 3072)
#define LOADX(st) glds4(xb + SP(st) * 128, xv0, xv1 + 16 * D - 1024, xv0 + 32 * D - 2048, xv1 + 48 * D - 3072, lds0 + LXR + ((st) % 3) * XSL + w * 4096)
    LOADW(0); LOADX(0);
    STAMP(1);
    LOADW(1); LOADX(1); LOADW(2); LOADX(2);
    __builtin_amdgcn_s_setprio(PRIO_A1);
    LAS unsigned* sy = (LAS unsigned*)(lds + LSY);
    const __amdgpu_buffer_rsrc_t rs = __builtin_amdgcn_make_buffer_rsrc(ws, 0, WS_END, 0x00020000);
    const int xs = (r32 >> 1) & 7;
    LAS const char* xp[4];
#pragma unroll
    for (int ks = 0; ks < 4; ++ks) xp[ks] = lds + LXR + w * 4096 + r32 * 128 + (((2 * ks + hi) ^ xs) * 16);
    LAS const char* wp = lds + (8 * hi + ((lane & 15) >> 2)) * 64 + ((lane >> 4) & 1) * 32 + (lane & 3) * 8 + wsh * 2;
    f32x16 aq = {}, ak = {}, av = {};
    bf16x8 xf[2][4]; s16x4 fr[4][6];
#define RX(st, ks) xf[(st) & 1][ks] = FR(xp[ks], ((st) % 3) * XSL)
#define RW(st, ks, j) fr[ks][j] = trread(wp + ((st) & 3) * WSL + (ks) * 3072 + ((((j) >> 1) + 1) % 3) * 1024 + ((j) & 1) * 256)
#define MK(st, ks) ak = MFMA_BF(cat8(fr[ks][0], fr[ks][1]), xf[(st) & 1][ks], ak); SBAR()
#define MV(st, ks) av = MFMA_BF(xf[(st) & 1][ks], cat8(fr[ks][2], fr[ks][3]), av); SBAR()
#define MQ(st, ks) aq = MFMA_BF(cat8(fr[ks][4], fr[ks][5]), xf[(st) & 1][ks], aq); SBAR()
#define QSTEP(st, ks) MK(st, ks); RW(st, ks + 1, 0); RW(st, ks + 1, 1); SBAR(); MV(st, ks); RW(st, ks + 1, 2); RW(st, ks + 1, 3); SBAR(); MQ(st, ks); RW(st, ks + 1, 4); RW(st, ks + 1, 5); SBAR()
    u64 tag = 0;
    asm volatile("s_waitcnt vmcnt(14)" ::: "memory");
    arrive(sy + SY_F); await(sy + SY_F, 4);
    STAMP(16);
    RX(0, 0); RW(0, 0, 0); RW(0, 0, 1); RW(0, 0, 2); RW(0, 0, 3); RW(0, 0, 4); RW(0, 0, 5); RX(0, 1); RX(0, 2); RX(0, 3); SBAR();
#pragma unroll
    for (int st = 0; st < 7; ++st) {
        QSTEP(st, 0); if (st < 4) LOADW(st + 3);
        QSTEP(st, 1); if (st < 4) LOADX(st + 3);
        QSTEP(st, 2);
        if (st < 4) asm volatile("s_waitcnt vmcnt(14)" ::: "memory"); else if (st == 4) asm volatile("s_waitcnt vmcnt(7)" ::: "memory"); else if (st == 5) drain();
        arrive(sy + SY_F + 1 + st); SBAR();
        MK(st, 3); await(sy + SY_F + 1 + st, 4);
        if (st < 6) { RX(st + 1, 0); RW(st + 1, 0, 0); RW(st + 1, 0, 1); RW(st + 1, 0, 2); RW(st + 1, 0, 3); } SBAR();
        MV(st, 3); if (st < 6) { RW(st + 1, 0, 4); RW(st + 1, 0, 5); RX(st + 1, 1); } SBAR();
        MQ(st, 3); if (st < 6) { RX(st + 1, 2); RX(st + 1, 3); } SBAR();
        if (st == 2) tag = get_tag(sy);
    }
    STAMP(3);
    const float cq = 0.18898223650461362f * 1.4426950408889634f;
    const unsigned voff = lane * 16;
    u32x4 q0, q1, k0, k1, v0, v1;
    const unsigned tb = WS_KV + (unsigned)(head * 64 + 4 * grp + w) * 4096 + voff;
#pragma unroll
    for (int i = 0; i < 4; ++i) { k0[i] = pk_bf16(ak[2 * i], ak[2 * i + 1]); k1[i] = pk_bf16(ak[8 + 2 * i], ak[9 + 2 * i]); }
    if (hi) { k1[2] = 0x3f803f80u; k1[3] = 0x00003f80u; }
    BST(k0, tb); BST(k1, tb + 1024);
#pragma unroll
    for (int i = 0; i < 4; ++i) {
        v0[i] = pk_bf16(av[2 * i], av[2 * i + 1]); v1[i] = pk_bf16(av[8 + 2 * i], av[9 + 2 * i]);
        if (r32 >= DH) { v0[i] = v1[i] = (r32 == DH ? 0x3f803f80u : 0u); }
    }
    BST(v0, tb + 2048); BST(v1, tb + 3072);
#pragma unroll
    for (int i = 0; i < 4; ++i) { q0[i] = pk_bf16(aq[2 * i] * cq, aq[2 * i + 1] * cq); q1[i] = pk_bf16(aq[8 + 2 * i] * cq, aq[9 + 2 * i] * cq); }
    if (hi) { q1[2] = 0; q1[3] = 0; }
    const f32x16 zero = {};
    {
        f32x16 s = MFMA_BF(BC8(k0), BC8(q0), zero); s = MFMA_BF(BC8(k1), BC8(q1), s);
        float m = s[0];
#pragma unroll
        for (int i = 1; i < 16; ++i) m = fmaxf(m, s[i]);
        q1 = put_ref(q1, swapmax(m), hi);
    }
    { LAS u32x4* qo = (LAS u32x4*)(lds + LQ + w * 2048) + lane; qo[0] = q0; qo[64] = q1; }
    arrive(sy + SY_Q); await(sy + SY_Q, 4);
    __builtin_amdgcn_s_setprio(0);
    bf16x8 Q[4][2];
    Q[0][0] = BC8(q0); Q[0][1] = BC8(q1);
#pragma unroll
    for (int i = 1; i < 4; ++i) { LAS const char* p = lds + LQ + ((w + i) & 3) * 2048 + voff; Q[i][0] = FR(p, 0); Q[i][1] = FR(p, 1024); }
    u32x4 kv[3][4] = {{k0, k1, v0, v1}, {}, {}};
    f32x16 O[4] = {}, sa, sb;
    u32x4 p0, p1;
#define GRP(SC, i, P, j) do { float e0 = EX(SC[i]); SBAR(); float e1 = EX(SC[i + 1]); SBAR(); float e2 = EX(SC[i + 2]); SBAR(); float e3 = EX(SC[i + 3]); SBAR(); \
        P[j] = pk_bf16(e0, e1); SBAR(); P[j + 1] = pk_bf16(e2, e3); PIN(P); SBAR(); } while (0)
#define SLOT(SC, SN, cb, i, QK, PV1, LD) do { constexpr int nb_ = (i) < 3 ? (cb) : ((cb) + 1) % 3, pb_ = (i) > 0 ? (cb) : ((cb) + 2) % 3; \
        if (QK) SN = MFMA_BF(BC8(kv[nb_][0]), Q[((i) + 1) & 3][0], zero); SBAR(); GRP(SC, 0, p0, 0); \
        if (PV1) O[((i) + 3) & 3] = MFMA_BF(BC8(kv[pb_][3]), BC8(p1), O[((i) + 3) & 3]); SBAR(); \
        if (LD) { kv[((cb) + 2) % 3][i] = __builtin_amdgcn_raw_buffer_load_b128(rs, voff + (i) * 1024, kvo, 16); SBAR(); } GRP(SC, 4, p0, 2); \
        if (QK) SN = MFMA_BF(BC8(kv[nb_][1]), Q[((i) + 1) & 3][1], SN); SBAR(); GRP(SC, 8, p1, 0); \
        O[i] = MFMA_BF(BC8(kv[cb][2]), BC8(p0), O[i]); SBAR(); GRP(SC, 12, p1, 2); } while (0)
    const unsigned kvb = WS_KV + (unsigned)(head * 64 + w) * 4096;
    unsigned kvo = 0;
    sa = MFMA_BF(BC8(k0), Q[0][0], zero); sa = MFMA_BF(BC8(k1), Q[0][1], sa);
    SLOT(sa, sb, 0, 0, true, false, false); SLOT(sb, sa, 0, 1, true, true, false);
    drain();
    put_flag(ws, FL_KV + (unsigned)((head * 4 + w) * 16 + grp) * FS, tag, lane);
    STAMP(4);
    SLOT(sa, sb, 0, 2, true, true, false); SLOT(sb, sa, 0, 3, false, true, false);
    STAMP(5);
    wait_flags<2>(ws, FL_KV + (unsigned)((head * 4 + w) * 16 + lane) * FS, tag, lane < 16 && lane != grp);
    STAMP(6);
#pragma unroll
    for (int j = 1; j < 3; ++j)
#pragma unroll
        for (int f = 0; f < 4; ++f) kv[j][f] = __builtin_amdgcn_raw_buffer_load_b128(rs, voff + f * 1024, kvb + ((grp + j) & 15) * 16384, 16);
    arrive(sy + SY_G);
    sa = MFMA_BF(BC8(kv[1][0]), Q[0][0], zero); sa = MFMA_BF(BC8(kv[1][1]), Q[0][1], sa);
    STAMP(7);
    int nx = grp + 3;
#define TILE(cb) do { kvo = kvb + (nx & 15) * 16384; ++nx; SBAR(); \
        SLOT(sa, sb, cb, 0, true, true, true); SLOT(sb, sa, cb, 1, true, true, true); SLOT(sa, sb, cb, 2, true, true, true); SLOT(sb, sa, cb, 3, true, true, true); } while (0)
#pragma unroll 1
    for (int it = 0; it < 5; ++it) { TILE(1); TILE(2); TILE(0); }
    O[3] = MFMA_BF(BC8(kv[0][3]), BC8(p1), O[3]);
    STAMP(8);
    f32x16 o = O[0];
    {
#pragma unroll
        for (int i = 1; i < 4; ++i) {
            LAS char* ex = lds + LX + (((w + i) & 3) * 3 + i - 1) * 4096 + voff;
#pragma unroll
            for (int g = 0; g < 4; ++g) *(LAS f32x4*)(ex + g * 1024) = (f32x4){O[i][4 * g], O[i][4 * g + 1], O[i][4 * g + 2], O[i][4 * g + 3]};
        }
        arrive(sy + SY_X); await(sy + SY_X, 4);
        LAS const char* er = lds + LX + w * 3 * 4096 + voff;
#pragma unroll
        for (int i = 0; i < 3; ++i)
#pragma unroll
            for (int g = 0; g < 4; ++g) { const f32x4 r = *(LAS const f32x4*)(er + i * 4096 + g * 1024);
#pragma unroll
                for (int e = 0; e < 4; ++e) o[4 * g + e] += r[e]; }
        f32x2 chk = {0.f, 0.f};
#pragma unroll
        for (int i = 0; i < 8; ++i) chk = pfma((f32x2){o[2 * i], o[2 * i + 1]}, (f32x2){0.f, 0.f}, chk);
        if (__builtin_expect(__any(!(chk[0] + chk[1] == 0.f) || !(o[12] < 1e30f)), 0)) {
            const unsigned hb = WS_KV + (unsigned)head * 64 * 4096 + voff;
            u32x4 qr = __builtin_bit_cast(u32x4, Q[0][1]); if (hi) { qr[2] = 0; qr[3] = 0; }
            float mm = -INFINITY;
            wait_flags<4>(ws, FL_KV + (unsigned)(head * 64 + lane) * FS, tag, true);
#pragma unroll 1
            for (int t = 0; t < 64; ++t) {
                f32x16 s = MFMA_BF(BC8(BLD(hb + t * 4096)), Q[0][0], zero); s = MFMA_BF(BC8(BLD(hb + t * 4096 + 1024)), BC8(qr), s);
#pragma unroll
                for (int i = 0; i < 16; ++i) mm = fmaxf(mm, s[i]);
            }
            qr = put_ref(qr, swapmax(mm), hi);
            o = zero;
#pragma unroll 1
            for (int t = 0; t < 64; ++t) {
                f32x16 s = MFMA_BF(BC8(BLD(hb + t * 4096)), Q[0][0], zero); s = MFMA_BF(BC8(BLD(hb + t * 4096 + 1024)), BC8(qr), s);
#pragma unroll
                for (int i = 0; i < 4; ++i) { p0[i] = pk_bf16(EX(s[2 * i]), EX(s[2 * i + 1])); p1[i] = pk_bf16(EX(s[8 + 2 * i]), EX(s[9 + 2 * i])); }
                o = MFMA_BF(BC8(BLD(hb + t * 4096 + 2048)), BC8(p0), o); o = MFMA_BF(BC8(BLD(hb + t * 4096 + 3072)), BC8(p1), o);
            }
        }
    }
    {
        const auto r = __builtin_amdgcn_permlane32_swap(__float_as_uint(o[12]), __float_as_uint(o[12]), false, false);
        const float rl = __builtin_amdgcn_rcpf(__uint_as_float(r[1]));
        u32x4 o0, o1;
#pragma unroll
        for (int i = 0; i < 4; ++i) { o0[i] = pk_bf16(o[2 * i] * rl, o[2 * i + 1] * rl); o1[i] = pk_bf16(o[8 + 2 * i] * rl, o[9 + 2 * i] * rl); }
        if (hi) { o1[2] = 0; o1[3] = 0; }
        const unsigned fi = (unsigned)((4 * grp + w) * 16 + head);
        BST(o0, WS_O + fi * 2048 + voff); BST(o1, WS_O + fi * 2048 + 1024 + voff);
        STAMP(9);
        drain();
        put_flag(ws, FL_O + fi * FS, tag, lane);
    }
    STAMP(10);
}

DI void role_b(const uint16_t* __restrict__ wo, const uint16_t* __restrict__ u, const uint16_t* __restrict__ lar, const uint16_t* __restrict__ aim,
               const uint16_t* __restrict__ cre, const uint16_t* __restrict__ cim, const uint16_t* __restrict__ ldt, const uint16_t* __restrict__ dskip,
               uint16_t* __restrict__ out, char* __restrict__ ws, LAS char* lds, int w, int lane) {
    u64 tag;
    const unsigned lds0 = (unsigned)(uintptr_t)lds;
    LAS unsigned* sy = (LAS unsigned*)(lds + LSY);
    const __amdgpu_buffer_rsrc_t rs = __builtin_amdgcn_make_buffer_rsrc(ws, 0, WS_END, 0x00020000);
    const int b = blockIdx.x, xcd = b & 7, bi = b >> 3, r32 = lane & 31, hi = lane >> 5;
    const unsigned voff = lane * 16;
    LAS char* tab = lds + LB + w * TABW;
    const int qt = 8 * xcd + (bi >> 2), ct = 4 * (bi & 3) + w;
    bf16x8 wf[32];
#define LOADWF(i) wf[i] = BC8(__builtin_amdgcn_raw_buffer_load_b128(rs, voff, WS_WO + (unsigned)(((8 * w + (i)) & 31) * 14 + ct) * 1024, 16))
    if (xcd < 7) {
        const int cc = w >> 1, hh = w & 1, pair = 32 * xcd + bi, ch = 2 * pair + cc;
        LAS char* cb = lds + LB + cc * CBW;
        const unsigned po = (unsigned)(ch * NS + 128 * hh) / 2 + lane;
        const unsigned pl = ((const unsigned*)lar)[po], pa = ((const unsigned*)aim)[po], pr = ((const unsigned*)cre)[po], pi = ((const unsigned*)cim)[po];
        const unsigned uo = (unsigned)((64 * bi + 16 * w + (lane >> 3)) * D + 64 * xcd + (lane & 7) * 8) * 2;
        glds2(u, uo, uo + 8 * D * 2 - 1024, lds0 + LB + w * TABW + 4096);
        const u64 e0 = __hip_atomic_load((GAS u64*)ws, __ATOMIC_RELAXED, __HIP_MEMORY_SCOPE_AGENT);
        u32x2 gi[2][3];
#pragma unroll
        for (int ps = 0; ps < 2; ++ps)
#pragma unroll
            for (int i = 0; i < 3; ++i) gi[ps][i] = ((const u32x2*)g_idx.v[128 * hh + 64 * ps + lane])[i];
        const unsigned lw = ((const unsigned*)ldt)[pair], dw = ((const unsigned*)dskip)[pair];
        __builtin_amdgcn_s_setprio(PRIO_B);
        const float dt = rbf(expf(cc ? bf_hi(lw) : bf_lo(lw)));
        drain();
        STAMP(1);
        arrive(sy + SY_WO);
        if (w == 0 && lane == 0) __hip_atomic_store((LAS u64*)sy, mix(e0), __ATOMIC_RELAXED, __HIP_MEMORY_SCOPE_WORKGROUP);
        await(sy + SY_WO, 4);
        {
            u32x4 o0, o1; transpose_tile(lds + LB + 4096, TABW, tab, sy + SY_T1, w, lane, o0, o1);
            const unsigned to = WS_UT + (unsigned)((64 * xcd + 16 * w + (lane >> 3)) * S + 64 * bi) * 2 + (lane & 7) * 16;
            __builtin_amdgcn_s_setprio(3); BST(o0, to); BST(o1, to + 8 * S * 2); __builtin_amdgcn_s_setprio(PRIO_B);
        }
        const float cm = wave_max(fmaxf(fmaxf(fabsf(bf_lo(pr)), fabsf(bf_hi(pr))), fmaxf(fabsf(bf_lo(pi)), fabsf(bf_hi(pi)))));
        unsigned ce = __float_as_uint(cm) & 0x7f800000u; ce = ce < 0x01000000u ? 0x01000000u : (ce > 0x7e800000u ? 0x7e800000u : ce);
        const float cscale = __uint_as_float(0x7f000000u - ce), oscale = 2.0f * dt * __uint_as_float(ce);
        LAS unsigned* tw = (LAS unsigned*)(tab + lane * 4);
        LAS const char* trd = tab + r32 * TROW + hi * 16;
        f32x16 dacc = {};
        f32x2 z1, z8, z256, x0;
#define ANCH(vl, va, vr, vi) do { const float a2 = (-rbf(expf(vl)) * dt) * 1.4426950408889634f, rev = ((va) * dt) * 0.15915494309189535f; \
            z1 = anchor(a2, -rev, 1.f); z8 = anchor(a2, rev, 8.f); z256 = anchor(a2, -rev, 256.f); x0 = (f32x2){(vr) * cscale, -(vi) * cscale}; \
            PIN(z1); PIN(z8); PIN(z256); SBAR(); } while (0)
#define TW(row) tw[(row) * (TROW / 4)]
#define CHAIN() do { \
            const f32x2 z1s = rot(z1), zs = rot(z256), z8s = rot(z8); \
            f32x2 y[4], X[8]; y[1] = z8; y[2] = cmul(z8, z8, z8s); X[0] = x0; X[1] = cmul(x0, z1, z1s); f32x2 a = cmul(x0, z256, zs); \
            y[3] = cmul(y[2], z8, z8s); const f32x2 z32 = cmul(y[2], y[2], rot(y[2])), z32s = rot(z32); X[2] = cmul(X[1], z1, z1s); y[0] = z32; \
            f32x2 c4 = cmul(X[2], z256, zs); \
            unsigned c[2][7], d0 = pk_f16(X[0]), d1 = pk_f16(X[1]), d2 = pk_f16(y[1]); \
            c[0][0] = pk_f16(y[0]); c[0][1] = 0x3c00u; c[0][2] = pk_f16(y[2]); c[0][3] = pk_f16(y[3]); c[0][4] = pk_f16(X[2]); c[0][5] = pk_f16(a); c[0][6] = pk_f16(c4); \
            TW(0) = d0; TW(1) = d1; TW(22) = d2; SBAR(); \
            _Pragma("unroll") for (int i = 1; i < 9; ++i) { \
                f32x2 t0, t1, t2, t3, t4, t5, t6; const int j = i - 1; unsigned* pc = c[j & 1]; unsigned* nc = c[i & 1]; \
                if (i < 8) { if (i > 1) t0 = y[0].xx * z32; t1 = y[1].xx * z32; t2 = y[2].xx * z32; t3 = y[3].xx * z32; \
                    if (i < 6) t4 = X[i + 1].xx * z1; t5 = a.xx * z256; \
                    if (i == 3) t6 = X[4].xx * z256; if (i == 4 || i == 5) t6 = c4.xx * z256; if (i == 6) t6 = X[6].xx * z256; } SBAR(); \
                if (j != 1) TW(21 + (j ? 4 * j : 4)) = pc[0]; TW(21 + (j ? 4 * j + 1 : 0)) = pc[1]; TW(23 + 4 * j) = pc[2]; TW(24 + 4 * j) = pc[3]; \
                if (j < 6) TW(j + 2) = pc[4]; TW(j == 0 ? 8 : (j == 1 ? 12 : (j == 2 ? 14 : 13 + j))) = pc[5]; \
                if (j == 0) TW(9) = pc[6]; if (j == 3) TW(10) = pc[6]; if (j == 4) TW(13) = pc[6]; if (j == 5) TW(15) = pc[6]; if (j == 6) TW(11) = pc[6]; SBAR(); \
                if (i < 8) { if (i > 1) y[0] = pfma(y[0].yy, z32s, t0); y[1] = pfma(y[1].yy, z32s, t1); y[2] = pfma(y[2].yy, z32s, t2); y[3] = pfma(y[3].yy, z32s, t3); \
                    if (i < 6) X[i + 2] = pfma(X[i + 1].yy, z1s, t4); a = pfma(a.yy, zs, t5); \
                    if (i == 3) c4 = pfma(X[4].yy, zs, t6); if (i == 4 || i == 5) c4 = pfma(c4.yy, zs, t6); if (i == 6) c4 = pfma(X[6].yy, zs, t6); SBAR(); \
                    if (i > 1) nc[0] = pk_f16(y[0]); nc[1] = pk_f16(y[1]); nc[2] = pk_f16(y[2]); nc[3] = pk_f16(y[3]); \
                    if (i < 6) nc[4] = pk_f16(X[i + 2]); nc[5] = pk_f16(a); if (i >= 3 && i <= 6) nc[6] = pk_f16(c4); \
                    asm volatile("" :: "v"(pc[0]), "v"(pc[1]), "v"(pc[2]), "v"(pc[3]), "v"(pc[4]), "v"(pc[5]), "v"(pc[6])); SBAR(); } \
            } } while (0)
        f16x8 fa[8], fb[8];
#define RDT() do { _Pragma("unroll") for (int kk = 0; kk < 8; ++kk) { fa[kk] = *(LAS const f16x8*)(trd + kk * 32); fb[kk] = *(LAS const f16x8*)(trd + 21 * TROW + kk * 32); } SBAR(); } while (0)
#define MMT() do { _Pragma("unroll") for (int kk = 0; kk < 8; ++kk) dacc = MFMA_F16(fa[kk], fb[kk], dacc); } while (0)
        ANCH(bf_lo(pl), bf_lo(pa), bf_lo(pr), bf_lo(pi));
        tag = get_tag(sy); await(sy + SY_T1, 4);
        STAMP(13);
        CHAIN(); RDT();
        drain(); __builtin_amdgcn_s_setprio(3); put_flag(ws, FL_UT + (unsigned)(xcd * 128 + w * 32 + bi) * FS, tag, lane); __builtin_amdgcn_s_setprio(PRIO_B);
        STAMP(14);
        ANCH(bf_hi(pl), bf_hi(pa), bf_hi(pr), bf_hi(pi));
        MMT(); SBAR();
        CHAIN(); RDT();
        const unsigned utf = hi ? FL_WO + (unsigned)(w * 32 + r32) * FS : FL_UT + (unsigned)(xcd * 128 + (bi >> 3) * 32 + r32) * FS;
        const u64 utv = __hip_atomic_load((GAS u64*)(ws + utf), __ATOMIC_RELAXED, __HIP_MEMORY_SCOPE_AGENT); SBAR();
        MMT();
        STAMP(2);
        {
            LAS float* pt = (LAS float*)tab;
            *(LAS f32x4*)(pt + 8 * r32 + 4 * hi) = (f32x4){dacc[0] * oscale, dacc[1] * oscale, dacc[2] * oscale, dacc[3] * oscale};
            *(LAS f32x2*)(pt + (hi ? 384 + 2 * r32 : 256 + 4 * r32)) = (f32x2){dacc[4] * oscale, dacc[5] * oscale};
            *(LAS f32x2*)(pt + (hi ? 448 + 2 * r32 : 258 + 4 * r32)) = (f32x2){dacc[6] * oscale, dacc[7] * oscale};
            pt[hi ? 640 + r32 : 512 + r32] = dacc[8] * oscale;
            pt[hi ? 672 + r32 : 544 + r32] = dacc[9] * oscale;
            pt[hi ? 672 + r32 : 576 + r32] = dacc[10] * oscale;
            pt[hi ? 672 + r32 : 608 + r32] = dacc[11] * oscale;
        }
        LAS unsigned* ps_ = sy + SY_P + 5 * cc;
        arrive(ps_ + 0);
        LAS char* ui = cb + UOF;
        LAS char* kt = cb + KOF;
        await(ps_ + 0, 2);
#pragma unroll
        for (int ps = 0; ps < 2; ++ps) {
            const int v = 81 * hh + 64 * ps + lane;
            if (ps == 0 || lane < 17) {
                const f32x4 p0 = *(LAS const f32x4*)(cb + v * 16), p1 = *(LAS const f32x4*)(cb + TABW + v * 16);
                u32x2 r = {pk_bf16(p0[0] + p1[0], p0[1] + p1[1]), pk_bf16(p0[2] + p1[2], p0[3] + p1[3])};
                if (v == 161) r = (u32x2){0u, 0u};
                *(LAS u32x2*)(kt + v * 8) = r;
            }
        }
        arrive(ps_ + 1);
        if (__any(utv != tag)) wait_flags<4>(ws, utf, tag, true);
        CFENCE();
        const unsigned ub = WS_UT + (unsigned)(ch * S + 1024 * hh) * 2 + voff;
        const u32x4 u0 = BLD(ub), u1 = BLD(ub + 1024); SBAR();
        await(ps_ + 1, 2);
        STAMP(3);
        __builtin_amdgcn_s_setprio(PRIO_B2);
        const u32x4 z4 = {};
        {
            unsigned W[2][6];
#pragma unroll
            for (int ps = 0; ps < 2; ++ps)
#pragma unroll
                for (int i = 0; i < 6; ++i) { const unsigned gv = gi[ps][i >> 1][i & 1];
                    W[ps][i] = *(LAS const uint16_t*)(kt + (gv & 0xffffu));
                    if (i < 5) W[ps][i] |= (unsigned)*(LAS const uint16_t*)(kt + (gv >> 16)) << 16; }
            *(LAS u32x4*)(ui + (64 * hh + lane) * 16) = z4;
            if (lane < 16) *(LAS u32x4*)(ui + (128 + 16 * hh + lane) * 16) = z4;
            if (lane < 8) *(LAS u32x4*)(cb + (2 * hh + (lane >> 2)) * CPY + (256 + (lane & 3)) * 16) = z4;
#pragma unroll
            for (int ps = 0; ps < 2; ++ps) {
                unsigned V[5];
#pragma unroll
                for (int i = 0; i < 5; ++i) V[i] = __builtin_amdgcn_alignbit(W[ps][i + 1], W[ps][i], 16);
                LAS char* cp = cb + (128 * hh + 64 * ps + lane) * 16;
                *(LAS u32x4*)(cp) = (u32x4){W[ps][0], W[ps][1], W[ps][2], W[ps][3]};
                *(LAS u32x4*)(cp + CPY) = (u32x4){V[0], V[1], V[2], V[3]};
                *(LAS u32x4*)(cp + 2 * CPY) = (u32x4){W[ps][1], W[ps][2], W[ps][3], W[ps][4]};
                *(LAS u32x4*)(cp + 3 * CPY) = (u32x4){V[1], V[2], V[3], V[4]};
            }
        }
        *(LAS u32x4*)(ui + (32 + 32 * hh + (lane >> 2)) * 80 + (lane & 3) * 16) = u0;
        *(LAS u32x4*)(ui + (48 + 32 * hh + (lane >> 2)) * 80 + (lane & 3) * 16) = u1;
        arrive(ps_ + 2); await(ps_ + 2, 2);
        STAMP(4);
        f32x16 a0 = {}, a1 = {};
        {
            const int sb = 7 - (r32 & 7);
            LAS const char* pa = cb + (sb & 3) * CPY + (255 - (r32 >> 3) + hi) * 16 + (sb >> 2) * 8 - 64 * (16 * hh + 15) - 2048;
            LAS const char* pa2 = pa + 8; asm volatile("" : "+v"(pa2));
            LAS const char* pb = ui + (32 + r32 - 16 * hh - 15) * 80 + 16 * hi;
            s16x4 ta[2][2], tb[2][2]; bf16x8 b0[2], b1[2];
#define T4(q, off) (*(LAS const s16x4*)((q) + (off)))
#define CSTEP(i, j, RD) do { const int o_ = 64 * (15 - ((j) >> 1)) + 32 * ((j) & 1), u_ = 80 * (15 - ((j) >> 1)) + 32 * ((j) & 1); \
                a1 = MFMA_BF(cat8(ta[i][0], ta[i][1]), b1[i], a1); SBAR(); if (RD) { ta[1 - i][0] = T4(pa, o_ + 2048); ta[1 - i][1] = T4(pa2, o_ + 2048); } SBAR(); \
                a0 = MFMA_BF(cat8(ta[i][0], ta[i][1]), b0[i], a0); SBAR(); if (RD) { b1[1 - i] = FR(pb, u_ + 2560); b0[1 - i] = FR(pb, u_); } SBAR(); \
                a1 = MFMA_BF(cat8(tb[i][0], tb[i][1]), b0[i], a1); SBAR(); if (RD) { tb[1 - i][0] = T4(pa, o_); tb[1 - i][1] = T4(pa2, o_); } SBAR(); } while (0)
            ta[0][0] = T4(pa, 960 + 2048); ta[0][1] = T4(pa2, 960 + 2048); b1[0] = FR(pb, 1200 + 2560); b0[0] = FR(pb, 1200); tb[0][0] = T4(pa, 960); tb[0][1] = T4(pa2, 960);
#pragma unroll
            for (int j = 0; j < 32; ++j) {
                if (j == 31) arrive(ps_ + 3);
                CSTEP(j & 1, j + 1, j < 31);
                if (ct < 14) LOADWF(j);
                SBAR();
            }
        }
        STAMP(5);
        {
            u32x2 uu[4];
#pragma unroll
            for (int g = 0; g < 4; ++g) uu[g] = *(LAS const u32x2*)(ui + (32 + 32 * hh + r32) * 80 + 16 * g + 8 * hi);
            LAS char* ex = cb + r32 * 144 + hi * 16;
            const f32x16 snd = hh ? a0 : a1; const f32x16 y = hh ? a1 : a0;
            await(ps_ + 3, 2);
#pragma unroll
            for (int g = 0; g < 4; ++g) *(LAS f32x4*)(ex + hh * 4608 + g * 32) = (f32x4){snd[4 * g], snd[4 * g + 1], snd[4 * g + 2], snd[4 * g + 3]};
            arrive(ps_ + 4);
            const float dsk = cc ? bf_hi(dw) : bf_lo(dw);
            unsigned sk[8];
#pragma unroll
            for (int g = 0; g < 4; ++g)
#pragma unroll
                for (int e = 0; e < 2; ++e) sk[2 * g + e] = pk_bf16(bf_lo(uu[g][e]) * dsk, bf_hi(uu[g][e]) * dsk);
            await(ps_ + 4, 2);
            LAS char* sc = cb + 9216 + hh * 2048;
#pragma unroll
            for (int g = 0; g < 4; ++g) {
                const f32x4 p = *(LAS const f32x4*)(ex + (1 - hh) * 4608 + g * 32);
                u32x2 r;
#pragma unroll
                for (int e = 0; e < 2; ++e) {
                    const unsigned yb = pk_bf16(y[4 * g + 2 * e] + p[2 * e], y[4 * g + 2 * e + 1] + p[2 * e + 1]);
                    r[e] = pk_bf16(bf_lo(yb) + bf_lo(sk[2 * g + e]), bf_hi(yb) + bf_hi(sk[2 * g + e]));
                }
                *(LAS u32x2*)(sc + 64 * r32 + 16 * g + 8 * hi) = r;
            }
            const u32x4 o0 = *(LAS const u32x4*)(sc + voff), o1 = *(LAS const u32x4*)(sc + 1024 + voff);
            const unsigned bo = WS_BT + (unsigned)(ch * S + 1024 * hh) * 2 + voff;
            BST(o0, bo); BST(o1, bo + 1024);
        }
        arrive(sy + SY_BG);
        STAMP(6);
        drain();
        put_flag(ws, FL_BT + (unsigned)(xcd * 128 + bi * 4 + w) * FS, tag, lane);
    } else {
        {
            const unsigned nb = (bi & 1) ? 12 * 2 * D : 16 * 2 * D;
            unsigned v4[4];
#pragma unroll
            for (int i = 0; i < 4; ++i) { const unsigned o = (unsigned)(w * 4096 + i * 1024) + voff; v4[i] = (o < nb ? o : 0u) + 4096 - i * 1024; }
            glds4((const char*)(wo + (DH * (bi >> 1) + 16 * (bi & 1)) * D) - 4096, v4[0], v4[1], v4[2], v4[3], lds0 + LB + w * 4096);
        }
        const u64 e0 = __hip_atomic_load((GAS u64*)ws, __ATOMIC_RELAXED, __HIP_MEMORY_SCOPE_AGENT);
        drain();
        if (w == 0 && lane == 0) __hip_atomic_store((LAS u64*)sy, mix(e0), __ATOMIC_RELAXED, __HIP_MEMORY_SCOPE_WORKGROUP);
        arrive(sy + SY_WO); await(sy + SY_WO, 4);
        LAS const char* p = lds + LB + (4 * hi + ((lane & 15) >> 2)) * (2 * D) + ((lane >> 4) & 1) * 32 + (lane & 3) * 8;
#pragma unroll
        for (int i = 0; i < 4; ++i) {
            const int ct = w + 4 * i;
            if (ct < 14) {
                u32x4 o = __builtin_bit_cast(u32x4, cat8(trread(p + ct * 64), trread(p + 8 * 2 * D + ct * 64)));
                if (hi && (bi & 1)) { o[2] = 0; o[3] = 0; }
                BST(o, WS_WO + (unsigned)(bi * 14 + ct) * 1024 + voff);
            }
        }
        tag = get_tag(sy);
        drain();
        put_flag(ws, FL_WO + (unsigned)(w * 32 + bi) * FS, tag, lane);
        arrive(sy + SY_BG);
        await_slow(sy + SY_G, 4);
        wait_flags<8>(ws, FL_WO + (unsigned)(w * 32 + r32) * FS, tag, !hi);
        if (ct < 14) {
#pragma unroll
            for (int i = 0; i < 32; ++i) { LOADWF(i); __builtin_amdgcn_s_sleep(WOSL); SBAR(); }
        }
    }
    STAMP(7);
    const __amdgpu_buffer_rsrc_t ro = __builtin_amdgcn_make_buffer_rsrc(out, 0, S * 2 * D * 2, 0x00020000);
    if (xcd < 7) {
        wait_flags<16>(ws, FL_BT + (unsigned)(xcd * 128 + w * 32 + r32) * FS, tag, !hi);
        await(sy + SY_BG, 4);
        const unsigned bo = WS_BT + (unsigned)((64 * xcd + 16 * w + (lane >> 3)) * S + 64 * bi + (lane & 7) * 8) * 2;
        glds2c(ws, bo, bo + 8 * S * 2 - 1024, lds0 + LB + 32768 + w * 2048);
        drain();
        arrive(sy + SY_T2); await(sy + SY_T2, 4);
        u32x4 o0, o1; transpose_tile(lds + LB + 32768, 2048, lds + LB + 40960 + w * 2304, nullptr, w, lane, o0, o1);
        const unsigned oo = (unsigned)((64 * bi + 16 * w + (lane >> 3)) * 2 * D + D + 64 * xcd) * 2 + (lane & 7) * 16;
        __builtin_amdgcn_raw_buffer_store_b128(o0, ro, oo, 0, 16); __builtin_amdgcn_raw_buffer_store_b128(o1, ro, oo + 8 * 2 * D * 2, 0, 16);
    } else await(sy + SY_BG, 4);
    STAMP(8);
    await_slow(sy + SY_X, 4);
    wait_flags<2>(ws, FL_O + (unsigned)(qt * 16 + 4 * w + lane) * FS, tag, lane < 4);
    STAMP(9);
    u32x4 of[8];
#pragma unroll
    for (int i = 0; i < 8; ++i) of[i] = BLD(WS_O + (unsigned)(qt * 32 + 8 * w + i) * 1024 + voff);
#pragma unroll
    for (int i = 0; i < 8; ++i) *(LAS u32x4*)(lds + LB + (8 * w + i) * 1024 + voff) = of[i];
    arrive(sy + SY_B2);
    STAMP(10);
    f32x16 acc = {};
    if (ct < 14) {
#pragma unroll
        for (int i = 0; i < 8; ++i) acc = MFMA_BF(wf[i], BC8(of[i]), acc);
    }
    SBAR();
    await(sy + SY_B2, 4);
    STAMP(11);
    if (ct < 14) {
        LAS const char* po[3];
#pragma unroll
        for (int j = 0; j < 3; ++j) po[j] = lds + LB + ((w + 1 + j) & 3) * 8192 + voff;
        bf16x8 og[4];
#pragma unroll
        for (int i = 0; i < 3; ++i) og[i] = FR(po[0], i * 1024);
#pragma unroll
        for (int i = 0; i < 24; ++i) { if (i < 21) og[(i + 3) & 3] = FR(po[(i + 3) >> 3], ((i + 3) & 7) * 1024); SBAR(); acc = MFMA_BF(wf[8 + i], og[i & 3], acc); SBAR(); }
        LAS char* sc = lds + LB + 40960 + w * 2304;
#pragma unroll
        for (int g = 0; g < 4; ++g) *(LAS u32x2*)(sc + r32 * 72 + 16 * g + 8 * hi) = (u32x2){pk_bf16(acc[4 * g], acc[4 * g + 1]), pk_bf16(acc[4 * g + 2], acc[4 * g + 3])};
        LAS const char* sr = sc + (lane >> 2) * 72 + (lane & 3) * 16;
        const u32x2 a0 = *(LAS const u32x2*)sr, a1 = *(LAS const u32x2*)(sr + 8), b0 = *(LAS const u32x2*)(sr + 16 * 72), b1 = *(LAS const u32x2*)(sr + 16 * 72 + 8);
        const unsigned oo = (unsigned)((32 * qt + (lane >> 2)) * 2 * D + 32 * ct) * 2 + (lane & 3) * 16;
        __builtin_amdgcn_raw_buffer_store_b128((u32x4){a0[0], a0[1], a1[0], a1[1]}, ro, oo, 0, 16);
        __builtin_amdgcn_raw_buffer_store_b128((u32x4){b0[0], b0[1], b1[0], b1[1]}, ro, oo + 16 * 2 * D * 2, 0, 16);
    }
    if (b == 0 && w == 0) put_flag(ws, 0, tag, lane);
    STAMP(12);
}

__global__ __launch_bounds__(512, 2) void fused(const uint16_t* __restrict__ x, const uint16_t* __restrict__ wqkv, const uint16_t* __restrict__ wo,
                                               const uint16_t* __restrict__ u, const uint16_t* __restrict__ lar, const uint16_t* __restrict__ aim,
                                               const uint16_t* __restrict__ cre, const uint16_t* __restrict__ cim, const uint16_t* __restrict__ ldt,
                                               const uint16_t* __restrict__ dskip, uint16_t* __restrict__ out, char* __restrict__ ws) {
    extern __shared__ __attribute__((aligned(16))) char smem[];
    LAS char* lds = (LAS char*)smem;
    const int lane = threadIdx.x & 63, w = __builtin_amdgcn_readfirstlane(threadIdx.x >> 6);
    STAMP(0);
    if (w == 4) ((LAS unsigned*)(lds + LSY))[lane] = 0;
    asm volatile("" :: "s"(x), "s"(wqkv), "s"(wo), "s"(u), "s"(lar), "s"(aim), "s"(cre), "s"(cim), "s"(ldt), "s"(dskip), "s"(out), "s"(ws));
    asm volatile("s_waitcnt lgkmcnt(0)\n\ts_barrier" ::: "memory");
    if (w < 4) role_a(x, wqkv, ws, lds, w, lane);
    else role_b(wo, u, lar, aim, cre, cim, ldt, dskip, out, ws, lds, w - 4, lane);
}
}

extern "C" void kernel_launch(void* const* d_in, const int* in_sizes, int n_in, void* d_out, int out_size, void* d_ws, size_t ws_size, hipStream_t stream) {
    using namespace k;
    (void)hipFuncSetAttribute((const void*)fused, hipFuncAttributeMaxDynamicSharedMemorySize, LDS_BYTES);
    const uint16_t* const* in = (const uint16_t* const*)d_in;
    fused<<<256, 512, LDS_BYTES, stream>>>(in[0], in[1], in[2], in[3], in[4], in[5], in[6], in[7], in[8], in[9], (uint16_t*)d_out, (char*)d_ws);
}
```

```cpp
#define CODEPF 0
#include <hip/hip_runtime.h>
#include <hip/hip_bf16.h>
#include <stdint.h>

typedef short bf16x8 __attribute__((ext_vector_type(8)));
typedef _Float16 f16x8 __attribute__((ext_vector_type(8)));
typedef _Float16 f16x2 __attribute__((ext_vector_type(2)));
typedef __bf16 bf16x2 __attribute__((ext_vector_type(2)));
typedef short s16x4 __attribute__((ext_vector_type(4)));
typedef float f32x16 __attribute__((ext_vector_type(16)));
typedef float f32x4 __attribute__((ext_vector_type(4)));
typedef float f32x2 __attribute__((ext_vector_type(2)));
typedef unsigned u32x4 __attribute__((ext_vector_type(4)));
typedef unsigned u32x2 __attribute__((ext_vector_type(2)));
typedef unsigned long long u64;
#define LAS __attribute__((address_space(3)))
#define GAS __attribute__((address_space(1)))
#define DI __device__ __forceinline__
#define MFMA_BF(a, b, c) __builtin_amdgcn_mfma_f32_32x32x16_bf16((a), (b), (c), 0, 0, 0)
#define MFMA_F16(a, b, c) __builtin_amdgcn_mfma_f32_32x32x16_f16((a), (b), (c), 0, 0, 0)
#define SBAR() __builtin_amdgcn_sched_barrier(0)
#define CFENCE() asm volatile("" ::: "memory")
#define PIN(x) asm volatile("" : "+v"(x))
#define BC8(x) __builtin_bit_cast(bf16x8, (x))
#define EX(x) __builtin_amdgcn_exp2f(x)
#define PRIO_A1 0
#define PRIO_B 0
#define PRIO_B2 1
#define ROT 1
#define WOSL 2

namespace k {
#define STAMP(i)
constexpr int S = 2048, D = 448, DH = 28, NS = 256;
constexpr unsigned FS = 128;
constexpr unsigned FL_KV = FS, FL_O = FL_KV + 1024 * FS, FL_UT = FL_O + 1024 * FS, FL_BT = FL_UT + 896 * FS, FL_WO = FL_BT + 896 * FS;
constexpr unsigned WS_KV = 512u << 10;
constexpr unsigned WS_O = WS_KV + (4u << 20);
constexpr unsigned WS_WO = WS_O + (2u << 20);
constexpr unsigned WS_UT = WS_WO + (448u << 10);
constexpr unsigned WS_BT = WS_UT + D * S * 2;
constexpr unsigned WS_END = WS_BT + D * S * 2;
constexpr int WSL = 12288, LXR = 4 * WSL, XSL = 16384, LB = LXR + 3 * XSL;
constexpr int LQ = 0, LX = 8192;
constexpr int TROW = 272, TABW = 53 * TROW, CBW = 2 * TABW, CPY = 4160, UOF = 4 * CPY, KOF = CBW - 1312;
constexpr int LSY = LB + 2 * CBW, LDS_BYTES = LSY + 256;
static_assert(LDS_BYTES <= 163840 && (LSY & 15) == 0 && UOF + 7680 <= KOF, "lds");
enum { SY_TAG = 0, SY_F = 2, SY_P = 10, SY_WO = 20, SY_T1 = 21, SY_BG = 22, SY_B2 = 23, SY_Q = 24, SY_X = 25, SY_G = 26, SY_T2 = 27 };
constexpr int SPIN = 1 << 18;
constexpr unsigned CODE_LINES = 264;

struct IdxTab { uint16_t v[256][12]; };
constexpr IdxTab make_idx() {
    IdxTab t = {};
    for (int y = 0; y < 256; ++y) for (int e = 0; e < 12; ++e) {
        const int j = 2047 - 8 * y - e; int ix = 644;
        if (j >= 0) { const int sh = j >= 1024 ? 3 : (j >= 512 ? 2 : (j >= 256 ? 1 : 0));
            ix = sh ? 128 * sh + ((j + (1 << (sh - 1)) - 1 + ((j >> sh) & 1)) >> sh) : j; }
        t.v[y][e] = (uint16_t)(2 * ix);
    }
    return t;
}
__device__ const IdxTab g_idx __attribute__((aligned(16))) = make_idx();

DI unsigned pk_bf16(float a, float b) { f32x2 v = {a, b}; return __builtin_bit_cast(unsigned, __builtin_convertvector(v, bf16x2)); }
DI unsigned pk_f16(f32x2 v) { return __builtin_bit_cast(unsigned, __builtin_convertvector(v, f16x2)); }
DI float bf_lo(unsigned w) { return __uint_as_float(w << 16); }
DI float bf_hi(unsigned w) { return __uint_as_float(w & 0xffff0000u); }
DI float rbf(float x) { return bf_lo(pk_bf16(x, x)); }
#define GL0 "s_mov_b32 %0, m0\n\ts_mov_b32 m0, %2\n\ts_nop 0\n\t"
#define GL1(V, O, C) "global_load_lds_dwordx4 " V ", %1 offset:" #O C "\n\t"
#define GLE "s_mov_b32 m0, %0"
DI void glds4(const void* sb, unsigned v0, unsigned v1, unsigned v2, unsigned v3, unsigned lb) { unsigned sv;
    asm volatile(GL0 GL1("%3", 0, "") GL1("%4", 1024, "") GL1("%5", 2048, "") GL1("%6", 3072, "") GLE : "=&s"(sv) : "s"(sb), "s"(lb), "v"(v0), "v"(v1), "v"(v2), "v"(v3) : "memory"); }
DI void glds3(const void* sb, unsigned v0, unsigned v1, unsigned v2, unsigned lb) { unsigned sv;
    asm volatile(GL0 GL1("%3", 0, "") GL1("%4", 1024, "") GL1("%5", 2048, "") GLE : "=&s"(sv) : "s"(sb), "s"(lb), "v"(v0), "v"(v1), "v"(v2) : "memory"); }
DI void glds2(const void* sb, unsigned v0, unsigned v1, unsigned lb) { unsigned sv;
    asm volatile(GL0 GL1("%3", 0, "") GL1("%4", 1024, "") GLE : "=&s"(sv) : "s"(sb), "s"(lb), "v"(v0), "v"(v1) : "memory"); }
DI void glds2c(const void* sb, unsigned v0, unsigned v1, unsigned lb) { unsigned sv;
    asm volatile(GL0 GL1("%3", 0, " sc1") GL1("%4", 1024, " sc1") GLE : "=&s"(sv) : "s"(sb), "s"(lb), "v"(v0), "v"(v1) : "memory"); }
DI float wave_max(float v) {
#define DPPMAX(ctrl, rm) v = fmaxf(v, __uint_as_float(__builtin_amdgcn_update_dpp(0, __float_as_uint(v), ctrl, rm, 0xf, true)))
    DPPMAX(0x111, 0xf); DPPMAX(0x112, 0xf); DPPMAX(0x114, 0xf); DPPMAX(0x118, 0xf); DPPMAX(0x142, 0xa); DPPMAX(0x143, 0xc);
    return __uint_as_float(__builtin_amdgcn_readlane(__float_as_uint(v), 63));
}
DI s16x4 trread(LAS const char* p) { return __builtin_bit_cast(s16x4, __builtin_amdgcn_ds_read_tr16_b64_v4i16((LAS s16x4*)p)); }
DI bf16x8 cat8(s16x4 a, s16x4 b) { return __builtin_shufflevector(a, b, 0, 1, 2, 3, 4, 5, 6, 7); }
DI f32x2 pfma(f32x2 a, f32x2 b, f32x2 c) { return __builtin_elementwise_fma(a, b, c); }
DI f32x2 cmul(f32x2 a, f32x2 b, f32x2 bs) { return pfma(a.yy, bs, a.xx * b); }
DI f32x2 rot(f32x2 b) { return (f32x2){-b.y, b.x}; }
DI f32x2 anchor(float a2, float rev, float s) {
    const float mag = EX(a2 * s);
    float r = rev * s; r -= __builtin_rintf(r);
    return (f32x2){mag * __builtin_amdgcn_cosf(r), mag * __builtin_amdgcn_sinf(r)};
}
DI void arrive(LAS unsigned* c) { asm volatile("s_mov_b64 exec, 1\n\tds_add_u32 %0, %1\n\ts_mov_b64 exec, -1" :: "v"((unsigned)(uintptr_t)c), "v"(1u) : "memory"); }
DI void await(LAS unsigned* c, unsigned n) {
    unsigned t, i = SPIN;
    asm volatile("1:\n\tds_read_b32 %0, %2\n\ts_waitcnt lgkmcnt(0)\n\tv_cmp_gt_u32 vcc, %3, %0\n\ts_cbranch_vccz 2f\n\ts_sleep 1\n\ts_sub_u32 %1, %1, 1\n\ts_cbranch_scc0 1b\n2:"
                 : "=&v"(t), "+s"(i) : "v"((unsigned)(uintptr_t)c), "s"(n) : "vcc", "scc", "memory");
}
DI void await_slow(LAS unsigned* c, unsigned n) {
    unsigned t, i = SPIN;
    asm volatile("1:\n\tds_read_b32 %0, %2\n\ts_waitcnt lgkmcnt(0)\n\tv_cmp_gt_u32 vcc, %3, %0\n\ts_cbranch_vccz 2f\n\ts_sleep 3\n\ts_sub_u32 %1, %1, 1\n\ts_cbranch_scc0 1b\n2:"
                 : "=&v"(t), "+s"(i) : "v"((unsigned)(uintptr_t)c), "s"(n) : "vcc", "scc", "memory");
}
DI u64 get_tag(LAS unsigned* sy) {
    await(sy, 1);
    const u64 v = __hip_atomic_load((LAS u64*)sy, __ATOMIC_RELAXED, __HIP_MEMORY_SCOPE_WORKGROUP);
    return ((u64)(unsigned)__builtin_amdgcn_readfirstlane((unsigned)(v >> 32)) << 32) | (unsigned)__builtin_amdgcn_readfirstlane((unsigned)v);
}
DI void drain() { asm volatile("s_waitcnt vmcnt(0)" ::: "memory"); }
DI void put_flag(char* ws, unsigned off, u64 tag, int lane) { if (lane == 0) __hip_atomic_store((GAS u64*)(ws + off), tag, __ATOMIC_RELAXED, __HIP_MEMORY_SCOPE_AGENT); }
template <int SLP> DI void wait_flags(char* ws, unsigned off, u64 tag, bool pend) {
    GAS u64* p = (GAS u64*)(ws + off);
    CFENCE();
#pragma unroll 1
    for (int i = 0; i < SPIN; ++i) {
        if (pend) pend = __hip_atomic_load(p, __ATOMIC_RELAXED, __HIP_MEMORY_SCOPE_AGENT) != tag;
        if (!__any(pend)) break;
        __builtin_amdgcn_s_sleep(SLP);
    }
    CFENCE();
}
#define BST(v, off) __builtin_amdgcn_raw_buffer_store_b128((v), rs, (off), 0, 16)
#define BLD(off) __builtin_amdgcn_raw_buffer_load_b128(rs, (off), 0, 16)
#define FR(p, off) (*(LAS const bf16x8*)((p) + (off)))
DI float swapmax(float m) { const auto r = __builtin_amdgcn_permlane32_swap(__float_as_uint(m), __float_as_uint(m), false, false); return fmaxf(__uint_as_float(r[0]), __uint_as_float(r[1])); }
DI u32x4 put_ref(u32x4 q1, float m, int hi) {
    const float m1 = rbf(m), m2 = rbf(m - m1), m3 = m - m1 - m2;
    if (hi) { q1[2] = pk_bf16(-m1, -m2); q1[3] = pk_bf16(-m3, 0.f); }
    return q1;
}
DI u64 mix(u64 e) { u64 z = e + 0x9E3779B97F4A7C15ull; z = (z ^ (z >> 30)) * 0xBF58476D1CE4E5B9ull; z = (z ^ (z >> 27)) * 0x94D049BB133111EBull; return (z ^ (z >> 31)) | 1ull; }
DI void transpose_tile(LAS const char* stage, int pst, LAS char* scratch, LAS unsigned* done, int w, int lane, u32x4& o0, u32x4& o1) {
    LAS const char* p = stage + (lane >> 4) * pst + ((lane & 15) >> 2) * 128 + w * 32 + (lane & 3) * 8;
    const bf16x8 t0 = cat8(trread(p), trread(p + 512)), t1 = cat8(trread(p + 1024), trread(p + 1536));
    if (done) arrive(done);
    LAS char* sc = scratch + (lane & 15) * 144 + (lane >> 4) * 32;
    *(LAS bf16x8*)sc = t0; *(LAS bf16x8*)(sc + 16) = t1;
    LAS const char* sr = scratch + (lane >> 3) * 144 + (lane & 7) * 16;
    o0 = *(LAS const u32x4*)sr; o1 = *(LAS const u32x4*)(sr + 8 * 144);
}

DI void role_a(const uint16_t* __restrict__ x, const uint16_t* __restrict__ wqkv, char* __restrict__ ws, LAS char* lds, int w, int lane, unsigned pf) {
    const int head = 4 * (blockIdx.x & 3) + ((blockIdx.x >> 3) & 3), grp = 8 * ((blockIdx.x >> 2) & 1) + (blockIdx.x >> 5);
    const unsigned lds0 = (unsigned)(uintptr_t)lds;
    const int r32 = lane & 31, hi = lane >> 5;
    const int wsh = (head & 7) == 7 ? 4 : 0;
    const unsigned wv = (unsigned)((lane >> 2) * 3 * D + (lane & 3) * 8) * 2;
    const char* wb = (const char*)(wqkv + DH * head - wsh + (16 * w) * 3 * D) - 2048;
    const unsigned xv0 = (unsigned)((lane >> 3) * D + (((lane & 7) ^ (lane >> 4)) * 8)) * 2, xv1 = xv0 ^ 64;
    const char* xb = (const char*)(x + (128 * grp + 32 * w) * D);
    const int rho = ROT ? ((blockIdx.x >> 3) + ((blockIdx.x >> 1) & 3)) % 7 : 0;
#define SP(st) ((st) + rho >= 7 ? (st) + rho - 7 : (st) + rho)
#define LOADW(st) glds3(wb + SP(st) * (64 * 3 * D * 2), wv + 2048, wv + 2 * D + 1024, wv + 4 * D, lds0 + ((st) & 3) * WSL + w * 3072)
#define LOADX(st) glds4(xb + SP(st) * 128, xv0, xv1 + 16 * D - 1024, xv0 + 32 * D - 2048, xv1 + 48 * D - 3072, lds0 + LXR + ((st) % 3) * XSL + w * 4096)
    LOADW(0); LOADX(0);
    STAMP(1);
    LOADW(1); LOADX(1); LOADW(2); LOADX(2);
    __builtin_amdgcn_s_setprio(PRIO_A1);
    LAS unsigned* sy = (LAS unsigned*)(lds + LSY);
    const __amdgpu_buffer_rsrc_t rs = __builtin_amdgcn_make_buffer_rsrc(ws, 0, WS_END, 0x00020000);
    const int xs = (r32 >> 1) & 7;
    LAS const char* xp[4];
#pragma unroll
    for (int ks = 0; ks < 4; ++ks) xp[ks] = lds + LXR + w * 4096 + r32 * 128 + (((2 * ks + hi) ^ xs) * 16);
    LAS const char* wp = lds + (8 * hi + ((lane & 15) >> 2)) * 64 + ((lane >> 4) & 1) * 32 + (lane & 3) * 8 + wsh * 2;
    f32x16 aq = {}, ak = {}, av = {};
    bf16x8 xf[2][4]; s16x4 fr[4][6];
#define RX(st, ks) xf[(st) & 1][ks] = FR(xp[ks], ((st) % 3) * XSL)
#define RW(st, ks, j) fr[ks][j] = trread(wp + ((st) & 3) * WSL + (ks) * 3072 + ((((j) >> 1) + 1) % 3) * 1024 + ((j) & 1) * 256)
#define MK(st, ks) ak = MFMA_BF(cat8(fr[ks][0], fr[ks][1]), xf[(st) & 1][ks], ak); SBAR()
#define MV(st, ks) av = MFMA_BF(xf[(st) & 1][ks], cat8(fr[ks][2], fr[ks][3]), av); SBAR()
#define MQ(st, ks) aq = MFMA_BF(cat8(fr[ks][4], fr[ks][5]), xf[(st) & 1][ks], aq); SBAR()
#define QSTEP(st, ks) MK(st, ks); RW(st, ks + 1, 0); RW(st, ks + 1, 1); SBAR(); MV(st, ks); RW(st, ks + 1, 2); RW(st, ks + 1, 3); SBAR(); MQ(st, ks); RW(st, ks + 1, 4); RW(st, ks + 1, 5); SBAR()
    u64 tag = 0;
    asm volatile("s_waitcnt vmcnt(14)" :: "v"(pf) : "memory");
    arrive(sy + SY_F); await(sy + SY_F, 4);
    STAMP(16);
    RX(0, 0); RW(0, 0, 0); RW(0, 0, 1); RW(0, 0, 2); RW(0, 0, 3); RW(0, 0, 4); RW(0, 0, 5); RX(0, 1); RX(0, 2); RX(0, 3); SBAR();
#pragma unroll
    for (int st = 0; st < 7; ++st) {
        QSTEP(st, 0); if (st < 4) LOADW(st + 3);
        QSTEP(st, 1); if (st < 4) LOADX(st + 3);
        QSTEP(st, 2);
        if (st < 4) asm volatile("s_waitcnt vmcnt(14)" ::: "memory"); else if (st == 4) asm volatile("s_waitcnt vmcnt(7)" ::: "memory"); else if (st == 5) drain();
        arrive(sy + SY_F + 1 + st); SBAR();
        MK(st, 3); await(sy + SY_F + 1 + st, 4);
        if (st < 6) { RX(st + 1, 0); RW(st + 1, 0, 0); RW(st + 1, 0, 1); RW(st + 1, 0, 2); RW(st + 1, 0, 3); } SBAR();
        MV(st, 3); if (st < 6) { RW(st + 1, 0, 4); RW(st + 1, 0, 5); RX(st + 1, 1); } SBAR();
        MQ(st, 3); if (st < 6) { RX(st + 1, 2); RX(st + 1, 3); } SBAR();
        if (st == 2) tag = get_tag(sy);
    }
    STAMP(3);
    const float cq = 0.18898223650461362f * 1.4426950408889634f;
    const unsigned voff = lane * 16;
    u32x4 q0, q1, k0, k1, v0, v1;
    const unsigned tb = WS_KV + (unsigned)(head * 64 + 4 * grp + w) * 4096 + voff;
#pragma unroll
    for (int i = 0; i < 4; ++i) { k0[i] = pk_bf16(ak[2 * i], ak[2 * i + 1]); k1[i] = pk_bf16(ak[8 + 2 * i], ak[9 + 2 * i]); }
    if (hi) { k1[2] = 0x3f803f80u; k1[3] = 0x00003f80u; }
    BST(k0, tb); BST(k1, tb + 1024);
#pragma unroll
    for (int i = 0; i < 4; ++i) {
        v0[i] = pk_bf16(av[2 * i], av[2 * i + 1]); v1[i] = pk_bf16(av[8 + 2 * i], av[9 + 2 * i]);
        if (r32 >= DH) { v0[i] = v1[i] = (r32 == DH ? 0x3f803f80u : 0u); }
    }
    BST(v0, tb + 2048); BST(v1, tb + 3072);
#pragma unroll
    for (int i = 0; i < 4; ++i) { q0[i] = pk_bf16(aq[2 * i] * cq, aq[2 * i + 1] * cq); q1[i] = pk_bf16(aq[8 + 2 * i] * cq, aq[9 + 2 * i] * cq); }
    if (hi) { q1[2] = 0; q1[3] = 0; }
    const f32x16 zero = {};
    {
        f32x16 s = MFMA_BF(BC8(k0), BC8(q0), zero); s = MFMA_BF(BC8(k1), BC8(q1), s);
        float m = s[0];
#pragma unroll
        for (int i = 1; i < 16; ++i) m = fmaxf(m, s[i]);
        q1 = put_ref(q1, swapmax(m), hi);
    }
    { LAS u32x4* qo = (LAS u32x4*)(lds + LQ + w * 2048) + lane; qo[0] = q0; qo[64] = q1; }
    arrive(sy + SY_Q); await(sy + SY_Q, 4);
    __builtin_amdgcn_s_setprio(0);
    bf16x8 Q[4][2];
    Q[0][0] = BC8(q0); Q[0][1] = BC8(q1);
#pragma unroll
    for (int i = 1; i < 4; ++i) { LAS const char* p = lds + LQ + ((w + i) & 3) * 2048 + voff; Q[i][0] = FR(p, 0); Q[i][1] = FR(p, 1024); }
    u32x4 kv[3][4] = {{k0, k1, v0, v1}, {}, {}};
    f32x16 O[4] = {}, sa, sb;
    u32x4 p0, p1;
#define GRP(SC, i, P, j) do { float e0 = EX(SC[i]); SBAR(); float e1 = EX(SC[i + 1]); SBAR(); float e2 = EX(SC[i + 2]); SBAR(); float e3 = EX(SC[i + 3]); SBAR(); \
        P[j] = pk_bf16(e0, e1); SBAR(); P[j + 1] = pk_bf16(e2, e3); PIN(P); SBAR(); } while (0)
#define SLOT(SC, SN, cb, i, QK, PV1, LD) do { constexpr int nb_ = (i) < 3 ? (cb) : ((cb) + 1) % 3, pb_ = (i) > 0 ? (cb) : ((cb) + 2) % 3; \
        if (QK) SN = MFMA_BF(BC8(kv[nb_][0]), Q[((i) + 1) & 3][0], zero); SBAR(); GRP(SC, 0, p0, 0); \
        if (PV1) O[((i) + 3) & 3] = MFMA_BF(BC8(kv[pb_][3]), BC8(p1), O[((i) + 3) & 3]); SBAR(); \
        if (LD) { kv[((cb) + 2) % 3][i] = __builtin_amdgcn_raw_buffer_load_b128(rs, voff + (i) * 1024, kvo, 16); SBAR(); } GRP(SC, 4, p0, 2); \
        if (QK) SN = MFMA_BF(BC8(kv[nb_][1]), Q[((i) + 1) & 3][1], SN); SBAR(); GRP(SC, 8, p1, 0); \
        O[i] = MFMA_BF(BC8(kv[cb][2]), BC8(p0), O[i]); SBAR(); GRP(SC, 12, p1, 2); } while (0)
    const unsigned kvb = WS_KV + (unsigned)(head * 64 + w) * 4096;
    unsigned kvo = 0;
    sa = MFMA_BF(BC8(k0), Q[0][0], zero); sa = MFMA_BF(BC8(k1), Q[0][1], sa);
    SLOT(sa, sb, 0, 0, true, false, false); SLOT(sb, sa, 0, 1, true, true, false);
    drain();
    put_flag(ws, FL_KV + (unsigned)((head * 4 + w) * 16 + grp) * FS, tag, lane);
    STAMP(4);
    SLOT(sa, sb, 0, 2, true, true, false); SLOT(sb, sa, 0, 3, false, true, false);
    STAMP(5);
    wait_flags<2>(ws, FL_KV + (unsigned)((head * 4 + w) * 16 + lane) * FS, tag, lane < 16 && lane != grp);
    STAMP(6);
#pragma unroll
    for (int j = 1; j < 3; ++j)
#pragma unroll
        for (int f = 0; f < 4; ++f) kv[j][f] = __builtin_amdgcn_raw_buffer_load_b128(rs, voff + f * 1024, kvb + ((grp + j) & 15) * 16384, 16);
    arrive(sy + SY_G);
    sa = MFMA_BF(BC8(kv[1][0]), Q[0][0], zero); sa = MFMA_BF(BC8(kv[1][1]), Q[0][1], sa);
    STAMP(7);
    int nx = grp + 3;
#define TILE(cb) do { kvo = kvb + (nx & 15) * 16384; ++nx; SBAR(); \
        SLOT(sa, sb, cb, 0, true, true, true); SLOT(sb, sa, cb, 1, true, true, true); SLOT(sa, sb, cb, 2, true, true, true); SLOT(sb, sa, cb, 3, true, true, true); } while (0)
#pragma unroll 1
    for (int it = 0; it < 5; ++it) { TILE(1); TILE(2); TILE(0); }
    O[3] = MFMA_BF(BC8(kv[0][3]), BC8(p1), O[3]);
    STAMP(8);
    f32x16 o = O[0];
    {
#pragma unroll
        for (int i = 1; i < 4; ++i) {
            LAS char* ex = lds + LX + (((w + i) & 3) * 3 + i - 1) * 4096 + voff;
#pragma unroll
            for (int g = 0; g < 4; ++g) *(LAS f32x4*)(ex + g * 1024) = (f32x4){O[i][4 * g], O[i][4 * g + 1], O[i][4 * g + 2], O[i][4 * g + 3]};
        }
        arrive(sy + SY_X); await(sy + SY_X, 4);
        LAS const char* er = lds + LX + w * 3 * 4096 + voff;
#pragma unroll
        for (int i = 0; i < 3; ++i)
#pragma unroll
            for (int g = 0; g < 4; ++g) { const f32x4 r = *(LAS const f32x4*)(er + i * 4096 + g * 1024);
#pragma unroll
                for (int e = 0; e < 4; ++e) o[4 * g + e] += r[e]; }
        f32x2 chk = {0.f, 0.f};
#pragma unroll
        for (int i = 0; i < 8; ++i) chk = pfma((f32x2){o[2 * i], o[2 * i + 1]}, (f32x2){0.f, 0.f}, chk);
        if (__builtin_expect(__any(!(chk[0] + chk[1] == 0.f) || !(o[12] < 1e30f)), 0)) {
            const unsigned hb = WS_KV + (unsigned)head * 64 * 4096 + voff;
            u32x4 qr = __builtin_bit_cast(u32x4, Q[0][1]); if (hi) { qr[2] = 0; qr[3] = 0; }
            float mm = -INFINITY;
            wait_flags<4>(ws, FL_KV + (unsigned)(head * 64 + lane) * FS, tag, true);
#pragma unroll 1
            for (int t = 0; t < 64; ++t) {
                f32x16 s = MFMA_BF(BC8(BLD(hb + t * 4096)), Q[0][0], zero); s = MFMA_BF(BC8(BLD(hb + t * 4096 + 1024)), BC8(qr), s);
#pragma unroll
                for (int i = 0; i < 16; ++i) mm = fmaxf(mm, s[i]);
            }
            qr = put_ref(qr, swapmax(mm), hi);
            o = zero;
#pragma unroll 1
            for (int t = 0; t < 64; ++t) {
                f32x16 s = MFMA_BF(BC8(BLD(hb + t * 4096)), Q[0][0], zero); s = MFMA_BF(BC8(BLD(hb + t * 4096 + 1024)), BC8(qr), s);
#pragma unroll
                for (int i = 0; i < 4; ++i) { p0[i] = pk_bf16(EX(s[2 * i]), EX(s[2 * i + 1])); p1[i] = pk_bf16(EX(s[8 + 2 * i]), EX(s[9 + 2 * i])); }
                o = MFMA_BF(BC8(BLD(hb + t * 4096 + 2048)), BC8(p0), o); o = MFMA_BF(BC8(BLD(hb + t * 4096 + 3072)), BC8(p1), o);
            }
        }
    }
    {
        const auto r = __builtin_amdgcn_permlane32_swap(__float_as_uint(o[12]), __float_as_uint(o[12]), false, false);
        const float rl = __builtin_amdgcn_rcpf(__uint_as_float(r[1]));
        u32x4 o0, o1;
#pragma unroll
        for (int i = 0; i < 4; ++i) { o0[i] = pk_bf16(o[2 * i] * rl, o[2 * i + 1] * rl); o1[i] = pk_bf16(o[8 + 2 * i] * rl, o[9 + 2 * i] * rl); }
        if (hi) { o1[2] = 0; o1[3] = 0; }
        const unsigned fi = (unsigned)((4 * grp + w) * 16 + head);
        BST(o0, WS_O + fi * 2048 + voff); BST(o1, WS_O + fi * 2048 + 1024 + voff);
        STAMP(9);
        drain();
        put_flag(ws, FL_O + fi * FS, tag, lane);
    }
    STAMP(10);
}

DI void role_b(const uint16_t* __restrict__ wo, const uint16_t* __restrict__ u, const uint16_t* __restrict__ lar, const uint16_t* __restrict__ aim,
               const uint16_t* __restrict__ cre, const uint16_t* __restrict__ cim, const uint16_t* __restrict__ ldt, const uint16_t* __restrict__ dskip,
               uint16_t* __restrict__ out, char* __restrict__ ws, LAS char* lds, int w, int lane, unsigned pf) {
    u64 tag;
    const unsigned lds0 = (unsigned)(uintptr_t)lds;
    LAS unsigned* sy = (LAS unsigned*)(lds + LSY);
    const __amdgpu_buffer_rsrc_t rs = __builtin_amdgcn_make_buffer_rsrc(ws, 0, WS_END, 0x00020000);
    const int b = blockIdx.x, xcd = b & 7, bi = b >> 3, r32 = lane & 31, hi = lane >> 5;
    const unsigned voff = lane * 16;
    LAS char* tab = lds + LB + w * TABW;
    const int qt = 8 * xcd + (bi >> 2), ct = 4 * (bi & 3) + w;
    bf16x8 wf[32];
#define LOADWF(i) wf[i] = BC8(__builtin_amdgcn_raw_buffer_load_b128(rs, voff, WS_WO + (unsigned)(((8 * w + (i)) & 31) * 14 + ct) * 1024, 16))
    if (xcd < 7) {
        const int cc = w >> 1, hh = w & 1, pair = 32 * xcd + bi, ch = 2 * pair + cc;
        LAS char* cb = lds + LB + cc * CBW;
        const unsigned po = (unsigned)(ch * NS + 128 * hh) / 2 + lane;
        const unsigned pl = ((const unsigned*)lar)[po], pa = ((const unsigned*)aim)[po], pr = ((const unsigned*)cre)[po], pi = ((const unsigned*)cim)[po];
        const unsigned uo = (unsigned)((64 * bi + 16 * w + (lane >> 3)) * D + 64 * xcd + (lane & 7) * 8) * 2;
        glds2(u, uo, uo + 8 * D * 2 - 1024, lds0 + LB + w * TABW + 4096);
        const u64 e0 = __hip_atomic_load((GAS u64*)ws, __ATOMIC_RELAXED, __HIP_MEMORY_SCOPE_AGENT);
        u32x2 gi[2][3];
#pragma unroll
        for (int ps = 0; ps < 2; ++ps)
#pragma unroll
            for (int i = 0; i < 3; ++i) gi[ps][i] = ((const u32x2*)g_idx.v[128 * hh + 64 * ps + lane])[i];
        const unsigned lw = ((const unsigned*)ldt)[pair], dw = ((const unsigned*)dskip)[pair];
        __builtin_amdgcn_s_setprio(PRIO_B);
        const float dt = rbf(expf(cc ? bf_hi(lw) : bf_lo(lw)));
        drain(); asm volatile("" :: "v"(pf));
        STAMP(1);
        arrive(sy + SY_WO);
        if (w == 0 && lane == 0) __hip_atomic_store((LAS u64*)sy, mix(e0), __ATOMIC_RELAXED, __HIP_MEMORY_SCOPE_WORKGROUP);
        await(sy + SY_WO, 4);
        {
            u32x4 o0, o1; transpose_tile(lds + LB + 4096, TABW, tab, sy + SY_T1, w, lane, o0, o1);
            const unsigned to = WS_UT + (unsigned)((64 * xcd + 16 * w + (lane >> 3)) * S + 64 * bi) * 2 + (lane & 7) * 16;
            __builtin_amdgcn_s_setprio(3); BST(o0, to); BST(o1, to + 8 * S * 2); __builtin_amdgcn_s_setprio(PRIO_B);
        }
        const float cm = wave_max(fmaxf(fmaxf(fabsf(bf_lo(pr)), fabsf(bf_hi(pr))), fmaxf(fabsf(bf_lo(pi)), fabsf(bf_hi(pi)))));
        unsigned ce = __float_as_uint(cm) & 0x7f800000u; ce = ce < 0x01000000u ? 0x01000000u : (ce > 0x7e800000u ? 0x7e800000u : ce);
        const float cscale = __uint_as_float(0x7f000000u - ce), oscale = 2.0f * dt * __uint_as_float(ce);
        LAS unsigned* tw = (LAS unsigned*)(tab + lane * 4);
        LAS const char* trd = tab + r32 * TROW + hi * 16;
        f32x16 dacc = {};
        f32x2 z1, z8, z256, x0;
#define ANCH(vl, va, vr, vi) do { const float a2 = (-rbf(expf(vl)) * dt) * 1.4426950408889634f, rev = ((va) * dt) * 0.15915494309189535f; \
            z1 = anchor(a2, -rev, 1.f); z8 = anchor(a2, rev, 8.f); z256 = anchor(a2, -rev, 256.f); x0 = (f32x2){(vr) * cscale, -(vi) * cscale}; \
            PIN(z1); PIN(z8); PIN(z256); SBAR(); } while (0)
#define TW(row) tw[(row) * (TROW / 4)]
#define CHAIN() do { \
            const f32x2 z1s = rot(z1), zs = rot(z256), z8s = rot(z8); \
            f32x2 y[4], X[8]; y[1] = z8; y[2] = cmul(z8, z8, z8s); X[0] = x0; X[1] = cmul(x0, z1, z1s); f32x2 a = cmul(x0, z256, zs); \
            y[3] = cmul(y[2], z8, z8s); const f32x2 z32 = cmul(y[2], y[2], rot(y[2])), z32s = rot(z32); X[2] = cmul(X[1], z1, z1s); y[0] = z32; \
            f32x2 c4 = cmul(X[2], z256, zs); \
            unsigned c[2][7], d0 = pk_f16(X[0]), d1 = pk_f16(X[1]), d2 = pk_f16(y[1]); \
            c[0][0] = pk_f16(y[0]); c[0][1] = 0x3c00u; c[0][2] = pk_f16(y[2]); c[0][3] = pk_f16(y[3]); c[0][4] = pk_f16(X[2]); c[0][5] = pk_f16(a); c[0][6] = pk_f16(c4); \
            TW(0) = d0; TW(1) = d1; TW(22) = d2; SBAR(); \
            _Pragma("unroll") for (int i = 1; i < 9; ++i) { \
                f32x2 t0, t1, t2, t3, t4, t5, t6; const int j = i - 1; unsigned* pc = c[j & 1]; unsigned* nc = c[i & 1]; \
                if (i < 8) { if (i > 1) t0 = y[0].xx * z32; t1 = y[1].xx * z32; t2 = y[2].xx * z32; t3 = y[3].xx * z32; \
                    if (i < 6) t4 = X[i + 1].xx * z1; t5 = a.xx * z256; \
                    if (i == 3) t6 = X[4].xx * z256; if (i == 4 || i == 5) t6 = c4.xx * z256; if (i == 6) t6 = X[6].xx * z256; } SBAR(); \
                if (j != 1) TW(21 + (j ? 4 * j : 4)) = pc[0]; TW(21 + (j ? 4 * j + 1 : 0)) = pc[1]; TW(23 + 4 * j) = pc[2]; TW(24 + 4 * j) = pc[3]; \
                if (j < 6) TW(j + 2) = pc[4]; TW(j == 0 ? 8 : (j == 1 ? 12 : (j == 2 ? 14 : 13 + j))) = pc[5]; \
                if (j == 0) TW(9) = pc[6]; if (j == 3) TW(10) = pc[6]; if (j == 4) TW(13) = pc[6]; if (j == 5) TW(15) = pc[6]; if (j == 6) TW(11) = pc[6]; SBAR(); \
                if (i < 8) { if (i > 1) y[0] = pfma(y[0].yy, z32s, t0); y[1] = pfma(y[1].yy, z32s, t1); y[2] = pfma(y[2].yy, z32s, t2); y[3] = pfma(y[3].yy, z32s, t3); \
                    if (i < 6) X[i + 2] = pfma(X[i + 1].yy, z1s, t4); a = pfma(a.yy, zs, t5); \
                    if (i == 3) c4 = pfma(X[4].yy, zs, t6); if (i == 4 || i == 5) c4 = pfma(c4.yy, zs, t6); if (i == 6) c4 = pfma(X[6].yy, zs, t6); SBAR(); \
                    if (i > 1) nc[0] = pk_f16(y[0]); nc[1] = pk_f16(y[1]); nc[2] = pk_f16(y[2]); nc[3] = pk_f16(y[3]); \
                    if (i < 6) nc[4] = pk_f16(X[i + 2]); nc[5] = pk_f16(a); if (i >= 3 && i <= 6) nc[6] = pk_f16(c4); \
                    asm volatile("" :: "v"(pc[0]), "v"(pc[1]), "v"(pc[2]), "v"(pc[3]), "v"(pc[4]), "v"(pc[5]), "v"(pc[6])); SBAR(); } \
            } } while (0)
        f16x8 fa[8], fb[8];
#define RDT() do { _Pragma("unroll") for (int kk = 0; kk < 8; ++kk) { fa[kk] = *(LAS const f16x8*)(trd + kk * 32); fb[kk] = *(LAS const f16x8*)(trd + 21 * TROW + kk * 32); } SBAR(); } while (0)
#define MMT() do { _Pragma("unroll") for (int kk = 0; kk < 8; ++kk) dacc = MFMA_F16(fa[kk], fb[kk], dacc); } while (0)
        ANCH(bf_lo(pl), bf_lo(pa), bf_lo(pr), bf_lo(pi));
        tag = get_tag(sy); await(sy + SY_T1, 4);
        STAMP(13);
        CHAIN(); RDT();
        drain(); __builtin_amdgcn_s_setprio(3); put_flag(ws, FL_UT + (unsigned)(xcd * 128 + w * 32 + bi) * FS, tag, lane); __builtin_amdgcn_s_setprio(PRIO_B);
        STAMP(14);
        ANCH(bf_hi(pl), bf_hi(pa), bf_hi(pr), bf_hi(pi));
        MMT(); SBAR();
        CHAIN(); RDT();
        const unsigned utf = hi ? FL_WO + (unsigned)(w * 32 + r32) * FS : FL_UT + (unsigned)(xcd * 128 + (bi >> 3) * 32 + r32) * FS;
        const u64 utv = __hip_atomic_load((GAS u64*)(ws + utf), __ATOMIC_RELAXED, __HIP_MEMORY_SCOPE_AGENT); SBAR();
        MMT();
        STAMP(2);
        {
            LAS float* pt = (LAS float*)tab;
            *(LAS f32x4*)(pt + 8 * r32 + 4 * hi) = (f32x4){dacc[0] * oscale, dacc[1] * oscale, dacc[2] * oscale, dacc[3] * oscale};
            *(LAS f32x2*)(pt + (hi ? 384 + 2 * r32 : 256 + 4 * r32)) = (f32x2){dacc[4] * oscale, dacc[5] * oscale};
            *(LAS f32x2*)(pt + (hi ? 448 + 2 * r32 : 258 + 4 * r32)) = (f32x2){dacc[6] * oscale, dacc[7] * oscale};
            pt[hi ? 640 + r32 : 512 + r32] = dacc[8] * oscale;
            pt[hi ? 672 + r32 : 544 + r32] = dacc[9] * oscale;
            pt[hi ? 672 + r32 : 576 + r32] = dacc[10] * oscale;
            pt[hi ? 672 + r32 : 608 + r32] = dacc[11] * oscale;
        }
        LAS unsigned* ps_ = sy + SY_P + 5 * cc;
        arrive(ps_ + 0);
        LAS char* ui = cb + UOF;
        LAS char* kt = cb + KOF;
        await(ps_ + 0, 2);
#pragma unroll
        for (int ps = 0; ps < 2; ++ps) {
            const int v = 81 * hh + 64 * ps + lane;
            if (ps == 0 || lane < 17) {
                const f32x4 p0 = *(LAS const f32x4*)(cb + v * 16), p1 = *(LAS const f32x4*)(cb + TABW + v * 16);
                u32x2 r = {pk_bf16(p0[0] + p1[0], p0[1] + p1[1]), pk_bf16(p0[2] + p1[2], p0[3] + p1[3])};
                if (v == 161) r = (u32x2){0u, 0u};
                *(LAS u32x2*)(kt + v * 8) = r;
            }
        }
        arrive(ps_ + 1);
        if (__any(utv != tag)) wait_flags<4>(ws, utf, tag, true);
        CFENCE();
        const unsigned ub = WS_UT + (unsigned)(ch * S + 1024 * hh) * 2 + voff;
        const u32x4 u0 = BLD(ub), u1 = BLD(ub + 1024); SBAR();
        await(ps_ + 1, 2);
        STAMP(3);
        __builtin_amdgcn_s_setprio(PRIO_B2);
        const u32x4 z4 = {};
        {
            unsigned W[2][6];
#pragma unroll
            for (int ps = 0; ps < 2; ++ps)
#pragma unroll
                for (int i = 0; i < 6; ++i) { const unsigned gv = gi[ps][i >> 1][i & 1];
                    W[ps][i] = *(LAS const uint16_t*)(kt + (gv & 0xffffu));
                    if (i < 5) W[ps][i] |= (unsigned)*(LAS const uint16_t*)(kt + (gv >> 16)) << 16; }
            *(LAS u32x4*)(ui + (64 * hh + lane) * 16) = z4;
            if (lane < 16) *(LAS u32x4*)(ui + (128 + 16 * hh + lane) * 16) = z4;
            if (lane < 8) *(LAS u32x4*)(cb + (2 * hh + (lane >> 2)) * CPY + (256 + (lane & 3)) * 16) = z4;
#pragma unroll
            for (int ps = 0; ps < 2; ++ps) {
                unsigned V[5];
#pragma unroll
                for (int i = 0; i < 5; ++i) V[i] = __builtin_amdgcn_alignbit(W[ps][i + 1], W[ps][i], 16);
                LAS char* cp = cb + (128 * hh + 64 * ps + lane) * 16;
                *(LAS u32x4*)(cp) = (u32x4){W[ps][0], W[ps][1], W[ps][2], W[ps][3]};
                *(LAS u32x4*)(cp + CPY) = (u32x4){V[0], V[1], V[2], V[3]};
                *(LAS u32x4*)(cp + 2 * CPY) = (u32x4){W[ps][1], W[ps][2], W[ps][3], W[ps][4]};
                *(LAS u32x4*)(cp + 3 * CPY) = (u32x4){V[1], V[2], V[3], V[4]};
            }
        }
        *(LAS u32x4*)(ui + (32 + 32 * hh + (lane >> 2)) * 80 + (lane & 3) * 16) = u0;
        *(LAS u32x4*)(ui + (48 + 32 * hh + (lane >> 2)) * 80 + (lane & 3) * 16) = u1;
        arrive(ps_ + 2); await(ps_ + 2, 2);
        STAMP(4);
        f32x16 a0 = {}, a1 = {};
        {
            const int sb = 7 - (r32 & 7);
            LAS const char* pa = cb + (sb & 3) * CPY + (255 - (r32 >> 3) + hi) * 16 + (sb >> 2) * 8 - 64 * (16 * hh + 15) - 2048;
            LAS const char* pa2 = pa + 8; asm volatile("" : "+v"(pa2));
            LAS const char* pb = ui + (32 + r32 - 16 * hh - 15) * 80 + 16 * hi;
            s16x4 ta[2][2], tb[2][2]; bf16x8 b0[2], b1[2];
#define T4(q, off) (*(LAS const s16x4*)((q) + (off)))
#define CSTEP(i, j, RD) do { const int o_ = 64 * (15 - ((j) >> 1)) + 32 * ((j) & 1), u_ = 80 * (15 - ((j) >> 1)) + 32 * ((j) & 1); \
                a1 = MFMA_BF(cat8(ta[i][0], ta[i][1]), b1[i], a1); SBAR(); if (RD) { ta[1 - i][0] = T4(pa, o_ + 2048); ta[1 - i][1] = T4(pa2, o_ + 2048); } SBAR(); \
                a0 = MFMA_BF(cat8(ta[i][0], ta[i][1]), b0[i], a0); SBAR(); if (RD) { b1[1 - i] = FR(pb, u_ + 2560); b0[1 - i] = FR(pb, u_); } SBAR(); \
                a1 = MFMA_BF(cat8(tb[i][0], tb[i][1]), b0[i], a1); SBAR(); if (RD) { tb[1 - i][0] = T4(pa, o_); tb[1 - i][1] = T4(pa2, o_); } SBAR(); } while (0)
            ta[0][0] = T4(pa, 960 + 2048); ta[0][1] = T4(pa2, 960 + 2048); b1[0] = FR(pb, 1200 + 2560); b0[0] = FR(pb, 1200); tb[0][0] = T4(pa, 960); tb[0][1] = T4(pa2, 960);
#pragma unroll
            for (int j = 0; j < 32; ++j) {
                if (j == 31) arrive(ps_ + 3);
                CSTEP(j & 1, j + 1, j < 31);
                if (ct < 14) LOADWF(j);
                SBAR();
            }
        }
        STAMP(5);
        {
            u32x2 uu[4];
#pragma unroll
            for (int g = 0; g < 4; ++g) uu[g] = *(LAS const u32x2*)(ui + (32 + 32 * hh + r32) * 80 + 16 * g + 8 * hi);
            LAS char* ex = cb + r32 * 144 + hi * 16;
            const f32x16 snd = hh ? a0 : a1; const f32x16 y = hh ? a1 : a0;
            await(ps_ + 3, 2);
#pragma unroll
            for (int g = 0; g < 4; ++g) *(LAS f32x4*)(ex + hh * 4608 + g * 32) = (f32x4){snd[4 * g], snd[4 * g + 1], snd[4 * g + 2], snd[4 * g + 3]};
            arrive(ps_ + 4);
            const float dsk = cc ? bf_hi(dw) : bf_lo(dw);
            unsigned sk[8];
#pragma unroll
            for (int g = 0; g < 4; ++g)
#pragma unroll
                for (int e = 0; e < 2; ++e) sk[2 * g + e] = pk_bf16(bf_lo(uu[g][e]) * dsk, bf_hi(uu[g][e]) * dsk);
            await(ps_ + 4, 2);
            LAS char* sc = cb + 9216 + hh * 2048;
#pragma unroll
            for (int g = 0; g < 4; ++g) {
                const f32x4 p = *(LAS const f32x4*)(ex + (1 - hh) * 4608 + g * 32);
                u32x2 r;
#pragma unroll
                for (int e = 0; e < 2; ++e) {
                    const unsigned yb = pk_bf16(y[4 * g + 2 * e] + p[2 * e], y[4 * g + 2 * e + 1] + p[2 * e + 1]);
                    r[e] = pk_bf16(bf_lo(yb) + bf_lo(sk[2 * g + e]), bf_hi(yb) + bf_hi(sk[2 * g + e]));
                }
                *(LAS u32x2*)(sc + 64 * r32 + 16 * g + 8 * hi) = r;
            }
            const u32x4 o0 = *(LAS const u32x4*)(sc + voff), o1 = *(LAS const u32x4*)(sc + 1024 + voff);
            const unsigned bo = WS_BT + (unsigned)(ch * S + 1024 * hh) * 2 + voff;
            BST(o0, bo); BST(o1, bo + 1024);
        }
        arrive(sy + SY_BG);
        STAMP(6);
        drain();
        put_flag(ws, FL_BT + (unsigned)(xcd * 128 + bi * 4 + w) * FS, tag, lane);
    } else {
        {
            const unsigned nb = (bi & 1) ? 12 * 2 * D : 16 * 2 * D;
            unsigned v4[4];
#pragma unroll
            for (int i = 0; i < 4; ++i) { const unsigned o = (unsigned)(w * 4096 + i * 1024) + voff; v4[i] = (o < nb ? o : 0u) + 4096 - i * 1024; }
            glds4((const char*)(wo + (DH * (bi >> 1) + 16 * (bi & 1)) * D) - 4096, v4[0], v4[1], v4[2], v4[3], lds0 + LB + w * 4096);
        }
        const u64 e0 = __hip_atomic_load((GAS u64*)ws, __ATOMIC_RELAXED, __HIP_MEMORY_SCOPE_AGENT);
        __builtin_amdgcn_s_setprio(0);
        drain(); asm volatile("" :: "v"(pf));
        if (w == 0 && lane == 0) __hip_atomic_store((LAS u64*)sy, mix(e0), __ATOMIC_RELAXED, __HIP_MEMORY_SCOPE_WORKGROUP);
        arrive(sy + SY_WO); await(sy + SY_WO, 4);
        LAS const char* p = lds + LB + (4 * hi + ((lane & 15) >> 2)) * (2 * D) + ((lane >> 4) & 1) * 32 + (lane & 3) * 8;
#pragma unroll
        for (int i = 0; i < 4; ++i) {
            const int ct = w + 4 * i;
            if (ct < 14) {
                u32x4 o = __builtin_bit_cast(u32x4, cat8(trread(p + ct * 64), trread(p + 8 * 2 * D + ct * 64)));
                if (hi && (bi & 1)) { o[2] = 0; o[3] = 0; }
                BST(o, WS_WO + (unsigned)(bi * 14 + ct) * 1024 + voff);
            }
        }
        tag = get_tag(sy);
        drain();
        put_flag(ws, FL_WO + (unsigned)(w * 32 + bi) * FS, tag, lane);
        arrive(sy + SY_BG);
        await_slow(sy + SY_G, 4);
        wait_flags<8>(ws, FL_WO + (unsigned)(w * 32 + r32) * FS, tag, !hi);
        if (ct < 14) {
#pragma unroll
            for (int i = 0; i < 32; ++i) { LOADWF(i); __builtin_amdgcn_s_sleep(WOSL); SBAR(); }
        }
    }
    STAMP(7);
    const __amdgpu_buffer_rsrc_t ro = __builtin_amdgcn_make_buffer_rsrc(out, 0, S * 2 * D * 2, 0x00020000);
    if (xcd < 7) {
        wait_flags<16>(ws, FL_BT + (unsigned)(xcd * 128 + w * 32 + r32) * FS, tag, !hi);
        await(sy + SY_BG, 4);
        const unsigned bo = WS_BT + (unsigned)((64 * xcd + 16 * w + (lane >> 3)) * S + 64 * bi + (lane & 7) * 8) * 2;
        glds2c(ws, bo, bo + 8 * S * 2 - 1024, lds0 + LB + 32768 + w * 2048);
        drain();
        arrive(sy + SY_T2); await(sy + SY_T2, 4);
        u32x4 o0, o1; transpose_tile(lds + LB + 32768, 2048, lds + LB + 40960 + w * 2304, nullptr, w, lane, o0, o1);
        const unsigned oo = (unsigned)((64 * bi + 16 * w + (lane >> 3)) * 2 * D + D + 64 * xcd) * 2 + (lane & 7) * 16;
        __builtin_amdgcn_raw_buffer_store_b128(o0, ro, oo, 0, 16); __builtin_amdgcn_raw_buffer_store_b128(o1, ro, oo + 8 * 2 * D * 2, 0, 16);
    } else await(sy + SY_BG, 4);
    STAMP(8);
    await_slow(sy + SY_X, 4);
    wait_flags<2>(ws, FL_O + (unsigned)(qt * 16 + 4 * w + lane) * FS, tag, lane < 4);
    STAMP(9);
    u32x4 of[8];
#pragma unroll
    for (int i = 0; i < 8; ++i) of[i] = BLD(WS_O + (unsigned)(qt * 32 + 8 * w + i) * 1024 + voff);
#pragma unroll
    for (int i = 0; i < 8; ++i) *(LAS u32x4*)(lds + LB + (8 * w + i) * 1024 + voff) = of[i];
    arrive(sy + SY_B2);
    STAMP(10);
    f32x16 acc = {};
    if (ct < 14) {
#pragma unroll
        for (int i = 0; i < 8; ++i) acc = MFMA_BF(wf[i], BC8(of[i]), acc);
    }
    SBAR();
    await(sy + SY_B2, 4);
    STAMP(11);
    if (ct < 14) {
        LAS const char* po[3];
#pragma unroll
        for (int j = 0; j < 3; ++j) po[j] = lds + LB + ((w + 1 + j) & 3) * 8192 + voff;
        bf16x8 og[4];
#pragma unroll
        for (int i = 0; i < 3; ++i) og[i] = FR(po[0], i * 1024);
#pragma unroll
        for (int i = 0; i < 24; ++i) { if (i < 21) og[(i + 3) & 3] = FR(po[(i + 3) >> 3], ((i + 3) & 7) * 1024); SBAR(); acc = MFMA_BF(wf[8 + i], og[i & 3], acc); SBAR(); }
        LAS char* sc = lds + LB + 40960 + w * 2304;
#pragma unroll
        for (int g = 0; g < 4; ++g) *(LAS u32x2*)(sc + r32 * 72 + 16 * g + 8 * hi) = (u32x2){pk_bf16(acc[4 * g], acc[4 * g + 1]), pk_bf16(acc[4 * g + 2], acc[4 * g + 3])};
        LAS const char* sr = sc + (lane >> 2) * 72 + (lane & 3) * 16;
        const u32x2 a0 = *(LAS const u32x2*)sr, a1 = *(LAS const u32x2*)(sr + 8), b0 = *(LAS const u32x2*)(sr + 16 * 72), b1 = *(LAS const u32x2*)(sr + 16 * 72 + 8);
        const unsigned oo = (unsigned)((32 * qt + (lane >> 2)) * 2 * D + 32 * ct) * 2 + (lane & 3) * 16;
        __builtin_amdgcn_raw_buffer_store_b128((u32x4){a0[0], a0[1], a1[0], a1[1]}, ro, oo, 0, 16);
        __builtin_amdgcn_raw_buffer_store_b128((u32x4){b0[0], b0[1], b1[0], b1[1]}, ro, oo + 16 * 2 * D * 2, 0, 16);
    }
    if (b == 0 && w == 0) put_flag(ws, 0, tag, lane);
    STAMP(12);
}

__global__ __launch_bounds__(512, 2) void fused(const uint16_t* __restrict__ x, const uint16_t* __restrict__ wqkv, const uint16_t* __restrict__ wo,
                                               const uint16_t* __restrict__ u, const uint16_t* __restrict__ lar, const uint16_t* __restrict__ aim,
                                               const uint16_t* __restrict__ cre, const uint16_t* __restrict__ cim, const uint16_t* __restrict__ ldt,
                                               const uint16_t* __restrict__ dskip, uint16_t* __restrict__ out, char* __restrict__ ws) {
    extern __shared__ __attribute__((aligned(16))) char smem[];
    LAS char* lds = (LAS char*)smem;
    const int lane = threadIdx.x & 63, w = __builtin_amdgcn_readfirstlane(threadIdx.x >> 6);
    STAMP(0);
    unsigned pf = 0;
    if (w >= 4) __builtin_amdgcn_s_setprio(3);
    if (w == 4) ((LAS unsigned*)(lds + LSY))[lane] = 0;
    asm volatile("" :: "s"(x), "s"(wqkv), "s"(wo), "s"(u), "s"(lar), "s"(aim), "s"(cre), "s"(cim), "s"(ldt), "s"(dskip), "s"(out), "s"(ws));
    asm volatile("s_waitcnt lgkmcnt(0)\n\ts_barrier" ::: "memory");
    if (w < 4) role_a(x, wqkv, ws, lds, w, lane, pf);
    else role_b(wo, u, lar, aim, cre, cim, ldt, dskip, out, ws, lds, w - 4, lane, pf);
}
}

extern "C" void kernel_launch(void* const* d_in, const int* in_sizes, int n_in, void* d_out, int out_size, void* d_ws, size_t ws_size, hipStream_t stream) {
    using namespace k;
    (void)hipFuncSetAttribute((const void*)fused, hipFuncAttributeMaxDynamicSharedMemorySize, LDS_BYTES);
    const uint16_t* const* in = (const uint16_t* const*)d_in;
    fused<<<256, 512, LDS_BYTES, stream>>>(in[0], in[1], in[2], in[3], in[4], in[5], in[6], in[7], in[8], in[9], (uint16_t*)d_out, (char*)d_ws);
}
```

```cpp
#define KVM 6u
#include <hip/hip_runtime.h>
#include <hip/hip_bf16.h>
#include <stdint.h>

typedef short bf16x8 __attribute__((ext_vector_type(8)));
typedef _Float16 f16x8 __attribute__((ext_vector_type(8)));
typedef _Float16 f16x2 __attribute__((ext_vector_type(2)));
typedef __bf16 bf16x2 __attribute__((ext_vector_type(2)));
typedef short s16x4 __attribute__((ext_vector_type(4)));
typedef float f32x16 __attribute__((ext_vector_type(16)));
typedef float f32x4 __attribute__((ext_vector_type(4)));
typedef float f32x2 __attribute__((ext_vector_type(2)));
typedef unsigned u32x4 __attribute__((ext_vector_type(4)));
typedef unsigned u32x2 __attribute__((ext_vector_type(2)));
typedef unsigned long long u64;
#define LAS __attribute__((address_space(3)))
#define GAS __attribute__((address_space(1)))
#define DI __device__ __forceinline__
#define MFMA_BF(a, b, c) __builtin_amdgcn_mfma_f32_32x32x16_bf16((a), (b), (c), 0, 0, 0)
#define MFMA_F16(a, b, c) __builtin_amdgcn_mfma_f32_32x32x16_f16((a), (b), (c), 0, 0, 0)
#define SBAR() __builtin_amdgcn_sched_barrier(0)
#define CFENCE() asm volatile("" ::: "memory")
#define PIN(x) asm volatile("" : "+v"(x))
#define BC8(x) __builtin_bit_cast(bf16x8, (x))
#define EX(x) __builtin_amdgcn_exp2f(x)
#define PRIO_A1 0
#define PRIO_B 0
#define PRIO_B2 1
#define ROT 1
#define HOLDB 0
#define PRIO_B3 1
#define PRIO_BC 0
#define WOSL 2

namespace k {
#define STAMP(i)
constexpr int S = 2048, D = 448, DH = 28, NS = 256;
constexpr unsigned FS = 128;
constexpr unsigned FL_KV = FS, FL_O = FL_KV + 1024 * FS, FL_UT = FL_O + 1024 * FS, FL_BT = FL_UT + 896 * FS, FL_WO = FL_BT + 896 * FS;
constexpr unsigned WS_KV = 512u << 10;
constexpr unsigned WS_O = WS_KV + (4u << 20);
constexpr unsigned WS_WO = WS_O + (2u << 20);
constexpr unsigned WS_UT = WS_WO + (448u << 10);
constexpr unsigned WS_BT = WS_UT + D * S * 2;
constexpr unsigned WS_END = WS_BT + D * S * 2;
constexpr int WSL = 12288, LXR = 4 * WSL, XSL = 16384, LB = LXR + 3 * XSL;
constexpr int LQ = 0, LX = 8192;
constexpr int TROW = 272, TABW = 53 * TROW, CBW = 2 * TABW, CPY = 4160, UOF = 4 * CPY, KOF = CBW - 1312;
constexpr int LSY = LB + 2 * CBW, LDS_BYTES = LSY + 256;
static_assert(LDS_BYTES <= 163840 && (LSY & 15) == 0 && UOF + 7680 <= KOF, "lds");
enum { SY_TAG = 0, SY_F = 2, SY_P = 10, SY_WO = 20, SY_T1 = 21, SY_BG = 22, SY_B2 = 23, SY_Q = 24, SY_X = 25, SY_G = 26, SY_T2 = 27, SY_H = 28 };
constexpr int SPIN = 1 << 18;

struct IdxTab { uint16_t v[256][12]; };
constexpr IdxTab make_idx() {
    IdxTab t = {};
    for (int y = 0; y < 256; ++y) for (int e = 0; e < 12; ++e) {
        const int j = 2047 - 8 * y - e; int ix = 644;
        if (j >= 0) { const int sh = j >= 1024 ? 3 : (j >= 512 ? 2 : (j >= 256 ? 1 : 0));
            ix = sh ? 128 * sh + ((j + (1 << (sh - 1)) - 1 + ((j >> sh) & 1)) >> sh) : j; }
        t.v[y][e] = (uint16_t)(2 * ix);
    }
    return t;
}
__device__ const IdxTab g_idx __attribute__((aligned(16))) = make_idx();

DI unsigned pk_bf16(float a, float b) { f32x2 v = {a, b}; return __builtin_bit_cast(unsigned, __builtin_convertvector(v, bf16x2)); }
DI unsigned pk_f16(f32x2 v) { return __builtin_bit_cast(unsigned, __builtin_convertvector(v, f16x2)); }
DI float bf_lo(unsigned w) { return __uint_as_float(w << 16); }
DI float bf_hi(unsigned w) { return __uint_as_float(w & 0xffff0000u); }
DI float rbf(float x) { return bf_lo(pk_bf16(x, x)); }
#define GL0 "s_mov_b32 %0, m0\n\ts_mov_b32 m0, %2\n\ts_nop 0\n\t"
#define GL1(V, O, C) "global_load_lds_dwordx4 " V ", %1 offset:" #O C "\n\t"
#define GLE "s_mov_b32 m0, %0"
DI void glds4(const void* sb, unsigned v0, unsigned v1, unsigned v2, unsigned v3, unsigned lb) { unsigned sv;
    asm volatile(GL0 GL1("%3", 0, "") GL1("%4", 1024, "") GL1("%5", 2048, "") GL1("%6", 3072, "") GLE : "=&s"(sv) : "s"(sb), "s"(lb), "v"(v0), "v"(v1), "v"(v2), "v"(v3) : "memory"); }
DI void glds3(const void* sb, unsigned v0, unsigned v1, unsigned v2, unsigned lb) { unsigned sv;
    asm volatile(GL0 GL1("%3", 0, "") GL1("%4", 1024, "") GL1("%5", 2048, "") GLE : "=&s"(sv) : "s"(sb), "s"(lb), "v"(v0), "v"(v1), "v"(v2) : "memory"); }
DI void glds2(const void* sb, unsigned v0, unsigned v1, unsigned lb) { unsigned sv;
    asm volatile(GL0 GL1("%3", 0, "") GL1("%4", 1024, "") GLE : "=&s"(sv) : "s"(sb), "s"(lb), "v"(v0), "v"(v1) : "memory"); }
DI void glds2c(const void* sb, unsigned v0, unsigned v1, unsigned lb) { unsigned sv;
    asm volatile(GL0 GL1("%3", 0, " sc1") GL1("%4", 1024, " sc1") GLE : "=&s"(sv) : "s"(sb), "s"(lb), "v"(v0), "v"(v1) : "memory"); }
DI float wave_max(float v) {
#define DPPMAX(ctrl, rm) v = fmaxf(v, __uint_as_float(__builtin_amdgcn_update_dpp(0, __float_as_uint(v), ctrl, rm, 0xf, true)))
    DPPMAX(0x111, 0xf); DPPMAX(0x112, 0xf); DPPMAX(0x114, 0xf); DPPMAX(0x118, 0xf); DPPMAX(0x142, 0xa); DPPMAX(0x143, 0xc);
    return __uint_as_float(__builtin_amdgcn_readlane(__float_as_uint(v), 63));
}
DI s16x4 trread(LAS const char* p) { return __builtin_bit_cast(s16x4, __builtin_amdgcn_ds_read_tr16_b64_v4i16((LAS s16x4*)p)); }
DI bf16x8 cat8(s16x4 a, s16x4 b) { return __builtin_shufflevector(a, b, 0, 1, 2, 3, 4, 5, 6, 7); }
DI f32x2 pfma(f32x2 a, f32x2 b, f32x2 c) { return __builtin_elementwise_fma(a, b, c); }
DI f32x2 cmul(f32x2 a, f32x2 b, f32x2 bs) { return pfma(a.yy, bs, a.xx * b); }
DI f32x2 rot(f32x2 b) { return (f32x2){-b.y, b.x}; }
DI f32x2 anchor(float a2, float rev, float s) {
    const float mag = EX(a2 * s);
    float r = rev * s; r -= __builtin_rintf(r);
    return (f32x2){mag * __builtin_amdgcn_cosf(r), mag * __builtin_amdgcn_sinf(r)};
}
DI void arrive(LAS unsigned* c) { asm volatile("s_mov_b64 exec, 1\n\tds_add_u32 %0, %1\n\ts_mov_b64 exec, -1" :: "v"((unsigned)(uintptr_t)c), "v"(1u) : "memory"); }
DI void await(LAS unsigned* c, unsigned n) {
    unsigned t, i = SPIN;
    asm volatile("1:\n\tds_read_b32 %0, %2\n\ts_waitcnt lgkmcnt(0)\n\tv_cmp_gt_u32 vcc, %3, %0\n\ts_cbranch_vccz 2f\n\ts_sleep 1\n\ts_sub_u32 %1, %1, 1\n\ts_cbranch_scc0 1b\n2:"
                 : "=&v"(t), "+s"(i) : "v"((unsigned)(uintptr_t)c), "s"(n) : "vcc", "scc", "memory");
}
DI void await_slow(LAS unsigned* c, unsigned n) {
    unsigned t, i = SPIN;
    asm volatile("1:\n\tds_read_b32 %0, %2\n\ts_waitcnt lgkmcnt(0)\n\tv_cmp_gt_u32 vcc, %3, %0\n\ts_cbranch_vccz 2f\n\ts_sleep 3\n\ts_sub_u32 %1, %1, 1\n\ts_cbranch_scc0 1b\n2:"
                 : "=&v"(t), "+s"(i) : "v"((unsigned)(uintptr_t)c), "s"(n) : "vcc", "scc", "memory");
}
DI u64 get_tag(LAS unsigned* sy) {
    await(sy, 1);
    const u64 v = __hip_atomic_load((LAS u64*)sy, __ATOMIC_RELAXED, __HIP_MEMORY_SCOPE_WORKGROUP);
    return ((u64)(unsigned)__builtin_amdgcn_readfirstlane((unsigned)(v >> 32)) << 32) | (unsigned)__builtin_amdgcn_readfirstlane((unsigned)v);
}
DI void drain() { asm volatile("s_waitcnt vmcnt(0)" ::: "memory"); }
DI void put_flag(char* ws, unsigned off, u64 tag, int lane) { if (lane == 0) __hip_atomic_store((GAS u64*)(ws + off), tag, __ATOMIC_RELAXED, __HIP_MEMORY_SCOPE_AGENT); }
template <int SLP> DI void wait_flags(char* ws, unsigned off, u64 tag, bool pend) {
    GAS u64* p = (GAS u64*)(ws + off);
    CFENCE();
#pragma unroll 1
    for (int i = 0; i < SPIN; ++i) {
        if (pend) pend = __hip_atomic_load(p, __ATOMIC_RELAXED, __HIP_MEMORY_SCOPE_AGENT) != tag;
        if (!__any(pend)) break;
        __builtin_amdgcn_s_sleep(SLP);
    }
    CFENCE();
}
#define BST(v, off) __builtin_amdgcn_raw_buffer_store_b128((v), rs, (off), 0, 16)
#define BLD(off) __builtin_amdgcn_raw_buffer_load_b128(rs, (off), 0, 16)
#define FR(p, off) (*(LAS const bf16x8*)((p) + (off)))
DI float swapmax(float m) { const auto r = __builtin_amdgcn_permlane32_swap(__float_as_uint(m), __float_as_uint(m), false, false); return fmaxf(__uint_as_float(r[0]), __uint_as_float(r[1])); }
DI u32x4 put_ref(u32x4 q1, float m, int hi) {
    const float m1 = rbf(m), m2 = rbf(m - m1), m3 = m - m1 - m2;
    if (hi) { q1[2] = pk_bf16(-m1, -m2); q1[3] = pk_bf16(-m3, 0.f); }
    return q1;
}
DI u64 mix(u64 e) { u64 z = e + 0x9E3779B97F4A7C15ull; z = (z ^ (z >> 30)) * 0xBF58476D1CE4E5B9ull; z = (z ^ (z >> 27)) * 0x94D049BB133111EBull; return (z ^ (z >> 31)) | 1ull; }
DI void transpose_tile(LAS const char* stage, int pst, LAS char* scratch, LAS unsigned* done, int w, int lane, u32x4& o0, u32x4& o1) {
    LAS const char* p = stage + (lane >> 4) * pst + ((lane & 15) >> 2) * 128 + w * 32 + (lane & 3) * 8;
    const bf16x8 t0 = cat8(trread(p), trread(p + 512)), t1 = cat8(trread(p + 1024), trread(p + 1536));
    if (done) arrive(done);
    LAS char* sc = scratch + (lane & 15) * 144 + (lane >> 4) * 32;
    *(LAS bf16x8*)sc = t0; *(LAS bf16x8*)(sc + 16) = t1;
    LAS const char* sr = scratch + (lane >> 3) * 144 + (lane & 7) * 16;
    o0 = *(LAS const u32x4*)sr; o1 = *(LAS const u32x4*)(sr + 8 * 144);
}

DI void role_a(const uint16_t* __restrict__ x, const uint16_t* __restrict__ wqkv, uint16_t* __restrict__ out, char* __restrict__ ws, LAS char* lds, int w, int lane) {
    const int head = 4 * (blockIdx.x & 3) + ((blockIdx.x >> 3) & 3), grp = 8 * ((blockIdx.x >> 2) & 1) + (blockIdx.x >> 5);
    const unsigned lds0 = (unsigned)(uintptr_t)lds;
    const int r32 = lane & 31, hi = lane >> 5;
    const int wsh = (head & 7) == 7 ? 4 : 0;
    const unsigned wv = (unsigned)((lane >> 2) * 3 * D + (lane & 3) * 8) * 2;
    const char* wb = (const char*)(wqkv + DH * head - wsh + (16 * w) * 3 * D) - 2048;
    const unsigned xv0 = (unsigned)((lane >> 3) * D + (((lane & 7) ^ (lane >> 4)) * 8)) * 2, xv1 = xv0 ^ 64;
    const char* xb = (const char*)(x + (128 * grp + 32 * w) * D);
    const int rho = ROT ? ((blockIdx.x >> 3) + ((blockIdx.x >> 1) & 3)) % 7 : 0;
#define SP(st) ((st) + rho >= 7 ? (st) + rho - 7 : (st) + rho)
#define LOADW(st) glds3(wb + SP(st) * (64 * 3 * D * 2), wv + 2048, wv + 2 * D + 1024, wv + 4 * D, lds0 + ((st) & 3) * WSL + w * 3072)
#define LOADX(st) glds4(xb + SP(st) * 128, xv0, xv1 + 16 * D - 1024, xv0 + 32 * D - 2048, xv1 + 48 * D - 3072, lds0 + LXR + ((st) % 3) * XSL + w * 4096)
    LOADW(0); LOADX(0);
    STAMP(1);
    LOADW(1); LOADX(1); LOADW(2); LOADX(2);
    __builtin_amdgcn_s_setprio(PRIO_A1);
    LAS unsigned* sy = (LAS unsigned*)(lds + LSY);
    const __amdgpu_buffer_rsrc_t rs = __builtin_amdgcn_make_buffer_rsrc(ws, 0, WS_END, 0x00020000);
    const int xs = (r32 >> 1) & 7;
    LAS const char* xp[4];
#pragma unroll
    for (int ks = 0; ks < 4; ++ks) xp[ks] = lds + LXR + w * 4096 + r32 * 128 + (((2 * ks + hi) ^ xs) * 16);
    LAS const char* wp = lds + (8 * hi + ((lane & 15) >> 2)) * 64 + ((lane >> 4) & 1) * 32 + (lane & 3) * 8 + wsh * 2;
    f32x16 aq = {}, ak = {}, av = {};
    bf16x8 xf[2][4]; s16x4 fr[4][6];
#define RX(st, ks) xf[(st) & 1][ks] = FR(xp[ks], ((st) % 3) * XSL)
#define RW(st, ks, j) fr[ks][j] = trread(wp + ((st) & 3) * WSL + (ks) * 3072 + ((((j) >> 1) + 1) % 3) * 1024 + ((j) & 1) * 256)
#define MK(st, ks) ak = MFMA_BF(cat8(fr[ks][0], fr[ks][1]), xf[(st) & 1][ks], ak); SBAR()
#define MV(st, ks) av = MFMA_BF(xf[(st) & 1][ks], cat8(fr[ks][2], fr[ks][3]), av); SBAR()
#define MQ(st, ks) aq = MFMA_BF(cat8(fr[ks][4], fr[ks][5]), xf[(st) & 1][ks], aq); SBAR()
#define QSTEP(st, ks) MK(st, ks); RW(st, ks + 1, 0); RW(st, ks + 1, 1); SBAR(); MV(st, ks); RW(st, ks + 1, 2); RW(st, ks + 1, 3); SBAR(); MQ(st, ks); RW(st, ks + 1, 4); RW(st, ks + 1, 5); SBAR()
    u64 tag = 0;
    asm volatile("s_waitcnt vmcnt(14)" ::: "memory");
    arrive(sy + SY_F); await(sy + SY_F, 4);
    STAMP(16);
    RX(0, 0); RW(0, 0, 0); RW(0, 0, 1); RW(0, 0, 2); RW(0, 0, 3); RW(0, 0, 4); RW(0, 0, 5); RX(0, 1); RX(0, 2); RX(0, 3); SBAR();
#pragma unroll
    for (int st = 0; st < 7; ++st) {
        QSTEP(st, 0); if (st < 4) LOADW(st + 3);
        QSTEP(st, 1); if (st < 4) LOADX(st + 3);
        QSTEP(st, 2);
        if (st < 4) asm volatile("s_waitcnt vmcnt(14)" ::: "memory"); else if (st == 4) asm volatile("s_waitcnt vmcnt(7)" ::: "memory"); else if (st == 5) drain();
        arrive(sy + SY_F + 1 + st); SBAR();
        MK(st, 3); await(sy + SY_F + 1 + st, 4);
        if (st < 6) { RX(st + 1, 0); RW(st + 1, 0, 0); RW(st + 1, 0, 1); RW(st + 1, 0, 2); RW(st + 1, 0, 3); } SBAR();
        MV(st, 3); if (st < 6) { RW(st + 1, 0, 4); RW(st + 1, 0, 5); RX(st + 1, 1); } SBAR();
        MQ(st, 3); if (st < 6) { RX(st + 1, 2); RX(st + 1, 3); } SBAR();
        if (st == 2) tag = get_tag(sy);
    }
    STAMP(3);
    const float cq = 0.18898223650461362f * 1.4426950408889634f;
    const unsigned voff = lane * 16;
    u32x4 q0, q1, k0, k1, v0, v1;
    const unsigned tb = WS_KV + (unsigned)(head * 64 + 4 * grp + w) * 4096 + voff;
#pragma unroll
    for (int i = 0; i < 4; ++i) { k0[i] = pk_bf16(ak[2 * i], ak[2 * i + 1]); k1[i] = pk_bf16(ak[8 + 2 * i], ak[9 + 2 * i]); }
    if (hi) { k1[2] = 0x3f803f80u; k1[3] = 0x00003f80u; }
    BST(k0, tb); BST(k1, tb + 1024);
#pragma unroll
    for (int i = 0; i < 4; ++i) {
        v0[i] = pk_bf16(av[2 * i], av[2 * i + 1]); v1[i] = pk_bf16(av[8 + 2 * i], av[9 + 2 * i]);
        if (r32 >= DH) { v0[i] = v1[i] = (r32 == DH ? 0x3f803f80u : 0u); }
    }
    BST(v0, tb + 2048); BST(v1, tb + 3072);
#pragma unroll
    for (int i = 0; i < 4; ++i) { q0[i] = pk_bf16(aq[2 * i] * cq, aq[2 * i + 1] * cq); q1[i] = pk_bf16(aq[8 + 2 * i] * cq, aq[9 + 2 * i] * cq); }
    if (hi) { q1[2] = 0; q1[3] = 0; }
    const f32x16 zero = {};
    {
        f32x16 s = MFMA_BF(BC8(k0), BC8(q0), zero); s = MFMA_BF(BC8(k1), BC8(q1), s);
        float m = s[0];
#pragma unroll
        for (int i = 1; i < 16; ++i) m = fmaxf(m, s[i]);
        q1 = put_ref(q1, swapmax(m), hi);
    }
    { LAS u32x4* qo = (LAS u32x4*)(lds + LQ + w * 2048) + lane; qo[0] = q0; qo[64] = q1; }
    arrive(sy + SY_Q); await(sy + SY_Q, 4);
    __builtin_amdgcn_s_setprio(0);
    bf16x8 Q[4][2];
    Q[0][0] = BC8(q0); Q[0][1] = BC8(q1);
#pragma unroll
    for (int i = 1; i < 4; ++i) { LAS const char* p = lds + LQ + ((w + i) & 3) * 2048 + voff; Q[i][0] = FR(p, 0); Q[i][1] = FR(p, 1024); }
    u32x4 kv[3][4] = {{k0, k1, v0, v1}, {}, {}};
    f32x16 O[4] = {}, sa, sb;
    u32x4 p0, p1;
#define GRP(SC, i, P, j) do { float e0 = EX(SC[i]); SBAR(); float e1 = EX(SC[i + 1]); SBAR(); float e2 = EX(SC[i + 2]); SBAR(); float e3 = EX(SC[i + 3]); SBAR(); \
        P[j] = pk_bf16(e0, e1); SBAR(); P[j + 1] = pk_bf16(e2, e3); PIN(P); SBAR(); } while (0)
#define SLOT(SC, SN, cb, i, QK, PV1, LD) do { constexpr int nb_ = (i) < 3 ? (cb) : ((cb) + 1) % 3, pb_ = (i) > 0 ? (cb) : ((cb) + 2) % 3; \
        if (QK) SN = MFMA_BF(BC8(kv[nb_][0]), Q[((i) + 1) & 3][0], zero); SBAR(); GRP(SC, 0, p0, 0); \
        if (PV1) O[((i) + 3) & 3] = MFMA_BF(BC8(kv[pb_][3]), BC8(p1), O[((i) + 3) & 3]); SBAR(); \
        if (LD) { kv[((cb) + 2) % 3][i] = __builtin_amdgcn_raw_buffer_load_b128(rs, voff + (i) * 1024, kvo, 16); SBAR(); } GRP(SC, 4, p0, 2); \
        if (QK) SN = MFMA_BF(BC8(kv[nb_][1]), Q[((i) + 1) & 3][1], SN); SBAR(); GRP(SC, 8, p1, 0); \
        O[i] = MFMA_BF(BC8(kv[cb][2]), BC8(p0), O[i]); SBAR(); GRP(SC, 12, p1, 2); } while (0)
    const unsigned kvb = WS_KV + (unsigned)(head * 64 + w) * 4096;
    unsigned kvo = 0;
    sa = MFMA_BF(BC8(k0), Q[0][0], zero); sa = MFMA_BF(BC8(k1), Q[0][1], sa);
    SLOT(sa, sb, 0, 0, true, false, false); SLOT(sb, sa, 0, 1, true, true, false);
    drain();
    put_flag(ws, FL_KV + (unsigned)((head * 4 + w) * 16 + grp) * FS, tag, lane);
    STAMP(4);
    SLOT(sa, sb, 0, 2, true, true, false); SLOT(sb, sa, 0, 3, false, true, false);
    STAMP(5);
    GAS u64* kvf = (GAS u64*)(ws + FL_KV + (unsigned)((head * 4 + w) * 16 + ((grp + lane) & 15)) * FS);
    bool pend = lane > 0 && lane < 16;
#define NEED(msk) do { CFENCE(); _Pragma("unroll 1") for (int i_ = 0; i_ < SPIN; ++i_) { \
        if (pend) pend = __hip_atomic_load(kvf, __ATOMIC_RELAXED, __HIP_MEMORY_SCOPE_AGENT) != tag; \
        miss = (unsigned)__ballot(pend); if (!(miss & (msk))) break; __builtin_amdgcn_s_sleep(2); } CFENCE(); } while (0)
    unsigned miss;
    NEED(KVM);
    STAMP(6);
#pragma unroll
    for (int j = 1; j < 3; ++j)
#pragma unroll
        for (int f = 0; f < 4; ++f) kv[j][f] = __builtin_amdgcn_raw_buffer_load_b128(rs, voff + f * 1024, kvb + ((grp + j) & 15) * 16384, 16);
    arrive(sy + SY_G);
    sa = MFMA_BF(BC8(kv[1][0]), Q[0][0], zero); sa = MFMA_BF(BC8(kv[1][1]), Q[0][1], sa);
    STAMP(7);
    int nx = grp + 3;
#define TILE(cb) do { kvo = kvb + (nx & 15) * 16384; ++nx; SBAR(); \
        SLOT(sa, sb, cb, 0, true, true, true); SLOT(sb, sa, cb, 1, true, true, true); SLOT(sa, sb, cb, 2, true, true, true); SLOT(sb, sa, cb, 3, true, true, true); } while (0)
#pragma unroll 1
    for (unsigned msk = 0x38; msk < 0x40000; msk <<= 3) { if (__builtin_expect(miss & msk, 0)) NEED(msk); if (HOLDB) arrive(sy + SY_H); TILE(1); TILE(2); TILE(0); }
    O[3] = MFMA_BF(BC8(kv[0][3]), BC8(p1), O[3]);
    STAMP(8);
    f32x16 o = O[0];
    {
#pragma unroll
        for (int i = 1; i < 4; ++i) {
            LAS char* ex = lds + LX + (((w + i) & 3) * 3 + i - 1) * 4096 + voff;
#pragma unroll
            for (int g = 0; g < 4; ++g) *(LAS f32x4*)(ex + g * 1024) = (f32x4){O[i][4 * g], O[i][4 * g + 1], O[i][4 * g + 2], O[i][4 * g + 3]};
        }
        arrive(sy + SY_X); await(sy + SY_X, 4);
        LAS const char* er = lds + LX + w * 3 * 4096 + voff;
#pragma unroll
        for (int i = 0; i < 3; ++i)
#pragma unroll
            for (int g = 0; g < 4; ++g) { const f32x4 r = *(LAS const f32x4*)(er + i * 4096 + g * 1024);
#pragma unroll
                for (int e = 0; e < 4; ++e) o[4 * g + e] += r[e]; }
        f32x2 chk = {0.f, 0.f};
#pragma unroll
        for (int i = 0; i < 8; ++i) chk = pfma((f32x2){o[2 * i], o[2 * i + 1]}, (f32x2){0.f, 0.f}, chk);
        if (__builtin_expect(__any(!(chk[0] + chk[1] == 0.f) || !(o[12] < 1e30f)), 0)) {
            const unsigned hb = WS_KV + (unsigned)head * 64 * 4096 + voff;
            u32x4 qr = __builtin_bit_cast(u32x4, Q[0][1]); if (hi) { qr[2] = 0; qr[3] = 0; }
            float mm = -INFINITY;
            wait_flags<4>(ws, FL_KV + (unsigned)(head * 64 + lane) * FS, tag, true);
#pragma unroll 1
            for (int t = 0; t < 64; ++t) {
                f32x16 s = MFMA_BF(BC8(BLD(hb + t * 4096)), Q[0][0], zero); s = MFMA_BF(BC8(BLD(hb + t * 4096 + 1024)), BC8(qr), s);
#pragma unroll
                for (int i = 0; i < 16; ++i) mm = fmaxf(mm, s[i]);
            }
            qr = put_ref(qr, swapmax(mm), hi);
            o = zero;
#pragma unroll 1
            for (int t = 0; t < 64; ++t) {
                f32x16 s = MFMA_BF(BC8(BLD(hb + t * 4096)), Q[0][0], zero); s = MFMA_BF(BC8(BLD(hb + t * 4096 + 1024)), BC8(qr), s);
#pragma unroll
                for (int i = 0; i < 4; ++i) { p0[i] = pk_bf16(EX(s[2 * i]), EX(s[2 * i + 1])); p1[i] = pk_bf16(EX(s[8 + 2 * i]), EX(s[9 + 2 * i])); }
                o = MFMA_BF(BC8(BLD(hb + t * 4096 + 2048)), BC8(p0), o); o = MFMA_BF(BC8(BLD(hb + t * 4096 + 3072)), BC8(p1), o);
            }
        }
    }
    {
        const auto r = __builtin_amdgcn_permlane32_swap(__float_as_uint(o[12]), __float_as_uint(o[12]), false, false);
        const float rl = __builtin_amdgcn_rcpf(__uint_as_float(r[1]));
        u32x4 o0, o1;
#pragma unroll
        for (int i = 0; i < 4; ++i) { o0[i] = pk_bf16(o[2 * i] * rl, o[2 * i + 1] * rl); o1[i] = pk_bf16(o[8 + 2 * i] * rl, o[9 + 2 * i] * rl); }
        if (hi) { o1[2] = 0; o1[3] = 0; }
        const unsigned fi = (unsigned)((4 * grp + w) * 16 + head);
        BST(o0, WS_O + fi * 2048 + voff); BST(o1, WS_O + fi * 2048 + 1024 + voff);
        STAMP(9);
        drain();
        put_flag(ws, FL_O + fi * FS, tag, lane);
    }
    STAMP(10);
    const int xcd = blockIdx.x & 7, bi = blockIdx.x >> 3;
    if (xcd < 7) {
        wait_flags<2>(ws, FL_BT + (unsigned)(xcd * 128 + w * 32 + r32) * FS, tag, !hi);
        STAMP(11);
        const unsigned bo = WS_BT + (unsigned)((64 * xcd + 16 * w + (lane >> 3)) * S + 64 * bi + (lane & 7) * 8) * 2;
        glds2c(ws, bo, bo + 8 * S * 2 - 1024, lds0 + 65536 + w * 2048);
        drain();
        arrive(sy + SY_T2); await(sy + SY_T2, 4);
        u32x4 o0, o1; transpose_tile(lds + 65536, 2048, lds + 73728 + w * 2304, nullptr, w, lane, o0, o1);
        const __amdgpu_buffer_rsrc_t ro = __builtin_amdgcn_make_buffer_rsrc(out, 0, S * 2 * D * 2, 0x00020000);
        const unsigned oo = (unsigned)((64 * bi + 16 * w + (lane >> 3)) * 2 * D + D + 64 * xcd) * 2 + (lane & 7) * 16;
        __builtin_amdgcn_raw_buffer_store_b128(o0, ro, oo, 0, 16); __builtin_amdgcn_raw_buffer_store_b128(o1, ro, oo + 8 * 2 * D * 2, 0, 16);
    }
    STAMP(12);
}

DI void role_b(const uint16_t* __restrict__ wo, const uint16_t* __restrict__ u, const uint16_t* __restrict__ lar, const uint16_t* __restrict__ aim,
               const uint16_t* __restrict__ cre, const uint16_t* __restrict__ cim, const uint16_t* __restrict__ ldt, const uint16_t* __restrict__ dskip,
               uint16_t* __restrict__ out, char* __restrict__ ws, LAS char* lds, int w, int lane) {
    u64 tag;
    const unsigned lds0 = (unsigned)(uintptr_t)lds;
    LAS unsigned* sy = (LAS unsigned*)(lds + LSY);
    const __amdgpu_buffer_rsrc_t rs = __builtin_amdgcn_make_buffer_rsrc(ws, 0, WS_END, 0x00020000);
    const int b = blockIdx.x, xcd = b & 7, bi = b >> 3, r32 = lane & 31, hi = lane >> 5;
    const unsigned voff = lane * 16;
    LAS char* tab = lds + LB + w * TABW;
    const int qt = 8 * xcd + (bi >> 2), ct = 4 * (bi & 3) + w;
    bf16x8 wf[32];
#define LOADWF(i) wf[i] = BC8(__builtin_amdgcn_raw_buffer_load_b128(rs, voff, WS_WO + (unsigned)(((8 * w + (i)) & 31) * 14 + ct) * 1024, 16))
    if (xcd < 7) {
        const int cc = w >> 1, hh = w & 1, pair = 32 * xcd + bi, ch = 2 * pair + cc;
        LAS char* cb = lds + LB + cc * CBW;
        const unsigned po = (unsigned)(ch * NS + 128 * hh) / 2 + lane;
        const unsigned pl = ((const unsigned*)lar)[po], pa = ((const unsigned*)aim)[po], pr = ((const unsigned*)cre)[po], pi = ((const unsigned*)cim)[po];
        const unsigned uo = (unsigned)((64 * bi + 16 * w + (lane >> 3)) * D + 64 * xcd + (lane & 7) * 8) * 2;
        glds2(u, uo, uo + 8 * D * 2 - 1024, lds0 + LB + w * TABW + 4096);
        const u64 e0 = __hip_atomic_load((GAS u64*)ws, __ATOMIC_RELAXED, __HIP_MEMORY_SCOPE_AGENT);
        u32x2 gi[2][3];
#pragma unroll
        for (int ps = 0; ps < 2; ++ps)
#pragma unroll
            for (int i = 0; i < 3; ++i) gi[ps][i] = ((const u32x2*)g_idx.v[128 * hh + 64 * ps + lane])[i];
        const unsigned lw = ((const unsigned*)ldt)[pair], dw = ((const unsigned*)dskip)[pair];
        const float dt = rbf(expf(cc ? bf_hi(lw) : bf_lo(lw)));
        drain();
        STAMP(1);
        arrive(sy + SY_WO);
        if (w == 0 && lane == 0) __hip_atomic_store((LAS u64*)sy, mix(e0), __ATOMIC_RELAXED, __HIP_MEMORY_SCOPE_WORKGROUP);
        await(sy + SY_WO, 4);
        {
            u32x4 o0, o1; transpose_tile(lds + LB + 4096, TABW, tab, sy + SY_T1, w, lane, o0, o1);
            const unsigned to = WS_UT + (unsigned)((64 * xcd + 16 * w + (lane >> 3)) * S + 64 * bi) * 2 + (lane & 7) * 16;
            __builtin_amdgcn_s_setprio(3); BST(o0, to); BST(o1, to + 8 * S * 2); __builtin_amdgcn_s_setprio(PRIO_B);
        }
        const float cm = wave_max(fmaxf(fmaxf(fabsf(bf_lo(pr)), fabsf(bf_hi(pr))), fmaxf(fabsf(bf_lo(pi)), fabsf(bf_hi(pi)))));
        unsigned ce = __float_as_uint(cm) & 0x7f800000u; ce = ce < 0x01000000u ? 0x01000000u : (ce > 0x7e800000u ? 0x7e800000u : ce);
        const float cscale = __uint_as_float(0x7f000000u - ce), oscale = 2.0f * dt * __uint_as_float(ce);
        LAS unsigned* tw = (LAS unsigned*)(tab + lane * 4);
        LAS const char* trd = tab + r32 * TROW + hi * 16;
        f32x16 dacc = {};
        f32x2 z1, z8, z256, x0;
#define ANCH(vl, va, vr, vi) do { const float a2 = (-rbf(expf(vl)) * dt) * 1.4426950408889634f, rev = ((va) * dt) * 0.15915494309189535f; \
            z1 = anchor(a2, -rev, 1.f); z8 = anchor(a2, rev, 8.f); z256 = anchor(a2, -rev, 256.f); x0 = (f32x2){(vr) * cscale, -(vi) * cscale}; \
            PIN(z1); PIN(z8); PIN(z256); SBAR(); } while (0)
#define TW(row) tw[(row) * (TROW / 4)]
#define CHAIN() do { \
            const f32x2 z1s = rot(z1), zs = rot(z256), z8s = rot(z8); \
            f32x2 y[4], X[8]; y[1] = z8; y[2] = cmul(z8, z8, z8s); X[0] = x0; X[1] = cmul(x0, z1, z1s); f32x2 a = cmul(x0, z256, zs); \
            y[3] = cmul(y[2], z8, z8s); const f32x2 z32 = cmul(y[2], y[2], rot(y[2])), z32s = rot(z32); X[2] = cmul(X[1], z1, z1s); y[0] = z32; \
            f32x2 c4 = cmul(X[2], z256, zs); \
            unsigned c[2][7], d0 = pk_f16(X[0]), d1 = pk_f16(X[1]), d2 = pk_f16(y[1]); \
            c[0][0] = pk_f16(y[0]); c[0][1] = 0x3c00u; c[0][2] = pk_f16(y[2]); c[0][3] = pk_f16(y[3]); c[0][4] = pk_f16(X[2]); c[0][5] = pk_f16(a); c[0][6] = pk_f16(c4); \
            TW(0) = d0; TW(1) = d1; TW(22) = d2; SBAR(); \
            _Pragma("unroll") for (int i = 1; i < 9; ++i) { \
                f32x2 t0, t1, t2, t3, t4, t5, t6; const int j = i - 1; unsigned* pc = c[j & 1]; unsigned* nc = c[i & 1]; \
                if (i < 8) { if (i > 1) t0 = y[0].xx * z32; t1 = y[1].xx * z32; t2 = y[2].xx * z32; t3 = y[3].xx * z32; \
                    if (i < 6) t4 = X[i + 1].xx * z1; t5 = a.xx * z256; \
                    if (i == 3) t6 = X[4].xx * z256; if (i == 4 || i == 5) t6 = c4.xx * z256; if (i == 6) t6 = X[6].xx * z256; } SBAR(); \
                if (j != 1) TW(21 + (j ? 4 * j : 4)) = pc[0]; TW(21 + (j ? 4 * j + 1 : 0)) = pc[1]; TW(23 + 4 * j) = pc[2]; TW(24 + 4 * j) = pc[3]; \
                if (j < 6) TW(j + 2) = pc[4]; TW(j == 0 ? 8 : (j == 1 ? 12 : (j == 2 ? 14 : 13 + j))) = pc[5]; \
                if (j == 0) TW(9) = pc[6]; if (j == 3) TW(10) = pc[6]; if (j == 4) TW(13) = pc[6]; if (j == 5) TW(15) = pc[6]; if (j == 6) TW(11) = pc[6]; SBAR(); \
                if (i < 8) { if (i > 1) y[0] = pfma(y[0].yy, z32s, t0); y[1] = pfma(y[1].yy, z32s, t1); y[2] = pfma(y[2].yy, z32s, t2); y[3] = pfma(y[3].yy, z32s, t3); \
                    if (i < 6) X[i + 2] = pfma(X[i + 1].yy, z1s, t4); a = pfma(a.yy, zs, t5); \
                    if (i == 3) c4 = pfma(X[4].yy, zs, t6); if (i == 4 || i == 5) c4 = pfma(c4.yy, zs, t6); if (i == 6) c4 = pfma(X[6].yy, zs, t6); SBAR(); \
                    if (i > 1) nc[0] = pk_f16(y[0]); nc[1] = pk_f16(y[1]); nc[2] = pk_f16(y[2]); nc[3] = pk_f16(y[3]); \
                    if (i < 6) nc[4] = pk_f16(X[i + 2]); nc[5] = pk_f16(a); if (i >= 3 && i <= 6) nc[6] = pk_f16(c4); \
                    asm volatile("" :: "v"(pc[0]), "v"(pc[1]), "v"(pc[2]), "v"(pc[3]), "v"(pc[4]), "v"(pc[5]), "v"(pc[6])); SBAR(); } \
            } } while (0)
        f16x8 fa[8], fb[8];
#define RDT() do { _Pragma("unroll") for (int kk = 0; kk < 8; ++kk) { fa[kk] = *(LAS const f16x8*)(trd + kk * 32); fb[kk] = *(LAS const f16x8*)(trd + 21 * TROW + kk * 32); } SBAR(); } while (0)
#define MMT() do { _Pragma("unroll") for (int kk = 0; kk < 8; ++kk) dacc = MFMA_F16(fa[kk], fb[kk], dacc); } while (0)
        ANCH(bf_lo(pl), bf_lo(pa), bf_lo(pr), bf_lo(pi));
        tag = get_tag(sy); await(sy + SY_T1, 4);
        STAMP(13);
        CHAIN(); RDT();
        drain(); __builtin_amdgcn_s_setprio(3); put_flag(ws, FL_UT + (unsigned)(xcd * 128 + w * 32 + bi) * FS, tag, lane); __builtin_amdgcn_s_setprio(PRIO_B);
        STAMP(14);
        ANCH(bf_hi(pl), bf_hi(pa), bf_hi(pr), bf_hi(pi));
        MMT(); SBAR();
        CHAIN(); RDT();
        const unsigned utf = hi ? FL_WO + (unsigned)(w * 32 + r32) * FS : FL_UT + (unsigned)(xcd * 128 + (bi >> 3) * 32 + r32) * FS;
        const u64 utv = __hip_atomic_load((GAS u64*)(ws + utf), __ATOMIC_RELAXED, __HIP_MEMORY_SCOPE_AGENT); SBAR();
        MMT();
        STAMP(2);
        {
            LAS float* pt = (LAS float*)tab;
            *(LAS f32x4*)(pt + 8 * r32 + 4 * hi) = (f32x4){dacc[0] * oscale, dacc[1] * oscale, dacc[2] * oscale, dacc[3] * oscale};
            *(LAS f32x2*)(pt + (hi ? 384 + 2 * r32 : 256 + 4 * r32)) = (f32x2){dacc[4] * oscale, dacc[5] * oscale};
            *(LAS f32x2*)(pt + (hi ? 448 + 2 * r32 : 258 + 4 * r32)) = (f32x2){dacc[6] * oscale, dacc[7] * oscale};
            pt[hi ? 640 + r32 : 512 + r32] = dacc[8] * oscale;
            pt[hi ? 672 + r32 : 544 + r32] = dacc[9] * oscale;
            pt[hi ? 672 + r32 : 576 + r32] = dacc[10] * oscale;
            pt[hi ? 672 + r32 : 608 + r32] = dacc[11] * oscale;
        }
        LAS unsigned* ps_ = sy + SY_P + 5 * cc;
        arrive(ps_ + 0);
        LAS char* ui = cb + UOF;
        LAS char* kt = cb + KOF;
        await(ps_ + 0, 2);
#pragma unroll
        for (int ps = 0; ps < 2; ++ps) {
            const int v = 81 * hh + 64 * ps + lane;
            if (ps == 0 || lane < 17) {
                const f32x4 p0 = *(LAS const f32x4*)(cb + v * 16), p1 = *(LAS const f32x4*)(cb + TABW + v * 16);
                u32x2 r = {pk_bf16(p0[0] + p1[0], p0[1] + p1[1]), pk_bf16(p0[2] + p1[2], p0[3] + p1[3])};
                if (v == 161) r = (u32x2){0u, 0u};
                *(LAS u32x2*)(kt + v * 8) = r;
            }
        }
        arrive(ps_ + 1);
        if (__any(utv != tag)) wait_flags<8>(ws, utf, tag, utv != tag);
        CFENCE();
        const unsigned ub = WS_UT + (unsigned)(ch * S + 1024 * hh) * 2 + voff;
        const u32x4 u0 = BLD(ub), u1 = BLD(ub + 1024); SBAR();
        await(ps_ + 1, 2);
        STAMP(3);
        __builtin_amdgcn_s_setprio(PRIO_B2);
        const u32x4 z4 = {};
        {
            unsigned W[2][6];
#pragma unroll
            for (int ps = 0; ps < 2; ++ps)
#pragma unroll
                for (int i = 0; i < 6; ++i) { const unsigned gv = gi[ps][i >> 1][i & 1];
                    W[ps][i] = *(LAS const uint16_t*)(kt + (gv & 0xffffu));
                    if (i < 5) W[ps][i] |= (unsigned)*(LAS const uint16_t*)(kt + (gv >> 16)) << 16; }
            *(LAS u32x4*)(ui + (64 * hh + lane) * 16) = z4;
            if (lane < 16) *(LAS u32x4*)(ui + (128 + 16 * hh + lane) * 16) = z4;
            if (lane < 8) *(LAS u32x4*)(cb + (2 * hh + (lane >> 2)) * CPY + (256 + (lane & 3)) * 16) = z4;
#pragma unroll
            for (int ps = 0; ps < 2; ++ps) {
                unsigned V[5];
#pragma unroll
                for (int i = 0; i < 5; ++i) V[i] = __builtin_amdgcn_alignbit(W[ps][i + 1], W[ps][i], 16);
                LAS char* cp = cb + (128 * hh + 64 * ps + lane) * 16;
                *(LAS u32x4*)(cp) = (u32x4){W[ps][0], W[ps][1], W[ps][2], W[ps][3]};
                *(LAS u32x4*)(cp + CPY) = (u32x4){V[0], V[1], V[2], V[3]};
                *(LAS u32x4*)(cp + 2 * CPY) = (u32x4){W[ps][1], W[ps][2], W[ps][3], W[ps][4]};
                *(LAS u32x4*)(cp + 3 * CPY) = (u32x4){V[1], V[2], V[3], V[4]};
            }
        }
        *(LAS u32x4*)(ui + (32 + 32 * hh + (lane >> 2)) * 80 + (lane & 3) * 16) = u0;
        *(LAS u32x4*)(ui + (48 + 32 * hh + (lane >> 2)) * 80 + (lane & 3) * 16) = u1;
        arrive(ps_ + 2); await(ps_ + 2, 2);
        STAMP(4);
        __builtin_amdgcn_s_setprio(PRIO_BC);
        f32x16 a0 = {}, a1 = {};
        {
            const int sb = 7 - (r32 & 7);
            LAS const char* pa = cb + (sb & 3) * CPY + (255 - (r32 >> 3) + hi) * 16 + (sb >> 2) * 8 - 64 * (16 * hh + 15) - 2048;
            LAS const char* pa2 = pa + 8; asm volatile("" : "+v"(pa2));
            LAS const char* pb = ui + (32 + r32 - 16 * hh - 15) * 80 + 16 * hi;
            s16x4 ta[2][2], tb[2][2]; bf16x8 b0[2], b1[2];
#define T4(q, off) (*(LAS const s16x4*)((q) + (off)))
#define CSTEP(i, j, RD) do { const int o_ = 64 * (15 - ((j) >> 1)) + 32 * ((j) & 1), u_ = 80 * (15 - ((j) >> 1)) + 32 * ((j) & 1); \
                a1 = MFMA_BF(cat8(ta[i][0], ta[i][1]), b1[i], a1); SBAR(); if (RD) { ta[1 - i][0] = T4(pa, o_ + 2048); ta[1 - i][1] = T4(pa2, o_ + 2048); } SBAR(); \
                a0 = MFMA_BF(cat8(ta[i][0], ta[i][1]), b0[i], a0); SBAR(); if (RD) { b1[1 - i] = FR(pb, u_ + 2560); b0[1 - i] = FR(pb, u_); } SBAR(); \
                a1 = MFMA_BF(cat8(tb[i][0], tb[i][1]), b0[i], a1); SBAR(); if (RD) { tb[1 - i][0] = T4(pa, o_); tb[1 - i][1] = T4(pa2, o_); } SBAR(); } while (0)
            ta[0][0] = T4(pa, 960 + 2048); ta[0][1] = T4(pa2, 960 + 2048); b1[0] = FR(pb, 1200 + 2560); b0[0] = FR(pb, 1200); tb[0][0] = T4(pa, 960); tb[0][1] = T4(pa2, 960);
#pragma unroll
            for (int j = 0; j < 32; ++j) {
                if (j == 31) arrive(ps_ + 3);
                CSTEP(j & 1, j + 1, j < 31);
                if (ct < 14) LOADWF(j);
                if (HOLDB && (j & 3) == 3 && j < 31) { const unsigned h_ = __builtin_amdgcn_readfirstlane(__hip_atomic_load(sy + SY_H, __ATOMIC_RELAXED, __HIP_MEMORY_SCOPE_WORKGROUP)); if (h_ > 0 && h_ < 4 * HOLDB) await_slow(sy + SY_H, 4 * HOLDB); }
                SBAR();
            }
        }
        STAMP(5);
        __builtin_amdgcn_s_setprio(PRIO_B3);
        {
            u32x2 uu[4];
#pragma unroll
            for (int g = 0; g < 4; ++g) uu[g] = *(LAS const u32x2*)(ui + (32 + 32 * hh + r32) * 80 + 16 * g + 8 * hi);
            LAS char* ex = cb + r32 * 144 + hi * 16;
            const f32x16 snd = hh ? a0 : a1; const f32x16 y = hh ? a1 : a0;
            await(ps_ + 3, 2);
#pragma unroll
            for (int g = 0; g < 4; ++g) *(LAS f32x4*)(ex + hh * 4608 + g * 32) = (f32x4){snd[4 * g], snd[4 * g + 1], snd[4 * g + 2], snd[4 * g + 3]};
            arrive(ps_ + 4);
            const float dsk = cc ? bf_hi(dw) : bf_lo(dw);
            unsigned sk[8];
#pragma unroll
            for (int g = 0; g < 4; ++g)
#pragma unroll
                for (int e = 0; e < 2; ++e) sk[2 * g + e] = pk_bf16(bf_lo(uu[g][e]) * dsk, bf_hi(uu[g][e]) * dsk);
            await(ps_ + 4, 2);
            LAS char* sc = cb + 9216 + hh * 2048;
#pragma unroll
            for (int g = 0; g < 4; ++g) {
                const f32x4 p = *(LAS const f32x4*)(ex + (1 - hh) * 4608 + g * 32);
                u32x2 r;
#pragma unroll
                for (int e = 0; e < 2; ++e) {
                    const unsigned yb = pk_bf16(y[4 * g + 2 * e] + p[2 * e], y[4 * g + 2 * e + 1] + p[2 * e + 1]);
                    r[e] = pk_bf16(bf_lo(yb) + bf_lo(sk[2 * g + e]), bf_hi(yb) + bf_hi(sk[2 * g + e]));
                }
                *(LAS u32x2*)(sc + 64 * r32 + 16 * g + 8 * hi) = r;
            }
            const u32x4 o0 = *(LAS const u32x4*)(sc + voff), o1 = *(LAS const u32x4*)(sc + 1024 + voff);
            const unsigned bo = WS_BT + (unsigned)(ch * S + 1024 * hh) * 2 + voff;
            BST(o0, bo); BST(o1, bo + 1024);
        }
        arrive(sy + SY_BG);
        STAMP(6);
        drain();
        put_flag(ws, FL_BT + (unsigned)(xcd * 128 + bi * 4 + w) * FS, tag, lane);
    } else {
        {
            const unsigned nb = (bi & 1) ? 12 * 2 * D : 16 * 2 * D;
            unsigned v4[4];
#pragma unroll
            for (int i = 0; i < 4; ++i) { const unsigned o = (unsigned)(w * 4096 + i * 1024) + voff; v4[i] = (o < nb ? o : 0u) + 4096 - i * 1024; }
            glds4((const char*)(wo + (DH * (bi >> 1) + 16 * (bi & 1)) * D) - 4096, v4[0], v4[1], v4[2], v4[3], lds0 + LB + w * 4096);
        }
        const u64 e0 = __hip_atomic_load((GAS u64*)ws, __ATOMIC_RELAXED, __HIP_MEMORY_SCOPE_AGENT);
        drain();
        if (w == 0 && lane == 0) __hip_atomic_store((LAS u64*)sy, mix(e0), __ATOMIC_RELAXED, __HIP_MEMORY_SCOPE_WORKGROUP);
        arrive(sy + SY_WO); await(sy + SY_WO, 4);
        LAS const char* p = lds + LB + (4 * hi + ((lane & 15) >> 2)) * (2 * D) + ((lane >> 4) & 1) * 32 + (lane & 3) * 8;
#pragma unroll
        for (int i = 0; i < 4; ++i) {
            const int ct = w + 4 * i;
            if (ct < 14) {
                u32x4 o = __builtin_bit_cast(u32x4, cat8(trread(p + ct * 64), trread(p + 8 * 2 * D + ct * 64)));
                if (hi && (bi & 1)) { o[2] = 0; o[3] = 0; }
                BST(o, WS_WO + (unsigned)(bi * 14 + ct) * 1024 + voff);
            }
        }
        tag = get_tag(sy);
        drain();
        put_flag(ws, FL_WO + (unsigned)(w * 32 + bi) * FS, tag, lane);
        __builtin_amdgcn_s_setprio(0);
        arrive(sy + SY_BG);
        await_slow(sy + SY_G, 4);
        wait_flags<8>(ws, FL_WO + (unsigned)(w * 32 + r32) * FS, tag, !hi);
        if (ct < 14) {
#pragma unroll
            for (int i = 0; i < 32; ++i) { LOADWF(i); __builtin_amdgcn_s_sleep(WOSL); SBAR(); }
        }
    }
    STAMP(7);
    const __amdgpu_buffer_rsrc_t ro = __builtin_amdgcn_make_buffer_rsrc(out, 0, S * 2 * D * 2, 0x00020000);
    await(sy + SY_BG, 4);
    STAMP(8);
    await_slow(sy + SY_X, 4);
    wait_flags<2>(ws, FL_O + (unsigned)(qt * 16 + 4 * w + lane) * FS, tag, lane < 4);
    STAMP(9);
    u32x4 of[8];
#pragma unroll
    for (int i = 0; i < 8; ++i) of[i] = BLD(WS_O + (unsigned)(qt * 32 + 8 * w + i) * 1024 + voff);
#pragma unroll
    for (int i = 0; i < 8; ++i) *(LAS u32x4*)(lds + LB + (8 * w + i) * 1024 + voff) = of[i];
    arrive(sy + SY_B2);
    STAMP(10);
    f32x16 acc = {};
    if (ct < 14) {
#pragma unroll
        for (int i = 0; i < 8; ++i) acc = MFMA_BF(wf[i], BC8(of[i]), acc);
    }
    SBAR();
    await(sy + SY_B2, 4);
    STAMP(11);
    if (ct < 14) {
        LAS const char* po[3];
#pragma unroll
        for (int j = 0; j < 3; ++j) po[j] = lds + LB + ((w + 1 + j) & 3) * 8192 + voff;
        bf16x8 og[4];
#pragma unroll
        for (int i = 0; i < 3; ++i) og[i] = FR(po[0], i * 1024);
#pragma unroll
        for (int i = 0; i < 24; ++i) { if (i < 21) og[(i + 3) & 3] = FR(po[(i + 3) >> 3], ((i + 3) & 7) * 1024); SBAR(); acc = MFMA_BF(wf[8 + i], og[i & 3], acc); SBAR(); }
        LAS char* sc = lds + LB + 40960 + w * 2304;
#pragma unroll
        for (int g = 0; g < 4; ++g) *(LAS u32x2*)(sc + r32 * 72 + 16 * g + 8 * hi) = (u32x2){pk_bf16(acc[4 * g], acc[4 * g + 1]), pk_bf16(acc[4 * g + 2], acc[4 * g + 3])};
        LAS const char* sr = sc + (lane >> 2) * 72 + (lane & 3) * 16;
        const u32x2 a0 = *(LAS const u32x2*)sr, a1 = *(LAS const u32x2*)(sr + 8), b0 = *(LAS const u32x2*)(sr + 16 * 72), b1 = *(LAS const u32x2*)(sr + 16 * 72 + 8);
        const unsigned oo = (unsigned)((32 * qt + (lane >> 2)) * 2 * D + 32 * ct) * 2 + (lane & 3) * 16;
        __builtin_amdgcn_raw_buffer_store_b128((u32x4){a0[0], a0[1], a1[0], a1[1]}, ro, oo, 0, 16);
        __builtin_amdgcn_raw_buffer_store_b128((u32x4){b0[0], b0[1], b1[0], b1[1]}, ro, oo + 16 * 2 * D * 2, 0, 16);
    }
    if (b == 0 && w == 0) put_flag(ws, 0, tag, lane);
    STAMP(12);
}

__global__ __launch_bounds__(512, 2) void fused(const uint16_t* __restrict__ x, const uint16_t* __restrict__ wqkv, const uint16_t* __restrict__ wo,
                                               const uint16_t* __restrict__ u, const uint16_t* __restrict__ lar, const uint16_t* __restrict__ aim,
                                               const uint16_t* __restrict__ cre, const uint16_t* __restrict__ cim, const uint16_t* __restrict__ ldt,
                                               const uint16_t* __restrict__ dskip, uint16_t* __restrict__ out, char* __restrict__ ws) {
    extern __shared__ __attribute__((aligned(16))) char smem[];
    LAS char* lds = (LAS char*)smem;
    const int lane = threadIdx.x & 63, w = __builtin_amdgcn_readfirstlane(threadIdx.x >> 6);
    STAMP(0);
    if (w >= 4) __builtin_amdgcn_s_setprio(3);
    if (w == 4) ((LAS unsigned*)(lds + LSY))[lane] = 0;
    asm volatile("" :: "s"(x), "s"(wqkv), "s"(wo), "s"(u), "s"(lar), "s"(aim), "s"(cre), "s"(cim), "s"(ldt), "s"(dskip), "s"(out), "s"(ws));
    asm volatile("s_waitcnt lgkmcnt(0)\n\ts_barrier" ::: "memory");
    if (w < 4) role_a(x, wqkv, out, ws, lds, w, lane);
    else role_b(wo, u, lar, aim, cre, cim, ldt, dskip, out, ws, lds, w - 4, lane);
}
}

extern "C" void kernel_launch(void* const* d_in, const int* in_sizes, int n_in, void* d_out, int out_size, void* d_ws, size_t ws_size, hipStream_t stream) {
    using namespace k;
    (void)hipFuncSetAttribute((const void*)fused, hipFuncAttributeMaxDynamicSharedMemorySize, LDS_BYTES);
    const uint16_t* const* in = (const uint16_t* const*)d_in;
    fused<<<256, 512, LDS_BYTES, stream>>>(in[0], in[1], in[2], in[3], in[4], in[5], in[6], in[7], in[8], in[9], (uint16_t*)d_out, (char*)d_ws);
}
```

```cpp
#define HA 99
#define HB -1
#define KVM 6u
#include <hip/hip_runtime.h>
#include <hip/hip_bf16.h>
#include <stdint.h>

typedef short bf16x8 __attribute__((ext_vector_type(8)));
typedef _Float16 f16x8 __attribute__((ext_vector_type(8)));
typedef _Float16 f16x2 __attribute__((ext_vector_type(2)));
typedef __bf16 bf16x2 __attribute__((ext_vector_type(2)));
typedef short s16x4 __attribute__((ext_vector_type(4)));
typedef float f32x16 __attribute__((ext_vector_type(16)));
typedef float f32x4 __attribute__((ext_vector_type(4)));
typedef float f32x2 __attribute__((ext_vector_type(2)));
typedef unsigned u32x4 __attribute__((ext_vector_type(4)));
typedef unsigned u32x2 __attribute__((ext_vector_type(2)));
typedef unsigned long long u64;
#define LAS __attribute__((address_space(3)))
#define GAS __attribute__((address_space(1)))
#define DI __device__ __forceinline__
#define MFMA_BF(a, b, c) __builtin_amdgcn_mfma_f32_32x32x16_bf16((a), (b), (c), 0, 0, 0)
#define MFMA_F16(a, b, c) __builtin_amdgcn_mfma_f32_32x32x16_f16((a), (b), (c), 0, 0, 0)
#define SBAR() __builtin_amdgcn_sched_barrier(0)
#define CFENCE() asm volatile("" ::: "memory")
#define PIN(x) asm volatile("" : "+v"(x))
#define BC8(x) __builtin_bit_cast(bf16x8, (x))
#define EX(x) __builtin_amdgcn_exp2f(x)
#define PRIO_A1 0
#define PRIO_B 0
#define PRIO_B2 1
#define ROT 1
#define HOLDB 0
#define PRIO_B3 1
#define PRIO_BC 0
#define WOSL 2

namespace k {
#define STAMP(i) do { if ((i) == ((threadIdx.x >> 8) ? HB : HA) && (threadIdx.x & 255) == 0) ((LAS volatile unsigned*)(lds + LSY))[40] = (unsigned)(__builtin_amdgcn_s_memrealtime() - t_start); \
    if ((i) == 12 && (threadIdx.x >> 8)) { const u64 e_ = 2800 + ((LAS volatile unsigned*)(lds + LSY))[40]; while (__builtin_amdgcn_s_memrealtime() - t_start < e_) __builtin_amdgcn_s_sleep(2); } } while (0)
constexpr int S = 2048, D = 448, DH = 28, NS = 256;
constexpr unsigned FS = 128;
constexpr unsigned FL_KV = FS, FL_O = FL_KV + 1024 * FS, FL_UT = FL_O + 1024 * FS, FL_BT = FL_UT + 896 * FS, FL_WO = FL_BT + 896 * FS;
constexpr unsigned WS_KV = 512u << 10;
constexpr unsigned WS_O = WS_KV + (4u << 20);
constexpr unsigned WS_WO = WS_O + (2u << 20);
constexpr unsigned WS_UT = WS_WO + (448u << 10);
constexpr unsigned WS_BT = WS_UT + D * S * 2;
constexpr unsigned WS_END = WS_BT + D * S * 2;
constexpr int WSL = 12288, LXR = 4 * WSL, XSL = 16384, LB = LXR + 3 * XSL;
constexpr int LQ = 0, LX = 8192;
constexpr int TROW = 272, TABW = 53 * TROW, CBW = 2 * TABW, CPY = 4160, UOF = 4 * CPY, KOF = CBW - 1312;
constexpr int LSY = LB + 2 * CBW, LDS_BYTES = LSY + 256;
static_assert(LDS_BYTES <= 163840 && (LSY & 15) == 0 && UOF + 7680 <= KOF, "lds");
enum { SY_TAG = 0, SY_F = 2, SY_P = 10, SY_WO = 20, SY_T1 = 21, SY_BG = 22, SY_B2 = 23, SY_Q = 24, SY_X = 25, SY_G = 26, SY_T2 = 27, SY_H = 28 };
constexpr int SPIN = 1 << 18;

struct IdxTab { uint16_t v[256][12]; };
constexpr IdxTab make_idx() {
    IdxTab t = {};
    for (int y = 0; y < 256; ++y) for (int e = 0; e < 12; ++e) {
        const int j = 2047 - 8 * y - e; int ix = 644;
        if (j >= 0) { const int sh = j >= 1024 ? 3 : (j >= 512 ? 2 : (j >= 256 ? 1 : 0));
            ix = sh ? 128 * sh + ((j + (1 << (sh - 1)) - 1 + ((j >> sh) & 1)) >> sh) : j; }
        t.v[y][e] = (uint16_t)(2 * ix);
    }
    return t;
}
__device__ const IdxTab g_idx __attribute__((aligned(16))) = make_idx();

DI unsigned pk_bf16(float a, float b) { f32x2 v = {a, b}; return __builtin_bit_cast(unsigned, __builtin_convertvector(v, bf16x2)); }
DI unsigned pk_f16(f32x2 v) { return __builtin_bit_cast(unsigned, __builtin_convertvector(v, f16x2)); }
DI float bf_lo(unsigned w) { return __uint_as_float(w << 16); }
DI float bf_hi(unsigned w) { return __uint_as_float(w & 0xffff0000u); }
DI float rbf(float x) { return bf_lo(pk_bf16(x, x)); }
#define GL0 "s_mov_b32 %0, m0\n\ts_mov_b32 m0, %2\n\ts_nop 0\n\t"
#define GL1(V, O, C) "global_load_lds_dwordx4 " V ", %1 offset:" #O C "\n\t"
#define GLE "s_mov_b32 m0, %0"
DI void glds4(const void* sb, unsigned v0, unsigned v1, unsigned v2, unsigned v3, unsigned lb) { unsigned sv;
    asm volatile(GL0 GL1("%3", 0, "") GL1("%4", 1024, "") GL1("%5", 2048, "") GL1("%6", 3072, "") GLE : "=&s"(sv) : "s"(sb), "s"(lb), "v"(v0), "v"(v1), "v"(v2), "v"(v3) : "memory"); }
DI void glds3(const void* sb, unsigned v0, unsigned v1, unsigned v2, unsigned lb) { unsigned sv;
    asm volatile(GL0 GL1("%3", 0, "") GL1("%4", 1024, "") GL1("%5", 2048, "") GLE : "=&s"(sv) : "s"(sb), "s"(lb), "v"(v0), "v"(v1), "v"(v2) : "memory"); }
DI void glds2(const void* sb, unsigned v0, unsigned v1, unsigned lb) { unsigned sv;
    asm volatile(GL0 GL1("%3", 0, "") GL1("%4", 1024, "") GLE : "=&s"(sv) : "s"(sb), "s"(lb), "v"(v0), "v"(v1) : "memory"); }
DI void glds2c(const void* sb, unsigned v0, unsigned v1, unsigned lb) { unsigned sv;
    asm volatile(GL0 GL1("%3", 0, " sc1") GL1("%4", 1024, " sc1") GLE : "=&s"(sv) : "s"(sb), "s"(lb), "v"(v0), "v"(v1) : "memory"); }
DI float wave_max(float v) {
#define DPPMAX(ctrl, rm) v = fmaxf(v, __uint_as_float(__builtin_amdgcn_update_dpp(0, __float_as_uint(v), ctrl, rm, 0xf, true)))
    DPPMAX(0x111, 0xf); DPPMAX(0x112, 0xf); DPPMAX(0x114, 0xf); DPPMAX(0x118, 0xf); DPPMAX(0x142, 0xa); DPPMAX(0x143, 0xc);
    return __uint_as_float(__builtin_amdgcn_readlane(__float_as_uint(v), 63));
}
DI s16x4 trread(LAS const char* p) { return __builtin_bit_cast(s16x4, __builtin_amdgcn_ds_read_tr16_b64_v4i16((LAS s16x4*)p)); }
DI bf16x8 cat8(s16x4 a, s16x4 b) { return __builtin_shufflevector(a, b, 0, 1, 2, 3, 4, 5, 6, 7); }
DI f32x2 pfma(f32x2 a, f32x2 b, f32x2 c) { return __builtin_elementwise_fma(a, b, c); }
DI f32x2 cmul(f32x2 a, f32x2 b, f32x2 bs) { return pfma(a.yy, bs, a.xx * b); }
DI f32x2 rot(f32x2 b) { return (f32x2){-b.y, b.x}; }
DI f32x2 anchor(float a2, float rev, float s) {
    const float mag = EX(a2 * s);
    float r = rev * s; r -= __builtin_rintf(r);
    return (f32x2){mag * __builtin_amdgcn_cosf(r), mag * __builtin_amdgcn_sinf(r)};
}
DI void arrive(LAS unsigned* c) { asm volatile("s_mov_b64 exec, 1\n\tds_add_u32 %0, %1\n\ts_mov_b64 exec, -1" :: "v"((unsigned)(uintptr_t)c), "v"(1u) : "memory"); }
DI void await(LAS unsigned* c, unsigned n) {
    unsigned t, i = SPIN;
    asm volatile("1:\n\tds_read_b32 %0, %2\n\ts_waitcnt lgkmcnt(0)\n\tv_cmp_gt_u32 vcc, %3, %0\n\ts_cbranch_vccz 2f\n\ts_sleep 1\n\ts_sub_u32 %1, %1, 1\n\ts_cbranch_scc0 1b\n2:"
                 : "=&v"(t), "+s"(i) : "v"((unsigned)(uintptr_t)c), "s"(n) : "vcc", "scc", "memory");
}
DI void await_slow(LAS unsigned* c, unsigned n) {
    unsigned t, i = SPIN;
    asm volatile("1:\n\tds_read_b32 %0, %2\n\ts_waitcnt lgkmcnt(0)\n\tv_cmp_gt_u32 vcc, %3, %0\n\ts_cbranch_vccz 2f\n\ts_sleep 3\n\ts_sub_u32 %1, %1, 1\n\ts_cbranch_scc0 1b\n2:"
                 : "=&v"(t), "+s"(i) : "v"((unsigned)(uintptr_t)c), "s"(n) : "vcc", "scc", "memory");
}
DI u64 get_tag(LAS unsigned* sy) {
    await(sy, 1);
    const u64 v = __hip_atomic_load((LAS u64*)sy, __ATOMIC_RELAXED, __HIP_MEMORY_SCOPE_WORKGROUP);
    return ((u64)(unsigned)__builtin_amdgcn_readfirstlane((unsigned)(v >> 32)) << 32) | (unsigned)__builtin_amdgcn_readfirstlane((unsigned)v);
}
DI void drain() { asm volatile("s_waitcnt vmcnt(0)" ::: "memory"); }
DI void put_flag(char* ws, unsigned off, u64 tag, int lane) { if (lane == 0) __hip_atomic_store((GAS u64*)(ws + off), tag, __ATOMIC_RELAXED, __HIP_MEMORY_SCOPE_AGENT); }
template <int SLP> DI void wait_flags(char* ws, unsigned off, u64 tag, bool pend) {
    GAS u64* p = (GAS u64*)(ws + off);
    CFENCE();
#pragma unroll 1
    for (int i = 0; i < SPIN; ++i) {
        if (pend) pend = __hip_atomic_load(p, __ATOMIC_RELAXED, __HIP_MEMORY_SCOPE_AGENT) != tag;
        if (!__any(pend)) break;
        __builtin_amdgcn_s_sleep(SLP);
    }
    CFENCE();
}
#define BST(v, off) __builtin_amdgcn_raw_buffer_store_b128((v), rs, (off), 0, 16)
#define BLD(off) __builtin_amdgcn_raw_buffer_load_b128(rs, (off), 0, 16)
#define FR(p, off) (*(LAS const bf16x8*)((p) + (off)))
DI float swapmax(float m) { const auto r = __builtin_amdgcn_permlane32_swap(__float_as_uint(m), __float_as_uint(m), false, false); return fmaxf(__uint_as_float(r[0]), __uint_as_float(r[1])); }
DI u32x4 put_ref(u32x4 q1, float m, int hi) {
    const float m1 = rbf(m), m2 = rbf(m - m1), m3 = m - m1 - m2;
    if (hi) { q1[2] = pk_bf16(-m1, -m2); q1[3] = pk_bf16(-m3, 0.f); }
    return q1;
}
DI u64 mix(u64 e) { u64 z = e + 0x9E3779B97F4A7C15ull; z = (z ^ (z >> 30)) * 0xBF58476D1CE4E5B9ull; z = (z ^ (z >> 27)) * 0x94D049BB133111EBull; return (z ^ (z >> 31)) | 1ull; }
DI void transpose_tile(LAS const char* stage, int pst, LAS char* scratch, LAS unsigned* done, int w, int lane, u32x4& o0, u32x4& o1) {
    LAS const char* p = stage + (lane >> 4) * pst + ((lane & 15) >> 2) * 128 + w * 32 + (lane & 3) * 8;
    const bf16x8 t0 = cat8(trread(p), trread(p + 512)), t1 = cat8(trread(p + 1024), trread(p + 1536));
    if (done) arrive(done);
    LAS char* sc = scratch + (lane & 15) * 144 + (lane >> 4) * 32;
    *(LAS bf16x8*)sc = t0; *(LAS bf16x8*)(sc + 16) = t1;
    LAS const char* sr = scratch + (lane >> 3) * 144 + (lane & 7) * 16;
    o0 = *(LAS const u32x4*)sr; o1 = *(LAS const u32x4*)(sr + 8 * 144);
}

DI void role_a(const uint16_t* __restrict__ x, const uint16_t* __restrict__ wqkv, uint16_t* __restrict__ out, char* __restrict__ ws, LAS char* lds, int w, int lane, u64 t_start) {
    const int head = 4 * (blockIdx.x & 3) + ((blockIdx.x >> 3) & 3), grp = 8 * ((blockIdx.x >> 2) & 1) + (blockIdx.x >> 5);
    const unsigned lds0 = (unsigned)(uintptr_t)lds;
    const int r32 = lane & 31, hi = lane >> 5;
    const int wsh = (head & 7) == 7 ? 4 : 0;
    const unsigned wv = (unsigned)((lane >> 2) * 3 * D + (lane & 3) * 8) * 2;
    const char* wb = (const char*)(wqkv + DH * head - wsh + (16 * w) * 3 * D) - 2048;
    const unsigned xv0 = (unsigned)((lane >> 3) * D + (((lane & 7) ^ (lane >> 4)) * 8)) * 2, xv1 = xv0 ^ 64;
    const char* xb = (const char*)(x + (128 * grp + 32 * w) * D);
    const int rho = ROT ? ((blockIdx.x >> 3) + ((blockIdx.x >> 1) & 3)) % 7 : 0;
#define SP(st) ((st) + rho >= 7 ? (st) + rho - 7 : (st) + rho)
#define LOADW(st) glds3(wb + SP(st) * (64 * 3 * D * 2), wv + 2048, wv + 2 * D + 1024, wv + 4 * D, lds0 + ((st) & 3) * WSL + w * 3072)
#define LOADX(st) glds4(xb + SP(st) * 128, xv0, xv1 + 16 * D - 1024, xv0 + 32 * D - 2048, xv1 + 48 * D - 3072, lds0 + LXR + ((st) % 3) * XSL + w * 4096)
    STAMP(31);
    LOADW(0); LOADX(0);
    STAMP(1);
    LOADW(1); LOADX(1); LOADW(2); LOADX(2);
    __builtin_amdgcn_s_setprio(PRIO_A1);
    LAS unsigned* sy = (LAS unsigned*)(lds + LSY);
    const __amdgpu_buffer_rsrc_t rs = __builtin_amdgcn_make_buffer_rsrc(ws, 0, WS_END, 0x00020000);
    const int xs = (r32 >> 1) & 7;
    LAS const char* xp[4];
#pragma unroll
    for (int ks = 0; ks < 4; ++ks) xp[ks] = lds + LXR + w * 4096 + r32 * 128 + (((2 * ks + hi) ^ xs) * 16);
    LAS const char* wp = lds + (8 * hi + ((lane & 15) >> 2)) * 64 + ((lane >> 4) & 1) * 32 + (lane & 3) * 8 + wsh * 2;
    f32x16 aq = {}, ak = {}, av = {};
    bf16x8 xf[2][4]; s16x4 fr[4][6];
#define RX(st, ks) xf[(st) & 1][ks] = FR(xp[ks], ((st) % 3) * XSL)
#define RW(st, ks, j) fr[ks][j] = trread(wp + ((st) & 3) * WSL + (ks) * 3072 + ((((j) >> 1) + 1) % 3) * 1024 + ((j) & 1) * 256)
#define MK(st, ks) ak = MFMA_BF(cat8(fr[ks][0], fr[ks][1]), xf[(st) & 1][ks], ak); SBAR()
#define MV(st, ks) av = MFMA_BF(xf[(st) & 1][ks], cat8(fr[ks][2], fr[ks][3]), av); SBAR()
#define MQ(st, ks) aq = MFMA_BF(cat8(fr[ks][4], fr[ks][5]), xf[(st) & 1][ks], aq); SBAR()
#define QSTEP(st, ks) MK(st, ks); RW(st, ks + 1, 0); RW(st, ks + 1, 1); SBAR(); MV(st, ks); RW(st, ks + 1, 2); RW(st, ks + 1, 3); SBAR(); MQ(st, ks); RW(st, ks + 1, 4); RW(st, ks + 1, 5); SBAR()
    u64 tag = 0;
    asm volatile("s_waitcnt vmcnt(14)" ::: "memory");
    arrive(sy + SY_F); await(sy + SY_F, 4);
    STAMP(16);
    RX(0, 0); RW(0, 0, 0); RW(0, 0, 1); RW(0, 0, 2); RW(0, 0, 3); RW(0, 0, 4); RW(0, 0, 5); RX(0, 1); RX(0, 2); RX(0, 3); SBAR();
#pragma unroll
    for (int st = 0; st < 7; ++st) {
        QSTEP(st, 0); if (st < 4) LOADW(st + 3);
        QSTEP(st, 1); if (st < 4) LOADX(st + 3);
        QSTEP(st, 2);
        if (st < 4) asm volatile("s_waitcnt vmcnt(14)" ::: "memory"); else if (st == 4) asm volatile("s_waitcnt vmcnt(7)" ::: "memory"); else if (st == 5) drain();
        arrive(sy + SY_F + 1 + st); SBAR();
        MK(st, 3); await(sy + SY_F + 1 + st, 4);
        if (st < 6) { RX(st + 1, 0); RW(st + 1, 0, 0); RW(st + 1, 0, 1); RW(st + 1, 0, 2); RW(st + 1, 0, 3); } SBAR();
        MV(st, 3); if (st < 6) { RW(st + 1, 0, 4); RW(st + 1, 0, 5); RX(st + 1, 1); } SBAR();
        MQ(st, 3); if (st < 6) { RX(st + 1, 2); RX(st + 1, 3); } SBAR();
        if (st == 2) tag = get_tag(sy);
    }
    STAMP(3);
    const float cq = 0.18898223650461362f * 1.4426950408889634f;
    const unsigned voff = lane * 16;
    u32x4 q0, q1, k0, k1, v0, v1;
    const unsigned tb = WS_KV + (unsigned)(head * 64 + 4 * grp + w) * 4096 + voff;
#pragma unroll
    for (int i = 0; i < 4; ++i) { k0[i] = pk_bf16(ak[2 * i], ak[2 * i + 1]); k1[i] = pk_bf16(ak[8 + 2 * i], ak[9 + 2 * i]); }
    if (hi) { k1[2] = 0x3f803f80u; k1[3] = 0x00003f80u; }
    BST(k0, tb); BST(k1, tb + 1024);
#pragma unroll
    for (int i = 0; i < 4; ++i) {
        v0[i] = pk_bf16(av[2 * i], av[2 * i + 1]); v1[i] = pk_bf16(av[8 + 2 * i], av[9 + 2 * i]);
        if (r32 >= DH) { v0[i] = v1[i] = (r32 == DH ? 0x3f803f80u : 0u); }
    }
    BST(v0, tb + 2048); BST(v1, tb + 3072);
#pragma unroll
    for (int i = 0; i < 4; ++i) { q0[i] = pk_bf16(aq[2 * i] * cq, aq[2 * i + 1] * cq); q1[i] = pk_bf16(aq[8 + 2 * i] * cq, aq[9 + 2 * i] * cq); }
    if (hi) { q1[2] = 0; q1[3] = 0; }
    const f32x16 zero = {};
    {
        f32x16 s = MFMA_BF(BC8(k0), BC8(q0), zero); s = MFMA_BF(BC8(k1), BC8(q1), s);
        float m = s[0];
#pragma unroll
        for (int i = 1; i < 16; ++i) m = fmaxf(m, s[i]);
        q1 = put_ref(q1, swapmax(m), hi);
    }
    { LAS u32x4* qo = (LAS u32x4*)(lds + LQ + w * 2048) + lane; qo[0] = q0; qo[64] = q1; }
    arrive(sy + SY_Q); await(sy + SY_Q, 4);
    __builtin_amdgcn_s_setprio(0);
    bf16x8 Q[4][2];
    Q[0][0] = BC8(q0); Q[0][1] = BC8(q1);
#pragma unroll
    for (int i = 1; i < 4; ++i) { LAS const char* p = lds + LQ + ((w + i) & 3) * 2048 + voff; Q[i][0] = FR(p, 0); Q[i][1] = FR(p, 1024); }
    u32x4 kv[3][4] = {{k0, k1, v0, v1}, {}, {}};
    f32x16 O[4] = {}, sa, sb;
    u32x4 p0, p1;
#define GRP(SC, i, P, j) do { float e0 = EX(SC[i]); SBAR(); float e1 = EX(SC[i + 1]); SBAR(); float e2 = EX(SC[i + 2]); SBAR(); float e3 = EX(SC[i + 3]); SBAR(); \
        P[j] = pk_bf16(e0, e1); SBAR(); P[j + 1] = pk_bf16(e2, e3); PIN(P); SBAR(); } while (0)
#define SLOT(SC, SN, cb, i, QK, PV1, LD) do { constexpr int nb_ = (i) < 3 ? (cb) : ((cb) + 1) % 3, pb_ = (i) > 0 ? (cb) : ((cb) + 2) % 3; \
        if (QK) SN = MFMA_BF(BC8(kv[nb_][0]), Q[((i) + 1) & 3][0], zero); SBAR(); GRP(SC, 0, p0, 0); \
        if (PV1) O[((i) + 3) & 3] = MFMA_BF(BC8(kv[pb_][3]), BC8(p1), O[((i) + 3) & 3]); SBAR(); \
        if (LD) { kv[((cb) + 2) % 3][i] = __builtin_amdgcn_raw_buffer_load_b128(rs, voff + (i) * 1024, kvo, 16); SBAR(); } GRP(SC, 4, p0, 2); \
        if (QK) SN = MFMA_BF(BC8(kv[nb_][1]), Q[((i) + 1) & 3][1], SN); SBAR(); GRP(SC, 8, p1, 0); \
        O[i] = MFMA_BF(BC8(kv[cb][2]), BC8(p0), O[i]); SBAR(); GRP(SC, 12, p1, 2); } while (0)
    const unsigned kvb = WS_KV + (unsigned)(head * 64 + w) * 4096;
    unsigned kvo = 0;
    sa = MFMA_BF(BC8(k0), Q[0][0], zero); sa = MFMA_BF(BC8(k1), Q[0][1], sa);
    SLOT(sa, sb, 0, 0, true, false, false); SLOT(sb, sa, 0, 1, true, true, false);
    drain();
    put_flag(ws, FL_KV + (unsigned)((head * 4 + w) * 16 + grp) * FS, tag, lane);
    STAMP(4);
    SLOT(sa, sb, 0, 2, true, true, false); SLOT(sb, sa, 0, 3, false, true, false);
    STAMP(5);
    GAS u64* kvf = (GAS u64*)(ws + FL_KV + (unsigned)((head * 4 + w) * 16 + ((grp + lane) & 15)) * FS);
    bool pend = lane > 0 && lane < 16;
#define NEED(msk) do { CFENCE(); _Pragma("unroll 1") for (int i_ = 0; i_ < SPIN; ++i_) { \
        if (pend) pend = __hip_atomic_load(kvf, __ATOMIC_RELAXED, __HIP_MEMORY_SCOPE_AGENT) != tag; \
        miss = (unsigned)__ballot(pend); if (!(miss & (msk))) break; __builtin_amdgcn_s_sleep(2); } CFENCE(); } while (0)
    unsigned miss;
    NEED(KVM);
    STAMP(6);
#pragma unroll
    for (int j = 1; j < 3; ++j)
#pragma unroll
        for (int f = 0; f < 4; ++f) kv[j][f] = __builtin_amdgcn_raw_buffer_load_b128(rs, voff + f * 1024, kvb + ((grp + j) & 15) * 16384, 16);
    arrive(sy + SY_G);
    sa = MFMA_BF(BC8(kv[1][0]), Q[0][0], zero); sa = MFMA_BF(BC8(kv[1][1]), Q[0][1], sa);
    STAMP(7);
    int nx = grp + 3;
#define TILE(cb) do { kvo = kvb + (nx & 15) * 16384; ++nx; SBAR(); \
        SLOT(sa, sb, cb, 0, true, true, true); SLOT(sb, sa, cb, 1, true, true, true); SLOT(sa, sb, cb, 2, true, true, true); SLOT(sb, sa, cb, 3, true, true, true); } while (0)
#pragma unroll 1
    for (unsigned msk = 0x38; msk < 0x40000; msk <<= 3) { if (__builtin_expect(miss & msk, 0)) NEED(msk); if (HOLDB) arrive(sy + SY_H); TILE(1); TILE(2); TILE(0); }
    O[3] = MFMA_BF(BC8(kv[0][3]), BC8(p1), O[3]);
    STAMP(8);
    f32x16 o = O[0];
    {
#pragma unroll
        for (int i = 1; i < 4; ++i) {
            LAS char* ex = lds + LX + (((w + i) & 3) * 3 + i - 1) * 4096 + voff;
#pragma unroll
            for (int g = 0; g < 4; ++g) *(LAS f32x4*)(ex + g * 1024) = (f32x4){O[i][4 * g], O[i][4 * g + 1], O[i][4 * g + 2], O[i][4 * g + 3]};
        }
        arrive(sy + SY_X); await(sy + SY_X, 4);
        LAS const char* er = lds + LX + w * 3 * 4096 + voff;
#pragma unroll
        for (int i = 0; i < 3; ++i)
#pragma unroll
            for (int g = 0; g < 4; ++g) { const f32x4 r = *(LAS const f32x4*)(er + i * 4096 + g * 1024);
#pragma unroll
                for (int e = 0; e < 4; ++e) o[4 * g + e] += r[e]; }
        f32x2 chk = {0.f, 0.f};
#pragma unroll
        for (int i = 0; i < 8; ++i) chk = pfma((f32x2){o[2 * i], o[2 * i + 1]}, (f32x2){0.f, 0.f}, chk);
        if (__builtin_expect(__any(!(chk[0] + chk[1] == 0.f) || !(o[12] < 1e30f)), 0)) {
            const unsigned hb = WS_KV + (unsigned)head * 64 * 4096 + voff;
            u32x4 qr = __builtin_bit_cast(u32x4, Q[0][1]); if (hi) { qr[2] = 0; qr[3] = 0; }
            float mm = -INFINITY;
            wait_flags<4>(ws, FL_KV + (unsigned)(head * 64 + lane) * FS, tag, true);
#pragma unroll 1
            for (int t = 0; t < 64; ++t) {
                f32x16 s = MFMA_BF(BC8(BLD(hb + t * 4096)), Q[0][0], zero); s = MFMA_BF(BC8(BLD(hb + t * 4096 + 1024)), BC8(qr), s);
#pragma unroll
                for (int i = 0; i < 16; ++i) mm = fmaxf(mm, s[i]);
            }
            qr = put_ref(qr, swapmax(mm), hi);
            o = zero;
#pragma unroll 1
            for (int t = 0; t < 64; ++t) {
                f32x16 s = MFMA_BF(BC8(BLD(hb + t * 4096)), Q[0][0], zero); s = MFMA_BF(BC8(BLD(hb + t * 4096 + 1024)), BC8(qr), s);
#pragma unroll
                for (int i = 0; i < 4; ++i) { p0[i] = pk_bf16(EX(s[2 * i]), EX(s[2 * i + 1])); p1[i] = pk_bf16(EX(s[8 + 2 * i]), EX(s[9 + 2 * i])); }
                o = MFMA_BF(BC8(BLD(hb + t * 4096 + 2048)), BC8(p0), o); o = MFMA_BF(BC8(BLD(hb + t * 4096 + 3072)), BC8(p1), o);
            }
        }
    }
    {
        const auto r = __builtin_amdgcn_permlane32_swap(__float_as_uint(o[12]), __float_as_uint(o[12]), false, false);
        const float rl = __builtin_amdgcn_rcpf(__uint_as_float(r[1]));
        u32x4 o0, o1;
#pragma unroll
        for (int i = 0; i < 4; ++i) { o0[i] = pk_bf16(o[2 * i] * rl, o[2 * i + 1] * rl); o1[i] = pk_bf16(o[8 + 2 * i] * rl, o[9 + 2 * i] * rl); }
        if (hi) { o1[2] = 0; o1[3] = 0; }
        const unsigned fi = (unsigned)((4 * grp + w) * 16 + head);
        BST(o0, WS_O + fi * 2048 + voff); BST(o1, WS_O + fi * 2048 + 1024 + voff);
        STAMP(9);
        drain();
        put_flag(ws, FL_O + fi * FS, tag, lane);
    }
    STAMP(10);
    const int xcd = blockIdx.x & 7, bi = blockIdx.x >> 3;
    if (xcd < 7) {
        wait_flags<2>(ws, FL_BT + (unsigned)(xcd * 128 + w * 32 + r32) * FS, tag, !hi);
        STAMP(11);
        const unsigned bo = WS_BT + (unsigned)((64 * xcd + 16 * w + (lane >> 3)) * S + 64 * bi + (lane & 7) * 8) * 2;
        glds2c(ws, bo, bo + 8 * S * 2 - 1024, lds0 + 65536 + w * 2048);
        drain();
        arrive(sy + SY_T2); await(sy + SY_T2, 4);
        u32x4 o0, o1; transpose_tile(lds + 65536, 2048, lds + 73728 + w * 2304, nullptr, w, lane, o0, o1);
        const __amdgpu_buffer_rsrc_t ro = __builtin_amdgcn_make_buffer_rsrc(out, 0, S * 2 * D * 2, 0x00020000);
        const unsigned oo = (unsigned)((64 * bi + 16 * w + (lane >> 3)) * 2 * D + D + 64 * xcd) * 2 + (lane & 7) * 16;
        __builtin_amdgcn_raw_buffer_store_b128(o0, ro, oo, 0, 16); __builtin_amdgcn_raw_buffer_store_b128(o1, ro, oo + 8 * 2 * D * 2, 0, 16);
    }
    STAMP(12);
}

DI void role_b(const uint16_t* __restrict__ wo, const uint16_t* __restrict__ u, const uint16_t* __restrict__ lar, const uint16_t* __restrict__ aim,
               const uint16_t* __restrict__ cre, const uint16_t* __restrict__ cim, const uint16_t* __restrict__ ldt, const uint16_t* __restrict__ dskip,
               uint16_t* __restrict__ out, char* __restrict__ ws, LAS char* lds, int w, int lane, u64 t_start) {
    u64 tag;
    const unsigned lds0 = (unsigned)(uintptr_t)lds;
    LAS unsigned* sy = (LAS unsigned*)(lds + LSY);
    const __amdgpu_buffer_rsrc_t rs = __builtin_amdgcn_make_buffer_rsrc(ws, 0, WS_END, 0x00020000);
    const int b = blockIdx.x, xcd = b & 7, bi = b >> 3, r32 = lane & 31, hi = lane >> 5;
    const unsigned voff = lane * 16;
    LAS char* tab = lds + LB + w * TABW;
    const int qt = 8 * xcd + (bi >> 2), ct = 4 * (bi & 3) + w;
    bf16x8 wf[32];
#define LOADWF(i) wf[i] = BC8(__builtin_amdgcn_raw_buffer_load_b128(rs, voff, WS_WO + (unsigned)(((8 * w + (i)) & 31) * 14 + ct) * 1024, 16))
    if (xcd < 7) {
        const int cc = w >> 1, hh = w & 1, pair = 32 * xcd + bi, ch = 2 * pair + cc;
        LAS char* cb = lds + LB + cc * CBW;
        const unsigned po = (unsigned)(ch * NS + 128 * hh) / 2 + lane;
        const unsigned pl = ((const unsigned*)lar)[po], pa = ((const unsigned*)aim)[po], pr = ((const unsigned*)cre)[po], pi = ((const unsigned*)cim)[po];
        const unsigned uo = (unsigned)((64 * bi + 16 * w + (lane >> 3)) * D + 64 * xcd + (lane & 7) * 8) * 2;
        glds2(u, uo, uo + 8 * D * 2 - 1024, lds0 + LB + w * TABW + 4096);
        const u64 e0 = __hip_atomic_load((GAS u64*)ws, __ATOMIC_RELAXED, __HIP_MEMORY_SCOPE_AGENT);
        u32x2 gi[2][3];
#pragma unroll
        for (int ps = 0; ps < 2; ++ps)
#pragma unroll
            for (int i = 0; i < 3; ++i) gi[ps][i] = ((const u32x2*)g_idx.v[128 * hh + 64 * ps + lane])[i];
        const unsigned lw = ((const unsigned*)ldt)[pair], dw = ((const unsigned*)dskip)[pair];
        const float dt = rbf(expf(cc ? bf_hi(lw) : bf_lo(lw)));
        drain();
        STAMP(1);
        arrive(sy + SY_WO);
        if (w == 0 && lane == 0) __hip_atomic_store((LAS u64*)sy, mix(e0), __ATOMIC_RELAXED, __HIP_MEMORY_SCOPE_WORKGROUP);
        await(sy + SY_WO, 4);
        {
            u32x4 o0, o1; transpose_tile(lds + LB + 4096, TABW, tab, sy + SY_T1, w, lane, o0, o1);
            const unsigned to = WS_UT + (unsigned)((64 * xcd + 16 * w + (lane >> 3)) * S + 64 * bi) * 2 + (lane & 7) * 16;
            __builtin_amdgcn_s_setprio(3); BST(o0, to); BST(o1, to + 8 * S * 2); __builtin_amdgcn_s_setprio(PRIO_B);
        }
        const float cm = wave_max(fmaxf(fmaxf(fabsf(bf_lo(pr)), fabsf(bf_hi(pr))), fmaxf(fabsf(bf_lo(pi)), fabsf(bf_hi(pi)))));
        unsigned ce = __float_as_uint(cm) & 0x7f800000u; ce = ce < 0x01000000u ? 0x01000000u : (ce > 0x7e800000u ? 0x7e800000u : ce);
        const float cscale = __uint_as_float(0x7f000000u - ce), oscale = 2.0f * dt * __uint_as_float(ce);
        LAS unsigned* tw = (LAS unsigned*)(tab + lane * 4);
        LAS const char* trd = tab + r32 * TROW + hi * 16;
        f32x16 dacc = {};
        f32x2 z1, z8, z256, x0;
#define ANCH(vl, va, vr, vi) do { const float a2 = (-rbf(expf(vl)) * dt) * 1.4426950408889634f, rev = ((va) * dt) * 0.15915494309189535f; \
            z1 = anchor(a2, -rev, 1.f); z8 = anchor(a2, rev, 8.f); z256 = anchor(a2, -rev, 256.f); x0 = (f32x2){(vr) * cscale, -(vi) * cscale}; \
            PIN(z1); PIN(z8); PIN(z256); SBAR(); } while (0)
#define TW(row) tw[(row) * (TROW / 4)]
#define CHAIN() do { \
            const f32x2 z1s = rot(z1), zs = rot(z256), z8s = rot(z8); \
            f32x2 y[4], X[8]; y[1] = z8; y[2] = cmul(z8, z8, z8s); X[0] = x0; X[1] = cmul(x0, z1, z1s); f32x2 a = cmul(x0, z256, zs); \
            y[3] = cmul(y[2], z8, z8s); const f32x2 z32 = cmul(y[2], y[2], rot(y[2])), z32s = rot(z32); X[2] = cmul(X[1], z1, z1s); y[0] = z32; \
            f32x2 c4 = cmul(X[2], z256, zs); \
            unsigned c[2][7], d0 = pk_f16(X[0]), d1 = pk_f16(X[1]), d2 = pk_f16(y[1]); \
            c[0][0] = pk_f16(y[0]); c[0][1] = 0x3c00u; c[0][2] = pk_f16(y[2]); c[0][3] = pk_f16(y[3]); c[0][4] = pk_f16(X[2]); c[0][5] = pk_f16(a); c[0][6] = pk_f16(c4); \
            TW(0) = d0; TW(1) = d1; TW(22) = d2; SBAR(); \
            _Pragma("unroll") for (int i = 1; i < 9; ++i) { \
                f32x2 t0, t1, t2, t3, t4, t5, t6; const int j = i - 1; unsigned* pc = c[j & 1]; unsigned* nc = c[i & 1]; \
                if (i < 8) { if (i > 1) t0 = y[0].xx * z32; t1 = y[1].xx * z32; t2 = y[2].xx * z32; t3 = y[3].xx * z32; \
                    if (i < 6) t4 = X[i + 1].xx * z1; t5 = a.xx * z256; \
                    if (i == 3) t6 = X[4].xx * z256; if (i == 4 || i == 5) t6 = c4.xx * z256; if (i == 6) t6 = X[6].xx * z256; } SBAR(); \
                if (j != 1) TW(21 + (j ? 4 * j : 4)) = pc[0]; TW(21 + (j ? 4 * j + 1 : 0)) = pc[1]; TW(23 + 4 * j) = pc[2]; TW(24 + 4 * j) = pc[3]; \
                if (j < 6) TW(j + 2) = pc[4]; TW(j == 0 ? 8 : (j == 1 ? 12 : (j == 2 ? 14 : 13 + j))) = pc[5]; \
                if (j == 0) TW(9) = pc[6]; if (j == 3) TW(10) = pc[6]; if (j == 4) TW(13) = pc[6]; if (j == 5) TW(15) = pc[6]; if (j == 6) TW(11) = pc[6]; SBAR(); \
                if (i < 8) { if (i > 1) y[0] = pfma(y[0].yy, z32s, t0); y[1] = pfma(y[1].yy, z32s, t1); y[2] = pfma(y[2].yy, z32s, t2); y[3] = pfma(y[3].yy, z32s, t3); \
                    if (i < 6) X[i + 2] = pfma(X[i + 1].yy, z1s, t4); a = pfma(a.yy, zs, t5); \
                    if (i == 3) c4 = pfma(X[4].yy, zs, t6); if (i == 4 || i == 5) c4 = pfma(c4.yy, zs, t6); if (i == 6) c4 = pfma(X[6].yy, zs, t6); SBAR(); \
                    if (i > 1) nc[0] = pk_f16(y[0]); nc[1] = pk_f16(y[1]); nc[2] = pk_f16(y[2]); nc[3] = pk_f16(y[3]); \
                    if (i < 6) nc[4] = pk_f16(X[i + 2]); nc[5] = pk_f16(a); if (i >= 3 && i <= 6) nc[6] = pk_f16(c4); \
                    asm volatile("" :: "v"(pc[0]), "v"(pc[1]), "v"(pc[2]), "v"(pc[3]), "v"(pc[4]), "v"(pc[5]), "v"(pc[6])); SBAR(); } \
            } } while (0)
        f16x8 fa[8], fb[8];
#define RDT() do { _Pragma("unroll") for (int kk = 0; kk < 8; ++kk) { fa[kk] = *(LAS const f16x8*)(trd + kk * 32); fb[kk] = *(LAS const f16x8*)(trd + 21 * TROW + kk * 32); } SBAR(); } while (0)
#define MMT() do { _Pragma("unroll") for (int kk = 0; kk < 8; ++kk) dacc = MFMA_F16(fa[kk], fb[kk], dacc); } while (0)
        ANCH(bf_lo(pl), bf_lo(pa), bf_lo(pr), bf_lo(pi));
        tag = get_tag(sy); await(sy + SY_T1, 4);
        STAMP(13);
        CHAIN(); RDT();
        drain(); __builtin_amdgcn_s_setprio(3); put_flag(ws, FL_UT + (unsigned)(xcd * 128 + w * 32 + bi) * FS, tag, lane); __builtin_amdgcn_s_setprio(PRIO_B);
        STAMP(14);
        ANCH(bf_hi(pl), bf_hi(pa), bf_hi(pr), bf_hi(pi));
        MMT(); SBAR();
        CHAIN(); RDT();
        const unsigned utf = hi ? FL_WO + (unsigned)(w * 32 + r32) * FS : FL_UT + (unsigned)(xcd * 128 + (bi >> 3) * 32 + r32) * FS;
        const u64 utv = __hip_atomic_load((GAS u64*)(ws + utf), __ATOMIC_RELAXED, __HIP_MEMORY_SCOPE_AGENT); SBAR();
        MMT();
        STAMP(2);
        {
            LAS float* pt = (LAS float*)tab;
            *(LAS f32x4*)(pt + 8 * r32 + 4 * hi) = (f32x4){dacc[0] * oscale, dacc[1] * oscale, dacc[2] * oscale, dacc[3] * oscale};
            *(LAS f32x2*)(pt + (hi ? 384 + 2 * r32 : 256 + 4 * r32)) = (f32x2){dacc[4] * oscale, dacc[5] * oscale};
            *(LAS f32x2*)(pt + (hi ? 448 + 2 * r32 : 258 + 4 * r32)) = (f32x2){dacc[6] * oscale, dacc[7] * oscale};
            pt[hi ? 640 + r32 : 512 + r32] = dacc[8] * oscale;
            pt[hi ? 672 + r32 : 544 + r32] = dacc[9] * oscale;
            pt[hi ? 672 + r32 : 576 + r32] = dacc[10] * oscale;
            pt[hi ? 672 + r32 : 608 + r32] = dacc[11] * oscale;
        }
        LAS unsigned* ps_ = sy + SY_P + 5 * cc;
        arrive(ps_ + 0);
        LAS char* ui = cb + UOF;
        LAS char* kt = cb + KOF;
        await(ps_ + 0, 2);
#pragma unroll
        for (int ps = 0; ps < 2; ++ps) {
            const int v = 81 * hh + 64 * ps + lane;
            if (ps == 0 || lane < 17) {
                const f32x4 p0 = *(LAS const f32x4*)(cb + v * 16), p1 = *(LAS const f32x4*)(cb + TABW + v * 16);
                u32x2 r = {pk_bf16(p0[0] + p1[0], p0[1] + p1[1]), pk_bf16(p0[2] + p1[2], p0[3] + p1[3])};
                if (v == 161) r = (u32x2){0u, 0u};
                *(LAS u32x2*)(kt + v * 8) = r;
            }
        }
        arrive(ps_ + 1);
        if (__any(utv != tag)) wait_flags<8>(ws, utf, tag, utv != tag);
        CFENCE();
        const unsigned ub = WS_UT + (unsigned)(ch * S + 1024 * hh) * 2 + voff;
        const u32x4 u0 = BLD(ub), u1 = BLD(ub + 1024); SBAR();
        await(ps_ + 1, 2);
        STAMP(3);
        __builtin_amdgcn_s_setprio(PRIO_B2);
        const u32x4 z4 = {};
        {
            unsigned W[2][6];
#pragma unroll
            for (int ps = 0; ps < 2; ++ps)
#pragma unroll
                for (int i = 0; i < 6; ++i) { const unsigned gv = gi[ps][i >> 1][i & 1];
                    W[ps][i] = *(LAS const uint16_t*)(kt + (gv & 0xffffu));
                    if (i < 5) W[ps][i] |= (unsigned)*(LAS const uint16_t*)(kt + (gv >> 16)) << 16; }
            *(LAS u32x4*)(ui + (64 * hh + lane) * 16) = z4;
            if (lane < 16) *(LAS u32x4*)(ui + (128 + 16 * hh + lane) * 16) = z4;
            if (lane < 8) *(LAS u32x4*)(cb + (2 * hh + (lane >> 2)) * CPY + (256 + (lane & 3)) * 16) = z4;
#pragma unroll
            for (int ps = 0; ps < 2; ++ps) {
                unsigned V[5];
#pragma unroll
                for (int i = 0; i < 5; ++i) V[i] = __builtin_amdgcn_alignbit(W[ps][i + 1], W[ps][i], 16);
                LAS char* cp = cb + (128 * hh + 64 * ps + lane) * 16;
                *(LAS u32x4*)(cp) = (u32x4){W[ps][0], W[ps][1], W[ps][2], W[ps][3]};
                *(LAS u32x4*)(cp + CPY) = (u32x4){V[0], V[1], V[2], V[3]};
                *(LAS u32x4*)(cp + 2 * CPY) = (u32x4){W[ps][1], W[ps][2], W[ps][3], W[ps][4]};
                *(LAS u32x4*)(cp + 3 * CPY) = (u32x4){V[1], V[2], V[3], V[4]};
            }
        }
        *(LAS u32x4*)(ui + (32 + 32 * hh + (lane >> 2)) * 80 + (lane & 3) * 16) = u0;
        *(LAS u32x4*)(ui + (48 + 32 * hh + (lane >> 2)) * 80 + (lane & 3) * 16) = u1;
        arrive(ps_ + 2); await(ps_ + 2, 2);
        STAMP(4);
        __builtin_amdgcn_s_setprio(PRIO_BC);
        f32x16 a0 = {}, a1 = {};
        {
            const int sb = 7 - (r32 & 7);
            LAS const char* pa = cb + (sb & 3) * CPY + (255 - (r32 >> 3) + hi) * 16 + (sb >> 2) * 8 - 64 * (16 * hh + 15) - 2048;
            LAS const char* pa2 = pa + 8; asm volatile("" : "+v"(pa2));
            LAS const char* pb = ui + (32 + r32 - 16 * hh - 15) * 80 + 16 * hi;
            s16x4 ta[2][2], tb[2][2]; bf16x8 b0[2], b1[2];
#define T4(q, off) (*(LAS const s16x4*)((q) + (off)))
#define CSTEP(i, j, RD) do { const int o_ = 64 * (15 - ((j) >> 1)) + 32 * ((j) & 1), u_ = 80 * (15 - ((j) >> 1)) + 32 * ((j) & 1); \
                a1 = MFMA_BF(cat8(ta[i][0], ta[i][1]), b1[i], a1); SBAR(); if (RD) { ta[1 - i][0] = T4(pa, o_ + 2048); ta[1 - i][1] = T4(pa2, o_ + 2048); } SBAR(); \
                a0 = MFMA_BF(cat8(ta[i][0], ta[i][1]), b0[i], a0); SBAR(); if (RD) { b1[1 - i] = FR(pb, u_ + 2560); b0[1 - i] = FR(pb, u_); } SBAR(); \
                a1 = MFMA_BF(cat8(tb[i][0], tb[i][1]), b0[i], a1); SBAR(); if (RD) { tb[1 - i][0] = T4(pa, o_); tb[1 - i][1] = T4(pa2, o_); } SBAR(); } while (0)
            ta[0][0] = T4(pa, 960 + 2048); ta[0][1] = T4(pa2, 960 + 2048); b1[0] = FR(pb, 1200 + 2560); b0[0] = FR(pb, 1200); tb[0][0] = T4(pa, 960); tb[0][1] = T4(pa2, 960);
#pragma unroll
            for (int j = 0; j < 32; ++j) {
                if (j == 31) arrive(ps_ + 3);
                CSTEP(j & 1, j + 1, j < 31);
                if (ct < 14) LOADWF(j);
                if (HOLDB && (j & 3) == 3 && j < 31) { const unsigned h_ = __builtin_amdgcn_readfirstlane(__hip_atomic_load(sy + SY_H, __ATOMIC_RELAXED, __HIP_MEMORY_SCOPE_WORKGROUP)); if (h_ > 0 && h_ < 4 * HOLDB) await_slow(sy + SY_H, 4 * HOLDB); }
                SBAR();
            }
        }
        STAMP(5);
        __builtin_amdgcn_s_setprio(PRIO_B3);
        {
            u32x2 uu[4];
#pragma unroll
            for (int g = 0; g < 4; ++g) uu[g] = *(LAS const u32x2*)(ui + (32 + 32 * hh + r32) * 80 + 16 * g + 8 * hi);
            LAS char* ex = cb + r32 * 144 + hi * 16;
            const f32x16 snd = hh ? a0 : a1; const f32x16 y = hh ? a1 : a0;
            await(ps_ + 3, 2);
#pragma unroll
            for (int g = 0; g < 4; ++g) *(LAS f32x4*)(ex + hh * 4608 + g * 32) = (f32x4){snd[4 * g], snd[4 * g + 1], snd[4 * g + 2], snd[4 * g + 3]};
            arrive(ps_ + 4);
            const float dsk = cc ? bf_hi(dw) : bf_lo(dw);
            unsigned sk[8];
#pragma unroll
            for (int g = 0; g < 4; ++g)
#pragma unroll
                for (int e = 0; e < 2; ++e) sk[2 * g + e] = pk_bf16(bf_lo(uu[g][e]) * dsk, bf_hi(uu[g][e]) * dsk);
            await(ps_ + 4, 2);
            LAS char* sc = cb + 9216 + hh * 2048;
#pragma unroll
            for (int g = 0; g < 4; ++g) {
                const f32x4 p = *(LAS const f32x4*)(ex + (1 - hh) * 4608 + g * 32);
                u32x2 r;
#pragma unroll
                for (int e = 0; e < 2; ++e) {
                    const unsigned yb = pk_bf16(y[4 * g + 2 * e] + p[2 * e], y[4 * g + 2 * e + 1] + p[2 * e + 1]);
                    r[e] = pk_bf16(bf_lo(yb) + bf_lo(sk[2 * g + e]), bf_hi(yb) + bf_hi(sk[2 * g + e]));
                }
                *(LAS u32x2*)(sc + 64 * r32 + 16 * g + 8 * hi) = r;
            }
            const u32x4 o0 = *(LAS const u32x4*)(sc + voff), o1 = *(LAS const u32x4*)(sc + 1024 + voff);
            const unsigned bo = WS_BT + (unsigned)(ch * S + 1024 * hh) * 2 + voff;
            BST(o0, bo); BST(o1, bo + 1024);
        }
        arrive(sy + SY_BG);
        STAMP(6);
        drain();
        put_flag(ws, FL_BT + (unsigned)(xcd * 128 + bi * 4 + w) * FS, tag, lane);
    } else {
        {
            const unsigned nb = (bi & 1) ? 12 * 2 * D : 16 * 2 * D;
            unsigned v4[4];
#pragma unroll
            for (int i = 0; i < 4; ++i) { const unsigned o = (unsigned)(w * 4096 + i * 1024) + voff; v4[i] = (o < nb ? o : 0u) + 4096 - i * 1024; }
            glds4((const char*)(wo + (DH * (bi >> 1) + 16 * (bi & 1)) * D) - 4096, v4[0], v4[1], v4[2], v4[3], lds0 + LB + w * 4096);
        }
        const u64 e0 = __hip_atomic_load((GAS u64*)ws, __ATOMIC_RELAXED, __HIP_MEMORY_SCOPE_AGENT);
        drain();
        if (w == 0 && lane == 0) __hip_atomic_store((LAS u64*)sy, mix(e0), __ATOMIC_RELAXED, __HIP_MEMORY_SCOPE_WORKGROUP);
        arrive(sy + SY_WO); await(sy + SY_WO, 4);
        LAS const char* p = lds + LB + (4 * hi + ((lane & 15) >> 2)) * (2 * D) + ((lane >> 4) & 1) * 32 + (lane & 3) * 8;
#pragma unroll
        for (int i = 0; i < 4; ++i) {
            const int ct = w + 4 * i;
            if (ct < 14) {
                u32x4 o = __builtin_bit_cast(u32x4, cat8(trread(p + ct * 64), trread(p + 8 * 2 * D + ct * 64)));
                if (hi && (bi & 1)) { o[2] = 0; o[3] = 0; }
                BST(o, WS_WO + (unsigned)(bi * 14 + ct) * 1024 + voff);
            }
        }
        tag = get_tag(sy);
        drain();
        put_flag(ws, FL_WO + (unsigned)(w * 32 + bi) * FS, tag, lane);
        __builtin_amdgcn_s_setprio(0);
        arrive(sy + SY_BG);
        await_slow(sy + SY_G, 4);
        wait_flags<8>(ws, FL_WO + (unsigned)(w * 32 + r32) * FS, tag, !hi);
        if (ct < 14) {
#pragma unroll
            for (int i = 0; i < 32; ++i) { LOADWF(i); __builtin_amdgcn_s_sleep(WOSL); SBAR(); }
        }
    }
    STAMP(7);
    const __amdgpu_buffer_rsrc_t ro = __builtin_amdgcn_make_buffer_rsrc(out, 0, S * 2 * D * 2, 0x00020000);
    await(sy + SY_BG, 4);
    STAMP(8);
    await_slow(sy + SY_X, 4);
    wait_flags<2>(ws, FL_O + (unsigned)(qt * 16 + 4 * w + lane) * FS, tag, lane < 4);
    STAMP(9);
    u32x4 of[8];
#pragma unroll
    for (int i = 0; i < 8; ++i) of[i] = BLD(WS_O + (unsigned)(qt * 32 + 8 * w + i) * 1024 + voff);
#pragma unroll
    for (int i = 0; i < 8; ++i) *(LAS u32x4*)(lds + LB + (8 * w + i) * 1024 + voff) = of[i];
    arrive(sy + SY_B2);
    STAMP(10);
    f32x16 acc = {};
    if (ct < 14) {
#pragma unroll
        for (int i = 0; i < 8; ++i) acc = MFMA_BF(wf[i], BC8(of[i]), acc);
    }
    SBAR();
    await(sy + SY_B2, 4);
    STAMP(11);
    if (ct < 14) {
        LAS const char* po[3];
#pragma unroll
        for (int j = 0; j < 3; ++j) po[j] = lds + LB + ((w + 1 + j) & 3) * 8192 + voff;
        bf16x8 og[4];
#pragma unroll
        for (int i = 0; i < 3; ++i) og[i] = FR(po[0], i * 1024);
#pragma unroll
        for (int i = 0; i < 24; ++i) { if (i < 21) og[(i + 3) & 3] = FR(po[(i + 3) >> 3], ((i + 3) & 7) * 1024); SBAR(); acc = MFMA_BF(wf[8 + i], og[i & 3], acc); SBAR(); }
        LAS char* sc = lds + LB + 40960 + w * 2304;
#pragma unroll
        for (int g = 0; g < 4; ++g) *(LAS u32x2*)(sc + r32 * 72 + 16 * g + 8 * hi) = (u32x2){pk_bf16(acc[4 * g], acc[4 * g + 1]), pk_bf16(acc[4 * g + 2], acc[4 * g + 3])};
        LAS const char* sr = sc + (lane >> 2) * 72 + (lane & 3) * 16;
        const u32x2 a0 = *(LAS const u32x2*)sr, a1 = *(LAS const u32x2*)(sr + 8), b0 = *(LAS const u32x2*)(sr + 16 * 72), b1 = *(LAS const u32x2*)(sr + 16 * 72 + 8);
        const unsigned oo = (unsigned)((32 * qt + (lane >> 2)) * 2 * D + 32 * ct) * 2 + (lane & 3) * 16;
        __builtin_amdgcn_raw_buffer_store_b128((u32x4){a0[0], a0[1], a1[0], a1[1]}, ro, oo, 0, 16);
        __builtin_amdgcn_raw_buffer_store_b128((u32x4){b0[0], b0[1], b1[0], b1[1]}, ro, oo + 16 * 2 * D * 2, 0, 16);
    }
    if (b == 0 && w == 0) put_flag(ws, 0, tag, lane);
    STAMP(12);
}

__global__ __launch_bounds__(512, 2) void fused(const uint16_t* __restrict__ x, const uint16_t* __restrict__ wqkv, const uint16_t* __restrict__ wo,
                                               const uint16_t* __restrict__ u, const uint16_t* __restrict__ lar, const uint16_t* __restrict__ aim,
                                               const uint16_t* __restrict__ cre, const uint16_t* __restrict__ cim, const uint16_t* __restrict__ ldt,
                                               const uint16_t* __restrict__ dskip, uint16_t* __restrict__ out, char* __restrict__ ws) {
    extern __shared__ __attribute__((aligned(16))) char smem[];
    LAS char* lds = (LAS char*)smem;
    const int lane = threadIdx.x & 63, w = __builtin_amdgcn_readfirstlane(threadIdx.x >> 6);
    const u64 t_start = __builtin_amdgcn_s_memrealtime();
    if (w >= 4) __builtin_amdgcn_s_setprio(3);
    if (w == 4) ((LAS unsigned*)(lds + LSY))[lane] = 0;
    asm volatile("" :: "s"(x), "s"(wqkv), "s"(wo), "s"(u), "s"(lar), "s"(aim), "s"(cre), "s"(cim), "s"(ldt), "s"(dskip), "s"(out), "s"(ws));
    asm volatile("s_waitcnt lgkmcnt(0)\n\ts_barrier" ::: "memory");
    STAMP(30);
    if (w < 4) role_a(x, wqkv, out, ws, lds, w, lane, t_start);
    else role_b(wo, u, lar, aim, cre, cim, ldt, dskip, out, ws, lds, w - 4, lane, t_start);
}
}

extern "C" void kernel_launch(void* const* d_in, const int* in_sizes, int n_in, void* d_out, int out_size, void* d_ws, size_t ws_size, hipStream_t stream) {
    using namespace k;
    (void)hipFuncSetAttribute((const void*)fused, hipFuncAttributeMaxDynamicSharedMemorySize, LDS_BYTES);
    const uint16_t* const* in = (const uint16_t* const*)d_in;
    fused<<<256, 512, LDS_BYTES, stream>>>(in[0], in[1], in[2], in[3], in[4], in[5], in[6], in[7], in[8], in[9], (uint16_t*)d_out, (char*)d_ws);
}
```

```cpp
#include <hip/hip_runtime.h>
#include <hip/hip_bf16.h>
#include <stdint.h>

typedef short bf16x8 __attribute__((ext_vector_type(8)));
typedef _Float16 f16x8 __attribute__((ext_vector_type(8)));
typedef _Float16 f16x2 __attribute__((ext_vector_type(2)));
typedef __bf16 bf16x2 __attribute__((ext_vector_type(2)));
typedef short s16x4 __attribute__((ext_vector_type(4)));
typedef float f32x16 __attribute__((ext_vector_type(16)));
typedef float f32x4 __attribute__((ext_vector_type(4)));
typedef float f32x2 __attribute__((ext_vector_type(2)));
typedef unsigned u32x4 __attribute__((ext_vector_type(4)));
typedef unsigned u32x2 __attribute__((ext_vector_type(2)));
typedef unsigned long long u64;
#define LAS __attribute__((address_space(3)))
#define GAS __attribute__((address_space(1)))
#define DI __device__ __forceinline__
#define MFMA_BF(a, b, c) __builtin_amdgcn_mfma_f32_32x32x16_bf16((a), (b), (c), 0, 0, 0)
#define MFMA_F16(a, b, c) __builtin_amdgcn_mfma_f32_32x32x16_f16((a), (b), (c), 0, 0, 0)
#define SBAR() __builtin_amdgcn_sched_barrier(0)
#define CFENCE() asm volatile("" ::: "memory")
#define PIN(x) asm volatile("" : "+v"(x))
#define BC8(x) __builtin_bit_cast(bf16x8, (x))
#define EX(x) __builtin_amdgcn_exp2f(x)
#define PRIO_A1 0
#define PRIO_B 0
#define PRIO_B2 1
#define ROT 1
#define KVM 6u
#define HOLDB 0
#define PRIO_B3 1
#define PRIO_BC 0
#define FLJ 13
#define WOSL 2

namespace k {
#define STAMP(i)
constexpr int S = 2048, D = 448, DH = 28, NS = 256;
constexpr unsigned FS = 128;
constexpr unsigned FL_KV = FS, FL_O = FL_KV + 1024 * FS, FL_UT = FL_O + 1024 * FS, FL_BT = FL_UT + 896 * FS, FL_WO = FL_BT + 896 * FS;
constexpr unsigned WS_KV = 512u << 10;
constexpr unsigned WS_O = WS_KV + (4u << 20);
constexpr unsigned WS_WO = WS_O + (2u << 20);
constexpr unsigned WS_UT = WS_WO + (448u << 10);
constexpr unsigned WS_BT = WS_UT + D * S * 2;
constexpr unsigned WS_END = WS_BT + D * S * 2;
constexpr int WSL = 8192, LWQ = 4 * WSL, LXR = LWQ + 16384, XSL = 16384, LB = LXR + 3 * XSL;
constexpr int LQ = LXR, LX = 0;
constexpr int TROW = 272, TABW = 53 * TROW, CBW = 2 * TABW, CPY = 4160, UOF = 4 * CPY, KOF = CBW - 1312;
constexpr int LSY = LB + 2 * CBW, LDS_BYTES = LSY + 256;
static_assert(LDS_BYTES <= 163840 && (LSY & 15) == 0 && UOF + 7680 <= KOF, "lds");
enum { SY_TAG = 0, SY_F = 2, SY_P = 10, SY_WO = 20, SY_T1 = 21, SY_BG = 22, SY_B2 = 23, SY_Q = 24, SY_X = 25, SY_G = 26, SY_T2 = 27, SY_H = 28 };
constexpr int SPIN = 1 << 18;

struct IdxTab { uint16_t v[256][12]; };
constexpr IdxTab make_idx() {
    IdxTab t = {};
    for (int y = 0; y < 256; ++y) for (int e = 0; e < 12; ++e) {
        const int j = 2047 - 8 * y - e; int ix = 644;
        if (j >= 0) { const int sh = j >= 1024 ? 3 : (j >= 512 ? 2 : (j >= 256 ? 1 : 0));
            ix = sh ? 128 * sh + ((j + (1 << (sh - 1)) - 1 + ((j >> sh) & 1)) >> sh) : j; }
        t.v[y][e] = (uint16_t)(2 * ix);
    }
    return t;
}
__device__ const IdxTab g_idx __attribute__((aligned(16))) = make_idx();

DI unsigned pk_bf16(float a, float b) { f32x2 v = {a, b}; return __builtin_bit_cast(unsigned, __builtin_convertvector(v, bf16x2)); }
DI unsigned pk_f16(f32x2 v) { return __builtin_bit_cast(unsigned, __builtin_convertvector(v, f16x2)); }
DI float bf_lo(unsigned w) { return __uint_as_float(w << 16); }
DI float bf_hi(unsigned w) { return __uint_as_float(w & 0xffff0000u); }
DI float rbf(float x) { return bf_lo(pk_bf16(x, x)); }
#define GL0 "s_mov_b32 %0, m0\n\ts_mov_b32 m0, %2\n\ts_nop 0\n\t"
#define GL1(V, O, C) "global_load_lds_dwordx4 " V ", %1 offset:" #O C "\n\t"
#define GLE "s_mov_b32 m0, %0"
DI void glds4(const void* sb, unsigned v0, unsigned v1, unsigned v2, unsigned v3, unsigned lb) { unsigned sv;
    asm volatile(GL0 GL1("%3", 0, "") GL1("%4", 1024, "") GL1("%5", 2048, "") GL1("%6", 3072, "") GLE : "=&s"(sv) : "s"(sb), "s"(lb), "v"(v0), "v"(v1), "v"(v2), "v"(v3) : "memory"); }
DI void glds3(const void* sb, unsigned v0, unsigned v1, unsigned v2, unsigned lb) { unsigned sv;
    asm volatile(GL0 GL1("%3", 0, "") GL1("%4", 1024, "") GL1("%5", 2048, "") GLE : "=&s"(sv) : "s"(sb), "s"(lb), "v"(v0), "v"(v1), "v"(v2) : "memory"); }
DI void glds2(const void* sb, unsigned v0, unsigned v1, unsigned lb) { unsigned sv;
    asm volatile(GL0 GL1("%3", 0, "") GL1("%4", 1024, "") GLE : "=&s"(sv) : "s"(sb), "s"(lb), "v"(v0), "v"(v1) : "memory"); }
DI void glds1(const void* sb, unsigned v0, unsigned lb) { unsigned sv;
    asm volatile(GL0 GL1("%3", 0, "") GLE : "=&s"(sv) : "s"(sb), "s"(lb), "v"(v0) : "memory"); }
DI void glds2c(const void* sb, unsigned v0, unsigned v1, unsigned lb) { unsigned sv;
    asm volatile(GL0 GL1("%3", 0, " sc1") GL1("%4", 1024, " sc1") GLE : "=&s"(sv) : "s"(sb), "s"(lb), "v"(v0), "v"(v1) : "memory"); }
DI float wave_max(float v) {
#define DPPMAX(ctrl, rm) v = fmaxf(v, __uint_as_float(__builtin_amdgcn_update_dpp(0, __float_as_uint(v), ctrl, rm, 0xf, true)))
    DPPMAX(0x111, 0xf); DPPMAX(0x112, 0xf); DPPMAX(0x114, 0xf); DPPMAX(0x118, 0xf); DPPMAX(0x142, 0xa); DPPMAX(0x143, 0xc);
    return __uint_as_float(__builtin_amdgcn_readlane(__float_as_uint(v), 63));
}
DI s16x4 trread(LAS const char* p) { return __builtin_bit_cast(s16x4, __builtin_amdgcn_ds_read_tr16_b64_v4i16((LAS s16x4*)p)); }
DI bf16x8 cat8(s16x4 a, s16x4 b) { return __builtin_shufflevector(a, b, 0, 1, 2, 3, 4, 5, 6, 7); }
DI f32x2 pfma(f32x2 a, f32x2 b, f32x2 c) { return __builtin_elementwise_fma(a, b, c); }
DI f32x2 cmul(f32x2 a, f32x2 b, f32x2 bs) { return pfma(a.yy, bs, a.xx * b); }
DI f32x2 rot(f32x2 b) { return (f32x2){-b.y, b.x}; }
DI f32x2 anchor(float a2, float rev, float s) {
    const float mag = EX(a2 * s);
    float r = rev * s; r -= __builtin_rintf(r);
    return (f32x2){mag * __builtin_amdgcn_cosf(r), mag * __builtin_amdgcn_sinf(r)};
}
DI void arrive(LAS unsigned* c) { asm volatile("s_mov_b64 exec, 1\n\tds_add_u32 %0, %1\n\ts_mov_b64 exec, -1" :: "v"((unsigned)(uintptr_t)c), "v"(1u) : "memory"); }
DI void await(LAS unsigned* c, unsigned n) {
    unsigned t, i = SPIN;
    asm volatile("1:\n\tds_read_b32 %0, %2\n\ts_waitcnt lgkmcnt(0)\n\tv_cmp_gt_u32 vcc, %3, %0\n\ts_cbranch_vccz 2f\n\ts_sleep 1\n\ts_sub_u32 %1, %1, 1\n\ts_cbranch_scc0 1b\n2:"
                 : "=&v"(t), "+s"(i) : "v"((unsigned)(uintptr_t)c), "s"(n) : "vcc", "scc", "memory");
}
DI void await_slow(LAS unsigned* c, unsigned n) {
    unsigned t, i = SPIN;
    asm volatile("1:\n\tds_read_b32 %0, %2\n\ts_waitcnt lgkmcnt(0)\n\tv_cmp_gt_u32 vcc, %3, %0\n\ts_cbranch_vccz 2f\n\ts_sleep 3\n\ts_sub_u32 %1, %1, 1\n\ts_cbranch_scc0 1b\n2:"
                 : "=&v"(t), "+s"(i) : "v"((unsigned)(uintptr_t)c), "s"(n) : "vcc", "scc", "memory");
}
DI u64 get_tag(LAS unsigned* sy) {
    await(sy, 1);
    const u64 v = __hip_atomic_load((LAS u64*)sy, __ATOMIC_RELAXED, __HIP_MEMORY_SCOPE_WORKGROUP);
    return ((u64)(unsigned)__builtin_amdgcn_readfirstlane((unsigned)(v >> 32)) << 32) | (unsigned)__builtin_amdgcn_readfirstlane((unsigned)v);
}
DI void drain() { asm volatile("s_waitcnt vmcnt(0)" ::: "memory"); }
DI void put_flag(char* ws, unsigned off, u64 tag, int lane) { if (lane == 0) __hip_atomic_store((GAS u64*)(ws + off), tag, __ATOMIC_RELAXED, __HIP_MEMORY_SCOPE_AGENT); }
template <int SLP> DI void wait_flags(char* ws, unsigned off, u64 tag, bool pend) {
    GAS u64* p = (GAS u64*)(ws + off);
    CFENCE();
#pragma unroll 1
    for (int i = 0; i < SPIN; ++i) {
        if (pend) pend = __hip_atomic_load(p, __ATOMIC_RELAXED, __HIP_MEMORY_SCOPE_AGENT) != tag;
        if (!__any(pend)) break;
        __builtin_amdgcn_s_sleep(SLP);
    }
    CFENCE();
}
#define BST(v, off) __builtin_amdgcn_raw_buffer_store_b128((v), rs, (off), 0, 16)
#define BLD(off) __builtin_amdgcn_raw_buffer_load_b128(rs, (off), 0, 16)
#define FR(p, off) (*(LAS const bf16x8*)((p) + (off)))
DI float swapmax(float m) { const auto r = __builtin_amdgcn_permlane32_swap(__float_as_uint(m), __float_as_uint(m), false, false); return fmaxf(__uint_as_float(r[0]), __uint_as_float(r[1])); }
DI u32x4 put_ref(u32x4 q1, float m, int hi) {
    const float m1 = rbf(m), m2 = rbf(m - m1), m3 = m - m1 - m2;
    if (hi) { q1[2] = pk_bf16(-m1, -m2); q1[3] = pk_bf16(-m3, 0.f); }
    return q1;
}
DI u64 mix(u64 e) { u64 z = e + 0x9E3779B97F4A7C15ull; z = (z ^ (z >> 30)) * 0xBF58476D1CE4E5B9ull; z = (z ^ (z >> 27)) * 0x94D049BB133111EBull; return (z ^ (z >> 31)) | 1ull; }
DI void transpose_tile(LAS const char* stage, int pst, LAS char* scratch, LAS unsigned* done, int w, int lane, u32x4& o0, u32x4& o1) {
    LAS const char* p = stage + (lane >> 4) * pst + ((lane & 15) >> 2) * 128 + w * 32 + (lane & 3) * 8;
    const bf16x8 t0 = cat8(trread(p), trread(p + 512)), t1 = cat8(trread(p + 1024), trread(p + 1536));
    if (done) arrive(done);
    LAS char* sc = scratch + (lane & 15) * 144 + (lane >> 4) * 32;
    *(LAS bf16x8*)sc = t0; *(LAS bf16x8*)(sc + 16) = t1;
    LAS const char* sr = scratch + (lane >> 3) * 144 + (lane & 7) * 16;
    o0 = *(LAS const u32x4*)sr; o1 = *(LAS const u32x4*)(sr + 8 * 144);
}

DI void role_a(const uint16_t* __restrict__ x, const uint16_t* __restrict__ wqkv, uint16_t* __restrict__ out, char* __restrict__ ws, LAS char* lds, int w, int lane) {
    const int head = 4 * (blockIdx.x & 3) + ((blockIdx.x >> 3) & 3), grp = 8 * ((blockIdx.x >> 2) & 1) + (blockIdx.x >> 5);
    const unsigned lds0 = (unsigned)(uintptr_t)lds;
    const int r32 = lane & 31, hi = lane >> 5;
    const int wsh = (head & 7) == 7 ? 4 : 0;
    const unsigned wv = (unsigned)((lane >> 2) * 3 * D + (lane & 3) * 8) * 2;
    const char* wb = (const char*)(wqkv + DH * head - wsh + (16 * w) * 3 * D);
    const unsigned xv0 = (unsigned)((lane >> 3) * D + (((lane & 7) ^ (lane >> 4)) * 8)) * 2, xv1 = xv0 ^ 64;
    const char* xb = (const char*)(x + (128 * grp + 32 * w) * D);
    const int rho = ROT ? ((blockIdx.x >> 3) + ((blockIdx.x >> 1) & 3)) % 7 : 0;
#define SP(st) ((st) + rho >= 7 ? (st) + rho - 7 : (st) + rho)
#define WQO(st) ((st) < 4 ? LWQ + (st) * 4096 : ((st) < 6 ? 3 * WSL + ((st) - 4) * 4096 : 0))
#define LOADW(st) glds2(wb + SP(st) * (64 * 3 * D * 2), wv + 2 * D, wv + 4 * D - 1024, lds0 + ((st) & 3) * WSL + w * 2048)
#define LOADQ(st) glds1(wb + SP(st) * (64 * 3 * D * 2), wv, lds0 + WQO(st) + w * 1024)
#define LOADX(st) glds4(xb + SP(st) * 128, xv0, xv1 + 16 * D - 1024, xv0 + 32 * D - 2048, xv1 + 48 * D - 3072, lds0 + LXR + ((st) % 3) * XSL + w * 4096)
    LOADW(0); LOADX(0);
    STAMP(1);
    LOADW(1); LOADX(1); LOADW(2); LOADX(2);
    __builtin_amdgcn_s_setprio(PRIO_A1);
    LAS unsigned* sy = (LAS unsigned*)(lds + LSY);
    const __amdgpu_buffer_rsrc_t rs = __builtin_amdgcn_make_buffer_rsrc(ws, 0, WS_END, 0x00020000);
    const int xs = (r32 >> 1) & 7;
    LAS const char* xp[4];
#pragma unroll
    for (int ks = 0; ks < 4; ++ks) xp[ks] = lds + LXR + w * 4096 + r32 * 128 + (((2 * ks + hi) ^ xs) * 16);
    LAS const char* wp = lds + (8 * hi + ((lane & 15) >> 2)) * 64 + ((lane >> 4) & 1) * 32 + (lane & 3) * 8 + wsh * 2;
    f32x16 aq = {}, ak = {}, av = {};
    bf16x8 xf[7][4]; s16x4 fr[4][4];
#define RX(st, ks) xf[st][ks] = FR(xp[ks], ((st) % 3) * XSL)
#define RW(st, ks, j) fr[ks][j] = trread(wp + ((st) & 3) * WSL + (ks) * 2048 + ((j) >> 1) * 1024 + ((j) & 1) * 256)
#define MK(st, ks) ak = MFMA_BF(cat8(fr[ks][0], fr[ks][1]), xf[st][ks], ak); SBAR()
#define MV(st, ks) av = MFMA_BF(xf[st][ks], cat8(fr[ks][2], fr[ks][3]), av); SBAR()
#define QSTEP(st, ks) MK(st, ks); RW(st, ks + 1, 0); RW(st, ks + 1, 1); SBAR(); MV(st, ks); RW(st, ks + 1, 2); RW(st, ks + 1, 3); SBAR()
    u64 tag = 0;
    asm volatile("s_waitcnt vmcnt(12)" ::: "memory");
    arrive(sy + SY_F); await(sy + SY_F, 4);
    STAMP(16);
    RX(0, 0); RW(0, 0, 0); RW(0, 0, 1); RW(0, 0, 2); RW(0, 0, 3); RX(0, 1); RX(0, 2); RX(0, 3); SBAR();
#pragma unroll
    for (int st = 0; st < 7; ++st) {
        QSTEP(st, 0); if (st < 4) LOADW(st + 3); if (st == 4) { LOADQ(4); LOADQ(5); } if (st == 5) LOADQ(6);
        QSTEP(st, 1); if (st < 4) LOADX(st + 3);
        QSTEP(st, 2); if (st == 3) { LOADQ(0); LOADQ(1); LOADQ(2); LOADQ(3); }
        if (st < 6) {
            if (st == 3) asm volatile("s_waitcnt vmcnt(16)" ::: "memory"); else if (st == 5) asm volatile("s_waitcnt vmcnt(7)" ::: "memory"); else asm volatile("s_waitcnt vmcnt(12)" ::: "memory");
            arrive(sy + SY_F + 1 + st); SBAR();
        }
        MK(st, 3);
        if (st < 6) { await(sy + SY_F + 1 + st, 4); RX(st + 1, 0); RW(st + 1, 0, 0); RW(st + 1, 0, 1); RW(st + 1, 0, 2); RW(st + 1, 0, 3); } SBAR();
        MV(st, 3); if (st < 6) { RX(st + 1, 1); RX(st + 1, 2); RX(st + 1, 3); } SBAR();
        if (st == 2) tag = get_tag(sy);
    }
    STAMP(3);
    const float cq = 0.18898223650461362f * 1.4426950408889634f;
    const unsigned voff = lane * 16;
    u32x4 q0, q1, k0, k1, v0, v1;
    const unsigned tb = WS_KV + (unsigned)(head * 64 + 4 * grp + w) * 4096 + voff;
#pragma unroll
    for (int i = 0; i < 4; ++i) { k0[i] = pk_bf16(ak[2 * i], ak[2 * i + 1]); k1[i] = pk_bf16(ak[8 + 2 * i], ak[9 + 2 * i]); }
    if (hi) { k1[2] = 0x3f803f80u; k1[3] = 0x00003f80u; }
    BST(k0, tb); BST(k1, tb + 1024);
#pragma unroll
    for (int i = 0; i < 4; ++i) {
        v0[i] = pk_bf16(av[2 * i], av[2 * i + 1]); v1[i] = pk_bf16(av[8 + 2 * i], av[9 + 2 * i]);
        if (r32 >= DH) { v0[i] = v1[i] = (r32 == DH ? 0x3f803f80u : 0u); }
    }
    BST(v0, tb + 2048); BST(v1, tb + 3072);
    asm volatile("s_waitcnt vmcnt(4)" ::: "memory");
    arrive(sy + SY_F + 7); await(sy + SY_F + 7, 4);
    {
        s16x4 fq[4][2];
#define RQ(j) do { fq[(j) & 3][0] = trread(wp + WQO((j) >> 2) + ((j) & 3) * 1024); fq[(j) & 3][1] = trread(wp + WQO((j) >> 2) + ((j) & 3) * 1024 + 256); } while (0)
        RQ(0); RQ(1); RQ(2); SBAR();
#pragma unroll
        for (int j = 0; j < 28; ++j) {
            aq = MFMA_BF(cat8(fq[j & 3][0], fq[j & 3][1]), xf[j >> 2][j & 3], aq); SBAR(); if (j < 25) RQ(j + 3); SBAR();
            if (j == FLJ) { drain(); put_flag(ws, FL_KV + (unsigned)((head * 4 + w) * 16 + grp) * FS, tag, lane); STAMP(4); }
        }
    }
    STAMP(17);
#pragma unroll
    for (int i = 0; i < 4; ++i) { q0[i] = pk_bf16(aq[2 * i] * cq, aq[2 * i + 1] * cq); q1[i] = pk_bf16(aq[8 + 2 * i] * cq, aq[9 + 2 * i] * cq); }
    if (hi) { q1[2] = 0; q1[3] = 0; }
    GAS u64* kvf = (GAS u64*)(ws + FL_KV + (unsigned)((head * 4 + w) * 16 + ((grp + lane) & 15)) * FS);
    const f32x16 zero = {};
    {
        f32x16 s = MFMA_BF(BC8(k0), BC8(q0), zero); s = MFMA_BF(BC8(k1), BC8(q1), s);
        float m = s[0];
#pragma unroll
        for (int i = 1; i < 16; ++i) m = fmaxf(m, s[i]);
        q1 = put_ref(q1, swapmax(m), hi);
    }
    { LAS u32x4* qo = (LAS u32x4*)(lds + LQ + w * 2048) + lane; qo[0] = q0; qo[64] = q1; }
    arrive(sy + SY_Q);
    const u64 kv1 = __hip_atomic_load(kvf, __ATOMIC_RELAXED, __HIP_MEMORY_SCOPE_AGENT); SBAR();
    await(sy + SY_Q, 4);
    __builtin_amdgcn_s_setprio(0);
    bf16x8 Q[4][2];
    Q[0][0] = BC8(q0); Q[0][1] = BC8(q1);
#pragma unroll
    for (int i = 1; i < 4; ++i) { LAS const char* p = lds + LQ + ((w + i) & 3) * 2048 + voff; Q[i][0] = FR(p, 0); Q[i][1] = FR(p, 1024); }
    u32x4 kv[3][4] = {{k0, k1, v0, v1}, {}, {}};
    f32x16 O[4] = {}, sa, sb;
    u32x4 p0, p1;
#define GRP(SC, i, P, j) do { float e0 = EX(SC[i]); SBAR(); float e1 = EX(SC[i + 1]); SBAR(); float e2 = EX(SC[i + 2]); SBAR(); float e3 = EX(SC[i + 3]); SBAR(); \
        P[j] = pk_bf16(e0, e1); SBAR(); P[j + 1] = pk_bf16(e2, e3); PIN(P); SBAR(); } while (0)
#define SLOT(SC, SN, cb, i, QK, PV1, LD) do { constexpr int nb_ = (i) < 3 ? (cb) : ((cb) + 1) % 3, pb_ = (i) > 0 ? (cb) : ((cb) + 2) % 3; \
        if (QK) SN = MFMA_BF(BC8(kv[nb_][0]), Q[((i) + 1) & 3][0], zero); SBAR(); GRP(SC, 0, p0, 0); \
        if (PV1) O[((i) + 3) & 3] = MFMA_BF(BC8(kv[pb_][3]), BC8(p1), O[((i) + 3) & 3]); SBAR(); \
        if (LD) { kv[((cb) + 2) % 3][i] = __builtin_amdgcn_raw_buffer_load_b128(rs, voff + (i) * 1024, kvo, 16); SBAR(); } GRP(SC, 4, p0, 2); \
        if (QK) SN = MFMA_BF(BC8(kv[nb_][1]), Q[((i) + 1) & 3][1], SN); SBAR(); GRP(SC, 8, p1, 0); \
        O[i] = MFMA_BF(BC8(kv[cb][2]), BC8(p0), O[i]); SBAR(); GRP(SC, 12, p1, 2); } while (0)
    const unsigned kvb = WS_KV + (unsigned)(head * 64 + w) * 4096;
    unsigned kvo = 0;
    STAMP(5);
    bool pend = lane > 0 && lane < 16 && kv1 != tag;
#define NEED(msk) do { CFENCE(); _Pragma("unroll 1") for (int i_ = 0; i_ < SPIN; ++i_) { \
        if (pend) pend = __hip_atomic_load(kvf, __ATOMIC_RELAXED, __HIP_MEMORY_SCOPE_AGENT) != tag; \
        miss = (unsigned)__ballot(pend); if (!(miss & (msk))) break; __builtin_amdgcn_s_sleep(2); } CFENCE(); } while (0)
    unsigned miss = (unsigned)__ballot(pend);
    if (miss & KVM) NEED(KVM);
    CFENCE();
    STAMP(6);
#pragma unroll
    for (int j = 1; j < 3; ++j)
#pragma unroll
        for (int f = 0; f < 4; ++f) kv[j][f] = __builtin_amdgcn_raw_buffer_load_b128(rs, voff + f * 1024, kvb + ((grp + j) & 15) * 16384, 16);
    arrive(sy + SY_G);
    sa = MFMA_BF(BC8(k0), Q[0][0], zero); sa = MFMA_BF(BC8(k1), Q[0][1], sa);
    SLOT(sa, sb, 0, 0, true, false, false); SLOT(sb, sa, 0, 1, true, true, false); SLOT(sa, sb, 0, 2, true, true, false); SLOT(sb, sa, 0, 3, true, true, false);
    STAMP(7);
    int nx = grp + 3;
#define TILE(cb) do { kvo = kvb + (nx & 15) * 16384; ++nx; SBAR(); \
        SLOT(sa, sb, cb, 0, true, true, true); SLOT(sb, sa, cb, 1, true, true, true); SLOT(sa, sb, cb, 2, true, true, true); SLOT(sb, sa, cb, 3, true, true, true); } while (0)
#pragma unroll 1
    for (unsigned msk = 0x38; msk < 0x40000; msk <<= 3) { if (__builtin_expect(miss & msk, 0)) NEED(msk); if (HOLDB) arrive(sy + SY_H); TILE(1); TILE(2); TILE(0); }
    O[3] = MFMA_BF(BC8(kv[0][3]), BC8(p1), O[3]);
    STAMP(8);
    f32x16 o = O[0];
    {
#pragma unroll
        for (int i = 1; i < 4; ++i) {
            LAS char* ex = lds + LX + (((w + i) & 3) * 3 + i - 1) * 4096 + voff;
#pragma unroll
            for (int g = 0; g < 4; ++g) *(LAS f32x4*)(ex + g * 1024) = (f32x4){O[i][4 * g], O[i][4 * g + 1], O[i][4 * g + 2], O[i][4 * g + 3]};
        }
        arrive(sy + SY_X); await(sy + SY_X, 4);
        LAS const char* er = lds + LX + w * 3 * 4096 + voff;
#pragma unroll
        for (int i = 0; i < 3; ++i)
#pragma unroll
            for (int g = 0; g < 4; ++g) { const f32x4 r = *(LAS const f32x4*)(er + i * 4096 + g * 1024);
#pragma unroll
                for (int e = 0; e < 4; ++e) o[4 * g + e] += r[e]; }
        f32x2 chk = {0.f, 0.f};
#pragma unroll
        for (int i = 0; i < 8; ++i) chk = pfma((f32x2){o[2 * i], o[2 * i + 1]}, (f32x2){0.f, 0.f}, chk);
        if (__builtin_expect(__any(!(chk[0] + chk[1] == 0.f) || !(o[12] < 1e30f)), 0)) {
            const unsigned hb = WS_KV + (unsigned)head * 64 * 4096 + voff;
            u32x4 qr = __builtin_bit_cast(u32x4, Q[0][1]); if (hi) { qr[2] = 0; qr[3] = 0; }
            float mm = -INFINITY;
            wait_flags<4>(ws, FL_KV + (unsigned)(head * 64 + lane) * FS, tag, true);
#pragma unroll 1
            for (int t = 0; t < 64; ++t) {
                f32x16 s = MFMA_BF(BC8(BLD(hb + t * 4096)), Q[0][0], zero); s = MFMA_BF(BC8(BLD(hb + t * 4096 + 1024)), BC8(qr), s);
#pragma unroll
                for (int i = 0; i < 16; ++i) mm = fmaxf(mm, s[i]);
            }
            qr = put_ref(qr, swapmax(mm), hi);
            o = zero;
#pragma unroll 1
            for (int t = 0; t < 64; ++t) {
                f32x16 s = MFMA_BF(BC8(BLD(hb + t * 4096)), Q[0][0], zero); s = MFMA_BF(BC8(BLD(hb + t * 4096 + 1024)), BC8(qr), s);
#pragma unroll
                for (int i = 0; i < 4; ++i) { p0[i] = pk_bf16(EX(s[2 * i]), EX(s[2 * i + 1])); p1[i] = pk_bf16(EX(s[8 + 2 * i]), EX(s[9 + 2 * i])); }
                o = MFMA_BF(BC8(BLD(hb + t * 4096 + 2048)), BC8(p0), o); o = MFMA_BF(BC8(BLD(hb + t * 4096 + 3072)), BC8(p1), o);
            }
        }
    }
    {
        const auto r = __builtin_amdgcn_permlane32_swap(__float_as_uint(o[12]), __float_as_uint(o[12]), false, false);
        const float rl = __builtin_amdgcn_rcpf(__uint_as_float(r[1]));
        u32x4 o0, o1;
#pragma unroll
        for (int i = 0; i < 4; ++i) { o0[i] = pk_bf16(o[2 * i] * rl, o[2 * i + 1] * rl); o1[i] = pk_bf16(o[8 + 2 * i] * rl, o[9 + 2 * i] * rl); }
        if (hi) { o1[2] = 0; o1[3] = 0; }
        const unsigned fi = (unsigned)((4 * grp + w) * 16 + head);
        BST(o0, WS_O + fi * 2048 + voff); BST(o1, WS_O + fi * 2048 + 1024 + voff);
        STAMP(9);
        drain();
        put_flag(ws, FL_O + fi * FS, tag, lane);
    }
    STAMP(10);
    const int xcd = blockIdx.x & 7, bi = blockIdx.x >> 3;
    if (xcd < 7) {
        wait_flags<2>(ws, FL_BT + (unsigned)(xcd * 128 + w * 32 + r32) * FS, tag, !hi);
        STAMP(11);
        const unsigned bo = WS_BT + (unsigned)((64 * xcd + 16 * w + (lane >> 3)) * S + 64 * bi + (lane & 7) * 8) * 2;
        glds2c(ws, bo, bo + 8 * S * 2 - 1024, lds0 + 65536 + w * 2048);
        drain();
        arrive(sy + SY_T2); await(sy + SY_T2, 4);
        u32x4 o0, o1; transpose_tile(lds + 65536, 2048, lds + 73728 + w * 2304, nullptr, w, lane, o0, o1);
        const __amdgpu_buffer_rsrc_t ro = __builtin_amdgcn_make_buffer_rsrc(out, 0, S * 2 * D * 2, 0x00020000);
        const unsigned oo = (unsigned)((64 * bi + 16 * w + (lane >> 3)) * 2 * D + D + 64 * xcd) * 2 + (lane & 7) * 16;
        __builtin_amdgcn_raw_buffer_store_b128(o0, ro, oo, 0, 16); __builtin_amdgcn_raw_buffer_store_b128(o1, ro, oo + 8 * 2 * D * 2, 0, 16);
    }
    STAMP(12);
}

DI void role_b(const uint16_t* __restrict__ wo, const uint16_t* __restrict__ u, const uint16_t* __restrict__ lar, const uint16_t* __restrict__ aim,
               const uint16_t* __restrict__ cre, const uint16_t* __restrict__ cim, const uint16_t* __restrict__ ldt, const uint16_t* __restrict__ dskip,
               uint16_t* __restrict__ out, char* __restrict__ ws, LAS char* lds, int w, int lane) {
    u64 tag;
    const unsigned lds0 = (unsigned)(uintptr_t)lds;
    LAS unsigned* sy = (LAS unsigned*)(lds + LSY);
    const __amdgpu_buffer_rsrc_t rs = __builtin_amdgcn_make_buffer_rsrc(ws, 0, WS_END, 0x00020000);
    const int b = blockIdx.x, xcd = b & 7, bi = b >> 3, r32 = lane & 31, hi = lane >> 5;
    const unsigned voff = lane * 16;
    LAS char* tab = lds + LB + w * TABW;
    const int qt = 8 * xcd + (bi >> 2), ct = 4 * (bi & 3) + w;
    bf16x8 wf[32];
#define LOADWF(i) wf[i] = BC8(__builtin_amdgcn_raw_buffer_load_b128(rs, voff, WS_WO + (unsigned)(((8 * w + (i)) & 31) * 14 + ct) * 1024, 16))
    if (xcd < 7) {
        const int cc = w >> 1, hh = w & 1, pair = 32 * xcd + bi, ch = 2 * pair + cc;
        LAS char* cb = lds + LB + cc * CBW;
        const unsigned po = (unsigned)(ch * NS + 128 * hh) / 2 + lane;
        const unsigned pl = ((const unsigned*)lar)[po], pa = ((const unsigned*)aim)[po], pr = ((const unsigned*)cre)[po], pi = ((const unsigned*)cim)[po];
        const unsigned uo = (unsigned)((64 * bi + 16 * w + (lane >> 3)) * D + 64 * xcd + (lane & 7) * 8) * 2;
        glds2(u, uo, uo + 8 * D * 2 - 1024, lds0 + LB + w * TABW + 4096);
        const u64 e0 = __hip_atomic_load((GAS u64*)ws, __ATOMIC_RELAXED, __HIP_MEMORY_SCOPE_AGENT);
        u32x2 gi[2][3];
#pragma unroll
        for (int ps = 0; ps < 2; ++ps)
#pragma unroll
            for (int i = 0; i < 3; ++i) gi[ps][i] = ((const u32x2*)g_idx.v[128 * hh + 64 * ps + lane])[i];
        const unsigned lw = ((const unsigned*)ldt)[pair], dw = ((const unsigned*)dskip)[pair];
        const float dt = rbf(expf(cc ? bf_hi(lw) : bf_lo(lw)));
        drain();
        STAMP(1);
        arrive(sy + SY_WO);
        if (w == 0 && lane == 0) __hip_atomic_store((LAS u64*)sy, mix(e0), __ATOMIC_RELAXED, __HIP_MEMORY_SCOPE_WORKGROUP);
        await(sy + SY_WO, 4);
        {
            u32x4 o0, o1; transpose_tile(lds + LB + 4096, TABW, tab, sy + SY_T1, w, lane, o0, o1);
            const unsigned to = WS_UT + (unsigned)((64 * xcd + 16 * w + (lane >> 3)) * S + 64 * bi) * 2 + (lane & 7) * 16;
            __builtin_amdgcn_s_setprio(3); BST(o0, to); BST(o1, to + 8 * S * 2); __builtin_amdgcn_s_setprio(PRIO_B);
        }
        const float cm = wave_max(fmaxf(fmaxf(fabsf(bf_lo(pr)), fabsf(bf_hi(pr))), fmaxf(fabsf(bf_lo(pi)), fabsf(bf_hi(pi)))));
        unsigned ce = __float_as_uint(cm) & 0x7f800000u; ce = ce < 0x01000000u ? 0x01000000u : (ce > 0x7e800000u ? 0x7e800000u : ce);
        const float cscale = __uint_as_float(0x7f000000u - ce), oscale = 2.0f * dt * __uint_as_float(ce);
        LAS unsigned* tw = (LAS unsigned*)(tab + lane * 4);
        LAS const char* trd = tab + r32 * TROW + hi * 16;
        f32x16 dacc = {};
        f32x2 z1, z8, z256, x0;
#define ANCH(vl, va, vr, vi) do { const float a2 = (-rbf(expf(vl)) * dt) * 1.4426950408889634f, rev = ((va) * dt) * 0.15915494309189535f; \
            z1 = anchor(a2, -rev, 1.f); z8 = anchor(a2, rev, 8.f); z256 = anchor(a2, -rev, 256.f); x0 = (f32x2){(vr) * cscale, -(vi) * cscale}; \
            PIN(z1); PIN(z8); PIN(z256); SBAR(); } while (0)
#define TW(row) tw[(row) * (TROW / 4)]
#define CHAIN() do { \
            const f32x2 z1s = rot(z1), zs = rot(z256), z8s = rot(z8); \
            f32x2 y[4], X[8]; y[1] = z8; y[2] = cmul(z8, z8, z8s); X[0] = x0; X[1] = cmul(x0, z1, z1s); f32x2 a = cmul(x0, z256, zs); \
            y[3] = cmul(y[2], z8, z8s); const f32x2 z32 = cmul(y[2], y[2], rot(y[2])), z32s = rot(z32); X[2] = cmul(X[1], z1, z1s); y[0] = z32; \
            f32x2 c4 = cmul(X[2], z256, zs); \
            unsigned c[2][7], d0 = pk_f16(X[0]), d1 = pk_f16(X[1]), d2 = pk_f16(y[1]); \
            c[0][0] = pk_f16(y[0]); c[0][1] = 0x3c00u; c[0][2] = pk_f16(y[2]); c[0][3] = pk_f16(y[3]); c[0][4] = pk_f16(X[2]); c[0][5] = pk_f16(a); c[0][6] = pk_f16(c4); \
            TW(0) = d0; TW(1) = d1; TW(22) = d2; SBAR(); \
            _Pragma("unroll") for (int i = 1; i < 9; ++i) { \
                f32x2 t0, t1, t2, t3, t4, t5, t6; const int j = i - 1; unsigned* pc = c[j & 1]; unsigned* nc = c[i & 1]; \
                if (i < 8) { if (i > 1) t0 = y[0].xx * z32; t1 = y[1].xx * z32; t2 = y[2].xx * z32; t3 = y[3].xx * z32; \
                    if (i < 6) t4 = X[i + 1].xx * z1; t5 = a.xx * z256; \
                    if (i == 3) t6 = X[4].xx * z256; if (i == 4 || i == 5) t6 = c4.xx * z256; if (i == 6) t6 = X[6].xx * z256; } SBAR(); \
                if (j != 1) TW(21 + (j ? 4 * j : 4)) = pc[0]; TW(21 + (j ? 4 * j + 1 : 0)) = pc[1]; TW(23 + 4 * j) = pc[2]; TW(24 + 4 * j) = pc[3]; \
                if (j < 6) TW(j + 2) = pc[4]; TW(j == 0 ? 8 : (j == 1 ? 12 : (j == 2 ? 14 : 13 + j))) = pc[5]; \
                if (j == 0) TW(9) = pc[6]; if (j == 3) TW(10) = pc[6]; if (j == 4) TW(13) = pc[6]; if (j == 5) TW(15) = pc[6]; if (j == 6) TW(11) = pc[6]; SBAR(); \
                if (i < 8) { if (i > 1) y[0] = pfma(y[0].yy, z32s, t0); y[1] = pfma(y[1].yy, z32s, t1); y[2] = pfma(y[2].yy, z32s, t2); y[3] = pfma(y[3].yy, z32s, t3); \
                    if (i < 6) X[i + 2] = pfma(X[i + 1].yy, z1s, t4); a = pfma(a.yy, zs, t5); \
                    if (i == 3) c4 = pfma(X[4].yy, zs, t6); if (i == 4 || i == 5) c4 = pfma(c4.yy, zs, t6); if (i == 6) c4 = pfma(X[6].yy, zs, t6); SBAR(); \
                    if (i > 1) nc[0] = pk_f16(y[0]); nc[1] = pk_f16(y[1]); nc[2] = pk_f16(y[2]); nc[3] = pk_f16(y[3]); \
                    if (i < 6) nc[4] = pk_f16(X[i + 2]); nc[5] = pk_f16(a); if (i >= 3 && i <= 6) nc[6] = pk_f16(c4); \
                    asm volatile("" :: "v"(pc[0]), "v"(pc[1]), "v"(pc[2]), "v"(pc[3]), "v"(pc[4]), "v"(pc[5]), "v"(pc[6])); SBAR(); } \
            } } while (0)
        f16x8 fa[8], fb[8];
#define RDT() do { _Pragma("unroll") for (int kk = 0; kk < 8; ++kk) { fa[kk] = *(LAS const f16x8*)(trd + kk * 32); fb[kk] = *(LAS const f16x8*)(trd + 21 * TROW + kk * 32); } SBAR(); } while (0)
#define MMT() do { _Pragma("unroll") for (int kk = 0; kk < 8; ++kk) dacc = MFMA_F16(fa[kk], fb[kk], dacc); } while (0)
        ANCH(bf_lo(pl), bf_lo(pa), bf_lo(pr), bf_lo(pi));
        tag = get_tag(sy); await(sy + SY_T1, 4);
        STAMP(13);
        CHAIN(); RDT();
        drain(); __builtin_amdgcn_s_setprio(3); put_flag(ws, FL_UT + (unsigned)(xcd * 128 + w * 32 + bi) * FS, tag, lane); __builtin_amdgcn_s_setprio(PRIO_B);
        STAMP(14);
        ANCH(bf_hi(pl), bf_hi(pa), bf_hi(pr), bf_hi(pi));
        MMT(); SBAR();
        CHAIN(); RDT();
        const unsigned utf = hi ? FL_WO + (unsigned)(w * 32 + r32) * FS : FL_UT + (unsigned)(xcd * 128 + (bi >> 3) * 32 + r32) * FS;
        const u64 utv = __hip_atomic_load((GAS u64*)(ws + utf), __ATOMIC_RELAXED, __HIP_MEMORY_SCOPE_AGENT); SBAR();
        MMT();
        STAMP(2);
        {
            LAS float* pt = (LAS float*)tab;
            *(LAS f32x4*)(pt + 8 * r32 + 4 * hi) = (f32x4){dacc[0] * oscale, dacc[1] * oscale, dacc[2] * oscale, dacc[3] * oscale};
            *(LAS f32x2*)(pt + (hi ? 384 + 2 * r32 : 256 + 4 * r32)) = (f32x2){dacc[4] * oscale, dacc[5] * oscale};
            *(LAS f32x2*)(pt + (hi ? 448 + 2 * r32 : 258 + 4 * r32)) = (f32x2){dacc[6] * oscale, dacc[7] * oscale};
            pt[hi ? 640 + r32 : 512 + r32] = dacc[8] * oscale;
            pt[hi ? 672 + r32 : 544 + r32] = dacc[9] * oscale;
            pt[hi ? 672 + r32 : 576 + r32] = dacc[10] * oscale;
            pt[hi ? 672 + r32 : 608 + r32] = dacc[11] * oscale;
        }
        LAS unsigned* ps_ = sy + SY_P + 5 * cc;
        arrive(ps_ + 0);
        LAS char* ui = cb + UOF;
        LAS char* kt = cb + KOF;
        await(ps_ + 0, 2);
#pragma unroll
        for (int ps = 0; ps < 2; ++ps) {
            const int v = 81 * hh + 64 * ps + lane;
            if (ps == 0 || lane < 17) {
                const f32x4 p0 = *(LAS const f32x4*)(cb + v * 16), p1 = *(LAS const f32x4*)(cb + TABW + v * 16);
                u32x2 r = {pk_bf16(p0[0] + p1[0], p0[1] + p1[1]), pk_bf16(p0[2] + p1[2], p0[3] + p1[3])};
                if (v == 161) r = (u32x2){0u, 0u};
                *(LAS u32x2*)(kt + v * 8) = r;
            }
        }
        arrive(ps_ + 1);
        if (__any(utv != tag)) wait_flags<8>(ws, utf, tag, utv != tag);
        CFENCE();
        const unsigned ub = WS_UT + (unsigned)(ch * S + 1024 * hh) * 2 + voff;
        const u32x4 u0 = BLD(ub), u1 = BLD(ub + 1024); SBAR();
        await(ps_ + 1, 2);
        STAMP(3);
        __builtin_amdgcn_s_setprio(PRIO_B2);
        const u32x4 z4 = {};
        {
            unsigned W[2][6];
#pragma unroll
            for (int ps = 0; ps < 2; ++ps)
#pragma unroll
                for (int i = 0; i < 6; ++i) { const unsigned gv = gi[ps][i >> 1][i & 1];
                    W[ps][i] = *(LAS const uint16_t*)(kt + (gv & 0xffffu));
                    if (i < 5) W[ps][i] |= (unsigned)*(LAS const uint16_t*)(kt + (gv >> 16)) << 16; }
            *(LAS u32x4*)(ui + (64 * hh + lane) * 16) = z4;
            if (lane < 16) *(LAS u32x4*)(ui + (128 + 16 * hh + lane) * 16) = z4;
            if (lane < 8) *(LAS u32x4*)(cb + (2 * hh + (lane >> 2)) * CPY + (256 + (lane & 3)) * 16) = z4;
#pragma unroll
            for (int ps = 0; ps < 2; ++ps) {
                unsigned V[5];
#pragma unroll
                for (int i = 0; i < 5; ++i) V[i] = __builtin_amdgcn_alignbit(W[ps][i + 1], W[ps][i], 16);
                LAS char* cp = cb + (128 * hh + 64 * ps + lane) * 16;
                *(LAS u32x4*)(cp) = (u32x4){W[ps][0], W[ps][1], W[ps][2], W[ps][3]};
                *(LAS u32x4*)(cp + CPY) = (u32x4){V[0], V[1], V[2], V[3]};
                *(LAS u32x4*)(cp + 2 * CPY) = (u32x4){W[ps][1], W[ps][2], W[ps][3], W[ps][4]};
                *(LAS u32x4*)(cp + 3 * CPY) = (u32x4){V[1], V[2], V[3], V[4]};
            }
        }
        *(LAS u32x4*)(ui + (32 + 32 * hh + (lane >> 2)) * 80 + (lane & 3) * 16) = u0;
        *(LAS u32x4*)(ui + (48 + 32 * hh + (lane >> 2)) * 80 + (lane & 3) * 16) = u1;
        arrive(ps_ + 2); await(ps_ + 2, 2);
        STAMP(4);
        __builtin_amdgcn_s_setprio(PRIO_BC);
        f32x16 a0 = {}, a1 = {};
        {
            const int sb = 7 - (r32 & 7);
            LAS const char* pa = cb + (sb & 3) * CPY + (255 - (r32 >> 3) + hi) * 16 + (sb >> 2) * 8 - 64 * (16 * hh + 15) - 2048;
            LAS const char* pa2 = pa + 8; asm volatile("" : "+v"(pa2));
            LAS const char* pb = ui + (32 + r32 - 16 * hh - 15) * 80 + 16 * hi;
            s16x4 ta[2][2], tb[2][2]; bf16x8 b0[2], b1[2];
#define T4(q, off) (*(LAS const s16x4*)((q) + (off)))
#define CSTEP(i, j, RD) do { const int o_ = 64 * (15 - ((j) >> 1)) + 32 * ((j) & 1), u_ = 80 * (15 - ((j) >> 1)) + 32 * ((j) & 1); \
                a1 = MFMA_BF(cat8(ta[i][0], ta[i][1]), b1[i], a1); SBAR(); if (RD) { ta[1 - i][0] = T4(pa, o_ + 2048); ta[1 - i][1] = T4(pa2, o_ + 2048); } SBAR(); \
                a0 = MFMA_BF(cat8(ta[i][0], ta[i][1]), b0[i], a0); SBAR(); if (RD) { b1[1 - i] = FR(pb, u_ + 2560); b0[1 - i] = FR(pb, u_); } SBAR(); \
                a1 = MFMA_BF(cat8(tb[i][0], tb[i][1]), b0[i], a1); SBAR(); if (RD) { tb[1 - i][0] = T4(pa, o_); tb[1 - i][1] = T4(pa2, o_); } SBAR(); } while (0)
            ta[0][0] = T4(pa, 960 + 2048); ta[0][1] = T4(pa2, 960 + 2048); b1[0] = FR(pb, 1200 + 2560); b0[0] = FR(pb, 1200); tb[0][0] = T4(pa, 960); tb[0][1] = T4(pa2, 960);
#pragma unroll
            for (int j = 0; j < 32; ++j) {
                if (j == 31) arrive(ps_ + 3);
                CSTEP(j & 1, j + 1, j < 31);
                if (ct < 14) LOADWF(j);
                if (HOLDB && (j & 3) == 3 && j < 31) { const unsigned h_ = __builtin_amdgcn_readfirstlane(__hip_atomic_load(sy + SY_H, __ATOMIC_RELAXED, __HIP_MEMORY_SCOPE_WORKGROUP)); if (h_ > 0 && h_ < 4 * HOLDB) await_slow(sy + SY_H, 4 * HOLDB); }
                SBAR();
            }
        }
        STAMP(5);
        __builtin_amdgcn_s_setprio(PRIO_B3);
        {
            u32x2 uu[4];
#pragma unroll
            for (int g = 0; g < 4; ++g) uu[g] = *(LAS const u32x2*)(ui + (32 + 32 * hh + r32) * 80 + 16 * g + 8 * hi);
            LAS char* ex = cb + r32 * 144 + hi * 16;
            const f32x16 snd = hh ? a0 : a1; const f32x16 y = hh ? a1 : a0;
            await(ps_ + 3, 2);
#pragma unroll
            for (int g = 0; g < 4; ++g) *(LAS f32x4*)(ex + hh * 4608 + g * 32) = (f32x4){snd[4 * g], snd[4 * g + 1], snd[4 * g + 2], snd[4 * g + 3]};
            arrive(ps_ + 4);
            const float dsk = cc ? bf_hi(dw) : bf_lo(dw);
            unsigned sk[8];
#pragma unroll
            for (int g = 0; g < 4; ++g)
#pragma unroll
                for (int e = 0; e < 2; ++e) sk[2 * g + e] = pk_bf16(bf_lo(uu[g][e]) * dsk, bf_hi(uu[g][e]) * dsk);
            await(ps_ + 4, 2);
            LAS char* sc = cb + 9216 + hh * 2048;
#pragma unroll
            for (int g = 0; g < 4; ++g) {
                const f32x4 p = *(LAS const f32x4*)(ex + (1 - hh) * 4608 + g * 32);
                u32x2 r;
#pragma unroll
                for (int e = 0; e < 2; ++e) {
                    const unsigned yb = pk_bf16(y[4 * g + 2 * e] + p[2 * e], y[4 * g + 2 * e + 1] + p[2 * e + 1]);
                    r[e] = pk_bf16(bf_lo(yb) + bf_lo(sk[2 * g + e]), bf_hi(yb) + bf_hi(sk[2 * g + e]));
                }
                *(LAS u32x2*)(sc + 64 * r32 + 16 * g + 8 * hi) = r;
            }
            const u32x4 o0 = *(LAS const u32x4*)(sc + voff), o1 = *(LAS const u32x4*)(sc + 1024 + voff);
            const unsigned bo = WS_BT + (unsigned)(ch * S + 1024 * hh) * 2 + voff;
            BST(o0, bo); BST(o1, bo + 1024);
        }
        arrive(sy + SY_BG);
        STAMP(6);
        drain();
        put_flag(ws, FL_BT + (unsigned)(xcd * 128 + bi * 4 + w) * FS, tag, lane);
    } else {
        {
            const unsigned nb = (bi & 1) ? 12 * 2 * D : 16 * 2 * D;
            unsigned v4[4];
#pragma unroll
            for (int i = 0; i < 4; ++i) { const unsigned o = (unsigned)(w * 4096 + i * 1024) + voff; v4[i] = (o < nb ? o : 0u) + 4096 - i * 1024; }
            glds4((const char*)(wo + (DH * (bi >> 1) + 16 * (bi & 1)) * D) - 4096, v4[0], v4[1], v4[2], v4[3], lds0 + LB + w * 4096);
        }
        const u64 e0 = __hip_atomic_load((GAS u64*)ws, __ATOMIC_RELAXED, __HIP_MEMORY_SCOPE_AGENT);
        drain();
        if (w == 0 && lane == 0) __hip_atomic_store((LAS u64*)sy, mix(e0), __ATOMIC_RELAXED, __HIP_MEMORY_SCOPE_WORKGROUP);
        arrive(sy + SY_WO); await(sy + SY_WO, 4);
        LAS const char* p = lds + LB + (4 * hi + ((lane & 15) >> 2)) * (2 * D) + ((lane >> 4) & 1) * 32 + (lane & 3) * 8;
#pragma unroll
        for (int i = 0; i < 4; ++i) {
            const int ct = w + 4 * i;
            if (ct < 14) {
                u32x4 o = __builtin_bit_cast(u32x4, cat8(trread(p + ct * 64), trread(p + 8 * 2 * D + ct * 64)));
                if (hi && (bi & 1)) { o[2] = 0; o[3] = 0; }
                BST(o, WS_WO + (unsigned)(bi * 14 + ct) * 1024 + voff);
            }
        }
        tag = get_tag(sy);
        drain();
        put_flag(ws, FL_WO + (unsigned)(w * 32 + bi) * FS, tag, lane);
        __builtin_amdgcn_s_setprio(0);
        arrive(sy + SY_BG);
        await_slow(sy + SY_G, 4);
        wait_flags<8>(ws, FL_WO + (unsigned)(w * 32 + r32) * FS, tag, !hi);
        if (ct < 14) {
#pragma unroll
            for (int i = 0; i < 32; ++i) { LOADWF(i); __builtin_amdgcn_s_sleep(WOSL); SBAR(); }
        }
    }
    STAMP(7);
    const __amdgpu_buffer_rsrc_t ro = __builtin_amdgcn_make_buffer_rsrc(out, 0, S * 2 * D * 2, 0x00020000);
    await(sy + SY_BG, 4);
    STAMP(8);
    await_slow(sy + SY_X, 4);
    wait_flags<2>(ws, FL_O + (unsigned)(qt * 16 + 4 * w + lane) * FS, tag, lane < 4);
    STAMP(9);
    u32x4 of[8];
#pragma unroll
    for (int i = 0; i < 8; ++i) of[i] = BLD(WS_O + (unsigned)(qt * 32 + 8 * w + i) * 1024 + voff);
#pragma unroll
    for (int i = 0; i < 8; ++i) *(LAS u32x4*)(lds + LB + (8 * w + i) * 1024 + voff) = of[i];
    arrive(sy + SY_B2);
    STAMP(10);
    f32x16 acc = {};
    if (ct < 14) {
#pragma unroll
        for (int i = 0; i < 8; ++i) acc = MFMA_BF(wf[i], BC8(of[i]), acc);
    }
    SBAR();
    await(sy + SY_B2, 4);
    STAMP(11);
    if (ct < 14) {
        LAS const char* po[3];
#pragma unroll
        for (int j = 0; j < 3; ++j) po[j] = lds + LB + ((w + 1 + j) & 3) * 8192 + voff;
        bf16x8 og[4];
#pragma unroll
        for (int i = 0; i < 3; ++i) og[i] = FR(po[0], i * 1024);
#pragma unroll
        for (int i = 0; i < 24; ++i) { if (i < 21) og[(i + 3) & 3] = FR(po[(i + 3) >> 3], ((i + 3) & 7) * 1024); SBAR(); acc = MFMA_BF(wf[8 + i], og[i & 3], acc); SBAR(); }
        LAS char* sc = lds + LB + 40960 + w * 2304;
#pragma unroll
        for (int g = 0; g < 4; ++g) *(LAS u32x2*)(sc + r32 * 72 + 16 * g + 8 * hi) = (u32x2){pk_bf16(acc[4 * g], acc[4 * g + 1]), pk_bf16(acc[4 * g + 2], acc[4 * g + 3])};
        LAS const char* sr = sc + (lane >> 2) * 72 + (lane & 3) * 16;
        const u32x2 a0 = *(LAS const u32x2*)sr, a1 = *(LAS const u32x2*)(sr + 8), b0 = *(LAS const u32x2*)(sr + 16 * 72), b1 = *(LAS const u32x2*)(sr + 16 * 72 + 8);
        const unsigned oo = (unsigned)((32 * qt + (lane >> 2)) * 2 * D + 32 * ct) * 2 + (lane & 3) * 16;
        __builtin_amdgcn_raw_buffer_store_b128((u32x4){a0[0], a0[1], a1[0], a1[1]}, ro, oo, 0, 16);
        __builtin_amdgcn_raw_buffer_store_b128((u32x4){b0[0], b0[1], b1[0], b1[1]}, ro, oo + 16 * 2 * D * 2, 0, 16);
    }
    if (b == 0 && w == 0) put_flag(ws, 0, tag, lane);
    STAMP(12);
}

__global__ __launch_bounds__(512, 2) void fused(const uint16_t* __restrict__ x, const uint16_t* __restrict__ wqkv, const uint16_t* __restrict__ wo,
                                               const uint16_t* __restrict__ u, const uint16_t* __restrict__ lar, const uint16_t* __restrict__ aim,
                                               const uint16_t* __restrict__ cre, const uint16_t* __restrict__ cim, const uint16_t* __restrict__ ldt,
                                               const uint16_t* __restrict__ dskip, uint16_t* __restrict__ out, char* __restrict__ ws) {
    extern __shared__ __attribute__((aligned(16))) char smem[];
    LAS char* lds = (LAS char*)smem;
    const int lane = threadIdx.x & 63, w = __builtin_amdgcn_readfirstlane(threadIdx.x >> 6);
    STAMP(0);
    if (w >= 4) __builtin_amdgcn_s_setprio(3);
    if (w == 4) ((LAS unsigned*)(lds + LSY))[lane] = 0;
    asm volatile("" :: "s"(x), "s"(wqkv), "s"(wo), "s"(u), "s"(lar), "s"(aim), "s"(cre), "s"(cim), "s"(ldt), "s"(dskip), "s"(out), "s"(ws));
    asm volatile("s_waitcnt lgkmcnt(0)\n\ts_barrier" ::: "memory");
    if (w < 4) role_a(x, wqkv, out, ws, lds, w, lane);
    else role_b(wo, u, lar, aim, cre, cim, ldt, dskip, out, ws, lds, w - 4, lane);
}
}

extern "C" void kernel_launch(void* const* d_in, const int* in_sizes, int n_in, void* d_out, int out_size, void* d_ws, size_t ws_size, hipStream_t stream) {
    using namespace k;
    (void)hipFuncSetAttribute((const void*)fused, hipFuncAttributeMaxDynamicSharedMemorySize, LDS_BYTES);
    const uint16_t* const* in = (const uint16_t* const*)d_in;
    fused<<<256, 512, LDS_BYTES, stream>>>(in[0], in[1], in[2], in[3], in[4], in[5], in[6], in[7], in[8], in[9], (uint16_t*)d_out, (char*)d_ws);
}
```

```cpp
#include <hip/hip_runtime.h>
#include <hip/hip_bf16.h>
#include <stdint.h>

typedef short bf16x8 __attribute__((ext_vector_type(8)));
typedef _Float16 f16x8 __attribute__((ext_vector_type(8)));
typedef _Float16 f16x2 __attribute__((ext_vector_type(2)));
typedef __bf16 bf16x2 __attribute__((ext_vector_type(2)));
typedef short s16x4 __attribute__((ext_vector_type(4)));
typedef float f32x16 __attribute__((ext_vector_type(16)));
typedef float f32x4 __attribute__((ext_vector_type(4)));
typedef float f32x2 __attribute__((ext_vector_type(2)));
typedef unsigned u32x4 __attribute__((ext_vector_type(4)));
typedef unsigned u32x2 __attribute__((ext_vector_type(2)));
typedef unsigned long long u64;
#define LAS __attribute__((address_space(3)))
#define GAS __attribute__((address_space(1)))
#define DI __device__ __forceinline__
#define MFMA_BF(a, b, c) __builtin_amdgcn_mfma_f32_32x32x16_bf16((a), (b), (c), 0, 0, 0)
#define MFMA_F16(a, b, c) __builtin_amdgcn_mfma_f32_32x32x16_f16((a), (b), (c), 0, 0, 0)
#define SBAR() __builtin_amdgcn_sched_barrier(0)
#define CFENCE() asm volatile("" ::: "memory")
#define PIN(x) asm volatile("" : "+v"(x))
#define BC8(x) __builtin_bit_cast(bf16x8, (x))
#define EX(x) __builtin_amdgcn_exp2f(x)
#define PRIO_A1 0
#define PRIO_B 0
#define PRIO_B2 1
#define ROT 1
#define KVM 6u
#define HOLDB 0
#define PRIO_B3 1
#define PRIO_BC 0
#define FLJ 13
#define OSL 2
#define LATE2 1
#define WOSL 2

namespace k {
#define STAMP(i)
constexpr int S = 2048, D = 448, DH = 28, NS = 256;
constexpr unsigned FS = 128;
constexpr unsigned FL_KV = FS, FL_O = FL_KV + 1024 * FS, FL_UT = FL_O + 1024 * FS, FL_BT = FL_UT + 896 * FS, FL_WO = FL_BT + 896 * FS;
constexpr unsigned WS_KV = 512u << 10;
constexpr unsigned WS_O = WS_KV + (4u << 20);
constexpr unsigned WS_WO = WS_O + (2u << 20);
constexpr unsigned WS_UT = WS_WO + (448u << 10);
constexpr unsigned WS_BT = WS_UT + D * S * 2;
constexpr unsigned WS_END = WS_BT + D * S * 2;
constexpr int WSL = 8192, LWQ = 4 * WSL, LXR = LWQ + 16384, XSL = 16384, LB = LXR + 3 * XSL;
constexpr int LQ = LXR, LX = 0;
constexpr int TROW = 272, TABW = 53 * TROW, CBW = 2 * TABW, CPY = 4160, UOF = 4 * CPY, KOF = CBW - 1312;
constexpr int LSY = LB + 2 * CBW, LDS_BYTES = LSY + 256;
static_assert(LDS_BYTES <= 163840 && (LSY & 15) == 0 && UOF + 7680 <= KOF, "lds");
enum { SY_TAG = 0, SY_F = 2, SY_P = 10, SY_WO = 20, SY_T1 = 21, SY_BG = 22, SY_B2 = 23, SY_Q = 24, SY_X = 25, SY_G = 26, SY_T2 = 27, SY_H = 28 };
constexpr int SPIN = 1 << 18;

struct IdxTab { uint16_t v[256][12]; };
constexpr IdxTab make_idx() {
    IdxTab t = {};
    for (int y = 0; y < 256; ++y) for (int e = 0; e < 12; ++e) {
        const int j = 2047 - 8 * y - e; int ix = 644;
        if (j >= 0) { const int sh = j >= 1024 ? 3 : (j >= 512 ? 2 : (j >= 256 ? 1 : 0));
            ix = sh ? 128 * sh + ((j + (1 << (sh - 1)) - 1 + ((j >> sh) & 1)) >> sh) : j; }
        t.v[y][e] = (uint16_t)(2 * ix);
    }
    return t;
}
__device__ const IdxTab g_idx __attribute__((aligned(16))) = make_idx();

DI unsigned pk_bf16(float a, float b) { f32x2 v = {a, b}; return __builtin_bit_cast(unsigned, __builtin_convertvector(v, bf16x2)); }
DI unsigned pk_f16(f32x2 v) { return __builtin_bit_cast(unsigned, __builtin_convertvector(v, f16x2)); }
DI float bf_lo(unsigned w) { return __uint_as_float(w << 16); }
DI float bf_hi(unsigned w) { return __uint_as_float(w & 0xffff0000u); }
DI float rbf(float x) { return bf_lo(pk_bf16(x, x)); }
#define GL0 "s_mov_b32 %0, m0\n\ts_mov_b32 m0, %2\n\ts_nop 0\n\t"
#define GL1(V, O, C) "global_load_lds_dwordx4 " V ", %1 offset:" #O C "\n\t"
#define GLE "s_mov_b32 m0, %0"
DI void glds4(const void* sb, unsigned v0, unsigned v1, unsigned v2, unsigned v3, unsigned lb) { unsigned sv;
    asm volatile(GL0 GL1("%3", 0, "") GL1("%4", 1024, "") GL1("%5", 2048, "") GL1("%6", 3072, "") GLE : "=&s"(sv) : "s"(sb), "s"(lb), "v"(v0), "v"(v1), "v"(v2), "v"(v3) : "memory"); }
DI void glds3(const void* sb, unsigned v0, unsigned v1, unsigned v2, unsigned lb) { unsigned sv;
    asm volatile(GL0 GL1("%3", 0, "") GL1("%4", 1024, "") GL1("%5", 2048, "") GLE : "=&s"(sv) : "s"(sb), "s"(lb), "v"(v0), "v"(v1), "v"(v2) : "memory"); }
DI void glds2(const void* sb, unsigned v0, unsigned v1, unsigned lb) { unsigned sv;
    asm volatile(GL0 GL1("%3", 0, "") GL1("%4", 1024, "") GLE : "=&s"(sv) : "s"(sb), "s"(lb), "v"(v0), "v"(v1) : "memory"); }
DI void glds1(const void* sb, unsigned v0, unsigned lb) { unsigned sv;
    asm volatile(GL0 GL1("%3", 0, "") GLE : "=&s"(sv) : "s"(sb), "s"(lb), "v"(v0) : "memory"); }
DI void glds2c(const void* sb, unsigned v0, unsigned v1, unsigned lb) { unsigned sv;
    asm volatile(GL0 GL1("%3", 0, " sc1") GL1("%4", 1024, " sc1") GLE : "=&s"(sv) : "s"(sb), "s"(lb), "v"(v0), "v"(v1) : "memory"); }
DI float wave_max(float v) {
#define DPPMAX(ctrl, rm) v = fmaxf(v, __uint_as_float(__builtin_amdgcn_update_dpp(0, __float_as_uint(v), ctrl, rm, 0xf, true)))
    DPPMAX(0x111, 0xf); DPPMAX(0x112, 0xf); DPPMAX(0x114, 0xf); DPPMAX(0x118, 0xf); DPPMAX(0x142, 0xa); DPPMAX(0x143, 0xc);
    return __uint_as_float(__builtin_amdgcn_readlane(__float_as_uint(v), 63));
}
DI s16x4 trread(LAS const char* p) { return __builtin_bit_cast(s16x4, __builtin_amdgcn_ds_read_tr16_b64_v4i16((LAS s16x4*)p)); }
DI bf16x8 cat8(s16x4 a, s16x4 b) { return __builtin_shufflevector(a, b, 0, 1, 2, 3, 4, 5, 6, 7); }
DI f32x2 pfma(f32x2 a, f32x2 b, f32x2 c) { return __builtin_elementwise_fma(a, b, c); }
DI f32x2 cmul(f32x2 a, f32x2 b, f32x2 bs) { return pfma(a.yy, bs, a.xx * b); }
DI f32x2 rot(f32x2 b) { return (f32x2){-b.y, b.x}; }
DI f32x2 anchor(float a2, float rev, float s) {
    const float mag = EX(a2 * s);
    float r = rev * s; r -= __builtin_rintf(r);
    return (f32x2){mag * __builtin_amdgcn_cosf(r), mag * __builtin_amdgcn_sinf(r)};
}
DI void arrive(LAS unsigned* c) { asm volatile("s_mov_b64 exec, 1\n\tds_add_u32 %0, %1\n\ts_mov_b64 exec, -1" :: "v"((unsigned)(uintptr_t)c), "v"(1u) : "memory"); }
DI void await(LAS unsigned* c, unsigned n) {
    unsigned t, i = SPIN;
    asm volatile("1:\n\tds_read_b32 %0, %2\n\ts_waitcnt lgkmcnt(0)\n\tv_cmp_gt_u32 vcc, %3, %0\n\ts_cbranch_vccz 2f\n\ts_sleep 1\n\ts_sub_u32 %1, %1, 1\n\ts_cbranch_scc0 1b\n2:"
                 : "=&v"(t), "+s"(i) : "v"((unsigned)(uintptr_t)c), "s"(n) : "vcc", "scc", "memory");
}
DI void await_slow(LAS unsigned* c, unsigned n) {
    unsigned t, i = SPIN;
    asm volatile("1:\n\tds_read_b32 %0, %2\n\ts_waitcnt lgkmcnt(0)\n\tv_cmp_gt_u32 vcc, %3, %0\n\ts_cbranch_vccz 2f\n\ts_sleep 3\n\ts_sub_u32 %1, %1, 1\n\ts_cbranch_scc0 1b\n2:"
                 : "=&v"(t), "+s"(i) : "v"((unsigned)(uintptr_t)c), "s"(n) : "vcc", "scc", "memory");
}
DI u64 get_tag(LAS unsigned* sy) {
    await(sy, 1);
    const u64 v = __hip_atomic_load((LAS u64*)sy, __ATOMIC_RELAXED, __HIP_MEMORY_SCOPE_WORKGROUP);
    return ((u64)(unsigned)__builtin_amdgcn_readfirstlane((unsigned)(v >> 32)) << 32) | (unsigned)__builtin_amdgcn_readfirstlane((unsigned)v);
}
DI void drain() { asm volatile("s_waitcnt vmcnt(0)" ::: "memory"); }
DI void put_flag(char* ws, unsigned off, u64 tag, int lane) { if (lane == 0) __hip_atomic_store((GAS u64*)(ws + off), tag, __ATOMIC_RELAXED, __HIP_MEMORY_SCOPE_AGENT); }
template <int SLP> DI void wait_flags(char* ws, unsigned off, u64 tag, bool pend) {
    GAS u64* p = (GAS u64*)(ws + off);
    CFENCE();
#pragma unroll 1
    for (int i = 0; i < SPIN; ++i) {
        if (pend) pend = __hip_atomic_load(p, __ATOMIC_RELAXED, __HIP_MEMORY_SCOPE_AGENT) != tag;
        if (!__any(pend)) break;
        __builtin_amdgcn_s_sleep(SLP);
    }
    CFENCE();
}
#define BST(v, off) __builtin_amdgcn_raw_buffer_store_b128((v), rs, (off), 0, 16)
#define BLD(off) __builtin_amdgcn_raw_buffer_load_b128(rs, (off), 0, 16)
#define FR(p, off) (*(LAS const bf16x8*)((p) + (off)))
DI float swapmax(float m) { const auto r = __builtin_amdgcn_permlane32_swap(__float_as_uint(m), __float_as_uint(m), false, false); return fmaxf(__uint_as_float(r[0]), __uint_as_float(r[1])); }
DI u32x4 put_ref(u32x4 q1, float m, int hi) {
    const float m1 = rbf(m), m2 = rbf(m - m1), m3 = m - m1 - m2;
    if (hi) { q1[2] = pk_bf16(-m1, -m2); q1[3] = pk_bf16(-m3, 0.f); }
    return q1;
}
DI u64 mix(u64 v) {
    const u64 e = ((u64)(unsigned)__builtin_amdgcn_readfirstlane((unsigned)(v >> 32)) << 32) | (unsigned)__builtin_amdgcn_readfirstlane((unsigned)v);
    u64 z = e + 0x9E3779B97F4A7C15ull; z = (z ^ (z >> 30)) * 0xBF58476D1CE4E5B9ull; z = (z ^ (z >> 27)) * 0x94D049BB133111EBull; return (z ^ (z >> 31)) | 1ull; }
DI void transpose_tile(LAS const char* stage, int pst, LAS char* scratch, LAS unsigned* done, int w, int lane, u32x4& o0, u32x4& o1) {
    LAS const char* p = stage + (lane >> 4) * pst + ((lane & 15) >> 2) * 128 + w * 32 + (lane & 3) * 8;
    const bf16x8 t0 = cat8(trread(p), trread(p + 512)), t1 = cat8(trread(p + 1024), trread(p + 1536));
    if (done) arrive(done);
    LAS char* sc = scratch + (lane & 15) * 144 + (lane >> 4) * 32;
    *(LAS bf16x8*)sc = t0; *(LAS bf16x8*)(sc + 16) = t1;
    LAS const char* sr = scratch + (lane >> 3) * 144 + (lane & 7) * 16;
    o0 = *(LAS const u32x4*)sr; o1 = *(LAS const u32x4*)(sr + 8 * 144);
}

DI void role_a(const uint16_t* __restrict__ x, const uint16_t* __restrict__ wqkv, uint16_t* __restrict__ out, char* __restrict__ ws, LAS char* lds, int w, int lane) {
    const int head = 2 * (blockIdx.x & 7) + ((blockIdx.x >> 3) & 1), grp = blockIdx.x >> 4;
    const unsigned lds0 = (unsigned)(uintptr_t)lds;
    const int r32 = lane & 31, hi = lane >> 5;
    const int wsh = (head & 7) == 7 ? 4 : 0;
    const unsigned wv = (unsigned)((lane >> 2) * 3 * D + (lane & 3) * 8) * 2;
    const char* wb = (const char*)(wqkv + DH * head - wsh + (16 * w) * 3 * D);
    const unsigned xv0 = (unsigned)((lane >> 3) * D + (((lane & 7) ^ (lane >> 4)) * 8)) * 2, xv1 = xv0 ^ 64;
    const char* xb = (const char*)(x + (128 * grp + 32 * w) * D);
    const int rho = ROT ? ((blockIdx.x >> 3) + ((blockIdx.x >> 1) & 3)) % 7 : 0;
#define SP(st) ((st) + rho >= 7 ? (st) + rho - 7 : (st) + rho)
#define WQO(st) ((st) < 4 ? LWQ + (st) * 4096 : ((st) < 6 ? 3 * WSL + ((st) - 4) * 4096 : 0))
#define LOADW(st) glds2(wb + SP(st) * (64 * 3 * D * 2), wv + 2 * D, wv + 4 * D - 1024, lds0 + ((st) & 3) * WSL + w * 2048)
#define LOADQ(st) glds1(wb + SP(st) * (64 * 3 * D * 2), wv, lds0 + WQO(st) + w * 1024)
#define LOADX(st) glds4(xb + SP(st) * 128, xv0, xv1 + 16 * D - 1024, xv0 + 32 * D - 2048, xv1 + 48 * D - 3072, lds0 + LXR + ((st) % 3) * XSL + w * 4096)
    LOADW(0); LOADX(0);
    STAMP(1);
    LOADW(1); LOADX(1); if (!LATE2) { LOADW(2); LOADX(2); }
    __builtin_amdgcn_s_setprio(PRIO_A1);
    LAS unsigned* sy = (LAS unsigned*)(lds + LSY);
    const __amdgpu_buffer_rsrc_t rs = __builtin_amdgcn_make_buffer_rsrc(ws, 0, WS_END, 0x00020000);
    const int xs = (r32 >> 1) & 7;
    LAS const char* xp[4];
#pragma unroll
    for (int ks = 0; ks < 4; ++ks) xp[ks] = lds + LXR + w * 4096 + r32 * 128 + (((2 * ks + hi) ^ xs) * 16);
    LAS const char* wp = lds + (8 * hi + ((lane & 15) >> 2)) * 64 + ((lane >> 4) & 1) * 32 + (lane & 3) * 8 + wsh * 2;
    f32x16 aq = {}, ak = {}, av = {};
    bf16x8 xf[7][4]; s16x4 fr[4][4];
#define RX(st, ks) xf[st][ks] = FR(xp[ks], ((st) % 3) * XSL)
#define RW(st, ks, j) fr[ks][j] = trread(wp + ((st) & 3) * WSL + (ks) * 2048 + ((j) >> 1) * 1024 + ((j) & 1) * 256)
#define MK(st, ks) ak = MFMA_BF(cat8(fr[ks][0], fr[ks][1]), xf[st][ks], ak); SBAR()
#define MV(st, ks) av = MFMA_BF(xf[st][ks], cat8(fr[ks][2], fr[ks][3]), av); SBAR()
#define QSTEP(st, ks) MK(st, ks); RW(st, ks + 1, 0); RW(st, ks + 1, 1); SBAR(); MV(st, ks); RW(st, ks + 1, 2); RW(st, ks + 1, 3); SBAR()
    u64 tag = 0;
    if (LATE2) { asm volatile("s_waitcnt vmcnt(6)" ::: "memory"); arrive(sy + SY_F); LOADW(2); LOADX(2); await(sy + SY_F, 4); }
    else { asm volatile("s_waitcnt vmcnt(12)" ::: "memory"); arrive(sy + SY_F); await(sy + SY_F, 4); }
    STAMP(16);
    RX(0, 0); RW(0, 0, 0); RW(0, 0, 1); RW(0, 0, 2); RW(0, 0, 3); RX(0, 1); RX(0, 2); RX(0, 3); SBAR();
#pragma unroll
    for (int st = 0; st < 7; ++st) {
        QSTEP(st, 0); if (st < 4) LOADW(st + 3); if (st == 4) { LOADQ(4); LOADQ(5); } if (st == 5) LOADQ(6);
        QSTEP(st, 1); if (st < 4) LOADX(st + 3);
        QSTEP(st, 2); if (st == 3) { LOADQ(0); LOADQ(1); LOADQ(2); LOADQ(3); }
        if (st < 6) {
            if (st == 3) asm volatile("s_waitcnt vmcnt(16)" ::: "memory"); else if (st == 5) asm volatile("s_waitcnt vmcnt(7)" ::: "memory"); else asm volatile("s_waitcnt vmcnt(12)" ::: "memory");
            arrive(sy + SY_F + 1 + st); SBAR();
        }
        MK(st, 3);
        if (st < 6) { await(sy + SY_F + 1 + st, 4); RX(st + 1, 0); RW(st + 1, 0, 0); RW(st + 1, 0, 1); RW(st + 1, 0, 2); RW(st + 1, 0, 3); } SBAR();
        MV(st, 3); if (st < 6) { RX(st + 1, 1); RX(st + 1, 2); RX(st + 1, 3); } SBAR();
        if (st == 2) tag = get_tag(sy);
    }
    STAMP(3);
    const float cq = 0.18898223650461362f * 1.4426950408889634f;
    const unsigned voff = lane * 16;
    u32x4 q0, q1, k0, k1, v0, v1;
    const unsigned tb = WS_KV + (unsigned)(head * 64 + 4 * grp + w) * 4096 + voff;
#pragma unroll
    for (int i = 0; i < 4; ++i) { k0[i] = pk_bf16(ak[2 * i], ak[2 * i + 1]); k1[i] = pk_bf16(ak[8 + 2 * i], ak[9 + 2 * i]); }
    if (hi) { k1[2] = 0x3f803f80u; k1[3] = 0x00003f80u; }
    BST(k0, tb); BST(k1, tb + 1024);
#pragma unroll
    for (int i = 0; i < 4; ++i) {
        v0[i] = pk_bf16(av[2 * i], av[2 * i + 1]); v1[i] = pk_bf16(av[8 + 2 * i], av[9 + 2 * i]);
        if (r32 >= DH) { v0[i] = v1[i] = (r32 == DH ? 0x3f803f80u : 0u); }
    }
    BST(v0, tb + 2048); BST(v1, tb + 3072);
    asm volatile("s_waitcnt vmcnt(4)" ::: "memory");
    arrive(sy + SY_F + 7); await(sy + SY_F + 7, 4);
    {
        s16x4 fq[4][2];
#define RQ(j) do { fq[(j) & 3][0] = trread(wp + WQO((j) >> 2) + ((j) & 3) * 1024); fq[(j) & 3][1] = trread(wp + WQO((j) >> 2) + ((j) & 3) * 1024 + 256); } while (0)
        RQ(0); RQ(1); RQ(2); SBAR();
#pragma unroll
        for (int j = 0; j < 28; ++j) {
            aq = MFMA_BF(cat8(fq[j & 3][0], fq[j & 3][1]), xf[j >> 2][j & 3], aq); SBAR(); if (j < 25) RQ(j + 3); SBAR();
            if (j == FLJ) { drain(); put_flag(ws, FL_KV + (unsigned)((head * 4 + w) * 16 + grp) * FS, tag, lane); STAMP(4); }
        }
    }
    STAMP(17);
#pragma unroll
    for (int i = 0; i < 4; ++i) { q0[i] = pk_bf16(aq[2 * i] * cq, aq[2 * i + 1] * cq); q1[i] = pk_bf16(aq[8 + 2 * i] * cq, aq[9 + 2 * i] * cq); }
    if (hi) { q1[2] = 0; q1[3] = 0; }
    GAS u64* kvf = (GAS u64*)(ws + FL_KV + (unsigned)((head * 4 + w) * 16 + ((grp + lane) & 15)) * FS);
    const f32x16 zero = {};
    {
        f32x16 s = MFMA_BF(BC8(k0), BC8(q0), zero); s = MFMA_BF(BC8(k1), BC8(q1), s);
        float m = s[0];
#pragma unroll
        for (int i = 1; i < 16; ++i) m = fmaxf(m, s[i]);
        q1 = put_ref(q1, swapmax(m), hi);
    }
    { LAS u32x4* qo = (LAS u32x4*)(lds + LQ + w * 2048) + lane; qo[0] = q0; qo[64] = q1; }
    arrive(sy + SY_Q);
    const u64 kv1 = __hip_atomic_load(kvf, __ATOMIC_RELAXED, __HIP_MEMORY_SCOPE_AGENT); SBAR();
    await(sy + SY_Q, 4);
    __builtin_amdgcn_s_setprio(0);
    bf16x8 Q[4][2];
    Q[0][0] = BC8(q0); Q[0][1] = BC8(q1);
#pragma unroll
    for (int i = 1; i < 4; ++i) { LAS const char* p = lds + LQ + ((w + i) & 3) * 2048 + voff; Q[i][0] = FR(p, 0); Q[i][1] = FR(p, 1024); }
    u32x4 kv[4][4] = {{k0, k1, v0, v1}, {}, {}, {}};
    f32x16 O[4] = {}, sa, sb;
    u32x4 p0, p1 = {};
#define GRP(SC, i, P, j) do { float e0 = EX(SC[i]); SBAR(); float e1 = EX(SC[i + 1]); SBAR(); float e2 = EX(SC[i + 2]); SBAR(); float e3 = EX(SC[i + 3]); SBAR(); \
        P[j] = pk_bf16(e0, e1); SBAR(); P[j + 1] = pk_bf16(e2, e3); PIN(P); SBAR(); } while (0)
#define SLOT(SC, SN, cb, i, QK, PV1, LD) do { constexpr int nb_ = (i) < 3 ? (cb) : ((cb) + 1) % 4, pb_ = (i) > 0 ? (cb) : ((cb) + 3) % 4; \
        if (QK) SN = MFMA_BF(BC8(kv[nb_][0]), Q[((i) + 1) & 3][0], zero); SBAR(); GRP(SC, 0, p0, 0); \
        if (PV1) O[((i) + 3) & 3] = MFMA_BF(BC8(kv[pb_][3]), BC8(p1), O[((i) + 3) & 3]); SBAR(); \
        if (LD) { kv[((cb) + 3) % 4][i] = __builtin_amdgcn_raw_buffer_load_b128(rs, voff + (i) * 1024, kvo, 16); SBAR(); } GRP(SC, 4, p0, 2); \
        if (QK) SN = MFMA_BF(BC8(kv[nb_][1]), Q[((i) + 1) & 3][1], SN); SBAR(); GRP(SC, 8, p1, 0); \
        O[i] = MFMA_BF(BC8(kv[cb][2]), BC8(p0), O[i]); SBAR(); GRP(SC, 12, p1, 2); } while (0)
    const unsigned kvb = WS_KV + (unsigned)(head * 64 + w) * 4096;
    unsigned kvo = 0;
    STAMP(5);
    bool pend = lane > 0 && lane < 16 && kv1 != tag;
#define NEED(msk) do { CFENCE(); _Pragma("unroll 1") for (int i_ = 0; i_ < SPIN; ++i_) { \
        if (pend) pend = __hip_atomic_load(kvf, __ATOMIC_RELAXED, __HIP_MEMORY_SCOPE_AGENT) != tag; \
        miss = (unsigned)__ballot(pend); if (!(miss & (msk))) break; __builtin_amdgcn_s_sleep(2); } CFENCE(); } while (0)
    unsigned miss = (unsigned)__ballot(pend);
    if (miss & KVM) NEED(KVM);
    CFENCE();
    STAMP(6);
#pragma unroll
    for (int j = 1; j < 3; ++j)
#pragma unroll
        for (int f = 0; f < 4; ++f) kv[j][f] = __builtin_amdgcn_raw_buffer_load_b128(rs, voff + f * 1024, kvb + ((grp + j) & 15) * 16384, 16);
    arrive(sy + SY_G);
    sa = MFMA_BF(BC8(k0), Q[0][0], zero); sa = MFMA_BF(BC8(k1), Q[0][1], sa);
    STAMP(7);
    int nx = grp + 3;
#define TILE(cb) do { kvo = kvb + (nx & 15) * 16384; ++nx; SBAR(); \
        SLOT(sa, sb, cb, 0, true, true, true); SLOT(sb, sa, cb, 1, true, true, true); SLOT(sa, sb, cb, 2, true, true, true); SLOT(sb, sa, cb, 3, true, true, true); } while (0)
#pragma unroll 1
    for (unsigned msk = 0x78; msk < 0x80000; msk <<= 4) { if (__builtin_expect(miss & msk, 0)) NEED(msk); TILE(0); TILE(1); TILE(2); TILE(3); }
    O[3] = MFMA_BF(BC8(kv[3][3]), BC8(p1), O[3]);
    STAMP(8);
    f32x16 o = O[0];
    {
#pragma unroll
        for (int i = 1; i < 4; ++i) {
            LAS char* ex = lds + LX + (((w + i) & 3) * 3 + i - 1) * 4096 + voff;
#pragma unroll
            for (int g = 0; g < 4; ++g) *(LAS f32x4*)(ex + g * 1024) = (f32x4){O[i][4 * g], O[i][4 * g + 1], O[i][4 * g + 2], O[i][4 * g + 3]};
        }
        arrive(sy + SY_X); await(sy + SY_X, 4);
        LAS const char* er = lds + LX + w * 3 * 4096 + voff;
#pragma unroll
        for (int i = 0; i < 3; ++i)
#pragma unroll
            for (int g = 0; g < 4; ++g) { const f32x4 r = *(LAS const f32x4*)(er + i * 4096 + g * 1024);
#pragma unroll
                for (int e = 0; e < 4; ++e) o[4 * g + e] += r[e]; }
        f32x2 chk = {0.f, 0.f};
#pragma unroll
        for (int i = 0; i < 8; ++i) chk = pfma((f32x2){o[2 * i], o[2 * i + 1]}, (f32x2){0.f, 0.f}, chk);
        if (__builtin_expect(__any(!(chk[0] + chk[1] == 0.f) || !(o[12] < 1e30f)), 0)) {
            const unsigned hb = WS_KV + (unsigned)head * 64 * 4096 + voff;
            u32x4 qr = __builtin_bit_cast(u32x4, Q[0][1]); if (hi) { qr[2] = 0; qr[3] = 0; }
            float mm = -INFINITY;
            wait_flags<4>(ws, FL_KV + (unsigned)(head * 64 + lane) * FS, tag, true);
#pragma unroll 1
            for (int t = 0; t < 64; ++t) {
                f32x16 s = MFMA_BF(BC8(BLD(hb + t * 4096)), Q[0][0], zero); s = MFMA_BF(BC8(BLD(hb + t * 4096 + 1024)), BC8(qr), s);
#pragma unroll
                for (int i = 0; i < 16; ++i) mm = fmaxf(mm, s[i]);
            }
            qr = put_ref(qr, swapmax(mm), hi);
            o = zero;
#pragma unroll 1
            for (int t = 0; t < 64; ++t) {
                f32x16 s = MFMA_BF(BC8(BLD(hb + t * 4096)), Q[0][0], zero); s = MFMA_BF(BC8(BLD(hb + t * 4096 + 1024)), BC8(qr), s);
#pragma unroll
                for (int i = 0; i < 4; ++i) { p0[i] = pk_bf16(EX(s[2 * i]), EX(s[2 * i + 1])); p1[i] = pk_bf16(EX(s[8 + 2 * i]), EX(s[9 + 2 * i])); }
                o = MFMA_BF(BC8(BLD(hb + t * 4096 + 2048)), BC8(p0), o); o = MFMA_BF(BC8(BLD(hb + t * 4096 + 3072)), BC8(p1), o);
            }
        }
    }
    {
        const auto r = __builtin_amdgcn_permlane32_swap(__float_as_uint(o[12]), __float_as_uint(o[12]), false, false);
        const float rl = __builtin_amdgcn_rcpf(__uint_as_float(r[1]));
        u32x4 o0, o1;
#pragma unroll
        for (int i = 0; i < 4; ++i) { o0[i] = pk_bf16(o[2 * i] * rl, o[2 * i + 1] * rl); o1[i] = pk_bf16(o[8 + 2 * i] * rl, o[9 + 2 * i] * rl); }
        if (hi) { o1[2] = 0; o1[3] = 0; }
        const unsigned fi = (unsigned)((4 * grp + w) * 16 + head);
        BST(o0, WS_O + fi * 2048 + voff); BST(o1, WS_O + fi * 2048 + 1024 + voff);
        STAMP(9);
        drain();
        put_flag(ws, FL_O + fi * FS, tag, lane);
    }
    STAMP(10);
    const int xcd = blockIdx.x & 7, bi = blockIdx.x >> 3;
    if (xcd < 7) {
        wait_flags<2>(ws, FL_BT + (unsigned)(xcd * 128 + w * 32 + r32) * FS, tag, !hi);
        STAMP(11);
        const unsigned bo = WS_BT + (unsigned)((64 * xcd + 16 * w + (lane >> 3)) * S + 64 * bi + (lane & 7) * 8) * 2;
        glds2c(ws, bo, bo + 8 * S * 2 - 1024, lds0 + 65536 + w * 2048);
        drain();
        arrive(sy + SY_T2); await(sy + SY_T2, 4);
        u32x4 o0, o1; transpose_tile(lds + 65536, 2048, lds + 73728 + w * 2304, nullptr, w, lane, o0, o1);
        const __amdgpu_buffer_rsrc_t ro = __builtin_amdgcn_make_buffer_rsrc(out, 0, S * 2 * D * 2, 0x00020000);
        const unsigned oo = (unsigned)((64 * bi + 16 * w + (lane >> 3)) * 2 * D + D + 64 * xcd) * 2 + (lane & 7) * 16;
        __builtin_amdgcn_raw_buffer_store_b128(o0, ro, oo, 0, 16); __builtin_amdgcn_raw_buffer_store_b128(o1, ro, oo + 8 * 2 * D * 2, 0, 16);
    }
    STAMP(12);
}

DI void role_b(const uint16_t* __restrict__ wo, const uint16_t* __restrict__ u, const uint16_t* __restrict__ lar, const uint16_t* __restrict__ aim,
               const uint16_t* __restrict__ cre, const uint16_t* __restrict__ cim, const uint16_t* __restrict__ ldt, const uint16_t* __restrict__ dskip,
               uint16_t* __restrict__ out, char* __restrict__ ws, LAS char* lds, int w, int lane) {
    u64 tag;
    const unsigned lds0 = (unsigned)(uintptr_t)lds;
    LAS unsigned* sy = (LAS unsigned*)(lds + LSY);
    const __amdgpu_buffer_rsrc_t rs = __builtin_amdgcn_make_buffer_rsrc(ws, 0, WS_END, 0x00020000);
    const int b = blockIdx.x, xcd = b & 7, bi = b >> 3, r32 = lane & 31, hi = lane >> 5;
    const unsigned voff = lane * 16;
    LAS char* tab = lds + LB + w * TABW;
    const int qt = 8 * xcd + (bi >> 2), ct = 4 * (bi & 3) + w;
    bf16x8 wf[32];
#define LOADWF(i) wf[i] = BC8(__builtin_amdgcn_raw_buffer_load_b128(rs, voff, WS_WO + (unsigned)(((8 * w + (i)) & 31) * 14 + ct) * 1024, 16))
    if (xcd < 7) {
        const int cc = w >> 1, hh = w & 1, pair = 32 * xcd + bi, ch = 2 * pair + cc;
        LAS char* cb = lds + LB + cc * CBW;
        const unsigned po = (unsigned)(ch * NS + 128 * hh) / 2 + lane;
        const unsigned pl = ((const unsigned*)lar)[po], pa = ((const unsigned*)aim)[po], pr = ((const unsigned*)cre)[po], pi = ((const unsigned*)cim)[po];
        const unsigned uo = (unsigned)((64 * bi + 16 * w + (lane >> 3)) * D + 64 * xcd + (lane & 7) * 8) * 2;
        glds2(u, uo, uo + 8 * D * 2 - 1024, lds0 + LB + w * TABW + 4096);
        const u64 e0 = __hip_atomic_load((GAS u64*)ws, __ATOMIC_RELAXED, __HIP_MEMORY_SCOPE_AGENT);
        u32x2 gi[2][3];
#pragma unroll
        for (int ps = 0; ps < 2; ++ps)
#pragma unroll
            for (int i = 0; i < 3; ++i) gi[ps][i] = ((const u32x2*)g_idx.v[128 * hh + 64 * ps + lane])[i];
        const unsigned lw = ((const unsigned*)ldt)[pair], dw = ((const unsigned*)dskip)[pair];
        const float dt = rbf(expf(cc ? bf_hi(lw) : bf_lo(lw)));
        drain();
        STAMP(1);
        arrive(sy + SY_WO);
        tag = mix(e0);
        const float cm = wave_max(fmaxf(fmaxf(fabsf(bf_lo(pr)), fabsf(bf_hi(pr))), fmaxf(fabsf(bf_lo(pi)), fabsf(bf_hi(pi)))));
        unsigned ce = __float_as_uint(cm) & 0x7f800000u; ce = ce < 0x01000000u ? 0x01000000u : (ce > 0x7e800000u ? 0x7e800000u : ce);
        const float cscale = __uint_as_float(0x7f000000u - ce), oscale = 2.0f * dt * __uint_as_float(ce);
        LAS unsigned* tw = (LAS unsigned*)(tab + lane * 4);
        LAS const char* trd = tab + r32 * TROW + hi * 16;
        f32x16 dacc = {};
        f32x2 z1, z8, z256, x0;
#define ANCH(vl, va, vr, vi) do { const float a2 = (-rbf(expf(vl)) * dt) * 1.4426950408889634f, rev = ((va) * dt) * 0.15915494309189535f; \
            z1 = anchor(a2, -rev, 1.f); z8 = anchor(a2, rev, 8.f); z256 = anchor(a2, -rev, 256.f); x0 = (f32x2){(vr) * cscale, -(vi) * cscale}; \
            PIN(z1); PIN(z8); PIN(z256); SBAR(); } while (0)
#define TW(row) tw[(row) * (TROW / 4)]
#define CHAIN() do { \
            const f32x2 z1s = rot(z1), zs = rot(z256), z8s = rot(z8); \
            f32x2 y[4], X[8]; y[1] = z8; y[2] = cmul(z8, z8, z8s); X[0] = x0; X[1] = cmul(x0, z1, z1s); f32x2 a = cmul(x0, z256, zs); \
            y[3] = cmul(y[2], z8, z8s); const f32x2 z32 = cmul(y[2], y[2], rot(y[2])), z32s = rot(z32); X[2] = cmul(X[1], z1, z1s); y[0] = z32; \
            f32x2 c4 = cmul(X[2], z256, zs); \
            unsigned c[2][7], d0 = pk_f16(X[0]), d1 = pk_f16(X[1]), d2 = pk_f16(y[1]); \
            c[0][0] = pk_f16(y[0]); c[0][1] = 0x3c00u; c[0][2] = pk_f16(y[2]); c[0][3] = pk_f16(y[3]); c[0][4] = pk_f16(X[2]); c[0][5] = pk_f16(a); c[0][6] = pk_f16(c4); \
            TW(0) = d0; TW(1) = d1; TW(22) = d2; SBAR(); \
            _Pragma("unroll") for (int i = 1; i < 9; ++i) { \
                f32x2 t0, t1, t2, t3, t4, t5, t6; const int j = i - 1; unsigned* pc = c[j & 1]; unsigned* nc = c[i & 1]; \
                if (i < 8) { if (i > 1) t0 = y[0].xx * z32; t1 = y[1].xx * z32; t2 = y[2].xx * z32; t3 = y[3].xx * z32; \
                    if (i < 6) t4 = X[i + 1].xx * z1; t5 = a.xx * z256; \
                    if (i == 3) t6 = X[4].xx * z256; if (i == 4 || i == 5) t6 = c4.xx * z256; if (i == 6) t6 = X[6].xx * z256; } SBAR(); \
                if (j != 1) TW(21 + (j ? 4 * j : 4)) = pc[0]; TW(21 + (j ? 4 * j + 1 : 0)) = pc[1]; TW(23 + 4 * j) = pc[2]; TW(24 + 4 * j) = pc[3]; \
                if (j < 6) TW(j + 2) = pc[4]; TW(j == 0 ? 8 : (j == 1 ? 12 : (j == 2 ? 14 : 13 + j))) = pc[5]; \
                if (j == 0) TW(9) = pc[6]; if (j == 3) TW(10) = pc[6]; if (j == 4) TW(13) = pc[6]; if (j == 5) TW(15) = pc[6]; if (j == 6) TW(11) = pc[6]; SBAR(); \
                if (i < 8) { if (i > 1) y[0] = pfma(y[0].yy, z32s, t0); y[1] = pfma(y[1].yy, z32s, t1); y[2] = pfma(y[2].yy, z32s, t2); y[3] = pfma(y[3].yy, z32s, t3); \
                    if (i < 6) X[i + 2] = pfma(X[i + 1].yy, z1s, t4); a = pfma(a.yy, zs, t5); \
                    if (i == 3) c4 = pfma(X[4].yy, zs, t6); if (i == 4 || i == 5) c4 = pfma(c4.yy, zs, t6); if (i == 6) c4 = pfma(X[6].yy, zs, t6); SBAR(); \
                    if (i > 1) nc[0] = pk_f16(y[0]); nc[1] = pk_f16(y[1]); nc[2] = pk_f16(y[2]); nc[3] = pk_f16(y[3]); \
                    if (i < 6) nc[4] = pk_f16(X[i + 2]); nc[5] = pk_f16(a); if (i >= 3 && i <= 6) nc[6] = pk_f16(c4); \
                    asm volatile("" :: "v"(pc[0]), "v"(pc[1]), "v"(pc[2]), "v"(pc[3]), "v"(pc[4]), "v"(pc[5]), "v"(pc[6])); SBAR(); } \
            } } while (0)
        f16x8 fa[8], fb[8];
#define RDT() do { _Pragma("unroll") for (int kk = 0; kk < 8; ++kk) { fa[kk] = *(LAS const f16x8*)(trd + kk * 32); fb[kk] = *(LAS const f16x8*)(trd + 21 * TROW + kk * 32); } SBAR(); } while (0)
#define MMT() do { _Pragma("unroll") for (int kk = 0; kk < 8; ++kk) dacc = MFMA_F16(fa[kk], fb[kk], dacc); } while (0)
        ANCH(bf_lo(pl), bf_lo(pa), bf_lo(pr), bf_lo(pi));
        await(sy + SY_WO, 4);
        if (w == 0 && lane == 0) __hip_atomic_store((LAS u64*)sy, tag, __ATOMIC_RELAXED, __HIP_MEMORY_SCOPE_WORKGROUP);
        {
            u32x4 o0, o1; transpose_tile(lds + LB + 4096, TABW, tab, sy + SY_T1, w, lane, o0, o1);
            const unsigned to = WS_UT + (unsigned)((64 * xcd + 16 * w + (lane >> 3)) * S + 64 * bi) * 2 + (lane & 7) * 16;
            __builtin_amdgcn_s_setprio(3); BST(o0, to); BST(o1, to + 8 * S * 2); __builtin_amdgcn_s_setprio(PRIO_B);
        }
        await(sy + SY_T1, 4);
        STAMP(13);
        CHAIN(); RDT();
        drain(); __builtin_amdgcn_s_setprio(3); put_flag(ws, FL_UT + (unsigned)(xcd * 128 + w * 32 + bi) * FS, tag, lane); __builtin_amdgcn_s_setprio(PRIO_B);
        STAMP(14);
        ANCH(bf_hi(pl), bf_hi(pa), bf_hi(pr), bf_hi(pi));
        MMT(); SBAR();
        CHAIN(); RDT();
        const unsigned utf = hi ? FL_WO + (unsigned)(w * 32 + r32) * FS : FL_UT + (unsigned)(xcd * 128 + (bi >> 3) * 32 + r32) * FS;
        const u64 utv = __hip_atomic_load((GAS u64*)(ws + utf), __ATOMIC_RELAXED, __HIP_MEMORY_SCOPE_AGENT); SBAR();
        MMT();
        STAMP(2);
        {
            LAS float* pt = (LAS float*)tab;
            *(LAS f32x4*)(pt + 8 * r32 + 4 * hi) = (f32x4){dacc[0] * oscale, dacc[1] * oscale, dacc[2] * oscale, dacc[3] * oscale};
            *(LAS f32x2*)(pt + (hi ? 384 + 2 * r32 : 256 + 4 * r32)) = (f32x2){dacc[4] * oscale, dacc[5] * oscale};
            *(LAS f32x2*)(pt + (hi ? 448 + 2 * r32 : 258 + 4 * r32)) = (f32x2){dacc[6] * oscale, dacc[7] * oscale};
            pt[hi ? 640 + r32 : 512 + r32] = dacc[8] * oscale;
            pt[hi ? 672 + r32 : 544 + r32] = dacc[9] * oscale;
            pt[hi ? 672 + r32 : 576 + r32] = dacc[10] * oscale;
            pt[hi ? 672 + r32 : 608 + r32] = dacc[11] * oscale;
        }
        LAS unsigned* ps_ = sy + SY_P + 5 * cc;
        arrive(ps_ + 0);
        LAS char* ui = cb + UOF;
        LAS char* kt = cb + KOF;
        await(ps_ + 0, 2);
#pragma unroll
        for (int ps = 0; ps < 2; ++ps) {
            const int v = 81 * hh + 64 * ps + lane;
            if (ps == 0 || lane < 17) {
                const f32x4 p0 = *(LAS const f32x4*)(cb + v * 16), p1 = *(LAS const f32x4*)(cb + TABW + v * 16);
                u32x2 r = {pk_bf16(p0[0] + p1[0], p0[1] + p1[1]), pk_bf16(p0[2] + p1[2], p0[3] + p1[3])};
                if (v == 161) r = (u32x2){0u, 0u};
                *(LAS u32x2*)(kt + v * 8) = r;
            }
        }
        arrive(ps_ + 1);
        if (__any(utv != tag)) wait_flags<8>(ws, utf, tag, utv != tag);
        CFENCE();
        const unsigned ub = WS_UT + (unsigned)(ch * S + 1024 * hh) * 2 + voff;
        const u32x4 u0 = BLD(ub), u1 = BLD(ub + 1024); SBAR();
        await(ps_ + 1, 2);
        STAMP(3);
        __builtin_amdgcn_s_setprio(PRIO_B2);
        const u32x4 z4 = {};
        {
            unsigned W[2][6];
#pragma unroll
            for (int ps = 0; ps < 2; ++ps)
#pragma unroll
                for (int i = 0; i < 6; ++i) { const unsigned gv = gi[ps][i >> 1][i & 1];
                    W[ps][i] = *(LAS const uint16_t*)(kt + (gv & 0xffffu));
                    if (i < 5) W[ps][i] |= (unsigned)*(LAS const uint16_t*)(kt + (gv >> 16)) << 16; }
            *(LAS u32x4*)(ui + (64 * hh + lane) * 16) = z4;
            if (lane < 16) *(LAS u32x4*)(ui + (128 + 16 * hh + lane) * 16) = z4;
            if (lane < 8) *(LAS u32x4*)(cb + (2 * hh + (lane >> 2)) * CPY + (256 + (lane & 3)) * 16) = z4;
#pragma unroll
            for (int ps = 0; ps < 2; ++ps) {
                unsigned V[5];
#pragma unroll
                for (int i = 0; i < 5; ++i) V[i] = __builtin_amdgcn_alignbit(W[ps][i + 1], W[ps][i], 16);
                LAS char* cp = cb + (128 * hh + 64 * ps + lane) * 16;
                *(LAS u32x4*)(cp) = (u32x4){W[ps][0], W[ps][1], W[ps][2], W[ps][3]};
                *(LAS u32x4*)(cp + CPY) = (u32x4){V[0], V[1], V[2], V[3]};
                *(LAS u32x4*)(cp + 2 * CPY) = (u32x4){W[ps][1], W[ps][2], W[ps][3], W[ps][4]};
                *(LAS u32x4*)(cp + 3 * CPY) = (u32x4){V[1], V[2], V[3], V[4]};
            }
        }
        *(LAS u32x4*)(ui + (32 + 32 * hh + (lane >> 2)) * 80 + (lane & 3) * 16) = u0;
        *(LAS u32x4*)(ui + (48 + 32 * hh + (lane >> 2)) * 80 + (lane & 3) * 16) = u1;
        arrive(ps_ + 2); await(ps_ + 2, 2);
        STAMP(4);
        __builtin_amdgcn_s_setprio(PRIO_BC);
        f32x16 a0 = {}, a1 = {};
        {
            const int sb = 7 - (r32 & 7);
            LAS const char* pa = cb + (sb & 3) * CPY + (255 - (r32 >> 3) + hi) * 16 + (sb >> 2) * 8 - 64 * (16 * hh + 15) - 2048;
            LAS const char* pa2 = pa + 8; asm volatile("" : "+v"(pa2));
            LAS const char* pb = ui + (32 + r32 - 16 * hh - 15) * 80 + 16 * hi;
            s16x4 ta[2][2], tb[2][2]; bf16x8 b0[2], b1[2];
#define T4(q, off) (*(LAS const s16x4*)((q) + (off)))
#define CSTEP(i, j, RD) do { const int o_ = 64 * (15 - ((j) >> 1)) + 32 * ((j) & 1), u_ = 80 * (15 - ((j) >> 1)) + 32 * ((j) & 1); \
                a1 = MFMA_BF(cat8(ta[i][0], ta[i][1]), b1[i], a1); SBAR(); if (RD) { ta[1 - i][0] = T4(pa, o_ + 2048); ta[1 - i][1] = T4(pa2, o_ + 2048); } SBAR(); \
                a0 = MFMA_BF(cat8(ta[i][0], ta[i][1]), b0[i], a0); SBAR(); if (RD) { b1[1 - i] = FR(pb, u_ + 2560); b0[1 - i] = FR(pb, u_); } SBAR(); \
                a1 = MFMA_BF(cat8(tb[i][0], tb[i][1]), b0[i], a1); SBAR(); if (RD) { tb[1 - i][0] = T4(pa, o_); tb[1 - i][1] = T4(pa2, o_); } SBAR(); } while (0)
            ta[0][0] = T4(pa, 960 + 2048); ta[0][1] = T4(pa2, 960 + 2048); b1[0] = FR(pb, 1200 + 2560); b0[0] = FR(pb, 1200); tb[0][0] = T4(pa, 960); tb[0][1] = T4(pa2, 960);
#pragma unroll
            for (int j = 0; j < 32; ++j) {
                if (j == 31) arrive(ps_ + 3);
                CSTEP(j & 1, j + 1, j < 31);
                if (ct < 14) LOADWF(j);
                SBAR();
            }
        }
        STAMP(5);
        __builtin_amdgcn_s_setprio(PRIO_B3);
        {
            u32x2 uu[4];
#pragma unroll
            for (int g = 0; g < 4; ++g) uu[g] = *(LAS const u32x2*)(ui + (32 + 32 * hh + r32) * 80 + 16 * g + 8 * hi);
            LAS char* ex = cb + r32 * 144 + hi * 16;
            const f32x16 snd = hh ? a0 : a1; const f32x16 y = hh ? a1 : a0;
            await(ps_ + 3, 2);
#pragma unroll
            for (int g = 0; g < 4; ++g) *(LAS f32x4*)(ex + hh * 4608 + g * 32) = (f32x4){snd[4 * g], snd[4 * g + 1], snd[4 * g + 2], snd[4 * g + 3]};
            arrive(ps_ + 4);
            const float dsk = cc ? bf_hi(dw) : bf_lo(dw);
            unsigned sk[8];
#pragma unroll
            for (int g = 0; g < 4; ++g)
#pragma unroll
                for (int e = 0; e < 2; ++e) sk[2 * g + e] = pk_bf16(bf_lo(uu[g][e]) * dsk, bf_hi(uu[g][e]) * dsk);
            await(ps_ + 4, 2);
            LAS char* sc = cb + 9216 + hh * 2048;
#pragma unroll
            for (int g = 0; g < 4; ++g) {
                const f32x4 p = *(LAS const f32x4*)(ex + (1 - hh) * 4608 + g * 32);
                u32x2 r;
#pragma unroll
                for (int e = 0; e < 2; ++e) {
                    const unsigned yb = pk_bf16(y[4 * g + 2 * e] + p[2 * e], y[4 * g + 2 * e + 1] + p[2 * e + 1]);
                    r[e] = pk_bf16(bf_lo(yb) + bf_lo(sk[2 * g + e]), bf_hi(yb) + bf_hi(sk[2 * g + e]));
                }
                *(LAS u32x2*)(sc + 64 * r32 + 16 * g + 8 * hi) = r;
            }
            const u32x4 o0 = *(LAS const u32x4*)(sc + voff), o1 = *(LAS const u32x4*)(sc + 1024 + voff);
            const unsigned bo = WS_BT + (unsigned)(ch * S + 1024 * hh) * 2 + voff;
            BST(o0, bo); BST(o1, bo + 1024);
        }
        arrive(sy + SY_BG);
        STAMP(6);
        drain();
        put_flag(ws, FL_BT + (unsigned)(xcd * 128 + bi * 4 + w) * FS, tag, lane);
    } else {
        {
            const unsigned nb = (bi & 1) ? 12 * 2 * D : 16 * 2 * D;
            unsigned v4[4];
#pragma unroll
            for (int i = 0; i < 4; ++i) { const unsigned o = (unsigned)(w * 4096 + i * 1024) + voff; v4[i] = (o < nb ? o : 0u) + 4096 - i * 1024; }
            glds4((const char*)(wo + (DH * (bi >> 1) + 16 * (bi & 1)) * D) - 4096, v4[0], v4[1], v4[2], v4[3], lds0 + LB + w * 4096);
        }
        const u64 e0 = __hip_atomic_load((GAS u64*)ws, __ATOMIC_RELAXED, __HIP_MEMORY_SCOPE_AGENT);
        drain();
        tag = mix(e0);
        arrive(sy + SY_WO); await(sy + SY_WO, 4);
        if (w == 0 && lane == 0) __hip_atomic_store((LAS u64*)sy, tag, __ATOMIC_RELAXED, __HIP_MEMORY_SCOPE_WORKGROUP);
        LAS const char* p = lds + LB + (4 * hi + ((lane & 15) >> 2)) * (2 * D) + ((lane >> 4) & 1) * 32 + (lane & 3) * 8;
#pragma unroll
        for (int i = 0; i < 4; ++i) {
            const int ct = w + 4 * i;
            if (ct < 14) {
                u32x4 o = __builtin_bit_cast(u32x4, cat8(trread(p + ct * 64), trread(p + 8 * 2 * D + ct * 64)));
                if (hi && (bi & 1)) { o[2] = 0; o[3] = 0; }
                BST(o, WS_WO + (unsigned)(bi * 14 + ct) * 1024 + voff);
            }
        }
        drain();
        put_flag(ws, FL_WO + (unsigned)(w * 32 + bi) * FS, tag, lane);
        __builtin_amdgcn_s_setprio(0);
        arrive(sy + SY_BG);
        await_slow(sy + SY_G, 4);
        wait_flags<8>(ws, FL_WO + (unsigned)(w * 32 + r32) * FS, tag, !hi);
        if (ct < 14) {
#pragma unroll
            for (int i = 0; i < 32; ++i) { LOADWF(i); __builtin_amdgcn_s_sleep(WOSL); SBAR(); }
        }
    }
    STAMP(7);
    const __amdgpu_buffer_rsrc_t ro = __builtin_amdgcn_make_buffer_rsrc(out, 0, S * 2 * D * 2, 0x00020000);
    await(sy + SY_BG, 4);
    STAMP(8);
    await_slow(sy + SY_X, 4);
    wait_flags<OSL>(ws, FL_O + (unsigned)(qt * 16 + 4 * w + lane) * FS, tag, lane < 4);
    STAMP(9);
    u32x4 of[8];
#pragma unroll
    for (int i = 0; i < 8; ++i) of[i] = BLD(WS_O + (unsigned)(qt * 32 + 8 * w + i) * 1024 + voff);
#pragma unroll
    for (int i = 0; i < 8; ++i) *(LAS u32x4*)(lds + LB + (8 * w + i) * 1024 + voff) = of[i];
    arrive(sy + SY_B2);
    STAMP(10);
    f32x16 acc = {};
    if (ct < 14) {
#pragma unroll
        for (int i = 0; i < 8; ++i) acc = MFMA_BF(wf[i], BC8(of[i]), acc);
    }
    SBAR();
    await(sy + SY_B2, 4);
    STAMP(11);
    if (ct < 14) {
        LAS const char* po[3];
#pragma unroll
        for (int j = 0; j < 3; ++j) po[j] = lds + LB + ((w + 1 + j) & 3) * 8192 + voff;
        bf16x8 og[4];
#pragma unroll
        for (int i = 0; i < 3; ++i) og[i] = FR(po[0], i * 1024);
#pragma unroll
        for (int i = 0; i < 24; ++i) { if (i < 21) og[(i + 3) & 3] = FR(po[(i + 3) >> 3], ((i + 3) & 7) * 1024); SBAR(); acc = MFMA_BF(wf[8 + i], og[i & 3], acc); SBAR(); }
        LAS char* sc = lds + LB + 40960 + w * 2304;
#pragma unroll
        for (int g = 0; g < 4; ++g) *(LAS u32x2*)(sc + r32 * 72 + 16 * g + 8 * hi) = (u32x2){pk_bf16(acc[4 * g], acc[4 * g + 1]), pk_bf16(acc[4 * g + 2], acc[4 * g + 3])};
        LAS const char* sr = sc + (lane >> 2) * 72 + (lane & 3) * 16;
        const u32x2 a0 = *(LAS const u32x2*)sr, a1 = *(LAS const u32x2*)(sr + 8), b0 = *(LAS const u32x2*)(sr + 16 * 72), b1 = *(LAS const u32x2*)(sr + 16 * 72 + 8);
        const unsigned oo = (unsigned)((32 * qt + (lane >> 2)) * 2 * D + 32 * ct) * 2 + (lane & 3) * 16;
        __builtin_amdgcn_raw_buffer_store_b128((u32x4){a0[0], a0[1], a1[0], a1[1]}, ro, oo, 0, 16);
        __builtin_amdgcn_raw_buffer_store_b128((u32x4){b0[0], b0[1], b1[0], b1[1]}, ro, oo + 16 * 2 * D * 2, 0, 16);
    }
    if (b == 0 && w == 0) put_flag(ws, 0, tag, lane);
    STAMP(12);
}

__global__ __launch_bounds__(512, 2) void fused(const uint16_t* __restrict__ x, const uint16_t* __restrict__ wqkv, const uint16_t* __restrict__ wo,
                                               const uint16_t* __restrict__ u, const uint16_t* __restrict__ lar, const uint16_t* __restrict__ aim,
                                               const uint16_t* __restrict__ cre, const uint16_t* __restrict__ cim, const uint16_t* __restrict__ ldt,
                                               const uint16_t* __restrict__ dskip, uint16_t* __restrict__ out, char* __restrict__ ws) {
    extern __shared__ __attribute__((aligned(16))) char smem[];
    LAS char* lds = (LAS char*)smem;
    const int lane = threadIdx.x & 63, w = __builtin_amdgcn_readfirstlane(threadIdx.x >> 6);
    STAMP(0);
    if (w >= 4) __builtin_amdgcn_s_setprio(3);
    if (w == 4) ((LAS unsigned*)(lds + LSY))[lane] = 0;
    asm volatile("" :: "s"(x), "s"(wqkv), "s"(wo), "s"(u), "s"(lar), "s"(aim), "s"(cre), "s"(cim), "s"(ldt), "s"(dskip), "s"(out), "s"(ws));
    asm volatile("s_waitcnt lgkmcnt(0)\n\ts_barrier" ::: "memory");
    if (w < 4) role_a(x, wqkv, out, ws, lds, w, lane);
    else role_b(wo, u, lar, aim, cre, cim, ldt, dskip, out, ws, lds, w - 4, lane);
}
}

extern "C" void kernel_launch(void* const* d_in, const int* in_sizes, int n_in, void* d_out, int out_size, void* d_ws, size_t ws_size, hipStream_t stream) {
    using namespace k;
    (void)hipFuncSetAttribute((const void*)fused, hipFuncAttributeMaxDynamicSharedMemorySize, LDS_BYTES);
    const uint16_t* const* in = (const uint16_t* const*)d_in;
    fused<<<256, 512, LDS_BYTES, stream>>>(in[0], in[1], in[2], in[3], in[4], in[5], in[6], in[7], in[8], in[9], (uint16_t*)d_out, (char*)d_ws);
}
```

```cpp
#define HA 99
#define HB -1
#include <hip/hip_runtime.h>
#include <hip/hip_bf16.h>
#include <stdint.h>

typedef short bf16x8 __attribute__((ext_vector_type(8)));
typedef _Float16 f16x8 __attribute__((ext_vector_type(8)));
typedef _Float16 f16x2 __attribute__((ext_vector_type(2)));
typedef __bf16 bf16x2 __attribute__((ext_vector_type(2)));
typedef short s16x4 __attribute__((ext_vector_type(4)));
typedef float f32x16 __attribute__((ext_vector_type(16)));
typedef float f32x4 __attribute__((ext_vector_type(4)));
typedef float f32x2 __attribute__((ext_vector_type(2)));
typedef unsigned u32x4 __attribute__((ext_vector_type(4)));
typedef unsigned u32x2 __attribute__((ext_vector_type(2)));
typedef unsigned long long u64;
#define LAS __attribute__((address_space(3)))
#define GAS __attribute__((address_space(1)))
#define DI __device__ __forceinline__
#define MFMA_BF(a, b, c) __builtin_amdgcn_mfma_f32_32x32x16_bf16((a), (b), (c), 0, 0, 0)
#define MFMA_F16(a, b, c) __builtin_amdgcn_mfma_f32_32x32x16_f16((a), (b), (c), 0, 0, 0)
#define SBAR() __builtin_amdgcn_sched_barrier(0)
#define CFENCE() asm volatile("" ::: "memory")
#define PIN(x) asm volatile("" : "+v"(x))
#define BC8(x) __builtin_bit_cast(bf16x8, (x))
#define EX(x) __builtin_amdgcn_exp2f(x)
#define PRIO_A1 0
#define PRIO_B 0
#define PRIO_B2 1
#define ROT 1
#define KVM 6u
#define HOLDB 0
#define PRIO_B3 1
#define PRIO_BC 0
#define FLJ 13
#define OSL 2
#define LATE2 1
#define WOSL 2

namespace k {
#define STAMP(i) do { if ((i) == ((threadIdx.x >> 8) ? HB : HA) && (threadIdx.x & 255) == 0) ((LAS volatile unsigned*)(lds + LSY))[40] = (unsigned)(__builtin_amdgcn_s_memrealtime() - t_start); \
    if ((i) == 12 && (threadIdx.x >> 8)) { const u64 e_ = 2800 + ((LAS volatile unsigned*)(lds + LSY))[40]; while (__builtin_amdgcn_s_memrealtime() - t_start < e_) __builtin_amdgcn_s_sleep(2); } } while (0)
constexpr int S = 2048, D = 448, DH = 28, NS = 256;
constexpr unsigned FS = 128;
constexpr unsigned FL_KV = FS, FL_O = FL_KV + 1024 * FS, FL_UT = FL_O + 1024 * FS, FL_BT = FL_UT + 896 * FS, FL_WO = FL_BT + 896 * FS;
constexpr unsigned WS_KV = 512u << 10;
constexpr unsigned WS_O = WS_KV + (4u << 20);
constexpr unsigned WS_WO = WS_O + (2u << 20);
constexpr unsigned WS_UT = WS_WO + (448u << 10);
constexpr unsigned WS_BT = WS_UT + D * S * 2;
constexpr unsigned WS_END = WS_BT + D * S * 2;
constexpr int WSL = 8192, LWQ = 4 * WSL, LXR = LWQ + 16384, XSL = 16384, LB = LXR + 3 * XSL;
constexpr int LQ = LXR, LX = 0;
constexpr int TROW = 272, TABW = 53 * TROW, CBW = 2 * TABW, CPY = 4160, UOF = 4 * CPY, KOF = CBW - 1312;
constexpr int LSY = LB + 2 * CBW, LDS_BYTES = LSY + 256;
static_assert(LDS_BYTES <= 163840 && (LSY & 15) == 0 && UOF + 7680 <= KOF, "lds");
enum { SY_TAG = 0, SY_F = 2, SY_P = 10, SY_WO = 20, SY_T1 = 21, SY_BG = 22, SY_B2 = 23, SY_Q = 24, SY_X = 25, SY_G = 26, SY_T2 = 27, SY_H = 28 };
constexpr int SPIN = 1 << 18;

struct IdxTab { uint16_t v[256][12]; };
constexpr IdxTab make_idx() {
    IdxTab t = {};
    for (int y = 0; y < 256; ++y) for (int e = 0; e < 12; ++e) {
        const int j = 2047 - 8 * y - e; int ix = 644;
        if (j >= 0) { const int sh = j >= 1024 ? 3 : (j >= 512 ? 2 : (j >= 256 ? 1 : 0));
            ix = sh ? 128 * sh + ((j + (1 << (sh - 1)) - 1 + ((j >> sh) & 1)) >> sh) : j; }
        t.v[y][e] = (uint16_t)(2 * ix);
    }
    return t;
}
__device__ const IdxTab g_idx __attribute__((aligned(16))) = make_idx();

DI unsigned pk_bf16(float a, float b) { f32x2 v = {a, b}; return __builtin_bit_cast(unsigned, __builtin_convertvector(v, bf16x2)); }
DI unsigned pk_f16(f32x2 v) { return __builtin_bit_cast(unsigned, __builtin_convertvector(v, f16x2)); }
DI float bf_lo(unsigned w) { return __uint_as_float(w << 16); }
DI float bf_hi(unsigned w) { return __uint_as_float(w & 0xffff0000u); }
DI float rbf(float x) { return bf_lo(pk_bf16(x, x)); }
#define GL0 "s_mov_b32 %0, m0\n\ts_mov_b32 m0, %2\n\ts_nop 0\n\t"
#define GL1(V, O, C) "global_load_lds_dwordx4 " V ", %1 offset:" #O C "\n\t"
#define GLE "s_mov_b32 m0, %0"
DI void glds4(const void* sb, unsigned v0, unsigned v1, unsigned v2, unsigned v3, unsigned lb) { unsigned sv;
    asm volatile(GL0 GL1("%3", 0, "") GL1("%4", 1024, "") GL1("%5", 2048, "") GL1("%6", 3072, "") GLE : "=&s"(sv) : "s"(sb), "s"(lb), "v"(v0), "v"(v1), "v"(v2), "v"(v3) : "memory"); }
DI void glds3(const void* sb, unsigned v0, unsigned v1, unsigned v2, unsigned lb) { unsigned sv;
    asm volatile(GL0 GL1("%3", 0, "") GL1("%4", 1024, "") GL1("%5", 2048, "") GLE : "=&s"(sv) : "s"(sb), "s"(lb), "v"(v0), "v"(v1), "v"(v2) : "memory"); }
DI void glds2(const void* sb, unsigned v0, unsigned v1, unsigned lb) { unsigned sv;
    asm volatile(GL0 GL1("%3", 0, "") GL1("%4", 1024, "") GLE : "=&s"(sv) : "s"(sb), "s"(lb), "v"(v0), "v"(v1) : "memory"); }
DI void glds1(const void* sb, unsigned v0, unsigned lb) { unsigned sv;
    asm volatile(GL0 GL1("%3", 0, "") GLE : "=&s"(sv) : "s"(sb), "s"(lb), "v"(v0) : "memory"); }
DI void glds2c(const void* sb, unsigned v0, unsigned v1, unsigned lb) { unsigned sv;
    asm volatile(GL0 GL1("%3", 0, " sc1") GL1("%4", 1024, " sc1") GLE : "=&s"(sv) : "s"(sb), "s"(lb), "v"(v0), "v"(v1) : "memory"); }
DI float wave_max(float v) {
#define DPPMAX(ctrl, rm) v = fmaxf(v, __uint_as_float(__builtin_amdgcn_update_dpp(0, __float_as_uint(v), ctrl, rm, 0xf, true)))
    DPPMAX(0x111, 0xf); DPPMAX(0x112, 0xf); DPPMAX(0x114, 0xf); DPPMAX(0x118, 0xf); DPPMAX(0x142, 0xa); DPPMAX(0x143, 0xc);
    return __uint_as_float(__builtin_amdgcn_readlane(__float_as_uint(v), 63));
}
DI s16x4 trread(LAS const char* p) { return __builtin_bit_cast(s16x4, __builtin_amdgcn_ds_read_tr16_b64_v4i16((LAS s16x4*)p)); }
DI bf16x8 cat8(s16x4 a, s16x4 b) { return __builtin_shufflevector(a, b, 0, 1, 2, 3, 4, 5, 6, 7); }
DI f32x2 pfma(f32x2 a, f32x2 b, f32x2 c) { return __builtin_elementwise_fma(a, b, c); }
DI f32x2 cmul(f32x2 a, f32x2 b, f32x2 bs) { return pfma(a.yy, bs, a.xx * b); }
DI f32x2 rot(f32x2 b) { return (f32x2){-b.y, b.x}; }
DI f32x2 anchor(float a2, float rev, float s) {
    const float mag = EX(a2 * s);
    float r = rev * s; r -= __builtin_rintf(r);
    return (f32x2){mag * __builtin_amdgcn_cosf(r), mag * __builtin_amdgcn_sinf(r)};
}
DI void arrive(LAS unsigned* c) { asm volatile("s_mov_b64 exec, 1\n\tds_add_u32 %0, %1\n\ts_mov_b64 exec, -1" :: "v"((unsigned)(uintptr_t)c), "v"(1u) : "memory"); }
DI void await(LAS unsigned* c, unsigned n) {
    unsigned t, i = SPIN;
    asm volatile("1:\n\tds_read_b32 %0, %2\n\ts_waitcnt lgkmcnt(0)\n\tv_cmp_gt_u32 vcc, %3, %0\n\ts_cbranch_vccz 2f\n\ts_sleep 1\n\ts_sub_u32 %1, %1, 1\n\ts_cbranch_scc0 1b\n2:"
                 : "=&v"(t), "+s"(i) : "v"((unsigned)(uintptr_t)c), "s"(n) : "vcc", "scc", "memory");
}
DI void await_slow(LAS unsigned* c, unsigned n) {
    unsigned t, i = SPIN;
    asm volatile("1:\n\tds_read_b32 %0, %2\n\ts_waitcnt lgkmcnt(0)\n\tv_cmp_gt_u32 vcc, %3, %0\n\ts_cbranch_vccz 2f\n\ts_sleep 3\n\ts_sub_u32 %1, %1, 1\n\ts_cbranch_scc0 1b\n2:"
                 : "=&v"(t), "+s"(i) : "v"((unsigned)(uintptr_t)c), "s"(n) : "vcc", "scc", "memory");
}
DI u64 get_tag(LAS unsigned* sy) {
    await(sy, 1);
    const u64 v = __hip_atomic_load((LAS u64*)sy, __ATOMIC_RELAXED, __HIP_MEMORY_SCOPE_WORKGROUP);
    return ((u64)(unsigned)__builtin_amdgcn_readfirstlane((unsigned)(v >> 32)) << 32) | (unsigned)__builtin_amdgcn_readfirstlane((unsigned)v);
}
DI void drain() { asm volatile("s_waitcnt vmcnt(0)" ::: "memory"); }
DI void put_flag(char* ws, unsigned off, u64 tag, int lane) { if (lane == 0) __hip_atomic_store((GAS u64*)(ws + off), tag, __ATOMIC_RELAXED, __HIP_MEMORY_SCOPE_AGENT); }
template <int SLP> DI void wait_flags(char* ws, unsigned off, u64 tag, bool pend) {
    GAS u64* p = (GAS u64*)(ws + off);
    CFENCE();
#pragma unroll 1
    for (int i = 0; i < SPIN; ++i) {
        if (pend) pend = __hip_atomic_load(p, __ATOMIC_RELAXED, __HIP_MEMORY_SCOPE_AGENT) != tag;
        if (!__any(pend)) break;
        __builtin_amdgcn_s_sleep(SLP);
    }
    CFENCE();
}
#define BST(v, off) __builtin_amdgcn_raw_buffer_store_b128((v), rs, (off), 0, 16)
#define BLD(off) __builtin_amdgcn_raw_buffer_load_b128(rs, (off), 0, 16)
#define FR(p, off) (*(LAS const bf16x8*)((p) + (off)))
DI float swapmax(float m) { const auto r = __builtin_amdgcn_permlane32_swap(__float_as_uint(m), __float_as_uint(m), false, false); return fmaxf(__uint_as_float(r[0]), __uint_as_float(r[1])); }
DI u32x4 put_ref(u32x4 q1, float m, int hi) {
    const float m1 = rbf(m), m2 = rbf(m - m1), m3 = m - m1 - m2;
    if (hi) { q1[2] = pk_bf16(-m1, -m2); q1[3] = pk_bf16(-m3, 0.f); }
    return q1;
}
DI u64 mix(u64 v) {
    const u64 e = ((u64)(unsigned)__builtin_amdgcn_readfirstlane((unsigned)(v >> 32)) << 32) | (unsigned)__builtin_amdgcn_readfirstlane((unsigned)v);
    u64 z = e + 0x9E3779B97F4A7C15ull; z = (z ^ (z >> 30)) * 0xBF58476D1CE4E5B9ull; z = (z ^ (z >> 27)) * 0x94D049BB133111EBull; return (z ^ (z >> 31)) | 1ull; }
DI void transpose_tile(LAS const char* stage, int pst, LAS char* scratch, LAS unsigned* done, int w, int lane, u32x4& o0, u32x4& o1) {
    LAS const char* p = stage + (lane >> 4) * pst + ((lane & 15) >> 2) * 128 + w * 32 + (lane & 3) * 8;
    const bf16x8 t0 = cat8(trread(p), trread(p + 512)), t1 = cat8(trread(p + 1024), trread(p + 1536));
    if (done) arrive(done);
    LAS char* sc = scratch + (lane & 15) * 144 + (lane >> 4) * 32;
    *(LAS bf16x8*)sc = t0; *(LAS bf16x8*)(sc + 16) = t1;
    LAS const char* sr = scratch + (lane >> 3) * 144 + (lane & 7) * 16;
    o0 = *(LAS const u32x4*)sr; o1 = *(LAS const u32x4*)(sr + 8 * 144);
}

DI void role_a(const uint16_t* __restrict__ x, const uint16_t* __restrict__ wqkv, uint16_t* __restrict__ out, char* __restrict__ ws, LAS char* lds, int w, int lane, u64 t_start) {
    const int head = 2 * (blockIdx.x & 7) + ((blockIdx.x >> 3) & 1), grp = blockIdx.x >> 4;
    const unsigned lds0 = (unsigned)(uintptr_t)lds;
    const int r32 = lane & 31, hi = lane >> 5;
    const int wsh = (head & 7) == 7 ? 4 : 0;
    const unsigned wv = (unsigned)((lane >> 2) * 3 * D + (lane & 3) * 8) * 2;
    const char* wb = (const char*)(wqkv + DH * head - wsh + (16 * w) * 3 * D);
    const unsigned xv0 = (unsigned)((lane >> 3) * D + (((lane & 7) ^ (lane >> 4)) * 8)) * 2, xv1 = xv0 ^ 64;
    const char* xb = (const char*)(x + (128 * grp + 32 * w) * D);
    const int rho = ROT ? ((blockIdx.x >> 3) + ((blockIdx.x >> 1) & 3)) % 7 : 0;
#define SP(st) ((st) + rho >= 7 ? (st) + rho - 7 : (st) + rho)
#define WQO(st) ((st) < 4 ? LWQ + (st) * 4096 : ((st) < 6 ? 3 * WSL + ((st) - 4) * 4096 : 0))
#define LOADW(st) glds2(wb + SP(st) * (64 * 3 * D * 2), wv + 2 * D, wv + 4 * D - 1024, lds0 + ((st) & 3) * WSL + w * 2048)
#define LOADQ(st) glds1(wb + SP(st) * (64 * 3 * D * 2), wv, lds0 + WQO(st) + w * 1024)
#define LOADX(st) glds4(xb + SP(st) * 128, xv0, xv1 + 16 * D - 1024, xv0 + 32 * D - 2048, xv1 + 48 * D - 3072, lds0 + LXR + ((st) % 3) * XSL + w * 4096)
    LOADW(0); LOADX(0);
    STAMP(1);
    LOADW(1); LOADX(1); if (!LATE2) { LOADW(2); LOADX(2); }
    __builtin_amdgcn_s_setprio(PRIO_A1);
    LAS unsigned* sy = (LAS unsigned*)(lds + LSY);
    const __amdgpu_buffer_rsrc_t rs = __builtin_amdgcn_make_buffer_rsrc(ws, 0, WS_END, 0x00020000);
    const int xs = (r32 >> 1) & 7;
    LAS const char* xp[4];
#pragma unroll
    for (int ks = 0; ks < 4; ++ks) xp[ks] = lds + LXR + w * 4096 + r32 * 128 + (((2 * ks + hi) ^ xs) * 16);
    LAS const char* wp = lds + (8 * hi + ((lane & 15) >> 2)) * 64 + ((lane >> 4) & 1) * 32 + (lane & 3) * 8 + wsh * 2;
    f32x16 aq = {}, ak = {}, av = {};
    bf16x8 xf[7][4]; s16x4 fr[4][4];
#define RX(st, ks) xf[st][ks] = FR(xp[ks], ((st) % 3) * XSL)
#define RW(st, ks, j) fr[ks][j] = trread(wp + ((st) & 3) * WSL + (ks) * 2048 + ((j) >> 1) * 1024 + ((j) & 1) * 256)
#define MK(st, ks) ak = MFMA_BF(cat8(fr[ks][0], fr[ks][1]), xf[st][ks], ak); SBAR()
#define MV(st, ks) av = MFMA_BF(xf[st][ks], cat8(fr[ks][2], fr[ks][3]), av); SBAR()
#define QSTEP(st, ks) MK(st, ks); RW(st, ks + 1, 0); RW(st, ks + 1, 1); SBAR(); MV(st, ks); RW(st, ks + 1, 2); RW(st, ks + 1, 3); SBAR()
    u64 tag = 0;
    if (LATE2) { asm volatile("s_waitcnt vmcnt(6)" ::: "memory"); arrive(sy + SY_F); LOADW(2); LOADX(2); await(sy + SY_F, 4); }
    else { asm volatile("s_waitcnt vmcnt(12)" ::: "memory"); arrive(sy + SY_F); await(sy + SY_F, 4); }
    STAMP(16);
    RX(0, 0); RW(0, 0, 0); RW(0, 0, 1); RW(0, 0, 2); RW(0, 0, 3); RX(0, 1); RX(0, 2); RX(0, 3); SBAR();
#pragma unroll
    for (int st = 0; st < 7; ++st) {
        QSTEP(st, 0); if (st < 4) LOADW(st + 3); if (st == 4) { LOADQ(4); LOADQ(5); } if (st == 5) LOADQ(6);
        QSTEP(st, 1); if (st < 4) LOADX(st + 3);
        QSTEP(st, 2); if (st == 3) { LOADQ(0); LOADQ(1); LOADQ(2); LOADQ(3); }
        if (st < 6) {
            if (st == 3) asm volatile("s_waitcnt vmcnt(16)" ::: "memory"); else if (st == 5) asm volatile("s_waitcnt vmcnt(7)" ::: "memory"); else asm volatile("s_waitcnt vmcnt(12)" ::: "memory");
            arrive(sy + SY_F + 1 + st); SBAR();
        }
        MK(st, 3);
        if (st < 6) { await(sy + SY_F + 1 + st, 4); RX(st + 1, 0); RW(st + 1, 0, 0); RW(st + 1, 0, 1); RW(st + 1, 0, 2); RW(st + 1, 0, 3); } SBAR();
        MV(st, 3); if (st < 6) { RX(st + 1, 1); RX(st + 1, 2); RX(st + 1, 3); } SBAR();
        if (st == 2) tag = get_tag(sy);
    }
    STAMP(3);
    const float cq = 0.18898223650461362f * 1.4426950408889634f;
    const unsigned voff = lane * 16;
    u32x4 q0, q1, k0, k1, v0, v1;
    const unsigned tb = WS_KV + (unsigned)(head * 64 + 4 * grp + w) * 4096 + voff;
#pragma unroll
    for (int i = 0; i < 4; ++i) { k0[i] = pk_bf16(ak[2 * i], ak[2 * i + 1]); k1[i] = pk_bf16(ak[8 + 2 * i], ak[9 + 2 * i]); }
    if (hi) { k1[2] = 0x3f803f80u; k1[3] = 0x00003f80u; }
    BST(k0, tb); BST(k1, tb + 1024);
#pragma unroll
    for (int i = 0; i < 4; ++i) {
        v0[i] = pk_bf16(av[2 * i], av[2 * i + 1]); v1[i] = pk_bf16(av[8 + 2 * i], av[9 + 2 * i]);
        if (r32 >= DH) { v0[i] = v1[i] = (r32 == DH ? 0x3f803f80u : 0u); }
    }
    BST(v0, tb + 2048); BST(v1, tb + 3072);
    asm volatile("s_waitcnt vmcnt(4)" ::: "memory");
    arrive(sy + SY_F + 7); await(sy + SY_F + 7, 4);
    {
        s16x4 fq[4][2];
#define RQ(j) do { fq[(j) & 3][0] = trread(wp + WQO((j) >> 2) + ((j) & 3) * 1024); fq[(j) & 3][1] = trread(wp + WQO((j) >> 2) + ((j) & 3) * 1024 + 256); } while (0)
        RQ(0); RQ(1); RQ(2); SBAR();
#pragma unroll
        for (int j = 0; j < 28; ++j) {
            aq = MFMA_BF(cat8(fq[j & 3][0], fq[j & 3][1]), xf[j >> 2][j & 3], aq); SBAR(); if (j < 25) RQ(j + 3); SBAR();
            if (j == FLJ) { drain(); put_flag(ws, FL_KV + (unsigned)((head * 4 + w) * 16 + grp) * FS, tag, lane); STAMP(4); }
        }
    }
    STAMP(17);
#pragma unroll
    for (int i = 0; i < 4; ++i) { q0[i] = pk_bf16(aq[2 * i] * cq, aq[2 * i + 1] * cq); q1[i] = pk_bf16(aq[8 + 2 * i] * cq, aq[9 + 2 * i] * cq); }
    if (hi) { q1[2] = 0; q1[3] = 0; }
    GAS u64* kvf = (GAS u64*)(ws + FL_KV + (unsigned)((head * 4 + w) * 16 + ((grp + lane) & 15)) * FS);
    const f32x16 zero = {};
    {
        f32x16 s = MFMA_BF(BC8(k0), BC8(q0), zero); s = MFMA_BF(BC8(k1), BC8(q1), s);
        float m = s[0];
#pragma unroll
        for (int i = 1; i < 16; ++i) m = fmaxf(m, s[i]);
        q1 = put_ref(q1, swapmax(m), hi);
    }
    { LAS u32x4* qo = (LAS u32x4*)(lds + LQ + w * 2048) + lane; qo[0] = q0; qo[64] = q1; }
    arrive(sy + SY_Q);
    const u64 kv1 = __hip_atomic_load(kvf, __ATOMIC_RELAXED, __HIP_MEMORY_SCOPE_AGENT); SBAR();
    await(sy + SY_Q, 4);
    __builtin_amdgcn_s_setprio(0);
    bf16x8 Q[4][2];
    Q[0][0] = BC8(q0); Q[0][1] = BC8(q1);
#pragma unroll
    for (int i = 1; i < 4; ++i) { LAS const char* p = lds + LQ + ((w + i) & 3) * 2048 + voff; Q[i][0] = FR(p, 0); Q[i][1] = FR(p, 1024); }
    u32x4 kv[4][4] = {{k0, k1, v0, v1}, {}, {}, {}};
    f32x16 O[4] = {}, sa, sb;
    u32x4 p0, p1 = {};
#define GRP(SC, i, P, j) do { float e0 = EX(SC[i]); SBAR(); float e1 = EX(SC[i + 1]); SBAR(); float e2 = EX(SC[i + 2]); SBAR(); float e3 = EX(SC[i + 3]); SBAR(); \
        P[j] = pk_bf16(e0, e1); SBAR(); P[j + 1] = pk_bf16(e2, e3); PIN(P); SBAR(); } while (0)
#define SLOT(SC, SN, cb, i, QK, PV1, LD) do { constexpr int nb_ = (i) < 3 ? (cb) : ((cb) + 1) % 4, pb_ = (i) > 0 ? (cb) : ((cb) + 3) % 4; \
        if (QK) SN = MFMA_BF(BC8(kv[nb_][0]), Q[((i) + 1) & 3][0], zero); SBAR(); GRP(SC, 0, p0, 0); \
        if (PV1) O[((i) + 3) & 3] = MFMA_BF(BC8(kv[pb_][3]), BC8(p1), O[((i) + 3) & 3]); SBAR(); \
        if (LD) { kv[((cb) + 3) % 4][i] = __builtin_amdgcn_raw_buffer_load_b128(rs, voff + (i) * 1024, kvo, 16); SBAR(); } GRP(SC, 4, p0, 2); \
        if (QK) SN = MFMA_BF(BC8(kv[nb_][1]), Q[((i) + 1) & 3][1], SN); SBAR(); GRP(SC, 8, p1, 0); \
        O[i] = MFMA_BF(BC8(kv[cb][2]), BC8(p0), O[i]); SBAR(); GRP(SC, 12, p1, 2); } while (0)
    const unsigned kvb = WS_KV + (unsigned)(head * 64 + w) * 4096;
    unsigned kvo = 0;
    STAMP(5);
    bool pend = lane > 0 && lane < 16 && kv1 != tag;
#define NEED(msk) do { CFENCE(); _Pragma("unroll 1") for (int i_ = 0; i_ < SPIN; ++i_) { \
        if (pend) pend = __hip_atomic_load(kvf, __ATOMIC_RELAXED, __HIP_MEMORY_SCOPE_AGENT) != tag; \
        miss = (unsigned)__ballot(pend); if (!(miss & (msk))) break; __builtin_amdgcn_s_sleep(2); } CFENCE(); } while (0)
    unsigned miss = (unsigned)__ballot(pend);
    if (miss & KVM) NEED(KVM);
    CFENCE();
    STAMP(6);
#pragma unroll
    for (int j = 1; j < 3; ++j)
#pragma unroll
        for (int f = 0; f < 4; ++f) kv[j][f] = __builtin_amdgcn_raw_buffer_load_b128(rs, voff + f * 1024, kvb + ((grp + j) & 15) * 16384, 16);
    arrive(sy + SY_G);
    sa = MFMA_BF(BC8(k0), Q[0][0], zero); sa = MFMA_BF(BC8(k1), Q[0][1], sa);
    STAMP(7);
    int nx = grp + 3;
#define TILE(cb) do { kvo = kvb + (nx & 15) * 16384; ++nx; SBAR(); \
        SLOT(sa, sb, cb, 0, true, true, true); SLOT(sb, sa, cb, 1, true, true, true); SLOT(sa, sb, cb, 2, true, true, true); SLOT(sb, sa, cb, 3, true, true, true); } while (0)
#pragma unroll 1
    for (unsigned msk = 0x78; msk < 0x80000; msk <<= 4) { if (__builtin_expect(miss & msk, 0)) NEED(msk); TILE(0); TILE(1); TILE(2); TILE(3); }
    O[3] = MFMA_BF(BC8(kv[3][3]), BC8(p1), O[3]);
    STAMP(8);
    f32x16 o = O[0];
    {
#pragma unroll
        for (int i = 1; i < 4; ++i) {
            LAS char* ex = lds + LX + (((w + i) & 3) * 3 + i - 1) * 4096 + voff;
#pragma unroll
            for (int g = 0; g < 4; ++g) *(LAS f32x4*)(ex + g * 1024) = (f32x4){O[i][4 * g], O[i][4 * g + 1], O[i][4 * g + 2], O[i][4 * g + 3]};
        }
        arrive(sy + SY_X); await(sy + SY_X, 4);
        LAS const char* er = lds + LX + w * 3 * 4096 + voff;
#pragma unroll
        for (int i = 0; i < 3; ++i)
#pragma unroll
            for (int g = 0; g < 4; ++g) { const f32x4 r = *(LAS const f32x4*)(er + i * 4096 + g * 1024);
#pragma unroll
                for (int e = 0; e < 4; ++e) o[4 * g + e] += r[e]; }
        f32x2 chk = {0.f, 0.f};
#pragma unroll
        for (int i = 0; i < 8; ++i) chk = pfma((f32x2){o[2 * i], o[2 * i + 1]}, (f32x2){0.f, 0.f}, chk);
        if (__builtin_expect(__any(!(chk[0] + chk[1] == 0.f) || !(o[12] < 1e30f)), 0)) {
            const unsigned hb = WS_KV + (unsigned)head * 64 * 4096 + voff;
            u32x4 qr = __builtin_bit_cast(u32x4, Q[0][1]); if (hi) { qr[2] = 0; qr[3] = 0; }
            float mm = -INFINITY;
            wait_flags<4>(ws, FL_KV + (unsigned)(head * 64 + lane) * FS, tag, true);
#pragma unroll 1
            for (int t = 0; t < 64; ++t) {
                f32x16 s = MFMA_BF(BC8(BLD(hb + t * 4096)), Q[0][0], zero); s = MFMA_BF(BC8(BLD(hb + t * 4096 + 1024)), BC8(qr), s);
#pragma unroll
                for (int i = 0; i < 16; ++i) mm = fmaxf(mm, s[i]);
            }
            qr = put_ref(qr, swapmax(mm), hi);
            o = zero;
#pragma unroll 1
            for (int t = 0; t < 64; ++t) {
                f32x16 s = MFMA_BF(BC8(BLD(hb + t * 4096)), Q[0][0], zero); s = MFMA_BF(BC8(BLD(hb + t * 4096 + 1024)), BC8(qr), s);
#pragma unroll
                for (int i = 0; i < 4; ++i) { p0[i] = pk_bf16(EX(s[2 * i]), EX(s[2 * i + 1])); p1[i] = pk_bf16(EX(s[8 + 2 * i]), EX(s[9 + 2 * i])); }
                o = MFMA_BF(BC8(BLD(hb + t * 4096 + 2048)), BC8(p0), o); o = MFMA_BF(BC8(BLD(hb + t * 4096 + 3072)), BC8(p1), o);
            }
        }
    }
    {
        const auto r = __builtin_amdgcn_permlane32_swap(__float_as_uint(o[12]), __float_as_uint(o[12]), false, false);
        const float rl = __builtin_amdgcn_rcpf(__uint_as_float(r[1]));
        u32x4 o0, o1;
#pragma unroll
        for (int i = 0; i < 4; ++i) { o0[i] = pk_bf16(o[2 * i] * rl, o[2 * i + 1] * rl); o1[i] = pk_bf16(o[8 + 2 * i] * rl, o[9 + 2 * i] * rl); }
        if (hi) { o1[2] = 0; o1[3] = 0; }
        const unsigned fi = (unsigned)((4 * grp + w) * 16 + head);
        BST(o0, WS_O + fi * 2048 + voff); BST(o1, WS_O + fi * 2048 + 1024 + voff);
        STAMP(9);
        drain();
        put_flag(ws, FL_O + fi * FS, tag, lane);
    }
    STAMP(10);
    const int xcd = blockIdx.x & 7, bi = blockIdx.x >> 3;
    if (xcd < 7) {
        wait_flags<2>(ws, FL_BT + (unsigned)(xcd * 128 + w * 32 + r32) * FS, tag, !hi);
        STAMP(11);
        const unsigned bo = WS_BT + (unsigned)((64 * xcd + 16 * w + (lane >> 3)) * S + 64 * bi + (lane & 7) * 8) * 2;
        glds2c(ws, bo, bo + 8 * S * 2 - 1024, lds0 + 65536 + w * 2048);
        drain();
        arrive(sy + SY_T2); await(sy + SY_T2, 4);
        u32x4 o0, o1; transpose_tile(lds + 65536, 2048, lds + 73728 + w * 2304, nullptr, w, lane, o0, o1);
        const __amdgpu_buffer_rsrc_t ro = __builtin_amdgcn_make_buffer_rsrc(out, 0, S * 2 * D * 2, 0x00020000);
        const unsigned oo = (unsigned)((64 * bi + 16 * w + (lane >> 3)) * 2 * D + D + 64 * xcd) * 2 + (lane & 7) * 16;
        __builtin_amdgcn_raw_buffer_store_b128(o0, ro, oo, 0, 16); __builtin_amdgcn_raw_buffer_store_b128(o1, ro, oo + 8 * 2 * D * 2, 0, 16);
    }
    STAMP(12);
}

DI void role_b(const uint16_t* __restrict__ wo, const uint16_t* __restrict__ u, const uint16_t* __restrict__ lar, const uint16_t* __restrict__ aim,
               const uint16_t* __restrict__ cre, const uint16_t* __restrict__ cim, const uint16_t* __restrict__ ldt, const uint16_t* __restrict__ dskip,
               uint16_t* __restrict__ out, char* __restrict__ ws, LAS char* lds, int w, int lane, u64 t_start) {
    u64 tag;
    const unsigned lds0 = (unsigned)(uintptr_t)lds;
    LAS unsigned* sy = (LAS unsigned*)(lds + LSY);
    const __amdgpu_buffer_rsrc_t rs = __builtin_amdgcn_make_buffer_rsrc(ws, 0, WS_END, 0x00020000);
    const int b = blockIdx.x, xcd = b & 7, bi = b >> 3, r32 = lane & 31, hi = lane >> 5;
    const unsigned voff = lane * 16;
    LAS char* tab = lds + LB + w * TABW;
    const int qt = 8 * xcd + (bi >> 2), ct = 4 * (bi & 3) + w;
    bf16x8 wf[32];
#define LOADWF(i) wf[i] = BC8(__builtin_amdgcn_raw_buffer_load_b128(rs, voff, WS_WO + (unsigned)(((8 * w + (i)) & 31) * 14 + ct) * 1024, 16))
    if (xcd < 7) {
        const int cc = w >> 1, hh = w & 1, pair = 32 * xcd + bi, ch = 2 * pair + cc;
        LAS char* cb = lds + LB + cc * CBW;
        const unsigned po = (unsigned)(ch * NS + 128 * hh) / 2 + lane;
        const unsigned pl = ((const unsigned*)lar)[po], pa = ((const unsigned*)aim)[po], pr = ((const unsigned*)cre)[po], pi = ((const unsigned*)cim)[po];
        const unsigned uo = (unsigned)((64 * bi + 16 * w + (lane >> 3)) * D + 64 * xcd + (lane & 7) * 8) * 2;
        glds2(u, uo, uo + 8 * D * 2 - 1024, lds0 + LB + w * TABW + 4096);
        const u64 e0 = __hip_atomic_load((GAS u64*)ws, __ATOMIC_RELAXED, __HIP_MEMORY_SCOPE_AGENT);
        u32x2 gi[2][3];
#pragma unroll
        for (int ps = 0; ps < 2; ++ps)
#pragma unroll
            for (int i = 0; i < 3; ++i) gi[ps][i] = ((const u32x2*)g_idx.v[128 * hh + 64 * ps + lane])[i];
        const unsigned lw = ((const unsigned*)ldt)[pair], dw = ((const unsigned*)dskip)[pair];
        const float dt = rbf(expf(cc ? bf_hi(lw) : bf_lo(lw)));
        drain();
        STAMP(1);
        arrive(sy + SY_WO);
        tag = mix(e0);
        const float cm = wave_max(fmaxf(fmaxf(fabsf(bf_lo(pr)), fabsf(bf_hi(pr))), fmaxf(fabsf(bf_lo(pi)), fabsf(bf_hi(pi)))));
        unsigned ce = __float_as_uint(cm) & 0x7f800000u; ce = ce < 0x01000000u ? 0x01000000u : (ce > 0x7e800000u ? 0x7e800000u : ce);
        const float cscale = __uint_as_float(0x7f000000u - ce), oscale = 2.0f * dt * __uint_as_float(ce);
        LAS unsigned* tw = (LAS unsigned*)(tab + lane * 4);
        LAS const char* trd = tab + r32 * TROW + hi * 16;
        f32x16 dacc = {};
        f32x2 z1, z8, z256, x0;
#define ANCH(vl, va, vr, vi) do { const float a2 = (-rbf(expf(vl)) * dt) * 1.4426950408889634f, rev = ((va) * dt) * 0.15915494309189535f; \
            z1 = anchor(a2, -rev, 1.f); z8 = anchor(a2, rev, 8.f); z256 = anchor(a2, -rev, 256.f); x0 = (f32x2){(vr) * cscale, -(vi) * cscale}; \
            PIN(z1); PIN(z8); PIN(z256); SBAR(); } while (0)
#define TW(row) tw[(row) * (TROW / 4)]
#define CHAIN() do { \
            const f32x2 z1s = rot(z1), zs = rot(z256), z8s = rot(z8); \
            f32x2 y[4], X[8]; y[1] = z8; y[2] = cmul(z8, z8, z8s); X[0] = x0; X[1] = cmul(x0, z1, z1s); f32x2 a = cmul(x0, z256, zs); \
            y[3] = cmul(y[2], z8, z8s); const f32x2 z32 = cmul(y[2], y[2], rot(y[2])), z32s = rot(z32); X[2] = cmul(X[1], z1, z1s); y[0] = z32; \
            f32x2 c4 = cmul(X[2], z256, zs); \
            unsigned c[2][7], d0 = pk_f16(X[0]), d1 = pk_f16(X[1]), d2 = pk_f16(y[1]); \
            c[0][0] = pk_f16(y[0]); c[0][1] = 0x3c00u; c[0][2] = pk_f16(y[2]); c[0][3] = pk_f16(y[3]); c[0][4] = pk_f16(X[2]); c[0][5] = pk_f16(a); c[0][6] = pk_f16(c4); \
            TW(0) = d0; TW(1) = d1; TW(22) = d2; SBAR(); \
            _Pragma("unroll") for (int i = 1; i < 9; ++i) { \
                f32x2 t0, t1, t2, t3, t4, t5, t6; const int j = i - 1; unsigned* pc = c[j & 1]; unsigned* nc = c[i & 1]; \
                if (i < 8) { if (i > 1) t0 = y[0].xx * z32; t1 = y[1].xx * z32; t2 = y[2].xx * z32; t3 = y[3].xx * z32; \
                    if (i < 6) t4 = X[i + 1].xx * z1; t5 = a.xx * z256; \
                    if (i == 3) t6 = X[4].xx * z256; if (i == 4 || i == 5) t6 = c4.xx * z256; if (i == 6) t6 = X[6].xx * z256; } SBAR(); \
                if (j != 1) TW(21 + (j ? 4 * j : 4)) = pc[0]; TW(21 + (j ? 4 * j + 1 : 0)) = pc[1]; TW(23 + 4 * j) = pc[2]; TW(24 + 4 * j) = pc[3]; \
                if (j < 6) TW(j + 2) = pc[4]; TW(j == 0 ? 8 : (j == 1 ? 12 : (j == 2 ? 14 : 13 + j))) = pc[5]; \
                if (j == 0) TW(9) = pc[6]; if (j == 3) TW(10) = pc[6]; if (j == 4) TW(13) = pc[6]; if (j == 5) TW(15) = pc[6]; if (j == 6) TW(11) = pc[6]; SBAR(); \
                if (i < 8) { if (i > 1) y[0] = pfma(y[0].yy, z32s, t0); y[1] = pfma(y[1].yy, z32s, t1); y[2] = pfma(y[2].yy, z32s, t2); y[3] = pfma(y[3].yy, z32s, t3); \
                    if (i < 6) X[i + 2] = pfma(X[i + 1].yy, z1s, t4); a = pfma(a.yy, zs, t5); \
                    if (i == 3) c4 = pfma(X[4].yy, zs, t6); if (i == 4 || i == 5) c4 = pfma(c4.yy, zs, t6); if (i == 6) c4 = pfma(X[6].yy, zs, t6); SBAR(); \
                    if (i > 1) nc[0] = pk_f16(y[0]); nc[1] = pk_f16(y[1]); nc[2] = pk_f16(y[2]); nc[3] = pk_f16(y[3]); \
                    if (i < 6) nc[4] = pk_f16(X[i + 2]); nc[5] = pk_f16(a); if (i >= 3 && i <= 6) nc[6] = pk_f16(c4); \
                    asm volatile("" :: "v"(pc[0]), "v"(pc[1]), "v"(pc[2]), "v"(pc[3]), "v"(pc[4]), "v"(pc[5]), "v"(pc[6])); SBAR(); } \
            } } while (0)
        f16x8 fa[8], fb[8];
#define RDT() do { _Pragma("unroll") for (int kk = 0; kk < 8; ++kk) { fa[kk] = *(LAS const f16x8*)(trd + kk * 32); fb[kk] = *(LAS const f16x8*)(trd + 21 * TROW + kk * 32); } SBAR(); } while (0)
#define MMT() do { _Pragma("unroll") for (int kk = 0; kk < 8; ++kk) dacc = MFMA_F16(fa[kk], fb[kk], dacc); } while (0)
        ANCH(bf_lo(pl), bf_lo(pa), bf_lo(pr), bf_lo(pi));
        await(sy + SY_WO, 4);
        if (w == 0 && lane == 0) __hip_atomic_store((LAS u64*)sy, tag, __ATOMIC_RELAXED, __HIP_MEMORY_SCOPE_WORKGROUP);
        {
            u32x4 o0, o1; transpose_tile(lds + LB + 4096, TABW, tab, sy + SY_T1, w, lane, o0, o1);
            const unsigned to = WS_UT + (unsigned)((64 * xcd + 16 * w + (lane >> 3)) * S + 64 * bi) * 2 + (lane & 7) * 16;
            __builtin_amdgcn_s_setprio(3); BST(o0, to); BST(o1, to + 8 * S * 2); __builtin_amdgcn_s_setprio(PRIO_B);
        }
        await(sy + SY_T1, 4);
        STAMP(13);
        CHAIN(); RDT();
        drain(); __builtin_amdgcn_s_setprio(3); put_flag(ws, FL_UT + (unsigned)(xcd * 128 + w * 32 + bi) * FS, tag, lane); __builtin_amdgcn_s_setprio(PRIO_B);
        STAMP(14);
        ANCH(bf_hi(pl), bf_hi(pa), bf_hi(pr), bf_hi(pi));
        MMT(); SBAR();
        CHAIN(); RDT();
        const unsigned utf = hi ? FL_WO + (unsigned)(w * 32 + r32) * FS : FL_UT + (unsigned)(xcd * 128 + (bi >> 3) * 32 + r32) * FS;
        const u64 utv = __hip_atomic_load((GAS u64*)(ws + utf), __ATOMIC_RELAXED, __HIP_MEMORY_SCOPE_AGENT); SBAR();
        MMT();
        STAMP(2);
        {
            LAS float* pt = (LAS float*)tab;
            *(LAS f32x4*)(pt + 8 * r32 + 4 * hi) = (f32x4){dacc[0] * oscale, dacc[1] * oscale, dacc[2] * oscale, dacc[3] * oscale};
            *(LAS f32x2*)(pt + (hi ? 384 + 2 * r32 : 256 + 4 * r32)) = (f32x2){dacc[4] * oscale, dacc[5] * oscale};
            *(LAS f32x2*)(pt + (hi ? 448 + 2 * r32 : 258 + 4 * r32)) = (f32x2){dacc[6] * oscale, dacc[7] * oscale};
            pt[hi ? 640 + r32 : 512 + r32] = dacc[8] * oscale;
            pt[hi ? 672 + r32 : 544 + r32] = dacc[9] * oscale;
            pt[hi ? 672 + r32 : 576 + r32] = dacc[10] * oscale;
            pt[hi ? 672 + r32 : 608 + r32] = dacc[11] * oscale;
        }
        LAS unsigned* ps_ = sy + SY_P + 5 * cc;
        arrive(ps_ + 0);
        LAS char* ui = cb + UOF;
        LAS char* kt = cb + KOF;
        await(ps_ + 0, 2);
#pragma unroll
        for (int ps = 0; ps < 2; ++ps) {
            const int v = 81 * hh + 64 * ps + lane;
            if (ps == 0 || lane < 17) {
                const f32x4 p0 = *(LAS const f32x4*)(cb + v * 16), p1 = *(LAS const f32x4*)(cb + TABW + v * 16);
                u32x2 r = {pk_bf16(p0[0] + p1[0], p0[1] + p1[1]), pk_bf16(p0[2] + p1[2], p0[3] + p1[3])};
                if (v == 161) r = (u32x2){0u, 0u};
                *(LAS u32x2*)(kt + v * 8) = r;
            }
        }
        arrive(ps_ + 1);
        if (__any(utv != tag)) wait_flags<8>(ws, utf, tag, utv != tag);
        CFENCE();
        const unsigned ub = WS_UT + (unsigned)(ch * S + 1024 * hh) * 2 + voff;
        const u32x4 u0 = BLD(ub), u1 = BLD(ub + 1024); SBAR();
        await(ps_ + 1, 2);
        STAMP(3);
        __builtin_amdgcn_s_setprio(PRIO_B2);
        const u32x4 z4 = {};
        {
            unsigned W[2][6];
#pragma unroll
            for (int ps = 0; ps < 2; ++ps)
#pragma unroll
                for (int i = 0; i < 6; ++i) { const unsigned gv = gi[ps][i >> 1][i & 1];
                    W[ps][i] = *(LAS const uint16_t*)(kt + (gv & 0xffffu));
                    if (i < 5) W[ps][i] |= (unsigned)*(LAS const uint16_t*)(kt + (gv >> 16)) << 16; }
            *(LAS u32x4*)(ui + (64 * hh + lane) * 16) = z4;
            if (lane < 16) *(LAS u32x4*)(ui + (128 + 16 * hh + lane) * 16) = z4;
            if (lane < 8) *(LAS u32x4*)(cb + (2 * hh + (lane >> 2)) * CPY + (256 + (lane & 3)) * 16) = z4;
#pragma unroll
            for (int ps = 0; ps < 2; ++ps) {
                unsigned V[5];
#pragma unroll
                for (int i = 0; i < 5; ++i) V[i] = __builtin_amdgcn_alignbit(W[ps][i + 1], W[ps][i], 16);
                LAS char* cp = cb + (128 * hh + 64 * ps + lane) * 16;
                *(LAS u32x4*)(cp) = (u32x4){W[ps][0], W[ps][1], W[ps][2], W[ps][3]};
                *(LAS u32x4*)(cp + CPY) = (u32x4){V[0], V[1], V[2], V[3]};
                *(LAS u32x4*)(cp + 2 * CPY) = (u32x4){W[ps][1], W[ps][2], W[ps][3], W[ps][4]};
                *(LAS u32x4*)(cp + 3 * CPY) = (u32x4){V[1], V[2], V[3], V[4]};
            }
        }
        *(LAS u32x4*)(ui + (32 + 32 * hh + (lane >> 2)) * 80 + (lane & 3) * 16) = u0;
        *(LAS u32x4*)(ui + (48 + 32 * hh + (lane >> 2)) * 80 + (lane & 3) * 16) = u1;
        arrive(ps_ + 2); await(ps_ + 2, 2);
        STAMP(4);
        __builtin_amdgcn_s_setprio(PRIO_BC);
        f32x16 a0 = {}, a1 = {};
        {
            const int sb = 7 - (r32 & 7);
            LAS const char* pa = cb + (sb & 3) * CPY + (255 - (r32 >> 3) + hi) * 16 + (sb >> 2) * 8 - 64 * (16 * hh + 15) - 2048;
            LAS const char* pa2 = pa + 8; asm volatile("" : "+v"(pa2));
            LAS const char* pb = ui + (32 + r32 - 16 * hh - 15) * 80 + 16 * hi;
            s16x4 ta[2][2], tb[2][2]; bf16x8 b0[2], b1[2];
#define T4(q, off) (*(LAS const s16x4*)((q) + (off)))
#define CSTEP(i, j, RD) do { const int o_ = 64 * (15 - ((j) >> 1)) + 32 * ((j) & 1), u_ = 80 * (15 - ((j) >> 1)) + 32 * ((j) & 1); \
                a1 = MFMA_BF(cat8(ta[i][0], ta[i][1]), b1[i], a1); SBAR(); if (RD) { ta[1 - i][0] = T4(pa, o_ + 2048); ta[1 - i][1] = T4(pa2, o_ + 2048); } SBAR(); \
                a0 = MFMA_BF(cat8(ta[i][0], ta[i][1]), b0[i], a0); SBAR(); if (RD) { b1[1 - i] = FR(pb, u_ + 2560); b0[1 - i] = FR(pb, u_); } SBAR(); \
                a1 = MFMA_BF(cat8(tb[i][0], tb[i][1]), b0[i], a1); SBAR(); if (RD) { tb[1 - i][0] = T4(pa, o_); tb[1 - i][1] = T4(pa2, o_); } SBAR(); } while (0)
            ta[0][0] = T4(pa, 960 + 2048); ta[0][1] = T4(pa2, 960 + 2048); b1[0] = FR(pb, 1200 + 2560); b0[0] = FR(pb, 1200); tb[0][0] = T4(pa, 960); tb[0][1] = T4(pa2, 960);
#pragma unroll
            for (int j = 0; j < 32; ++j) {
                if (j == 31) arrive(ps_ + 3);
                CSTEP(j & 1, j + 1, j < 31);
                if (ct < 14) LOADWF(j);
                SBAR();
            }
        }
        STAMP(5);
        __builtin_amdgcn_s_setprio(PRIO_B3);
        {
            u32x2 uu[4];
#pragma unroll
            for (int g = 0; g < 4; ++g) uu[g] = *(LAS const u32x2*)(ui + (32 + 32 * hh + r32) * 80 + 16 * g + 8 * hi);
            LAS char* ex = cb + r32 * 144 + hi * 16;
            const f32x16 snd = hh ? a0 : a1; const f32x16 y = hh ? a1 : a0;
            await(ps_ + 3, 2);
#pragma unroll
            for (int g = 0; g < 4; ++g) *(LAS f32x4*)(ex + hh * 4608 + g * 32) = (f32x4){snd[4 * g], snd[4 * g + 1], snd[4 * g + 2], snd[4 * g + 3]};
            arrive(ps_ + 4);
            const float dsk = cc ? bf_hi(dw) : bf_lo(dw);
            unsigned sk[8];
#pragma unroll
            for (int g = 0; g < 4; ++g)
#pragma unroll
                for (int e = 0; e < 2; ++e) sk[2 * g + e] = pk_bf16(bf_lo(uu[g][e]) * dsk, bf_hi(uu[g][e]) * dsk);
            await(ps_ + 4, 2);
            LAS char* sc = cb + 9216 + hh * 2048;
#pragma unroll
            for (int g = 0; g < 4; ++g) {
                const f32x4 p = *(LAS const f32x4*)(ex + (1 - hh) * 4608 + g * 32);
                u32x2 r;
#pragma unroll
                for (int e = 0; e < 2; ++e) {
                    const unsigned yb = pk_bf16(y[4 * g + 2 * e] + p[2 * e], y[4 * g + 2 * e + 1] + p[2 * e + 1]);
                    r[e] = pk_bf16(bf_lo(yb) + bf_lo(sk[2 * g + e]), bf_hi(yb) + bf_hi(sk[2 * g + e]));
                }
                *(LAS u32x2*)(sc + 64 * r32 + 16 * g + 8 * hi) = r;
            }
            const u32x4 o0 = *(LAS const u32x4*)(sc + voff), o1 = *(LAS const u32x4*)(sc + 1024 + voff);
            const unsigned bo = WS_BT + (unsigned)(ch * S + 1024 * hh) * 2 + voff;
            BST(o0, bo); BST(o1, bo + 1024);
        }
        arrive(sy + SY_BG);
        STAMP(6);
        drain();
        put_flag(ws, FL_BT + (unsigned)(xcd * 128 + bi * 4 + w) * FS, tag, lane);
    } else {
        {
            const unsigned nb = (bi & 1) ? 12 * 2 * D : 16 * 2 * D;
            unsigned v4[4];
#pragma unroll
            for (int i = 0; i < 4; ++i) { const unsigned o = (unsigned)(w * 4096 + i * 1024) + voff; v4[i] = (o < nb ? o : 0u) + 4096 - i * 1024; }
            glds4((const char*)(wo + (DH * (bi >> 1) + 16 * (bi & 1)) * D) - 4096, v4[0], v4[1], v4[2], v4[3], lds0 + LB + w * 4096);
        }
        const u64 e0 = __hip_atomic_load((GAS u64*)ws, __ATOMIC_RELAXED, __HIP_MEMORY_SCOPE_AGENT);
        drain();
        tag = mix(e0);
        arrive(sy + SY_WO); await(sy + SY_WO, 4);
        if (w == 0 && lane == 0) __hip_atomic_store((LAS u64*)sy, tag, __ATOMIC_RELAXED, __HIP_MEMORY_SCOPE_WORKGROUP);
        LAS const char* p = lds + LB + (4 * hi + ((lane & 15) >> 2)) * (2 * D) + ((lane >> 4) & 1) * 32 + (lane & 3) * 8;
#pragma unroll
        for (int i = 0; i < 4; ++i) {
            const int ct = w + 4 * i;
            if (ct < 14) {
                u32x4 o = __builtin_bit_cast(u32x4, cat8(trread(p + ct * 64), trread(p + 8 * 2 * D + ct * 64)));
                if (hi && (bi & 1)) { o[2] = 0; o[3] = 0; }
                BST(o, WS_WO + (unsigned)(bi * 14 + ct) * 1024 + voff);
            }
        }
        drain();
        put_flag(ws, FL_WO + (unsigned)(w * 32 + bi) * FS, tag, lane);
        __builtin_amdgcn_s_setprio(0);
        arrive(sy + SY_BG);
        await_slow(sy + SY_G, 4);
        wait_flags<8>(ws, FL_WO + (unsigned)(w * 32 + r32) * FS, tag, !hi);
        if (ct < 14) {
#pragma unroll
            for (int i = 0; i < 32; ++i) { LOADWF(i); __builtin_amdgcn_s_sleep(WOSL); SBAR(); }
        }
    }
    STAMP(7);
    const __amdgpu_buffer_rsrc_t ro = __builtin_amdgcn_make_buffer_rsrc(out, 0, S * 2 * D * 2, 0x00020000);
    await(sy + SY_BG, 4);
    STAMP(8);
    await_slow(sy + SY_X, 4);
    wait_flags<OSL>(ws, FL_O + (unsigned)(qt * 16 + 4 * w + lane) * FS, tag, lane < 4);
    STAMP(9);
    u32x4 of[8];
#pragma unroll
    for (int i = 0; i < 8; ++i) of[i] = BLD(WS_O + (unsigned)(qt * 32 + 8 * w + i) * 1024 + voff);
#pragma unroll
    for (int i = 0; i < 8; ++i) *(LAS u32x4*)(lds + LB + (8 * w + i) * 1024 + voff) = of[i];
    arrive(sy + SY_B2);
    STAMP(10);
    f32x16 acc = {};
    if (ct < 14) {
#pragma unroll
        for (int i = 0; i < 8; ++i) acc = MFMA_BF(wf[i], BC8(of[i]), acc);
    }
    SBAR();
    await(sy + SY_B2, 4);
    STAMP(11);
    if (ct < 14) {
        LAS const char* po[3];
#pragma unroll
        for (int j = 0; j < 3; ++j) po[j] = lds + LB + ((w + 1 + j) & 3) * 8192 + voff;
        bf16x8 og[4];
#pragma unroll
        for (int i = 0; i < 3; ++i) og[i] = FR(po[0], i * 1024);
#pragma unroll
        for (int i = 0; i < 24; ++i) { if (i < 21) og[(i + 3) & 3] = FR(po[(i + 3) >> 3], ((i + 3) & 7) * 1024); SBAR(); acc = MFMA_BF(wf[8 + i], og[i & 3], acc); SBAR(); }
        LAS char* sc = lds + LB + 40960 + w * 2304;
#pragma unroll
        for (int g = 0; g < 4; ++g) *(LAS u32x2*)(sc + r32 * 72 + 16 * g + 8 * hi) = (u32x2){pk_bf16(acc[4 * g], acc[4 * g + 1]), pk_bf16(acc[4 * g + 2], acc[4 * g + 3])};
        LAS const char* sr = sc + (lane >> 2) * 72 + (lane & 3) * 16;
        const u32x2 a0 = *(LAS const u32x2*)sr, a1 = *(LAS const u32x2*)(sr + 8), b0 = *(LAS const u32x2*)(sr + 16 * 72), b1 = *(LAS const u32x2*)(sr + 16 * 72 + 8);
        const unsigned oo = (unsigned)((32 * qt + (lane >> 2)) * 2 * D + 32 * ct) * 2 + (lane & 3) * 16;
        __builtin_amdgcn_raw_buffer_store_b128((u32x4){a0[0], a0[1], a1[0], a1[1]}, ro, oo, 0, 16);
        __builtin_amdgcn_raw_buffer_store_b128((u32x4){b0[0], b0[1], b1[0], b1[1]}, ro, oo + 16 * 2 * D * 2, 0, 16);
    }
    if (b == 0 && w == 0) put_flag(ws, 0, tag, lane);
    STAMP(12);
}

__global__ __launch_bounds__(512, 2) void fused(const uint16_t* __restrict__ x, const uint16_t* __restrict__ wqkv, const uint16_t* __restrict__ wo,
                                               const uint16_t* __restrict__ u, const uint16_t* __restrict__ lar, const uint16_t* __restrict__ aim,
                                               const uint16_t* __restrict__ cre, const uint16_t* __restrict__ cim, const uint16_t* __restrict__ ldt,
                                               const uint16_t* __restrict__ dskip, uint16_t* __restrict__ out, char* __restrict__ ws) {
    extern __shared__ __attribute__((aligned(16))) char smem[];
    LAS char* lds = (LAS char*)smem;
    const int lane = threadIdx.x & 63, w = __builtin_amdgcn_readfirstlane(threadIdx.x >> 6);
    const u64 t_start = __builtin_amdgcn_s_memrealtime();
    if (w >= 4) __builtin_amdgcn_s_setprio(3);
    if (w == 4) ((LAS unsigned*)(lds + LSY))[lane] = 0;
    asm volatile("" :: "s"(x), "s"(wqkv), "s"(wo), "s"(u), "s"(lar), "s"(aim), "s"(cre), "s"(cim), "s"(ldt), "s"(dskip), "s"(out), "s"(ws));
    asm volatile("s_waitcnt lgkmcnt(0)\n\ts_barrier" ::: "memory");
    if (w < 4) role_a(x, wqkv, out, ws, lds, w, lane, t_start);
    else role_b(wo, u, lar, aim, cre, cim, ldt, dskip, out, ws, lds, w - 4, lane, t_start);
}
}

extern "C" void kernel_launch(void* const* d_in, const int* in_sizes, int n_in, void* d_out, int out_size, void* d_ws, size_t ws_size, hipStream_t stream) {
    using namespace k;
    (void)hipFuncSetAttribute((const void*)fused, hipFuncAttributeMaxDynamicSharedMemorySize, LDS_BYTES);
    const uint16_t* const* in = (const uint16_t* const*)d_in;
    fused<<<256, 512, LDS_BYTES, stream>>>(in[0], in[1], in[2], in[3], in[4], in[5], in[6], in[7], in[8], in[9], (uint16_t*)d_out, (char*)d_ws);
}
```

```cpp
#define R4_(x) x x x x
#define R8_(x) R4_(x) R4_(x)
#define R16_(x) R4_(R4_(x))
#define R256_(x) R16_(R16_(x))
#define HA 99
#define HB -1
#include <hip/hip_runtime.h>
#include <hip/hip_bf16.h>
#include <stdint.h>

typedef short bf16x8 __attribute__((ext_vector_type(8)));
typedef _Float16 f16x8 __attribute__((ext_vector_type(8)));
typedef _Float16 f16x2 __attribute__((ext_vector_type(2)));
typedef __bf16 bf16x2 __attribute__((ext_vector_type(2)));
typedef short s16x4 __attribute__((ext_vector_type(4)));
typedef float f32x16 __attribute__((ext_vector_type(16)));
typedef float f32x4 __attribute__((ext_vector_type(4)));
typedef float f32x2 __attribute__((ext_vector_type(2)));
typedef unsigned u32x4 __attribute__((ext_vector_type(4)));
typedef unsigned u32x2 __attribute__((ext_vector_type(2)));
typedef unsigned long long u64;
#define LAS __attribute__((address_space(3)))
#define GAS __attribute__((address_space(1)))
#define DI __device__ __forceinline__
#define MFMA_BF(a, b, c) __builtin_amdgcn_mfma_f32_32x32x16_bf16((a), (b), (c), 0, 0, 0)
#define MFMA_F16(a, b, c) __builtin_amdgcn_mfma_f32_32x32x16_f16((a), (b), (c), 0, 0, 0)
#define SBAR() __builtin_amdgcn_sched_barrier(0)
#define CFENCE() asm volatile("" ::: "memory")
#define PIN(x) asm volatile("" : "+v"(x))
#define BC8(x) __builtin_bit_cast(bf16x8, (x))
#define EX(x) __builtin_amdgcn_exp2f(x)
#define PRIO_A1 0
#define PRIO_B 0
#define PRIO_B2 1
#define ROT 1
#define KVM 6u
#define HOLDB 0
#define PRIO_B3 1
#define PRIO_BC 0
#define FLJ 13
#define OSL 2
#define LATE2 1
#define WOSL 2

namespace k {
#define STAMP(i) do { if ((i) == ((threadIdx.x >> 8) ? HB : HA) && (threadIdx.x & 255) == 0) ((LAS volatile unsigned*)(lds + LSY))[40] = (unsigned)(__builtin_amdgcn_s_memrealtime() - t_start); \
    if ((i) == 12 && (threadIdx.x >> 8)) { const u64 a_ = __builtin_amdgcn_s_memrealtime(); { float z_ = 1.f; asm volatile(R256_(R8_("v_fma_f32 %0, %0, %0, %0\n")) : "+v"(z_)); } const u64 e_ = 2800 + 4 * (__builtin_amdgcn_s_memrealtime() - a_); while (__builtin_amdgcn_s_memrealtime() - t_start < e_) __builtin_amdgcn_s_sleep(2); } } while (0)
constexpr int S = 2048, D = 448, DH = 28, NS = 256;
constexpr unsigned FS = 128;
constexpr unsigned FL_KV = FS, FL_O = FL_KV + 1024 * FS, FL_UT = FL_O + 1024 * FS, FL_BT = FL_UT + 896 * FS, FL_WO = FL_BT + 896 * FS;
constexpr unsigned WS_KV = 512u << 10;
constexpr unsigned WS_O = WS_KV + (4u << 20);
constexpr unsigned WS_WO = WS_O + (2u << 20);
constexpr unsigned WS_UT = WS_WO + (448u << 10);
constexpr unsigned WS_BT = WS_UT + D * S * 2;
constexpr unsigned WS_END = WS_BT + D * S * 2;
constexpr int WSL = 8192, LWQ = 4 * WSL, LXR = LWQ + 16384, XSL = 16384, LB = LXR + 3 * XSL;
constexpr int LQ = LXR, LX = 0;
constexpr int TROW = 272, TABW = 53 * TROW, CBW = 2 * TABW, CPY = 4160, UOF = 4 * CPY, KOF = CBW - 1312;
constexpr int LSY = LB + 2 * CBW, LDS_BYTES = LSY + 256;
static_assert(LDS_BYTES <= 163840 && (LSY & 15) == 0 && UOF + 7680 <= KOF, "lds");
enum { SY_TAG = 0, SY_F = 2, SY_P = 10, SY_WO = 20, SY_T1 = 21, SY_BG = 22, SY_B2 = 23, SY_Q = 24, SY_X = 25, SY_G = 26, SY_T2 = 27, SY_H = 28 };
constexpr int SPIN = 1 << 18;

struct IdxTab { uint16_t v[256][12]; };
constexpr IdxTab make_idx() {
    IdxTab t = {};
    for (int y = 0; y < 256; ++y) for (int e = 0; e < 12; ++e) {
        const int j = 2047 - 8 * y - e; int ix = 644;
        if (j >= 0) { const int sh = j >= 1024 ? 3 : (j >= 512 ? 2 : (j >= 256 ? 1 : 0));
            ix = sh ? 128 * sh + ((j + (1 << (sh - 1)) - 1 + ((j >> sh) & 1)) >> sh) : j; }
        t.v[y][e] = (uint16_t)(2 * ix);
    }
    return t;
}
__device__ const IdxTab g_idx __attribute__((aligned(16))) = make_idx();

DI unsigned pk_bf16(float a, float b) { f32x2 v = {a, b}; return __builtin_bit_cast(unsigned, __builtin_convertvector(v, bf16x2)); }
DI unsigned pk_f16(f32x2 v) { return __builtin_bit_cast(unsigned, __builtin_convertvector(v, f16x2)); }
DI float bf_lo(unsigned w) { return __uint_as_float(w << 16); }
DI float bf_hi(unsigned w) { return __uint_as_float(w & 0xffff0000u); }
DI float rbf(float x) { return bf_lo(pk_bf16(x, x)); }
#define GL0 "s_mov_b32 %0, m0\n\ts_mov_b32 m0, %2\n\ts_nop 0\n\t"
#define GL1(V, O, C) "global_load_lds_dwordx4 " V ", %1 offset:" #O C "\n\t"
#define GLE "s_mov_b32 m0, %0"
DI void glds4(const void* sb, unsigned v0, unsigned v1, unsigned v2, unsigned v3, unsigned lb) { unsigned sv;
    asm volatile(GL0 GL1("%3", 0, "") GL1("%4", 1024, "") GL1("%5", 2048, "") GL1("%6", 3072, "") GLE : "=&s"(sv) : "s"(sb), "s"(lb), "v"(v0), "v"(v1), "v"(v2), "v"(v3) : "memory"); }
DI void glds3(const void* sb, unsigned v0, unsigned v1, unsigned v2, unsigned lb) { unsigned sv;
    asm volatile(GL0 GL1("%3", 0, "") GL1("%4", 1024, "") GL1("%5", 2048, "") GLE : "=&s"(sv) : "s"(sb), "s"(lb), "v"(v0), "v"(v1), "v"(v2) : "memory"); }
DI void glds2(const void* sb, unsigned v0, unsigned v1, unsigned lb) { unsigned sv;
    asm volatile(GL0 GL1("%3", 0, "") GL1("%4", 1024, "") GLE : "=&s"(sv) : "s"(sb), "s"(lb), "v"(v0), "v"(v1) : "memory"); }
DI void glds1(const void* sb, unsigned v0, unsigned lb) { unsigned sv;
    asm volatile(GL0 GL1("%3", 0, "") GLE : "=&s"(sv) : "s"(sb), "s"(lb), "v"(v0) : "memory"); }
DI void glds2c(const void* sb, unsigned v0, unsigned v1, unsigned lb) { unsigned sv;
    asm volatile(GL0 GL1("%3", 0, " sc1") GL1("%4", 1024, " sc1") GLE : "=&s"(sv) : "s"(sb), "s"(lb), "v"(v0), "v"(v1) : "memory"); }
DI float wave_max(float v) {
#define DPPMAX(ctrl, rm) v = fmaxf(v, __uint_as_float(__builtin_amdgcn_update_dpp(0, __float_as_uint(v), ctrl, rm, 0xf, true)))
    DPPMAX(0x111, 0xf); DPPMAX(0x112, 0xf); DPPMAX(0x114, 0xf); DPPMAX(0x118, 0xf); DPPMAX(0x142, 0xa); DPPMAX(0x143, 0xc);
    return __uint_as_float(__builtin_amdgcn_readlane(__float_as_uint(v), 63));
}
DI s16x4 trread(LAS const char* p) { return __builtin_bit_cast(s16x4, __builtin_amdgcn_ds_read_tr16_b64_v4i16((LAS s16x4*)p)); }
DI bf16x8 cat8(s16x4 a, s16x4 b) { return __builtin_shufflevector(a, b, 0, 1, 2, 3, 4, 5, 6, 7); }
DI f32x2 pfma(f32x2 a, f32x2 b, f32x2 c) { return __builtin_elementwise_fma(a, b, c); }
DI f32x2 cmul(f32x2 a, f32x2 b, f32x2 bs) { return pfma(a.yy, bs, a.xx * b); }
DI f32x2 rot(f32x2 b) { return (f32x2){-b.y, b.x}; }
DI f32x2 anchor(float a2, float rev, float s) {
    const float mag = EX(a2 * s);
    float r = rev * s; r -= __builtin_rintf(r);
    return (f32x2){mag * __builtin_amdgcn_cosf(r), mag * __builtin_amdgcn_sinf(r)};
}
DI void arrive(LAS unsigned* c) { asm volatile("s_mov_b64 exec, 1\n\tds_add_u32 %0, %1\n\ts_mov_b64 exec, -1" :: "v"((unsigned)(uintptr_t)c), "v"(1u) : "memory"); }
DI void await(LAS unsigned* c, unsigned n) {
    unsigned t, i = SPIN;
    asm volatile("1:\n\tds_read_b32 %0, %2\n\ts_waitcnt lgkmcnt(0)\n\tv_cmp_gt_u32 vcc, %3, %0\n\ts_cbranch_vccz 2f\n\ts_sleep 1\n\ts_sub_u32 %1, %1, 1\n\ts_cbranch_scc0 1b\n2:"
                 : "=&v"(t), "+s"(i) : "v"((unsigned)(uintptr_t)c), "s"(n) : "vcc", "scc", "memory");
}
DI void await_slow(LAS unsigned* c, unsigned n) {
    unsigned t, i = SPIN;
    asm volatile("1:\n\tds_read_b32 %0, %2\n\ts_waitcnt lgkmcnt(0)\n\tv_cmp_gt_u32 vcc, %3, %0\n\ts_cbranch_vccz 2f\n\ts_sleep 3\n\ts_sub_u32 %1, %1, 1\n\ts_cbranch_scc0 1b\n2:"
                 : "=&v"(t), "+s"(i) : "v"((unsigned)(uintptr_t)c), "s"(n) : "vcc", "scc", "memory");
}
DI u64 get_tag(LAS unsigned* sy) {
    await(sy, 1);
    const u64 v = __hip_atomic_load((LAS u64*)sy, __ATOMIC_RELAXED, __HIP_MEMORY_SCOPE_WORKGROUP);
    return ((u64)(unsigned)__builtin_amdgcn_readfirstlane((unsigned)(v >> 32)) << 32) | (unsigned)__builtin_amdgcn_readfirstlane((unsigned)v);
}
DI void drain() { asm volatile("s_waitcnt vmcnt(0)" ::: "memory"); }
DI void put_flag(char* ws, unsigned off, u64 tag, int lane) { if (lane == 0) __hip_atomic_store((GAS u64*)(ws + off), tag, __ATOMIC_RELAXED, __HIP_MEMORY_SCOPE_AGENT); }
template <int SLP> DI void wait_flags(char* ws, unsigned off, u64 tag, bool pend) {
    GAS u64* p = (GAS u64*)(ws + off);
    CFENCE();
#pragma unroll 1
    for (int i = 0; i < SPIN; ++i) {
        if (pend) pend = __hip_atomic_load(p, __ATOMIC_RELAXED, __HIP_MEMORY_SCOPE_AGENT) != tag;
        if (!__any(pend)) break;
        __builtin_amdgcn_s_sleep(SLP);
    }
    CFENCE();
}
#define BST(v, off) __builtin_amdgcn_raw_buffer_store_b128((v), rs, (off), 0, 16)
#define BLD(off) __builtin_amdgcn_raw_buffer_load_b128(rs, (off), 0, 16)
#define FR(p, off) (*(LAS const bf16x8*)((p) + (off)))
DI float swapmax(float m) { const auto r = __builtin_amdgcn_permlane32_swap(__float_as_uint(m), __float_as_uint(m), false, false); return fmaxf(__uint_as_float(r[0]), __uint_as_float(r[1])); }
DI u32x4 put_ref(u32x4 q1, float m, int hi) {
    const float m1 = rbf(m), m2 = rbf(m - m1), m3 = m - m1 - m2;
    if (hi) { q1[2] = pk_bf16(-m1, -m2); q1[3] = pk_bf16(-m3, 0.f); }
    return q1;
}
DI u64 mix(u64 v) {
    const u64 e = ((u64)(unsigned)__builtin_amdgcn_readfirstlane((unsigned)(v >> 32)) << 32) | (unsigned)__builtin_amdgcn_readfirstlane((unsigned)v);
    u64 z = e + 0x9E3779B97F4A7C15ull; z = (z ^ (z >> 30)) * 0xBF58476D1CE4E5B9ull; z = (z ^ (z >> 27)) * 0x94D049BB133111EBull; return (z ^ (z >> 31)) | 1ull; }
DI void transpose_tile(LAS const char* stage, int pst, LAS char* scratch, LAS unsigned* done, int w, int lane, u32x4& o0, u32x4& o1) {
    LAS const char* p = stage + (lane >> 4) * pst + ((lane & 15) >> 2) * 128 + w * 32 + (lane & 3) * 8;
    const bf16x8 t0 = cat8(trread(p), trread(p + 512)), t1 = cat8(trread(p + 1024), trread(p + 1536));
    if (done) arrive(done);
    LAS char* sc = scratch + (lane & 15) * 144 + (lane >> 4) * 32;
    *(LAS bf16x8*)sc = t0; *(LAS bf16x8*)(sc + 16) = t1;
    LAS const char* sr = scratch + (lane >> 3) * 144 + (lane & 7) * 16;
    o0 = *(LAS const u32x4*)sr; o1 = *(LAS const u32x4*)(sr + 8 * 144);
}

DI void role_a(const uint16_t* __restrict__ x, const uint16_t* __restrict__ wqkv, uint16_t* __restrict__ out, char* __restrict__ ws, LAS char* lds, int w, int lane, u64 t_start) {
    const int head = 2 * (blockIdx.x & 7) + ((blockIdx.x >> 3) & 1), grp = blockIdx.x >> 4;
    const unsigned lds0 = (unsigned)(uintptr_t)lds;
    const int r32 = lane & 31, hi = lane >> 5;
    const int wsh = (head & 7) == 7 ? 4 : 0;
    const unsigned wv = (unsigned)((lane >> 2) * 3 * D + (lane & 3) * 8) * 2;
    const char* wb = (const char*)(wqkv + DH * head - wsh + (16 * w) * 3 * D);
    const unsigned xv0 = (unsigned)((lane >> 3) * D + (((lane & 7) ^ (lane >> 4)) * 8)) * 2, xv1 = xv0 ^ 64;
    const char* xb = (const char*)(x + (128 * grp + 32 * w) * D);
    const int rho = ROT ? ((blockIdx.x >> 3) + ((blockIdx.x >> 1) & 3)) % 7 : 0;
#define SP(st) ((st) + rho >= 7 ? (st) + rho - 7 : (st) + rho)
#define WQO(st) ((st) < 4 ? LWQ + (st) * 4096 : ((st) < 6 ? 3 * WSL + ((st) - 4) * 4096 : 0))
#define LOADW(st) glds2(wb + SP(st) * (64 * 3 * D * 2), wv + 2 * D, wv + 4 * D - 1024, lds0 + ((st) & 3) * WSL + w * 2048)
#define LOADQ(st) glds1(wb + SP(st) * (64 * 3 * D * 2), wv, lds0 + WQO(st) + w * 1024)
#define LOADX(st) glds4(xb + SP(st) * 128, xv0, xv1 + 16 * D - 1024, xv0 + 32 * D - 2048, xv1 + 48 * D - 3072, lds0 + LXR + ((st) % 3) * XSL + w * 4096)
    LOADW(0); LOADX(0);
    STAMP(1);
    LOADW(1); LOADX(1); if (!LATE2) { LOADW(2); LOADX(2); }
    __builtin_amdgcn_s_setprio(PRIO_A1);
    LAS unsigned* sy = (LAS unsigned*)(lds + LSY);
    const __amdgpu_buffer_rsrc_t rs = __builtin_amdgcn_make_buffer_rsrc(ws, 0, WS_END, 0x00020000);
    const int xs = (r32 >> 1) & 7;
    LAS const char* xp[4];
#pragma unroll
    for (int ks = 0; ks < 4; ++ks) xp[ks] = lds + LXR + w * 4096 + r32 * 128 + (((2 * ks + hi) ^ xs) * 16);
    LAS const char* wp = lds + (8 * hi + ((lane & 15) >> 2)) * 64 + ((lane >> 4) & 1) * 32 + (lane & 3) * 8 + wsh * 2;
    f32x16 aq = {}, ak = {}, av = {};
    bf16x8 xf[7][4]; s16x4 fr[4][4];
#define RX(st, ks) xf[st][ks] = FR(xp[ks], ((st) % 3) * XSL)
#define RW(st, ks, j) fr[ks][j] = trread(wp + ((st) & 3) * WSL + (ks) * 2048 + ((j) >> 1) * 1024 + ((j) & 1) * 256)
#define MK(st, ks) ak = MFMA_BF(cat8(fr[ks][0], fr[ks][1]), xf[st][ks], ak); SBAR()
#define MV(st, ks) av = MFMA_BF(xf[st][ks], cat8(fr[ks][2], fr[ks][3]), av); SBAR()
#define QSTEP(st, ks) MK(st, ks); RW(st, ks + 1, 0); RW(st, ks + 1, 1); SBAR(); MV(st, ks); RW(st, ks + 1, 2); RW(st, ks + 1, 3); SBAR()
    u64 tag = 0;
    if (LATE2) { asm volatile("s_waitcnt vmcnt(6)" ::: "memory"); arrive(sy + SY_F); LOADW(2); LOADX(2); await(sy + SY_F, 4); }
    else { asm volatile("s_waitcnt vmcnt(12)" ::: "memory"); arrive(sy + SY_F); await(sy + SY_F, 4); }
    STAMP(16);
    RX(0, 0); RW(0, 0, 0); RW(0, 0, 1); RW(0, 0, 2); RW(0, 0, 3); RX(0, 1); RX(0, 2); RX(0, 3); SBAR();
#pragma unroll
    for (int st = 0; st < 7; ++st) {
        QSTEP(st, 0); if (st < 4) LOADW(st + 3); if (st == 4) { LOADQ(4); LOADQ(5); } if (st == 5) LOADQ(6);
        QSTEP(st, 1); if (st < 4) LOADX(st + 3);
        QSTEP(st, 2); if (st == 3) { LOADQ(0); LOADQ(1); LOADQ(2); LOADQ(3); }
        if (st < 6) {
            if (st == 3) asm volatile("s_waitcnt vmcnt(16)" ::: "memory"); else if (st == 5) asm volatile("s_waitcnt vmcnt(7)" ::: "memory"); else asm volatile("s_waitcnt vmcnt(12)" ::: "memory");
            arrive(sy + SY_F + 1 + st); SBAR();
        }
        MK(st, 3);
        if (st < 6) { await(sy + SY_F + 1 + st, 4); RX(st + 1, 0); RW(st + 1, 0, 0); RW(st + 1, 0, 1); RW(st + 1, 0, 2); RW(st + 1, 0, 3); } SBAR();
        MV(st, 3); if (st < 6) { RX(st + 1, 1); RX(st + 1, 2); RX(st + 1, 3); } SBAR();
        if (st == 2) tag = get_tag(sy);
    }
    STAMP(3);
    const float cq = 0.18898223650461362f * 1.4426950408889634f;
    const unsigned voff = lane * 16;
    u32x4 q0, q1, k0, k1, v0, v1;
    const unsigned tb = WS_KV + (unsigned)(head * 64 + 4 * grp + w) * 4096 + voff;
#pragma unroll
    for (int i = 0; i < 4; ++i) { k0[i] = pk_bf16(ak[2 * i], ak[2 * i + 1]); k1[i] = pk_bf16(ak[8 + 2 * i], ak[9 + 2 * i]); }
    if (hi) { k1[2] = 0x3f803f80u; k1[3] = 0x00003f80u; }
    BST(k0, tb); BST(k1, tb + 1024);
#pragma unroll
    for (int i = 0; i < 4; ++i) {
        v0[i] = pk_bf16(av[2 * i], av[2 * i + 1]); v1[i] = pk_bf16(av[8 + 2 * i], av[9 + 2 * i]);
        if (r32 >= DH) { v0[i] = v1[i] = (r32 == DH ? 0x3f803f80u : 0u); }
    }
    BST(v0, tb + 2048); BST(v1, tb + 3072);
    asm volatile("s_waitcnt vmcnt(4)" ::: "memory");
    arrive(sy + SY_F + 7); await(sy + SY_F + 7, 4);
    {
        s16x4 fq[4][2];
#define RQ(j) do { fq[(j) & 3][0] = trread(wp + WQO((j) >> 2) + ((j) & 3) * 1024); fq[(j) & 3][1] = trread(wp + WQO((j) >> 2) + ((j) & 3) * 1024 + 256); } while (0)
        RQ(0); RQ(1); RQ(2); SBAR();
#pragma unroll
        for (int j = 0; j < 28; ++j) {
            aq = MFMA_BF(cat8(fq[j & 3][0], fq[j & 3][1]), xf[j >> 2][j & 3], aq); SBAR(); if (j < 25) RQ(j + 3); SBAR();
            if (j == FLJ) { drain(); put_flag(ws, FL_KV + (unsigned)((head * 4 + w) * 16 + grp) * FS, tag, lane); STAMP(4); }
        }
    }
    STAMP(17);
#pragma unroll
    for (int i = 0; i < 4; ++i) { q0[i] = pk_bf16(aq[2 * i] * cq, aq[2 * i + 1] * cq); q1[i] = pk_bf16(aq[8 + 2 * i] * cq, aq[9 + 2 * i] * cq); }
    if (hi) { q1[2] = 0; q1[3] = 0; }
    GAS u64* kvf = (GAS u64*)(ws + FL_KV + (unsigned)((head * 4 + w) * 16 + ((grp + lane) & 15)) * FS);
    const f32x16 zero = {};
    {
        f32x16 s = MFMA_BF(BC8(k0), BC8(q0), zero); s = MFMA_BF(BC8(k1), BC8(q1), s);
        float m = s[0];
#pragma unroll
        for (int i = 1; i < 16; ++i) m = fmaxf(m, s[i]);
        q1 = put_ref(q1, swapmax(m), hi);
    }
    { LAS u32x4* qo = (LAS u32x4*)(lds + LQ + w * 2048) + lane; qo[0] = q0; qo[64] = q1; }
    arrive(sy + SY_Q);
    const u64 kv1 = __hip_atomic_load(kvf, __ATOMIC_RELAXED, __HIP_MEMORY_SCOPE_AGENT); SBAR();
    await(sy + SY_Q, 4);
    __builtin_amdgcn_s_setprio(0);
    bf16x8 Q[4][2];
    Q[0][0] = BC8(q0); Q[0][1] = BC8(q1);
#pragma unroll
    for (int i = 1; i < 4; ++i) { LAS const char* p = lds + LQ + ((w + i) & 3) * 2048 + voff; Q[i][0] = FR(p, 0); Q[i][1] = FR(p, 1024); }
    u32x4 kv[4][4] = {{k0, k1, v0, v1}, {}, {}, {}};
    f32x16 O[4] = {}, sa, sb;
    u32x4 p0, p1 = {};
#define GRP(SC, i, P, j) do { float e0 = EX(SC[i]); SBAR(); float e1 = EX(SC[i + 1]); SBAR(); float e2 = EX(SC[i + 2]); SBAR(); float e3 = EX(SC[i + 3]); SBAR(); \
        P[j] = pk_bf16(e0, e1); SBAR(); P[j + 1] = pk_bf16(e2, e3); PIN(P); SBAR(); } while (0)
#define SLOT(SC, SN, cb, i, QK, PV1, LD) do { constexpr int nb_ = (i) < 3 ? (cb) : ((cb) + 1) % 4, pb_ = (i) > 0 ? (cb) : ((cb) + 3) % 4; \
        if (QK) SN = MFMA_BF(BC8(kv[nb_][0]), Q[((i) + 1) & 3][0], zero); SBAR(); GRP(SC, 0, p0, 0); \
        if (PV1) O[((i) + 3) & 3] = MFMA_BF(BC8(kv[pb_][3]), BC8(p1), O[((i) + 3) & 3]); SBAR(); \
        if (LD) { kv[((cb) + 3) % 4][i] = __builtin_amdgcn_raw_buffer_load_b128(rs, voff + (i) * 1024, kvo, 16); SBAR(); } GRP(SC, 4, p0, 2); \
        if (QK) SN = MFMA_BF(BC8(kv[nb_][1]), Q[((i) + 1) & 3][1], SN); SBAR(); GRP(SC, 8, p1, 0); \
        O[i] = MFMA_BF(BC8(kv[cb][2]), BC8(p0), O[i]); SBAR(); GRP(SC, 12, p1, 2); } while (0)
    const unsigned kvb = WS_KV + (unsigned)(head * 64 + w) * 4096;
    unsigned kvo = 0;
    STAMP(5);
    bool pend = lane > 0 && lane < 16 && kv1 != tag;
#define NEED(msk) do { CFENCE(); _Pragma("unroll 1") for (int i_ = 0; i_ < SPIN; ++i_) { \
        if (pend) pend = __hip_atomic_load(kvf, __ATOMIC_RELAXED, __HIP_MEMORY_SCOPE_AGENT) != tag; \
        miss = (unsigned)__ballot(pend); if (!(miss & (msk))) break; __builtin_amdgcn_s_sleep(2); } CFENCE(); } while (0)
    unsigned miss = (unsigned)__ballot(pend);
    if (miss & KVM) NEED(KVM);
    CFENCE();
    STAMP(6);
#pragma unroll
    for (int j = 1; j < 3; ++j)
#pragma unroll
        for (int f = 0; f < 4; ++f) kv[j][f] = __builtin_amdgcn_raw_buffer_load_b128(rs, voff + f * 1024, kvb + ((grp + j) & 15) * 16384, 16);
    arrive(sy + SY_G);
    sa = MFMA_BF(BC8(k0), Q[0][0], zero); sa = MFMA_BF(BC8(k1), Q[0][1], sa);
    STAMP(7);
    int nx = grp + 3;
#define TILE(cb) do { kvo = kvb + (nx & 15) * 16384; ++nx; SBAR(); \
        SLOT(sa, sb, cb, 0, true, true, true); SLOT(sb, sa, cb, 1, true, true, true); SLOT(sa, sb, cb, 2, true, true, true); SLOT(sb, sa, cb, 3, true, true, true); } while (0)
#pragma unroll 1
    for (unsigned msk = 0x78; msk < 0x80000; msk <<= 4) { if (__builtin_expect(miss & msk, 0)) NEED(msk); TILE(0); TILE(1); TILE(2); TILE(3); }
    O[3] = MFMA_BF(BC8(kv[3][3]), BC8(p1), O[3]);
    STAMP(8);
    f32x16 o = O[0];
    {
#pragma unroll
        for (int i = 1; i < 4; ++i) {
            LAS char* ex = lds + LX + (((w + i) & 3) * 3 + i - 1) * 4096 + voff;
#pragma unroll
            for (int g = 0; g < 4; ++g) *(LAS f32x4*)(ex + g * 1024) = (f32x4){O[i][4 * g], O[i][4 * g + 1], O[i][4 * g + 2], O[i][4 * g + 3]};
        }
        arrive(sy + SY_X); await(sy + SY_X, 4);
        LAS const char* er = lds + LX + w * 3 * 4096 + voff;
#pragma unroll
        for (int i = 0; i < 3; ++i)
#pragma unroll
            for (int g = 0; g < 4; ++g) { const f32x4 r = *(LAS const f32x4*)(er + i * 4096 + g * 1024);
#pragma unroll
                for (int e = 0; e < 4; ++e) o[4 * g + e] += r[e]; }
        f32x2 chk = {0.f, 0.f};
#pragma unroll
        for (int i = 0; i < 8; ++i) chk = pfma((f32x2){o[2 * i], o[2 * i + 1]}, (f32x2){0.f, 0.f}, chk);
        if (__builtin_expect(__any(!(chk[0] + chk[1] == 0.f) || !(o[12] < 1e30f)), 0)) {
            const unsigned hb = WS_KV + (unsigned)head * 64 * 4096 + voff;
            u32x4 qr = __builtin_bit_cast(u32x4, Q[0][1]); if (hi) { qr[2] = 0; qr[3] = 0; }
            float mm = -INFINITY;
            wait_flags<4>(ws, FL_KV + (unsigned)(head * 64 + lane) * FS, tag, true);
#pragma unroll 1
            for (int t = 0; t < 64; ++t) {
                f32x16 s = MFMA_BF(BC8(BLD(hb + t * 4096)), Q[0][0], zero); s = MFMA_BF(BC8(BLD(hb + t * 4096 + 1024)), BC8(qr), s);
#pragma unroll
                for (int i = 0; i < 16; ++i) mm = fmaxf(mm, s[i]);
            }
            qr = put_ref(qr, swapmax(mm), hi);
            o = zero;
#pragma unroll 1
            for (int t = 0; t < 64; ++t) {
                f32x16 s = MFMA_BF(BC8(BLD(hb + t * 4096)), Q[0][0], zero); s = MFMA_BF(BC8(BLD(hb + t * 4096 + 1024)), BC8(qr), s);
#pragma unroll
                for (int i = 0; i < 4; ++i) { p0[i] = pk_bf16(EX(s[2 * i]), EX(s[2 * i + 1])); p1[i] = pk_bf16(EX(s[8 + 2 * i]), EX(s[9 + 2 * i])); }
                o = MFMA_BF(BC8(BLD(hb + t * 4096 + 2048)), BC8(p0), o); o = MFMA_BF(BC8(BLD(hb + t * 4096 + 3072)), BC8(p1), o);
            }
        }
    }
    {
        const auto r = __builtin_amdgcn_permlane32_swap(__float_as_uint(o[12]), __float_as_uint(o[12]), false, false);
        const float rl = __builtin_amdgcn_rcpf(__uint_as_float(r[1]));
        u32x4 o0, o1;
#pragma unroll
        for (int i = 0; i < 4; ++i) { o0[i] = pk_bf16(o[2 * i] * rl, o[2 * i + 1] * rl); o1[i] = pk_bf16(o[8 + 2 * i] * rl, o[9 + 2 * i] * rl); }
        if (hi) { o1[2] = 0; o1[3] = 0; }
        const unsigned fi = (unsigned)((4 * grp + w) * 16 + head);
        BST(o0, WS_O + fi * 2048 + voff); BST(o1, WS_O + fi * 2048 + 1024 + voff);
        STAMP(9);
        drain();
        put_flag(ws, FL_O + fi * FS, tag, lane);
    }
    STAMP(10);
    const int xcd = blockIdx.x & 7, bi = blockIdx.x >> 3;
    if (xcd < 7) {
        wait_flags<2>(ws, FL_BT + (unsigned)(xcd * 128 + w * 32 + r32) * FS, tag, !hi);
        STAMP(11);
        const unsigned bo = WS_BT + (unsigned)((64 * xcd + 16 * w + (lane >> 3)) * S + 64 * bi + (lane & 7) * 8) * 2;
        glds2c(ws, bo, bo + 8 * S * 2 - 1024, lds0 + 65536 + w * 2048);
        drain();
        arrive(sy + SY_T2); await(sy + SY_T2, 4);
        u32x4 o0, o1; transpose_tile(lds + 65536, 2048, lds + 73728 + w * 2304, nullptr, w, lane, o0, o1);
        const __amdgpu_buffer_rsrc_t ro = __builtin_amdgcn_make_buffer_rsrc(out, 0, S * 2 * D * 2, 0x00020000);
        const unsigned oo = (unsigned)((64 * bi + 16 * w + (lane >> 3)) * 2 * D + D + 64 * xcd) * 2 + (lane & 7) * 16;
        __builtin_amdgcn_raw_buffer_store_b128(o0, ro, oo, 0, 16); __builtin_amdgcn_raw_buffer_store_b128(o1, ro, oo + 8 * 2 * D * 2, 0, 16);
    }
    STAMP(12);
}

DI void role_b(const uint16_t* __restrict__ wo, const uint16_t* __restrict__ u, const uint16_t* __restrict__ lar, const uint16_t* __restrict__ aim,
               const uint16_t* __restrict__ cre, const uint16_t* __restrict__ cim, const uint16_t* __restrict__ ldt, const uint16_t* __restrict__ dskip,
               uint16_t* __restrict__ out, char* __restrict__ ws, LAS char* lds, int w, int lane, u64 t_start) {
    u64 tag;
    const unsigned lds0 = (unsigned)(uintptr_t)lds;
    LAS unsigned* sy = (LAS unsigned*)(lds + LSY);
    const __amdgpu_buffer_rsrc_t rs = __builtin_amdgcn_make_buffer_rsrc(ws, 0, WS_END, 0x00020000);
    const int b = blockIdx.x, xcd = b & 7, bi = b >> 3, r32 = lane & 31, hi = lane >> 5;
    const unsigned voff = lane * 16;
    LAS char* tab = lds + LB + w * TABW;
    const int qt = 8 * xcd + (bi >> 2), ct = 4 * (bi & 3) + w;
    bf16x8 wf[32];
#define LOADWF(i) wf[i] = BC8(__builtin_amdgcn_raw_buffer_load_b128(rs, voff, WS_WO + (unsigned)(((8 * w + (i)) & 31) * 14 + ct) * 1024, 16))
    if (xcd < 7) {
        const int cc = w >> 1, hh = w & 1, pair = 32 * xcd + bi, ch = 2 * pair + cc;
        LAS char* cb = lds + LB + cc * CBW;
        const unsigned po = (unsigned)(ch * NS + 128 * hh) / 2 + lane;
        const unsigned pl = ((const unsigned*)lar)[po], pa = ((const unsigned*)aim)[po], pr = ((const unsigned*)cre)[po], pi = ((const unsigned*)cim)[po];
        const unsigned uo = (unsigned)((64 * bi + 16 * w + (lane >> 3)) * D + 64 * xcd + (lane & 7) * 8) * 2;
        glds2(u, uo, uo + 8 * D * 2 - 1024, lds0 + LB + w * TABW + 4096);
        const u64 e0 = __hip_atomic_load((GAS u64*)ws, __ATOMIC_RELAXED, __HIP_MEMORY_SCOPE_AGENT);
        u32x2 gi[2][3];
#pragma unroll
        for (int ps = 0; ps < 2; ++ps)
#pragma unroll
            for (int i = 0; i < 3; ++i) gi[ps][i] = ((const u32x2*)g_idx.v[128 * hh + 64 * ps + lane])[i];
        const unsigned lw = ((const unsigned*)ldt)[pair], dw = ((const unsigned*)dskip)[pair];
        const float dt = rbf(expf(cc ? bf_hi(lw) : bf_lo(lw)));
        drain();
        STAMP(1);
        arrive(sy + SY_WO);
        tag = mix(e0);
        const float cm = wave_max(fmaxf(fmaxf(fabsf(bf_lo(pr)), fabsf(bf_hi(pr))), fmaxf(fabsf(bf_lo(pi)), fabsf(bf_hi(pi)))));
        unsigned ce = __float_as_uint(cm) & 0x7f800000u; ce = ce < 0x01000000u ? 0x01000000u : (ce > 0x7e800000u ? 0x7e800000u : ce);
        const float cscale = __uint_as_float(0x7f000000u - ce), oscale = 2.0f * dt * __uint_as_float(ce);
        LAS unsigned* tw = (LAS unsigned*)(tab + lane * 4);
        LAS const char* trd = tab + r32 * TROW + hi * 16;
        f32x16 dacc = {};
        f32x2 z1, z8, z256, x0;
#define ANCH(vl, va, vr, vi) do { const float a2 = (-rbf(expf(vl)) * dt) * 1.4426950408889634f, rev = ((va) * dt) * 0.15915494309189535f; \
            z1 = anchor(a2, -rev, 1.f); z8 = anchor(a2, rev, 8.f); z256 = anchor(a2, -rev, 256.f); x0 = (f32x2){(vr) * cscale, -(vi) * cscale}; \
            PIN(z1); PIN(z8); PIN(z256); SBAR(); } while (0)
#define TW(row) tw[(row) * (TROW / 4)]
#define CHAIN() do { \
            const f32x2 z1s = rot(z1), zs = rot(z256), z8s = rot(z8); \
            f32x2 y[4], X[8]; y[1] = z8; y[2] = cmul(z8, z8, z8s); X[0] = x0; X[1] = cmul(x0, z1, z1s); f32x2 a = cmul(x0, z256, zs); \
            y[3] = cmul(y[2], z8, z8s); const f32x2 z32 = cmul(y[2], y[2], rot(y[2])), z32s = rot(z32); X[2] = cmul(X[1], z1, z1s); y[0] = z32; \
            f32x2 c4 = cmul(X[2], z256, zs); \
            unsigned c[2][7], d0 = pk_f16(X[0]), d1 = pk_f16(X[1]), d2 = pk_f16(y[1]); \
            c[0][0] = pk_f16(y[0]); c[0][1] = 0x3c00u; c[0][2] = pk_f16(y[2]); c[0][3] = pk_f16(y[3]); c[0][4] = pk_f16(X[2]); c[0][5] = pk_f16(a); c[0][6] = pk_f16(c4); \
            TW(0) = d0; TW(1) = d1; TW(22) = d2; SBAR(); \
            _Pragma("unroll") for (int i = 1; i < 9; ++i) { \
                f32x2 t0, t1, t2, t3, t4, t5, t6; const int j = i - 1; unsigned* pc = c[j & 1]; unsigned* nc = c[i & 1]; \
                if (i < 8) { if (i > 1) t0 = y[0].xx * z32; t1 = y[1].xx * z32; t2 = y[2].xx * z32; t3 = y[3].xx * z32; \
                    if (i < 6) t4 = X[i + 1].xx * z1; t5 = a.xx * z256; \
                    if (i == 3) t6 = X[4].xx * z256; if (i == 4 || i == 5) t6 = c4.xx * z256; if (i == 6) t6 = X[6].xx * z256; } SBAR(); \
                if (j != 1) TW(21 + (j ? 4 * j : 4)) = pc[0]; TW(21 + (j ? 4 * j + 1 : 0)) = pc[1]; TW(23 + 4 * j) = pc[2]; TW(24 + 4 * j) = pc[3]; \
                if (j < 6) TW(j + 2) = pc[4]; TW(j == 0 ? 8 : (j == 1 ? 12 : (j == 2 ? 14 : 13 + j))) = pc[5]; \
                if (j == 0) TW(9) = pc[6]; if (j == 3) TW(10) = pc[6]; if (j == 4) TW(13) = pc[6]; if (j == 5) TW(15) = pc[6]; if (j == 6) TW(11) = pc[6]; SBAR(); \
                if (i < 8) { if (i > 1) y[0] = pfma(y[0].yy, z32s, t0); y[1] = pfma(y[1].yy, z32s, t1); y[2] = pfma(y[2].yy, z32s, t2); y[3] = pfma(y[3].yy, z32s, t3); \
                    if (i < 6) X[i + 2] = pfma(X[i + 1].yy, z1s, t4); a = pfma(a.yy, zs, t5); \
                    if (i == 3) c4 = pfma(X[4].yy, zs, t6); if (i == 4 || i == 5) c4 = pfma(c4.yy, zs, t6); if (i == 6) c4 = pfma(X[6].yy, zs, t6); SBAR(); \
                    if (i > 1) nc[0] = pk_f16(y[0]); nc[1] = pk_f16(y[1]); nc[2] = pk_f16(y[2]); nc[3] = pk_f16(y[3]); \
                    if (i < 6) nc[4] = pk_f16(X[i + 2]); nc[5] = pk_f16(a); if (i >= 3 && i <= 6) nc[6] = pk_f16(c4); \
                    asm volatile("" :: "v"(pc[0]), "v"(pc[1]), "v"(pc[2]), "v"(pc[3]), "v"(pc[4]), "v"(pc[5]), "v"(pc[6])); SBAR(); } \
            } } while (0)
        f16x8 fa[8], fb[8];
#define RDT() do { _Pragma("unroll") for (int kk = 0; kk < 8; ++kk) { fa[kk] = *(LAS const f16x8*)(trd + kk * 32); fb[kk] = *(LAS const f16x8*)(trd + 21 * TROW + kk * 32); } SBAR(); } while (0)
#define MMT() do { _Pragma("unroll") for (int kk = 0; kk < 8; ++kk) dacc = MFMA_F16(fa[kk], fb[kk], dacc); } while (0)
        ANCH(bf_lo(pl), bf_lo(pa), bf_lo(pr), bf_lo(pi));
        await(sy + SY_WO, 4);
        if (w == 0 && lane == 0) __hip_atomic_store((LAS u64*)sy, tag, __ATOMIC_RELAXED, __HIP_MEMORY_SCOPE_WORKGROUP);
        {
            u32x4 o0, o1; transpose_tile(lds + LB + 4096, TABW, tab, sy + SY_T1, w, lane, o0, o1);
            const unsigned to = WS_UT + (unsigned)((64 * xcd + 16 * w + (lane >> 3)) * S + 64 * bi) * 2 + (lane & 7) * 16;
            __builtin_amdgcn_s_setprio(3); BST(o0, to); BST(o1, to + 8 * S * 2); __builtin_amdgcn_s_setprio(PRIO_B);
        }
        await(sy + SY_T1, 4);
        STAMP(13);
        CHAIN(); RDT();
        drain(); __builtin_amdgcn_s_setprio(3); put_flag(ws, FL_UT + (unsigned)(xcd * 128 + w * 32 + bi) * FS, tag, lane); __builtin_amdgcn_s_setprio(PRIO_B);
        STAMP(14);
        ANCH(bf_hi(pl), bf_hi(pa), bf_hi(pr), bf_hi(pi));
        MMT(); SBAR();
        CHAIN(); RDT();
        const unsigned utf = hi ? FL_WO + (unsigned)(w * 32 + r32) * FS : FL_UT + (unsigned)(xcd * 128 + (bi >> 3) * 32 + r32) * FS;
        const u64 utv = __hip_atomic_load((GAS u64*)(ws + utf), __ATOMIC_RELAXED, __HIP_MEMORY_SCOPE_AGENT); SBAR();
        MMT();
        STAMP(2);
        {
            LAS float* pt = (LAS float*)tab;
            *(LAS f32x4*)(pt + 8 * r32 + 4 * hi) = (f32x4){dacc[0] * oscale, dacc[1] * oscale, dacc[2] * oscale, dacc[3] * oscale};
            *(LAS f32x2*)(pt + (hi ? 384 + 2 * r32 : 256 + 4 * r32)) = (f32x2){dacc[4] * oscale, dacc[5] * oscale};
            *(LAS f32x2*)(pt + (hi ? 448 + 2 * r32 : 258 + 4 * r32)) = (f32x2){dacc[6] * oscale, dacc[7] * oscale};
            pt[hi ? 640 + r32 : 512 + r32] = dacc[8] * oscale;
            pt[hi ? 672 + r32 : 544 + r32] = dacc[9] * oscale;
            pt[hi ? 672 + r32 : 576 + r32] = dacc[10] * oscale;
            pt[hi ? 672 + r32 : 608 + r32] = dacc[11] * oscale;
        }
        LAS unsigned* ps_ = sy + SY_P + 5 * cc;
        arrive(ps_ + 0);
        LAS char* ui = cb + UOF;
        LAS char* kt = cb + KOF;
        await(ps_ + 0, 2);
#pragma unroll
        for (int ps = 0; ps < 2; ++ps) {
            const int v = 81 * hh + 64 * ps + lane;
            if (ps == 0 || lane < 17) {
                const f32x4 p0 = *(LAS const f32x4*)(cb + v * 16), p1 = *(LAS const f32x4*)(cb + TABW + v * 16);
                u32x2 r = {pk_bf16(p0[0] + p1[0], p0[1] + p1[1]), pk_bf16(p0[2] + p1[2], p0[3] + p1[3])};
                if (v == 161) r = (u32x2){0u, 0u};
                *(LAS u32x2*)(kt + v * 8) = r;
            }
        }
        arrive(ps_ + 1);
        if (__any(utv != tag)) wait_flags<8>(ws, utf, tag, utv != tag);
        CFENCE();
        const unsigned ub = WS_UT + (unsigned)(ch * S + 1024 * hh) * 2 + voff;
        const u32x4 u0 = BLD(ub), u1 = BLD(ub + 1024); SBAR();
        await(ps_ + 1, 2);
        STAMP(3);
        __builtin_amdgcn_s_setprio(PRIO_B2);
        const u32x4 z4 = {};
        {
            unsigned W[2][6];
#pragma unroll
            for (int ps = 0; ps < 2; ++ps)
#pragma unroll
                for (int i = 0; i < 6; ++i) { const unsigned gv = gi[ps][i >> 1][i & 1];
                    W[ps][i] = *(LAS const uint16_t*)(kt + (gv & 0xffffu));
                    if (i < 5) W[ps][i] |= (unsigned)*(LAS const uint16_t*)(kt + (gv >> 16)) << 16; }
            *(LAS u32x4*)(ui + (64 * hh + lane) * 16) = z4;
            if (lane < 16) *(LAS u32x4*)(ui + (128 + 16 * hh + lane) * 16) = z4;
            if (lane < 8) *(LAS u32x4*)(cb + (2 * hh + (lane >> 2)) * CPY + (256 + (lane & 3)) * 16) = z4;
#pragma unroll
            for (int ps = 0; ps < 2; ++ps) {
                unsigned V[5];
#pragma unroll
                for (int i = 0; i < 5; ++i) V[i] = __builtin_amdgcn_alignbit(W[ps][i + 1], W[ps][i], 16);
                LAS char* cp = cb + (128 * hh + 64 * ps + lane) * 16;
                *(LAS u32x4*)(cp) = (u32x4){W[ps][0], W[ps][1], W[ps][2], W[ps][3]};
                *(LAS u32x4*)(cp + CPY) = (u32x4){V[0], V[1], V[2], V[3]};
                *(LAS u32x4*)(cp + 2 * CPY) = (u32x4){W[ps][1], W[ps][2], W[ps][3], W[ps][4]};
                *(LAS u32x4*)(cp + 3 * CPY) = (u32x4){V[1], V[2], V[3], V[4]};
            }
        }
        *(LAS u32x4*)(ui + (32 + 32 * hh + (lane >> 2)) * 80 + (lane & 3) * 16) = u0;
        *(LAS u32x4*)(ui + (48 + 32 * hh + (lane >> 2)) * 80 + (lane & 3) * 16) = u1;
        arrive(ps_ + 2); await(ps_ + 2, 2);
        STAMP(4);
        __builtin_amdgcn_s_setprio(PRIO_BC);
        f32x16 a0 = {}, a1 = {};
        {
            const int sb = 7 - (r32 & 7);
            LAS const char* pa = cb + (sb & 3) * CPY + (255 - (r32 >> 3) + hi) * 16 + (sb >> 2) * 8 - 64 * (16 * hh + 15) - 2048;
            LAS const char* pa2 = pa + 8; asm volatile("" : "+v"(pa2));
            LAS const char* pb = ui + (32 + r32 - 16 * hh - 15) * 80 + 16 * hi;
            s16x4 ta[2][2], tb[2][2]; bf16x8 b0[2], b1[2];
#define T4(q, off) (*(LAS const s16x4*)((q) + (off)))
#define CSTEP(i, j, RD) do { const int o_ = 64 * (15 - ((j) >> 1)) + 32 * ((j) & 1), u_ = 80 * (15 - ((j) >> 1)) + 32 * ((j) & 1); \
                a1 = MFMA_BF(cat8(ta[i][0], ta[i][1]), b1[i], a1); SBAR(); if (RD) { ta[1 - i][0] = T4(pa, o_ + 2048); ta[1 - i][1] = T4(pa2, o_ + 2048); } SBAR(); \
                a0 = MFMA_BF(cat8(ta[i][0], ta[i][1]), b0[i], a0); SBAR(); if (RD) { b1[1 - i] = FR(pb, u_ + 2560); b0[1 - i] = FR(pb, u_); } SBAR(); \
                a1 = MFMA_BF(cat8(tb[i][0], tb[i][1]), b0[i], a1); SBAR(); if (RD) { tb[1 - i][0] = T4(pa, o_); tb[1 - i][1] = T4(pa2, o_); } SBAR(); } while (0)
            ta[0][0] = T4(pa, 960 + 2048); ta[0][1] = T4(pa2, 960 + 2048); b1[0] = FR(pb, 1200 + 2560); b0[0] = FR(pb, 1200); tb[0][0] = T4(pa, 960); tb[0][1] = T4(pa2, 960);
#pragma unroll
            for (int j = 0; j < 32; ++j) {
                if (j == 31) arrive(ps_ + 3);
                CSTEP(j & 1, j + 1, j < 31);
                if (ct < 14) LOADWF(j);
                SBAR();
            }
        }
        STAMP(5);
        __builtin_amdgcn_s_setprio(PRIO_B3);
        {
            u32x2 uu[4];
#pragma unroll
            for (int g = 0; g < 4; ++g) uu[g] = *(LAS const u32x2*)(ui + (32 + 32 * hh + r32) * 80 + 16 * g + 8 * hi);
            LAS char* ex = cb + r32 * 144 + hi * 16;
            const f32x16 snd = hh ? a0 : a1; const f32x16 y = hh ? a1 : a0;
            await(ps_ + 3, 2);
#pragma unroll
            for (int g = 0; g < 4; ++g) *(LAS f32x4*)(ex + hh * 4608 + g * 32) = (f32x4){snd[4 * g], snd[4 * g + 1], snd[4 * g + 2], snd[4 * g + 3]};
            arrive(ps_ + 4);
            const float dsk = cc ? bf_hi(dw) : bf_lo(dw);
            unsigned sk[8];
#pragma unroll
            for (int g = 0; g < 4; ++g)
#pragma unroll
                for (int e = 0; e < 2; ++e) sk[2 * g + e] = pk_bf16(bf_lo(uu[g][e]) * dsk, bf_hi(uu[g][e]) * dsk);
            await(ps_ + 4, 2);
            LAS char* sc = cb + 9216 + hh * 2048;
#pragma unroll
            for (int g = 0; g < 4; ++g) {
                const f32x4 p = *(LAS const f32x4*)(ex + (1 - hh) * 4608 + g * 32);
                u32x2 r;
#pragma unroll
                for (int e = 0; e < 2; ++e) {
                    const unsigned yb = pk_bf16(y[4 * g + 2 * e] + p[2 * e], y[4 * g + 2 * e + 1] + p[2 * e + 1]);
                    r[e] = pk_bf16(bf_lo(yb) + bf_lo(sk[2 * g + e]), bf_hi(yb) + bf_hi(sk[2 * g + e]));
                }
                *(LAS u32x2*)(sc + 64 * r32 + 16 * g + 8 * hi) = r;
            }
            const u32x4 o0 = *(LAS const u32x4*)(sc + voff), o1 = *(LAS const u32x4*)(sc + 1024 + voff);
            const unsigned bo = WS_BT + (unsigned)(ch * S + 1024 * hh) * 2 + voff;
            BST(o0, bo); BST(o1, bo + 1024);
        }
        arrive(sy + SY_BG);
        STAMP(6);
        drain();
        put_flag(ws, FL_BT + (unsigned)(xcd * 128 + bi * 4 + w) * FS, tag, lane);
    } else {
        {
            const unsigned nb = (bi & 1) ? 12 * 2 * D : 16 * 2 * D;
            unsigned v4[4];
#pragma unroll
            for (int i = 0; i < 4; ++i) { const unsigned o = (unsigned)(w * 4096 + i * 1024) + voff; v4[i] = (o < nb ? o : 0u) + 4096 - i * 1024; }
            glds4((const char*)(wo + (DH * (bi >> 1) + 16 * (bi & 1)) * D) - 4096, v4[0], v4[1], v4[2], v4[3], lds0 + LB + w * 4096);
        }
        const u64 e0 = __hip_atomic_load((GAS u64*)ws, __ATOMIC_RELAXED, __HIP_MEMORY_SCOPE_AGENT);
        drain();
        tag = mix(e0);
        arrive(sy + SY_WO); await(sy + SY_WO, 4);
        if (w == 0 && lane == 0) __hip_atomic_store((LAS u64*)sy, tag, __ATOMIC_RELAXED, __HIP_MEMORY_SCOPE_WORKGROUP);
        LAS const char* p = lds + LB + (4 * hi + ((lane & 15) >> 2)) * (2 * D) + ((lane >> 4) & 1) * 32 + (lane & 3) * 8;
#pragma unroll
        for (int i = 0; i < 4; ++i) {
            const int ct = w + 4 * i;
            if (ct < 14) {
                u32x4 o = __builtin_bit_cast(u32x4, cat8(trread(p + ct * 64), trread(p + 8 * 2 * D + ct * 64)));
                if (hi && (bi & 1)) { o[2] = 0; o[3] = 0; }
                BST(o, WS_WO + (unsigned)(bi * 14 + ct) * 1024 + voff);
            }
        }
        drain();
        put_flag(ws, FL_WO + (unsigned)(w * 32 + bi) * FS, tag, lane);
        __builtin_amdgcn_s_setprio(0);
        arrive(sy + SY_BG);
        await_slow(sy + SY_G, 4);
        wait_flags<8>(ws, FL_WO + (unsigned)(w * 32 + r32) * FS, tag, !hi);
        if (ct < 14) {
#pragma unroll
            for (int i = 0; i < 32; ++i) { LOADWF(i); __builtin_amdgcn_s_sleep(WOSL); SBAR(); }
        }
    }
    STAMP(7);
    const __amdgpu_buffer_rsrc_t ro = __builtin_amdgcn_make_buffer_rsrc(out, 0, S * 2 * D * 2, 0x00020000);
    await(sy + SY_BG, 4);
    STAMP(8);
    await_slow(sy + SY_X, 4);
    wait_flags<OSL>(ws, FL_O + (unsigned)(qt * 16 + 4 * w + lane) * FS, tag, lane < 4);
    STAMP(9);
    u32x4 of[8];
#pragma unroll
    for (int i = 0; i < 8; ++i) of[i] = BLD(WS_O + (unsigned)(qt * 32 + 8 * w + i) * 1024 + voff);
#pragma unroll
    for (int i = 0; i < 8; ++i) *(LAS u32x4*)(lds + LB + (8 * w + i) * 1024 + voff) = of[i];
    arrive(sy + SY_B2);
    STAMP(10);
    f32x16 acc = {};
    if (ct < 14) {
#pragma unroll
        for (int i = 0; i < 8; ++i) acc = MFMA_BF(wf[i], BC8(of[i]), acc);
    }
    SBAR();
    await(sy + SY_B2, 4);
    STAMP(11);
    if (ct < 14) {
        LAS const char* po[3];
#pragma unroll
        for (int j = 0; j < 3; ++j) po[j] = lds + LB + ((w + 1 + j) & 3) * 8192 + voff;
        bf16x8 og[4];
#pragma unroll
        for (int i = 0; i < 3; ++i) og[i] = FR(po[0], i * 1024);
#pragma unroll
        for (int i = 0; i < 24; ++i) { if (i < 21) og[(i + 3) & 3] = FR(po[(i + 3) >> 3], ((i + 3) & 7) * 1024); SBAR(); acc = MFMA_BF(wf[8 + i], og[i & 3], acc); SBAR(); }
        LAS char* sc = lds + LB + 40960 + w * 2304;
#pragma unroll
        for (int g = 0; g < 4; ++g) *(LAS u32x2*)(sc + r32 * 72 + 16 * g + 8 * hi) = (u32x2){pk_bf16(acc[4 * g], acc[4 * g + 1]), pk_bf16(acc[4 * g + 2], acc[4 * g + 3])};
        LAS const char* sr = sc + (lane >> 2) * 72 + (lane & 3) * 16;
        const u32x2 a0 = *(LAS const u32x2*)sr, a1 = *(LAS const u32x2*)(sr + 8), b0 = *(LAS const u32x2*)(sr + 16 * 72), b1 = *(LAS const u32x2*)(sr + 16 * 72 + 8);
        const unsigned oo = (unsigned)((32 * qt + (lane >> 2)) * 2 * D + 32 * ct) * 2 + (lane & 3) * 16;
        __builtin_amdgcn_raw_buffer_store_b128((u32x4){a0[0], a0[1], a1[0], a1[1]}, ro, oo, 0, 16);
        __builtin_amdgcn_raw_buffer_store_b128((u32x4){b0[0], b0[1], b1[0], b1[1]}, ro, oo + 16 * 2 * D * 2, 0, 16);
    }
    if (b == 0 && w == 0) put_flag(ws, 0, tag, lane);
    STAMP(12);
}

__global__ __launch_bounds__(512, 2) void fused(const uint16_t* __restrict__ x, const uint16_t* __restrict__ wqkv, const uint16_t* __restrict__ wo,
                                               const uint16_t* __restrict__ u, const uint16_t* __restrict__ lar, const uint16_t* __restrict__ aim,
                                               const uint16_t* __restrict__ cre, const uint16_t* __restrict__ cim, const uint16_t* __restrict__ ldt,
                                               const uint16_t* __restrict__ dskip, uint16_t* __restrict__ out, char* __restrict__ ws) {
    extern __shared__ __attribute__((aligned(16))) char smem[];
    LAS char* lds = (LAS char*)smem;
    const int lane = threadIdx.x & 63, w = __builtin_amdgcn_readfirstlane(threadIdx.x >> 6);
    const u64 t_start = __builtin_amdgcn_s_memrealtime();
    { unsigned pf; const unsigned ln = ((blockIdx.x & ~7u) + (threadIdx.x >> 6)) * 4 + (threadIdx.x & 3);
      asm volatile("global_load_dword %0, %1, %2\n\ts_waitcnt vmcnt(0)" : "=&v"(pf) : "v"((ln < 780 ? ln : 779) * 128), "s"(__builtin_amdgcn_s_getpc() & ~255ull) : "memory"); }
    if (w >= 4) __builtin_amdgcn_s_setprio(3);
    if (w == 4) ((LAS unsigned*)(lds + LSY))[lane] = 0;
    asm volatile("" :: "s"(x), "s"(wqkv), "s"(wo), "s"(u), "s"(lar), "s"(aim), "s"(cre), "s"(cim), "s"(ldt), "s"(dskip), "s"(out), "s"(ws));
    asm volatile("s_waitcnt lgkmcnt(0)\n\ts_barrier" ::: "memory");
    if (w < 4) role_a(x, wqkv, out, ws, lds, w, lane, t_start);
    else role_b(wo, u, lar, aim, cre, cim, ldt, dskip, out, ws, lds, w - 4, lane, t_start);
}
}

extern "C" void kernel_launch(void* const* d_in, const int* in_sizes, int n_in, void* d_out, int out_size, void* d_ws, size_t ws_size, hipStream_t stream) {
    using namespace k;
    (void)hipFuncSetAttribute((const void*)fused, hipFuncAttributeMaxDynamicSharedMemorySize, LDS_BYTES);
    const uint16_t* const* in = (const uint16_t* const*)d_in;
    fused<<<256, 512, LDS_BYTES, stream>>>(in[0], in[1], in[2], in[3], in[4], in[5], in[6], in[7], in[8], in[9], (uint16_t*)d_out, (char*)d_ws);
}
```

```cpp
#include <hip/hip_runtime.h>
#include <hip/hip_bf16.h>
#include <stdint.h>

typedef short bf16x8 __attribute__((ext_vector_type(8)));
typedef _Float16 f16x8 __attribute__((ext_vector_type(8)));
typedef _Float16 f16x2 __attribute__((ext_vector_type(2)));
typedef __bf16 bf16x2 __attribute__((ext_vector_type(2)));
typedef short s16x4 __attribute__((ext_vector_type(4)));
typedef float f32x16 __attribute__((ext_vector_type(16)));
typedef float f32x4 __attribute__((ext_vector_type(4)));
typedef float f32x2 __attribute__((ext_vector_type(2)));
typedef unsigned u32x4 __attribute__((ext_vector_type(4)));
typedef unsigned u32x2 __attribute__((ext_vector_type(2)));
typedef unsigned long long u64;
#define LAS __attribute__((address_space(3)))
#define GAS __attribute__((address_space(1)))
#define DI __device__ __forceinline__
#define MFMA_BF(a, b, c) __builtin_amdgcn_mfma_f32_32x32x16_bf16((a), (b), (c), 0, 0, 0)
#define MFMA_F16(a, b, c) __builtin_amdgcn_mfma_f32_32x32x16_f16((a), (b), (c), 0, 0, 0)
#define SBAR() __builtin_amdgcn_sched_barrier(0)
#define CFENCE() asm volatile("" ::: "memory")
#define PIN(x) asm volatile("" : "+v"(x))
#define BC8(x) __builtin_bit_cast(bf16x8, (x))
#define EX(x) __builtin_amdgcn_exp2f(x)
#define PRIO_A1 0
#define PRIO_B 0
#define PRIO_B2 1
#define ROT 1
#define KVM 6u
#define HOLDB 0
#define PRIO_B3 1
#define PRIO_BC 0
#define FLJ 13
#define OSL 2
#define LATE2 1
#define CODEPF 1
#define WOSL 2

namespace k {
#define STAMP(i)
constexpr int S = 2048, D = 448, DH = 28, NS = 256;
constexpr unsigned FS = 128;
constexpr unsigned FL_KV = FS, FL_O = FL_KV + 1024 * FS, FL_UT = FL_O + 1024 * FS, FL_BT = FL_UT + 896 * FS, FL_WO = FL_BT + 896 * FS;
constexpr unsigned WS_KV = 512u << 10;
constexpr unsigned WS_O = WS_KV + (4u << 20);
constexpr unsigned WS_WO = WS_O + (2u << 20);
constexpr unsigned WS_UT = WS_WO + (448u << 10);
constexpr unsigned WS_BT = WS_UT + D * S * 2;
constexpr unsigned WS_END = WS_BT + D * S * 2;
constexpr int WSL = 8192, LWQ = 4 * WSL, LXR = LWQ + 16384, XSL = 16384, LB = LXR + 3 * XSL;
constexpr int LQ = LXR, LX = 0;
constexpr int TROW = 272, TABW = 53 * TROW, CBW = 2 * TABW, CPY = 4160, UOF = 4 * CPY, KOF = CBW - 1312;
constexpr int LSY = LB + 2 * CBW, LDS_BYTES = LSY + 256;
static_assert(LDS_BYTES <= 163840 && (LSY & 15) == 0 && UOF + 7680 <= KOF, "lds");
enum { SY_TAG = 0, SY_F = 2, SY_P = 10, SY_WO = 20, SY_T1 = 21, SY_BG = 22, SY_B2 = 23, SY_Q = 24, SY_X = 25, SY_G = 26, SY_T2 = 27, SY_H = 28 };
constexpr int SPIN = 1 << 18;
constexpr unsigned CODE_LINES = 272;

struct IdxTab { uint16_t v[256][12]; };
constexpr IdxTab make_idx() {
    IdxTab t = {};
    for (int y = 0; y < 256; ++y) for (int e = 0; e < 12; ++e) {
        const int j = 2047 - 8 * y - e; int ix = 644;
        if (j >= 0) { const int sh = j >= 1024 ? 3 : (j >= 512 ? 2 : (j >= 256 ? 1 : 0));
            ix = sh ? 128 * sh + ((j + (1 << (sh - 1)) - 1 + ((j >> sh) & 1)) >> sh) : j; }
        t.v[y][e] = (uint16_t)(2 * ix);
    }
    return t;
}
__device__ const IdxTab g_idx __attribute__((aligned(16))) = make_idx();

DI unsigned pk_bf16(float a, float b) { f32x2 v = {a, b}; return __builtin_bit_cast(unsigned, __builtin_convertvector(v, bf16x2)); }
DI unsigned pk_f16(f32x2 v) { return __builtin_bit_cast(unsigned, __builtin_convertvector(v, f16x2)); }
DI float bf_lo(unsigned w) { return __uint_as_float(w << 16); }
DI float bf_hi(unsigned w) { return __uint_as_float(w & 0xffff0000u); }
DI float rbf(float x) { return bf_lo(pk_bf16(x, x)); }
#define GL0 "s_mov_b32 %0, m0\n\ts_mov_b32 m0, %2\n\ts_nop 0\n\t"
#define GL1(V, O, C) "global_load_lds_dwordx4 " V ", %1 offset:" #O C "\n\t"
#define GLE "s_mov_b32 m0, %0"
DI void glds4(const void* sb, unsigned v0, unsigned v1, unsigned v2, unsigned v3, unsigned lb) { unsigned sv;
    asm volatile(GL0 GL1("%3", 0, "") GL1("%4", 1024, "") GL1("%5", 2048, "") GL1("%6", 3072, "") GLE : "=&s"(sv) : "s"(sb), "s"(lb), "v"(v0), "v"(v1), "v"(v2), "v"(v3) : "memory"); }
DI void glds3(const void* sb, unsigned v0, unsigned v1, unsigned v2, unsigned lb) { unsigned sv;
    asm volatile(GL0 GL1("%3", 0, "") GL1("%4", 1024, "") GL1("%5", 2048, "") GLE : "=&s"(sv) : "s"(sb), "s"(lb), "v"(v0), "v"(v1), "v"(v2) : "memory"); }
DI void glds2(const void* sb, unsigned v0, unsigned v1, unsigned lb) { unsigned sv;
    asm volatile(GL0 GL1("%3", 0, "") GL1("%4", 1024, "") GLE : "=&s"(sv) : "s"(sb), "s"(lb), "v"(v0), "v"(v1) : "memory"); }
DI void glds1(const void* sb, unsigned v0, unsigned lb) { unsigned sv;
    asm volatile(GL0 GL1("%3", 0, "") GLE : "=&s"(sv) : "s"(sb), "s"(lb), "v"(v0) : "memory"); }
DI void glds2c(const void* sb, unsigned v0, unsigned v1, unsigned lb) { unsigned sv;
    asm volatile(GL0 GL1("%3", 0, " sc1") GL1("%4", 1024, " sc1") GLE : "=&s"(sv) : "s"(sb), "s"(lb), "v"(v0), "v"(v1) : "memory"); }
DI float wave_max(float v) {
#define DPPMAX(ctrl, rm) v = fmaxf(v, __uint_as_float(__builtin_amdgcn_update_dpp(0, __float_as_uint(v), ctrl, rm, 0xf, true)))
    DPPMAX(0x111, 0xf); DPPMAX(0x112, 0xf); DPPMAX(0x114, 0xf); DPPMAX(0x118, 0xf); DPPMAX(0x142, 0xa); DPPMAX(0x143, 0xc);
    return __uint_as_float(__builtin_amdgcn_readlane(__float_as_uint(v), 63));
}
DI s16x4 trread(LAS const char* p) { return __builtin_bit_cast(s16x4, __builtin_amdgcn_ds_read_tr16_b64_v4i16((LAS s16x4*)p)); }
DI bf16x8 cat8(s16x4 a, s16x4 b) { return __builtin_shufflevector(a, b, 0, 1, 2, 3, 4, 5, 6, 7); }
DI f32x2 pfma(f32x2 a, f32x2 b, f32x2 c) { return __builtin_elementwise_fma(a, b, c); }
DI f32x2 cmul(f32x2 a, f32x2 b, f32x2 bs) { return pfma(a.yy, bs, a.xx * b); }
DI f32x2 rot(f32x2 b) { return (f32x2){-b.y, b.x}; }
DI f32x2 anchor(float a2, float rev, float s) {
    const float mag = EX(a2 * s);
    float r = rev * s; r -= __builtin_rintf(r);
    return (f32x2){mag * __builtin_amdgcn_cosf(r), mag * __builtin_amdgcn_sinf(r)};
}
DI void arrive(LAS unsigned* c) { asm volatile("s_mov_b64 exec, 1\n\tds_add_u32 %0, %1\n\ts_mov_b64 exec, -1" :: "v"((unsigned)(uintptr_t)c), "v"(1u) : "memory"); }
DI void await(LAS unsigned* c, unsigned n) {
    unsigned t, i = SPIN;
    asm volatile("1:\n\tds_read_b32 %0, %2\n\ts_waitcnt lgkmcnt(0)\n\tv_cmp_gt_u32 vcc, %3, %0\n\ts_cbranch_vccz 2f\n\ts_sleep 1\n\ts_sub_u32 %1, %1, 1\n\ts_cbranch_scc0 1b\n2:"
                 : "=&v"(t), "+s"(i) : "v"((unsigned)(uintptr_t)c), "s"(n) : "vcc", "scc", "memory");
}
DI void await_slow(LAS unsigned* c, unsigned n) {
    unsigned t, i = SPIN;
    asm volatile("1:\n\tds_read_b32 %0, %2\n\ts_waitcnt lgkmcnt(0)\n\tv_cmp_gt_u32 vcc, %3, %0\n\ts_cbranch_vccz 2f\n\ts_sleep 3\n\ts_sub_u32 %1, %1, 1\n\ts_cbranch_scc0 1b\n2:"
                 : "=&v"(t), "+s"(i) : "v"((unsigned)(uintptr_t)c), "s"(n) : "vcc", "scc", "memory");
}
DI u64 get_tag(LAS unsigned* sy) {
    await(sy, 1);
    const u64 v = __hip_atomic_load((LAS u64*)sy, __ATOMIC_RELAXED, __HIP_MEMORY_SCOPE_WORKGROUP);
    return ((u64)(unsigned)__builtin_amdgcn_readfirstlane((unsigned)(v >> 32)) << 32) | (unsigned)__builtin_amdgcn_readfirstlane((unsigned)v);
}
DI void drain() { asm volatile("s_waitcnt vmcnt(0)" ::: "memory"); }
DI void put_flag(char* ws, unsigned off, u64 tag, int lane) { if (lane == 0) __hip_atomic_store((GAS u64*)(ws + off), tag, __ATOMIC_RELAXED, __HIP_MEMORY_SCOPE_AGENT); }
template <int SLP> DI void wait_flags(char* ws, unsigned off, u64 tag, bool pend) {
    GAS u64* p = (GAS u64*)(ws + off);
    CFENCE();
#pragma unroll 1
    for (int i = 0; i < SPIN; ++i) {
        if (pend) pend = __hip_atomic_load(p, __ATOMIC_RELAXED, __HIP_MEMORY_SCOPE_AGENT) != tag;
        if (!__any(pend)) break;
        __builtin_amdgcn_s_sleep(SLP);
    }
    CFENCE();
}
#define BST(v, off) __builtin_amdgcn_raw_buffer_store_b128((v), rs, (off), 0, 16)
#define BLD(off) __builtin_amdgcn_raw_buffer_load_b128(rs, (off), 0, 16)
#define FR(p, off) (*(LAS const bf16x8*)((p) + (off)))
DI float swapmax(float m) { const auto r = __builtin_amdgcn_permlane32_swap(__float_as_uint(m), __float_as_uint(m), false, false); return fmaxf(__uint_as_float(r[0]), __uint_as_float(r[1])); }
DI u32x4 put_ref(u32x4 q1, float m, int hi) {
    const float m1 = rbf(m), m2 = rbf(m - m1), m3 = m - m1 - m2;
    if (hi) { q1[2] = pk_bf16(-m1, -m2); q1[3] = pk_bf16(-m3, 0.f); }
    return q1;
}
DI u64 mix(u64 v) {
    const u64 e = ((u64)(unsigned)__builtin_amdgcn_readfirstlane((unsigned)(v >> 32)) << 32) | (unsigned)__builtin_amdgcn_readfirstlane((unsigned)v);
    u64 z = e + 0x9E3779B97F4A7C15ull; z = (z ^ (z >> 30)) * 0xBF58476D1CE4E5B9ull; z = (z ^ (z >> 27)) * 0x94D049BB133111EBull; return (z ^ (z >> 31)) | 1ull; }
DI void transpose_tile(LAS const char* stage, int pst, LAS char* scratch, LAS unsigned* done, int w, int lane, u32x4& o0, u32x4& o1) {
    LAS const char* p = stage + (lane >> 4) * pst + ((lane & 15) >> 2) * 128 + w * 32 + (lane & 3) * 8;
    const bf16x8 t0 = cat8(trread(p), trread(p + 512)), t1 = cat8(trread(p + 1024), trread(p + 1536));
    if (done) arrive(done);
    LAS char* sc = scratch + (lane & 15) * 144 + (lane >> 4) * 32;
    *(LAS bf16x8*)sc = t0; *(LAS bf16x8*)(sc + 16) = t1;
    LAS const char* sr = scratch + (lane >> 3) * 144 + (lane & 7) * 16;
    o0 = *(LAS const u32x4*)sr; o1 = *(LAS const u32x4*)(sr + 8 * 144);
}

DI void role_a(const uint16_t* __restrict__ x, const uint16_t* __restrict__ wqkv, uint16_t* __restrict__ out, char* __restrict__ ws, LAS char* lds, int w, int lane) {
    const int head = 2 * (blockIdx.x & 7) + ((blockIdx.x >> 3) & 1), grp = blockIdx.x >> 4;
    const unsigned lds0 = (unsigned)(uintptr_t)lds;
    const int r32 = lane & 31, hi = lane >> 5;
    const int wsh = (head & 7) == 7 ? 4 : 0;
    const unsigned wv = (unsigned)((lane >> 2) * 3 * D + (lane & 3) * 8) * 2;
    const char* wb = (const char*)(wqkv + DH * head - wsh + (16 * w) * 3 * D);
    const unsigned xv0 = (unsigned)((lane >> 3) * D + (((lane & 7) ^ (lane >> 4)) * 8)) * 2, xv1 = xv0 ^ 64;
    const char* xb = (const char*)(x + (128 * grp + 32 * w) * D);
    const int rho = ROT ? ((blockIdx.x >> 3) + ((blockIdx.x >> 1) & 3)) % 7 : 0;
#define SP(st) ((st) + rho >= 7 ? (st) + rho - 7 : (st) + rho)
#define WQO(st) ((st) < 4 ? LWQ + (st) * 4096 : ((st) < 6 ? 3 * WSL + ((st) - 4) * 4096 : 0))
#define LOADW(st) glds2(wb + SP(st) * (64 * 3 * D * 2), wv + 2 * D, wv + 4 * D - 1024, lds0 + ((st) & 3) * WSL + w * 2048)
#define LOADQ(st) glds1(wb + SP(st) * (64 * 3 * D * 2), wv, lds0 + WQO(st) + w * 1024)
#define LOADX(st) glds4(xb + SP(st) * 128, xv0, xv1 + 16 * D - 1024, xv0 + 32 * D - 2048, xv1 + 48 * D - 3072, lds0 + LXR + ((st) % 3) * XSL + w * 4096)
    LOADW(0); LOADX(0);
    STAMP(1);
    LOADW(1); LOADX(1); if (!LATE2) { LOADW(2); LOADX(2); }
    __builtin_amdgcn_s_setprio(PRIO_A1);
    LAS unsigned* sy = (LAS unsigned*)(lds + LSY);
    const __amdgpu_buffer_rsrc_t rs = __builtin_amdgcn_make_buffer_rsrc(ws, 0, WS_END, 0x00020000);
    const int xs = (r32 >> 1) & 7;
    LAS const char* xp[4];
#pragma unroll
    for (int ks = 0; ks < 4; ++ks) xp[ks] = lds + LXR + w * 4096 + r32 * 128 + (((2 * ks + hi) ^ xs) * 16);
    LAS const char* wp = lds + (8 * hi + ((lane & 15) >> 2)) * 64 + ((lane >> 4) & 1) * 32 + (lane & 3) * 8 + wsh * 2;
    f32x16 aq = {}, ak = {}, av = {};
    bf16x8 xf[7][4]; s16x4 fr[4][4];
#define RX(st, ks) xf[st][ks] = FR(xp[ks], ((st) % 3) * XSL)
#define RW(st, ks, j) fr[ks][j] = trread(wp + ((st) & 3) * WSL + (ks) * 2048 + ((j) >> 1) * 1024 + ((j) & 1) * 256)
#define MK(st, ks) ak = MFMA_BF(cat8(fr[ks][0], fr[ks][1]), xf[st][ks], ak); SBAR()
#define MV(st, ks) av = MFMA_BF(xf[st][ks], cat8(fr[ks][2], fr[ks][3]), av); SBAR()
#define QSTEP(st, ks) MK(st, ks); RW(st, ks + 1, 0); RW(st, ks + 1, 1); SBAR(); MV(st, ks); RW(st, ks + 1, 2); RW(st, ks + 1, 3); SBAR()
    u64 tag = 0;
    if (LATE2) { asm volatile("s_waitcnt vmcnt(6)" ::: "memory"); arrive(sy + SY_F); LOADW(2); LOADX(2); await(sy + SY_F, 4); }
    else { asm volatile("s_waitcnt vmcnt(12)" ::: "memory"); arrive(sy + SY_F); await(sy + SY_F, 4); }
    STAMP(16);
    RX(0, 0); RW(0, 0, 0); RW(0, 0, 1); RW(0, 0, 2); RW(0, 0, 3); RX(0, 1); RX(0, 2); RX(0, 3); SBAR();
#pragma unroll
    for (int st = 0; st < 7; ++st) {
        QSTEP(st, 0); if (st < 4) LOADW(st + 3); if (st == 4) { LOADQ(4); LOADQ(5); } if (st == 5) LOADQ(6);
        QSTEP(st, 1); if (st < 4) LOADX(st + 3);
        QSTEP(st, 2); if (st == 3) { LOADQ(0); LOADQ(1); LOADQ(2); LOADQ(3); }
        if (st < 6) {
            if (st == 3) asm volatile("s_waitcnt vmcnt(16)" ::: "memory"); else if (st == 5) asm volatile("s_waitcnt vmcnt(7)" ::: "memory"); else asm volatile("s_waitcnt vmcnt(12)" ::: "memory");
            arrive(sy + SY_F + 1 + st); SBAR();
        }
        MK(st, 3);
        if (st < 6) { await(sy + SY_F + 1 + st, 4); RX(st + 1, 0); RW(st + 1, 0, 0); RW(st + 1, 0, 1); RW(st + 1, 0, 2); RW(st + 1, 0, 3); } SBAR();
        MV(st, 3); if (st < 6) { RX(st + 1, 1); RX(st + 1, 2); RX(st + 1, 3); } SBAR();
        if (st == 2) tag = get_tag(sy);
    }
    STAMP(3);
    const float cq = 0.18898223650461362f * 1.4426950408889634f;
    const unsigned voff = lane * 16;
    u32x4 q0, q1, k0, k1, v0, v1;
    const unsigned tb = WS_KV + (unsigned)(head * 64 + 4 * grp + w) * 4096 + voff;
#pragma unroll
    for (int i = 0; i < 4; ++i) { k0[i] = pk_bf16(ak[2 * i], ak[2 * i + 1]); k1[i] = pk_bf16(ak[8 + 2 * i], ak[9 + 2 * i]); }
    if (hi) { k1[2] = 0x3f803f80u; k1[3] = 0x00003f80u; }
    BST(k0, tb); BST(k1, tb + 1024);
#pragma unroll
    for (int i = 0; i < 4; ++i) {
        v0[i] = pk_bf16(av[2 * i], av[2 * i + 1]); v1[i] = pk_bf16(av[8 + 2 * i], av[9 + 2 * i]);
        if (r32 >= DH) { v0[i] = v1[i] = (r32 == DH ? 0x3f803f80u : 0u); }
    }
    BST(v0, tb + 2048); BST(v1, tb + 3072);
    asm volatile("s_waitcnt vmcnt(4)" ::: "memory");
    arrive(sy + SY_F + 7); await(sy + SY_F + 7, 4);
    {
        s16x4 fq[4][2];
#define RQ(j) do { fq[(j) & 3][0] = trread(wp + WQO((j) >> 2) + ((j) & 3) * 1024); fq[(j) & 3][1] = trread(wp + WQO((j) >> 2) + ((j) & 3) * 1024 + 256); } while (0)
        RQ(0); RQ(1); RQ(2); SBAR();
#pragma unroll
        for (int j = 0; j < 28; ++j) {
            aq = MFMA_BF(cat8(fq[j & 3][0], fq[j & 3][1]), xf[j >> 2][j & 3], aq); SBAR(); if (j < 25) RQ(j + 3); SBAR();
            if (j == FLJ) { drain(); put_flag(ws, FL_KV + (unsigned)((head * 4 + w) * 16 + grp) * FS, tag, lane); STAMP(4); }
        }
    }
    STAMP(17);
#pragma unroll
    for (int i = 0; i < 4; ++i) { q0[i] = pk_bf16(aq[2 * i] * cq, aq[2 * i + 1] * cq); q1[i] = pk_bf16(aq[8 + 2 * i] * cq, aq[9 + 2 * i] * cq); }
    if (hi) { q1[2] = 0; q1[3] = 0; }
    GAS u64* kvf = (GAS u64*)(ws + FL_KV + (unsigned)((head * 4 + w) * 16 + ((grp + lane) & 15)) * FS);
    const f32x16 zero = {};
    {
        f32x16 s = MFMA_BF(BC8(k0), BC8(q0), zero); s = MFMA_BF(BC8(k1), BC8(q1), s);
        float m = s[0];
#pragma unroll
        for (int i = 1; i < 16; ++i) m = fmaxf(m, s[i]);
        q1 = put_ref(q1, swapmax(m), hi);
    }
    { LAS u32x4* qo = (LAS u32x4*)(lds + LQ + w * 2048) + lane; qo[0] = q0; qo[64] = q1; }
    arrive(sy + SY_Q);
    const u64 kv1 = __hip_atomic_load(kvf, __ATOMIC_RELAXED, __HIP_MEMORY_SCOPE_AGENT); SBAR();
    await(sy + SY_Q, 4);
    __builtin_amdgcn_s_setprio(0);
    bf16x8 Q[4][2];
    Q[0][0] = BC8(q0); Q[0][1] = BC8(q1);
#pragma unroll
    for (int i = 1; i < 4; ++i) { LAS const char* p = lds + LQ + ((w + i) & 3) * 2048 + voff; Q[i][0] = FR(p, 0); Q[i][1] = FR(p, 1024); }
    u32x4 kv[4][4] = {{k0, k1, v0, v1}, {}, {}, {}};
    f32x16 O[4] = {}, sa, sb;
    u32x4 p0, p1 = {};
#define GRP(SC, i, P, j) do { float e0 = EX(SC[i]); SBAR(); float e1 = EX(SC[i + 1]); SBAR(); float e2 = EX(SC[i + 2]); SBAR(); float e3 = EX(SC[i + 3]); SBAR(); \
        P[j] = pk_bf16(e0, e1); SBAR(); P[j + 1] = pk_bf16(e2, e3); PIN(P); SBAR(); } while (0)
#define SLOT(SC, SN, cb, i, QK, PV1, LD) do { constexpr int nb_ = (i) < 3 ? (cb) : ((cb) + 1) % 4, pb_ = (i) > 0 ? (cb) : ((cb) + 3) % 4; \
        if (QK) SN = MFMA_BF(BC8(kv[nb_][0]), Q[((i) + 1) & 3][0], zero); SBAR(); GRP(SC, 0, p0, 0); \
        if (PV1) O[((i) + 3) & 3] = MFMA_BF(BC8(kv[pb_][3]), BC8(p1), O[((i) + 3) & 3]); SBAR(); \
        if (LD) { kv[((cb) + 3) % 4][i] = __builtin_amdgcn_raw_buffer_load_b128(rs, voff + (i) * 1024, kvo, 16); SBAR(); } GRP(SC, 4, p0, 2); \
        if (QK) SN = MFMA_BF(BC8(kv[nb_][1]), Q[((i) + 1) & 3][1], SN); SBAR(); GRP(SC, 8, p1, 0); \
        O[i] = MFMA_BF(BC8(kv[cb][2]), BC8(p0), O[i]); SBAR(); GRP(SC, 12, p1, 2); } while (0)
    const unsigned kvb = WS_KV + (unsigned)(head * 64 + w) * 4096;
    unsigned kvo = 0;
    STAMP(5);
    bool pend = lane > 0 && lane < 16 && kv1 != tag;
#define NEED(msk) do { CFENCE(); _Pragma("unroll 1") for (int i_ = 0; i_ < SPIN; ++i_) { \
        if (pend) pend = __hip_atomic_load(kvf, __ATOMIC_RELAXED, __HIP_MEMORY_SCOPE_AGENT) != tag; \
        miss = (unsigned)__ballot(pend); if (!(miss & (msk))) break; __builtin_amdgcn_s_sleep(2); } CFENCE(); } while (0)
    unsigned miss = (unsigned)__ballot(pend);
    if (miss & KVM) NEED(KVM);
    CFENCE();
    STAMP(6);
#pragma unroll
    for (int j = 1; j < 3; ++j)
#pragma unroll
        for (int f = 0; f < 4; ++f) kv[j][f] = __builtin_amdgcn_raw_buffer_load_b128(rs, voff + f * 1024, kvb + ((grp + j) & 15) * 16384, 16);
    arrive(sy + SY_G);
    sa = MFMA_BF(BC8(k0), Q[0][0], zero); sa = MFMA_BF(BC8(k1), Q[0][1], sa);
    STAMP(7);
    int nx = grp + 3;
#define TILE(cb) do { kvo = kvb + (nx & 15) * 16384; ++nx; SBAR(); \
        SLOT(sa, sb, cb, 0, true, true, true); SLOT(sb, sa, cb, 1, true, true, true); SLOT(sa, sb, cb, 2, true, true, true); SLOT(sb, sa, cb, 3, true, true, true); } while (0)
#pragma unroll 1
    for (unsigned msk = 0x78; msk < 0x80000; msk <<= 4) { if (__builtin_expect(miss & msk, 0)) NEED(msk); TILE(0); TILE(1); TILE(2); TILE(3); }
    O[3] = MFMA_BF(BC8(kv[3][3]), BC8(p1), O[3]);
    STAMP(8);
    f32x16 o = O[0];
    {
#pragma unroll
        for (int i = 1; i < 4; ++i) {
            LAS char* ex = lds + LX + (((w + i) & 3) * 3 + i - 1) * 4096 + voff;
#pragma unroll
            for (int g = 0; g < 4; ++g) *(LAS f32x4*)(ex + g * 1024) = (f32x4){O[i][4 * g], O[i][4 * g + 1], O[i][4 * g + 2], O[i][4 * g + 3]};
        }
        arrive(sy + SY_X); await(sy + SY_X, 4);
        LAS const char* er = lds + LX + w * 3 * 4096 + voff;
#pragma unroll
        for (int i = 0; i < 3; ++i)
#pragma unroll
            for (int g = 0; g < 4; ++g) { const f32x4 r = *(LAS const f32x4*)(er + i * 4096 + g * 1024);
#pragma unroll
                for (int e = 0; e < 4; ++e) o[4 * g + e] += r[e]; }
        f32x2 chk = {0.f, 0.f};
#pragma unroll
        for (int i = 0; i < 8; ++i) chk = pfma((f32x2){o[2 * i], o[2 * i + 1]}, (f32x2){0.f, 0.f}, chk);
        if (__builtin_expect(__any(!(chk[0] + chk[1] == 0.f) || !(o[12] < 1e30f)), 0)) {
            const unsigned hb = WS_KV + (unsigned)head * 64 * 4096 + voff;
            u32x4 qr = __builtin_bit_cast(u32x4, Q[0][1]); if (hi) { qr[2] = 0; qr[3] = 0; }
            float mm = -INFINITY;
            wait_flags<4>(ws, FL_KV + (unsigned)(head * 64 + lane) * FS, tag, true);
#pragma unroll 1
            for (int t = 0; t < 64; ++t) {
                f32x16 s = MFMA_BF(BC8(BLD(hb + t * 4096)), Q[0][0], zero); s = MFMA_BF(BC8(BLD(hb + t * 4096 + 1024)), BC8(qr), s);
#pragma unroll
                for (int i = 0; i < 16; ++i) mm = fmaxf(mm, s[i]);
            }
            qr = put_ref(qr, swapmax(mm), hi);
            o = zero;
#pragma unroll 1
            for (int t = 0; t < 64; ++t) {
                f32x16 s = MFMA_BF(BC8(BLD(hb + t * 4096)), Q[0][0], zero); s = MFMA_BF(BC8(BLD(hb + t * 4096 + 1024)), BC8(qr), s);
#pragma unroll
                for (int i = 0; i < 4; ++i) { p0[i] = pk_bf16(EX(s[2 * i]), EX(s[2 * i + 1])); p1[i] = pk_bf16(EX(s[8 + 2 * i]), EX(s[9 + 2 * i])); }
                o = MFMA_BF(BC8(BLD(hb + t * 4096 + 2048)), BC8(p0), o); o = MFMA_BF(BC8(BLD(hb + t * 4096 + 3072)), BC8(p1), o);
            }
        }
    }
    {
        const auto r = __builtin_amdgcn_permlane32_swap(__float_as_uint(o[12]), __float_as_uint(o[12]), false, false);
        const float rl = __builtin_amdgcn_rcpf(__uint_as_float(r[1]));
        u32x4 o0, o1;
#pragma unroll
        for (int i = 0; i < 4; ++i) { o0[i] = pk_bf16(o[2 * i] * rl, o[2 * i + 1] * rl); o1[i] = pk_bf16(o[8 + 2 * i] * rl, o[9 + 2 * i] * rl); }
        if (hi) { o1[2] = 0; o1[3] = 0; }
        const unsigned fi = (unsigned)((4 * grp + w) * 16 + head);
        BST(o0, WS_O + fi * 2048 + voff); BST(o1, WS_O + fi * 2048 + 1024 + voff);
        STAMP(9);
        drain();
        put_flag(ws, FL_O + fi * FS, tag, lane);
    }
    STAMP(10);
    const int xcd = blockIdx.x & 7, bi = blockIdx.x >> 3;
    if (xcd < 7) {
        wait_flags<2>(ws, FL_BT + (unsigned)(xcd * 128 + w * 32 + r32) * FS, tag, !hi);
        STAMP(11);
        const unsigned bo = WS_BT + (unsigned)((64 * xcd + 16 * w + (lane >> 3)) * S + 64 * bi + (lane & 7) * 8) * 2;
        glds2c(ws, bo, bo + 8 * S * 2 - 1024, lds0 + 65536 + w * 2048);
        drain();
        arrive(sy + SY_T2); await(sy + SY_T2, 4);
        u32x4 o0, o1; transpose_tile(lds + 65536, 2048, lds + 73728 + w * 2304, nullptr, w, lane, o0, o1);
        const __amdgpu_buffer_rsrc_t ro = __builtin_amdgcn_make_buffer_rsrc(out, 0, S * 2 * D * 2, 0x00020000);
        const unsigned oo = (unsigned)((64 * bi + 16 * w + (lane >> 3)) * 2 * D + D + 64 * xcd) * 2 + (lane & 7) * 16;
        __builtin_amdgcn_raw_buffer_store_b128(o0, ro, oo, 0, 16); __builtin_amdgcn_raw_buffer_store_b128(o1, ro, oo + 8 * 2 * D * 2, 0, 16);
    }
    STAMP(12);
}

DI void role_b(const uint16_t* __restrict__ wo, const uint16_t* __restrict__ u, const uint16_t* __restrict__ lar, const uint16_t* __restrict__ aim,
               const uint16_t* __restrict__ cre, const uint16_t* __restrict__ cim, const uint16_t* __restrict__ ldt, const uint16_t* __restrict__ dskip,
               uint16_t* __restrict__ out, char* __restrict__ ws, LAS char* lds, int w, int lane, u64 code0) {
    u64 tag;
    const unsigned lds0 = (unsigned)(uintptr_t)lds;
    LAS unsigned* sy = (LAS unsigned*)(lds + LSY);
    const __amdgpu_buffer_rsrc_t rs = __builtin_amdgcn_make_buffer_rsrc(ws, 0, WS_END, 0x00020000);
    const int b = blockIdx.x, xcd = b & 7, bi = b >> 3, r32 = lane & 31, hi = lane >> 5;
    const unsigned voff = lane * 16;
    LAS char* tab = lds + LB + w * TABW;
    unsigned pf = 0;
#define CODE_PF() asm volatile("global_load_dword %0, %1, %2" : "+v"(pf) : "v"(min((unsigned)((bi * 4 + w) * 3 + (lane < 3 ? lane : 0)), CODE_LINES - 1) * 128), "s"(code0) : "memory")
    const int qt = 8 * xcd + (bi >> 2), ct = 4 * (bi & 3) + w;
    bf16x8 wf[32];
#define LOADWF(i) wf[i] = BC8(__builtin_amdgcn_raw_buffer_load_b128(rs, voff, WS_WO + (unsigned)(((8 * w + (i)) & 31) * 14 + ct) * 1024, 16))
    if (xcd < 7) {
        const int cc = w >> 1, hh = w & 1, pair = 32 * xcd + bi, ch = 2 * pair + cc;
        LAS char* cb = lds + LB + cc * CBW;
        const unsigned po = (unsigned)(ch * NS + 128 * hh) / 2 + lane;
        const unsigned pl = ((const unsigned*)lar)[po], pa = ((const unsigned*)aim)[po], pr = ((const unsigned*)cre)[po], pi = ((const unsigned*)cim)[po];
        const unsigned uo = (unsigned)((64 * bi + 16 * w + (lane >> 3)) * D + 64 * xcd + (lane & 7) * 8) * 2;
        glds2(u, uo, uo + 8 * D * 2 - 1024, lds0 + LB + w * TABW + 4096);
        const u64 e0 = __hip_atomic_load((GAS u64*)ws, __ATOMIC_RELAXED, __HIP_MEMORY_SCOPE_AGENT);
        u32x2 gi[2][3];
#pragma unroll
        for (int ps = 0; ps < 2; ++ps)
#pragma unroll
            for (int i = 0; i < 3; ++i) gi[ps][i] = ((const u32x2*)g_idx.v[128 * hh + 64 * ps + lane])[i];
        if (CODEPF) CODE_PF();
        const unsigned lw = ((const unsigned*)ldt)[pair], dw = ((const unsigned*)dskip)[pair];
        const float dt = rbf(expf(cc ? bf_hi(lw) : bf_lo(lw)));
        drain(); asm volatile("" :: "v"(pf));
        STAMP(1);
        arrive(sy + SY_WO);
        tag = mix(e0);
        const float cm = wave_max(fmaxf(fmaxf(fabsf(bf_lo(pr)), fabsf(bf_hi(pr))), fmaxf(fabsf(bf_lo(pi)), fabsf(bf_hi(pi)))));
        unsigned ce = __float_as_uint(cm) & 0x7f800000u; ce = ce < 0x01000000u ? 0x01000000u : (ce > 0x7e800000u ? 0x7e800000u : ce);
        const float cscale = __uint_as_float(0x7f000000u - ce), oscale = 2.0f * dt * __uint_as_float(ce);
        LAS unsigned* tw = (LAS unsigned*)(tab + lane * 4);
        LAS const char* trd = tab + r32 * TROW + hi * 16;
        f32x16 dacc = {};
        f32x2 z1, z8, z256, x0;
#define ANCH(vl, va, vr, vi) do { const float a2 = (-rbf(expf(vl)) * dt) * 1.4426950408889634f, rev = ((va) * dt) * 0.15915494309189535f; \
            z1 = anchor(a2, -rev, 1.f); z8 = anchor(a2, rev, 8.f); z256 = anchor(a2, -rev, 256.f); x0 = (f32x2){(vr) * cscale, -(vi) * cscale}; \
            PIN(z1); PIN(z8); PIN(z256); SBAR(); } while (0)
#define TW(row) tw[(row) * (TROW / 4)]
#define CHAIN() do { \
            const f32x2 z1s = rot(z1), zs = rot(z256), z8s = rot(z8); \
            f32x2 y[4], X[8]; y[1] = z8; y[2] = cmul(z8, z8, z8s); X[0] = x0; X[1] = cmul(x0, z1, z1s); f32x2 a = cmul(x0, z256, zs); \
            y[3] = cmul(y[2], z8, z8s); const f32x2 z32 = cmul(y[2], y[2], rot(y[2])), z32s = rot(z32); X[2] = cmul(X[1], z1, z1s); y[0] = z32; \
            f32x2 c4 = cmul(X[2], z256, zs); \
            unsigned c[2][7], d0 = pk_f16(X[0]), d1 = pk_f16(X[1]), d2 = pk_f16(y[1]); \
            c[0][0] = pk_f16(y[0]); c[0][1] = 0x3c00u; c[0][2] = pk_f16(y[2]); c[0][3] = pk_f16(y[3]); c[0][4] = pk_f16(X[2]); c[0][5] = pk_f16(a); c[0][6] = pk_f16(c4); \
            TW(0) = d0; TW(1) = d1; TW(22) = d2; SBAR(); \
            _Pragma("unroll") for (int i = 1; i < 9; ++i) { \
                f32x2 t0, t1, t2, t3, t4, t5, t6; const int j = i - 1; unsigned* pc = c[j & 1]; unsigned* nc = c[i & 1]; \
                if (i < 8) { if (i > 1) t0 = y[0].xx * z32; t1 = y[1].xx * z32; t2 = y[2].xx * z32; t3 = y[3].xx * z32; \
                    if (i < 6) t4 = X[i + 1].xx * z1; t5 = a.xx * z256; \
                    if (i == 3) t6 = X[4].xx * z256; if (i == 4 || i == 5) t6 = c4.xx * z256; if (i == 6) t6 = X[6].xx * z256; } SBAR(); \
                if (j != 1) TW(21 + (j ? 4 * j : 4)) = pc[0]; TW(21 + (j ? 4 * j + 1 : 0)) = pc[1]; TW(23 + 4 * j) = pc[2]; TW(24 + 4 * j) = pc[3]; \
                if (j < 6) TW(j + 2) = pc[4]; TW(j == 0 ? 8 : (j == 1 ? 12 : (j == 2 ? 14 : 13 + j))) = pc[5]; \
                if (j == 0) TW(9) = pc[6]; if (j == 3) TW(10) = pc[6]; if (j == 4) TW(13) = pc[6]; if (j == 5) TW(15) = pc[6]; if (j == 6) TW(11) = pc[6]; SBAR(); \
                if (i < 8) { if (i > 1) y[0] = pfma(y[0].yy, z32s, t0); y[1] = pfma(y[1].yy, z32s, t1); y[2] = pfma(y[2].yy, z32s, t2); y[3] = pfma(y[3].yy, z32s, t3); \
                    if (i < 6) X[i + 2] = pfma(X[i + 1].yy, z1s, t4); a = pfma(a.yy, zs, t5); \
                    if (i == 3) c4 = pfma(X[4].yy, zs, t6); if (i == 4 || i == 5) c4 = pfma(c4.yy, zs, t6); if (i == 6) c4 = pfma(X[6].yy, zs, t6); SBAR(); \
                    if (i > 1) nc[0] = pk_f16(y[0]); nc[1] = pk_f16(y[1]); nc[2] = pk_f16(y[2]); nc[3] = pk_f16(y[3]); \
                    if (i < 6) nc[4] = pk_f16(X[i + 2]); nc[5] = pk_f16(a); if (i >= 3 && i <= 6) nc[6] = pk_f16(c4); \
                    asm volatile("" :: "v"(pc[0]), "v"(pc[1]), "v"(pc[2]), "v"(pc[3]), "v"(pc[4]), "v"(pc[5]), "v"(pc[6])); SBAR(); } \
            } } while (0)
        f16x8 fa[8], fb[8];
#define RDT() do { _Pragma("unroll") for (int kk = 0; kk < 8; ++kk) { fa[kk] = *(LAS const f16x8*)(trd + kk * 32); fb[kk] = *(LAS const f16x8*)(trd + 21 * TROW + kk * 32); } SBAR(); } while (0)
#define MMT() do { _Pragma("unroll") for (int kk = 0; kk < 8; ++kk) dacc = MFMA_F16(fa[kk], fb[kk], dacc); } while (0)
        ANCH(bf_lo(pl), bf_lo(pa), bf_lo(pr), bf_lo(pi));
        await(sy + SY_WO, 4);
        if (w == 0 && lane == 0) __hip_atomic_store((LAS u64*)sy, tag, __ATOMIC_RELAXED, __HIP_MEMORY_SCOPE_WORKGROUP);
        {
            u32x4 o0, o1; transpose_tile(lds + LB + 4096, TABW, tab, sy + SY_T1, w, lane, o0, o1);
            const unsigned to = WS_UT + (unsigned)((64 * xcd + 16 * w + (lane >> 3)) * S + 64 * bi) * 2 + (lane & 7) * 16;
            __builtin_amdgcn_s_setprio(3); BST(o0, to); BST(o1, to + 8 * S * 2); __builtin_amdgcn_s_setprio(PRIO_B);
        }
        await(sy + SY_T1, 4);
        STAMP(13);
        CHAIN(); RDT();
        drain(); __builtin_amdgcn_s_setprio(3); put_flag(ws, FL_UT + (unsigned)(xcd * 128 + w * 32 + bi) * FS, tag, lane); __builtin_amdgcn_s_setprio(PRIO_B);
        STAMP(14);
        ANCH(bf_hi(pl), bf_hi(pa), bf_hi(pr), bf_hi(pi));
        MMT(); SBAR();
        CHAIN(); RDT();
        const unsigned utf = hi ? FL_WO + (unsigned)(w * 32 + r32) * FS : FL_UT + (unsigned)(xcd * 128 + (bi >> 3) * 32 + r32) * FS;
        const u64 utv = __hip_atomic_load((GAS u64*)(ws + utf), __ATOMIC_RELAXED, __HIP_MEMORY_SCOPE_AGENT); SBAR();
        MMT();
        STAMP(2);
        {
            LAS float* pt = (LAS float*)tab;
            *(LAS f32x4*)(pt + 8 * r32 + 4 * hi) = (f32x4){dacc[0] * oscale, dacc[1] * oscale, dacc[2] * oscale, dacc[3] * oscale};
            *(LAS f32x2*)(pt + (hi ? 384 + 2 * r32 : 256 + 4 * r32)) = (f32x2){dacc[4] * oscale, dacc[5] * oscale};
            *(LAS f32x2*)(pt + (hi ? 448 + 2 * r32 : 258 + 4 * r32)) = (f32x2){dacc[6] * oscale, dacc[7] * oscale};
            pt[hi ? 640 + r32 : 512 + r32] = dacc[8] * oscale;
            pt[hi ? 672 + r32 : 544 + r32] = dacc[9] * oscale;
            pt[hi ? 672 + r32 : 576 + r32] = dacc[10] * oscale;
            pt[hi ? 672 + r32 : 608 + r32] = dacc[11] * oscale;
        }
        LAS unsigned* ps_ = sy + SY_P + 5 * cc;
        arrive(ps_ + 0);
        LAS char* ui = cb + UOF;
        LAS char* kt = cb + KOF;
        await(ps_ + 0, 2);
#pragma unroll
        for (int ps = 0; ps < 2; ++ps) {
            const int v = 81 * hh + 64 * ps + lane;
            if (ps == 0 || lane < 17) {
                const f32x4 p0 = *(LAS const f32x4*)(cb + v * 16), p1 = *(LAS const f32x4*)(cb + TABW + v * 16);
                u32x2 r = {pk_bf16(p0[0] + p1[0], p0[1] + p1[1]), pk_bf16(p0[2] + p1[2], p0[3] + p1[3])};
                if (v == 161) r = (u32x2){0u, 0u};
                *(LAS u32x2*)(kt + v * 8) = r;
            }
        }
        arrive(ps_ + 1);
        if (__any(utv != tag)) wait_flags<8>(ws, utf, tag, utv != tag);
        CFENCE();
        const unsigned ub = WS_UT + (unsigned)(ch * S + 1024 * hh) * 2 + voff;
        const u32x4 u0 = BLD(ub), u1 = BLD(ub + 1024); SBAR();
        await(ps_ + 1, 2);
        STAMP(3);
        __builtin_amdgcn_s_setprio(PRIO_B2);
        const u32x4 z4 = {};
        {
            unsigned W[2][6];
#pragma unroll
            for (int ps = 0; ps < 2; ++ps)
#pragma unroll
                for (int i = 0; i < 6; ++i) { const unsigned gv = gi[ps][i >> 1][i & 1];
                    W[ps][i] = *(LAS const uint16_t*)(kt + (gv & 0xffffu));
                    if (i < 5) W[ps][i] |= (unsigned)*(LAS const uint16_t*)(kt + (gv >> 16)) << 16; }
            *(LAS u32x4*)(ui + (64 * hh + lane) * 16) = z4;
            if (lane < 16) *(LAS u32x4*)(ui + (128 + 16 * hh + lane) * 16) = z4;
            if (lane < 8) *(LAS u32x4*)(cb + (2 * hh + (lane >> 2)) * CPY + (256 + (lane & 3)) * 16) = z4;
#pragma unroll
            for (int ps = 0; ps < 2; ++ps) {
                unsigned V[5];
#pragma unroll
                for (int i = 0; i < 5; ++i) V[i] = __builtin_amdgcn_alignbit(W[ps][i + 1], W[ps][i], 16);
                LAS char* cp = cb + (128 * hh + 64 * ps + lane) * 16;
                *(LAS u32x4*)(cp) = (u32x4){W[ps][0], W[ps][1], W[ps][2], W[ps][3]};
                *(LAS u32x4*)(cp + CPY) = (u32x4){V[0], V[1], V[2], V[3]};
                *(LAS u32x4*)(cp + 2 * CPY) = (u32x4){W[ps][1], W[ps][2], W[ps][3], W[ps][4]};
                *(LAS u32x4*)(cp + 3 * CPY) = (u32x4){V[1], V[2], V[3], V[4]};
            }
        }
        *(LAS u32x4*)(ui + (32 + 32 * hh + (lane >> 2)) * 80 + (lane & 3) * 16) = u0;
        *(LAS u32x4*)(ui + (48 + 32 * hh + (lane >> 2)) * 80 + (lane & 3) * 16) = u1;
        arrive(ps_ + 2); await(ps_ + 2, 2);
        STAMP(4);
        __builtin_amdgcn_s_setprio(PRIO_BC);
        f32x16 a0 = {}, a1 = {};
        {
            const int sb = 7 - (r32 & 7);
            LAS const char* pa = cb + (sb & 3) * CPY + (255 - (r32 >> 3) + hi) * 16 + (sb >> 2) * 8 - 64 * (16 * hh + 15) - 2048;
            LAS const char* pa2 = pa + 8; asm volatile("" : "+v"(pa2));
            LAS const char* pb = ui + (32 + r32 - 16 * hh - 15) * 80 + 16 * hi;
            s16x4 ta[2][2], tb[2][2]; bf16x8 b0[2], b1[2];
#define T4(q, off) (*(LAS const s16x4*)((q) + (off)))
#define CSTEP(i, j, RD) do { const int o_ = 64 * (15 - ((j) >> 1)) + 32 * ((j) & 1), u_ = 80 * (15 - ((j) >> 1)) + 32 * ((j) & 1); \
                a1 = MFMA_BF(cat8(ta[i][0], ta[i][1]), b1[i], a1); SBAR(); if (RD) { ta[1 - i][0] = T4(pa, o_ + 2048); ta[1 - i][1] = T4(pa2, o_ + 2048); } SBAR(); \
                a0 = MFMA_BF(cat8(ta[i][0], ta[i][1]), b0[i], a0); SBAR(); if (RD) { b1[1 - i] = FR(pb, u_ + 2560); b0[1 - i] = FR(pb, u_); } SBAR(); \
                a1 = MFMA_BF(cat8(tb[i][0], tb[i][1]), b0[i], a1); SBAR(); if (RD) { tb[1 - i][0] = T4(pa, o_); tb[1 - i][1] = T4(pa2, o_); } SBAR(); } while (0)
            ta[0][0] = T4(pa, 960 + 2048); ta[0][1] = T4(pa2, 960 + 2048); b1[0] = FR(pb, 1200 + 2560); b0[0] = FR(pb, 1200); tb[0][0] = T4(pa, 960); tb[0][1] = T4(pa2, 960);
#pragma unroll
            for (int j = 0; j < 32; ++j) {
                if (j == 31) arrive(ps_ + 3);
                CSTEP(j & 1, j + 1, j < 31);
                if (ct < 14) LOADWF(j);
                SBAR();
            }
        }
        STAMP(5);
        __builtin_amdgcn_s_setprio(PRIO_B3);
        {
            u32x2 uu[4];
#pragma unroll
            for (int g = 0; g < 4; ++g) uu[g] = *(LAS const u32x2*)(ui + (32 + 32 * hh + r32) * 80 + 16 * g + 8 * hi);
            LAS char* ex = cb + r32 * 144 + hi * 16;
            const f32x16 snd = hh ? a0 : a1; const f32x16 y = hh ? a1 : a0;
            await(ps_ + 3, 2);
#pragma unroll
            for (int g = 0; g < 4; ++g) *(LAS f32x4*)(ex + hh * 4608 + g * 32) = (f32x4){snd[4 * g], snd[4 * g + 1], snd[4 * g + 2], snd[4 * g + 3]};
            arrive(ps_ + 4);
            const float dsk = cc ? bf_hi(dw) : bf_lo(dw);
            unsigned sk[8];
#pragma unroll
            for (int g = 0; g < 4; ++g)
#pragma unroll
                for (int e = 0; e < 2; ++e) sk[2 * g + e] = pk_bf16(bf_lo(uu[g][e]) * dsk, bf_hi(uu[g][e]) * dsk);
            await(ps_ + 4, 2);
            LAS char* sc = cb + 9216 + hh * 2048;
#pragma unroll
            for (int g = 0; g < 4; ++g) {
                const f32x4 p = *(LAS const f32x4*)(ex + (1 - hh) * 4608 + g * 32);
                u32x2 r;
#pragma unroll
                for (int e = 0; e < 2; ++e) {
                    const unsigned yb = pk_bf16(y[4 * g + 2 * e] + p[2 * e], y[4 * g + 2 * e + 1] + p[2 * e + 1]);
                    r[e] = pk_bf16(bf_lo(yb) + bf_lo(sk[2 * g + e]), bf_hi(yb) + bf_hi(sk[2 * g + e]));
                }
                *(LAS u32x2*)(sc + 64 * r32 + 16 * g + 8 * hi) = r;
            }
            const u32x4 o0 = *(LAS const u32x4*)(sc + voff), o1 = *(LAS const u32x4*)(sc + 1024 + voff);
            const unsigned bo = WS_BT + (unsigned)(ch * S + 1024 * hh) * 2 + voff;
            BST(o0, bo); BST(o1, bo + 1024);
        }
        arrive(sy + SY_BG);
        STAMP(6);
        drain();
        put_flag(ws, FL_BT + (unsigned)(xcd * 128 + bi * 4 + w) * FS, tag, lane);
    } else {
        {
            const unsigned nb = (bi & 1) ? 12 * 2 * D : 16 * 2 * D;
            unsigned v4[4];
#pragma unroll
            for (int i = 0; i < 4; ++i) { const unsigned o = (unsigned)(w * 4096 + i * 1024) + voff; v4[i] = (o < nb ? o : 0u) + 4096 - i * 1024; }
            glds4((const char*)(wo + (DH * (bi >> 1) + 16 * (bi & 1)) * D) - 4096, v4[0], v4[1], v4[2], v4[3], lds0 + LB + w * 4096);
        }
        const u64 e0 = __hip_atomic_load((GAS u64*)ws, __ATOMIC_RELAXED, __HIP_MEMORY_SCOPE_AGENT);
        if (CODEPF) CODE_PF();
        drain(); asm volatile("" :: "v"(pf));
        tag = mix(e0);
        arrive(sy + SY_WO); await(sy + SY_WO, 4);
        if (w == 0 && lane == 0) __hip_atomic_store((LAS u64*)sy, tag, __ATOMIC_RELAXED, __HIP_MEMORY_SCOPE_WORKGROUP);
        LAS const char* p = lds + LB + (4 * hi + ((lane & 15) >> 2)) * (2 * D) + ((lane >> 4) & 1) * 32 + (lane & 3) * 8;
#pragma unroll
        for (int i = 0; i < 4; ++i) {
            const int ct = w + 4 * i;
            if (ct < 14) {
                u32x4 o = __builtin_bit_cast(u32x4, cat8(trread(p + ct * 64), trread(p + 8 * 2 * D + ct * 64)));
                if (hi && (bi & 1)) { o[2] = 0; o[3] = 0; }
                BST(o, WS_WO + (unsigned)(bi * 14 + ct) * 1024 + voff);
            }
        }
        drain();
        put_flag(ws, FL_WO + (unsigned)(w * 32 + bi) * FS, tag, lane);
        __builtin_amdgcn_s_setprio(0);
        arrive(sy + SY_BG);
        await_slow(sy + SY_G, 4);
        wait_flags<8>(ws, FL_WO + (unsigned)(w * 32 + r32) * FS, tag, !hi);
        if (ct < 14) {
#pragma unroll
            for (int i = 0; i < 32; ++i) { LOADWF(i); __builtin_amdgcn_s_sleep(WOSL); SBAR(); }
        }
    }
    STAMP(7);
    const __amdgpu_buffer_rsrc_t ro = __builtin_amdgcn_make_buffer_rsrc(out, 0, S * 2 * D * 2, 0x00020000);
    await(sy + SY_BG, 4);
    STAMP(8);
    await_slow(sy + SY_X, 4);
    wait_flags<OSL>(ws, FL_O + (unsigned)(qt * 16 + 4 * w + lane) * FS, tag, lane < 4);
    STAMP(9);
    u32x4 of[8];
#pragma unroll
    for (int i = 0; i < 8; ++i) of[i] = BLD(WS_O + (unsigned)(qt * 32 + 8 * w + i) * 1024 + voff);
#pragma unroll
    for (int i = 0; i < 8; ++i) *(LAS u32x4*)(lds + LB + (8 * w + i) * 1024 + voff) = of[i];
    arrive(sy + SY_B2);
    STAMP(10);
    f32x16 acc = {};
    if (ct < 14) {
#pragma unroll
        for (int i = 0; i < 8; ++i) acc = MFMA_BF(wf[i], BC8(of[i]), acc);
    }
    SBAR();
    await(sy + SY_B2, 4);
    STAMP(11);
    if (ct < 14) {
        LAS const char* po[3];
#pragma unroll
        for (int j = 0; j < 3; ++j) po[j] = lds + LB + ((w + 1 + j) & 3) * 8192 + voff;
        bf16x8 og[4];
#pragma unroll
        for (int i = 0; i < 3; ++i) og[i] = FR(po[0], i * 1024);
#pragma unroll
        for (int i = 0; i < 24; ++i) { if (i < 21) og[(i + 3) & 3] = FR(po[(i + 3) >> 3], ((i + 3) & 7) * 1024); SBAR(); acc = MFMA_BF(wf[8 + i], og[i & 3], acc); SBAR(); }
        LAS char* sc = lds + LB + 40960 + w * 2304;
#pragma unroll
        for (int g = 0; g < 4; ++g) *(LAS u32x2*)(sc + r32 * 72 + 16 * g + 8 * hi) = (u32x2){pk_bf16(acc[4 * g], acc[4 * g + 1]), pk_bf16(acc[4 * g + 2], acc[4 * g + 3])};
        LAS const char* sr = sc + (lane >> 2) * 72 + (lane & 3) * 16;
        const u32x2 a0 = *(LAS const u32x2*)sr, a1 = *(LAS const u32x2*)(sr + 8), b0 = *(LAS const u32x2*)(sr + 16 * 72), b1 = *(LAS const u32x2*)(sr + 16 * 72 + 8);
        const unsigned oo = (unsigned)((32 * qt + (lane >> 2)) * 2 * D + 32 * ct) * 2 + (lane & 3) * 16;
        __builtin_amdgcn_raw_buffer_store_b128((u32x4){a0[0], a0[1], a1[0], a1[1]}, ro, oo, 0, 16);
        __builtin_amdgcn_raw_buffer_store_b128((u32x4){b0[0], b0[1], b1[0], b1[1]}, ro, oo + 16 * 2 * D * 2, 0, 16);
    }
    if (b == 0 && w == 0) put_flag(ws, 0, tag, lane);
    STAMP(12);
}

__global__ __launch_bounds__(512, 2) void fused(const uint16_t* __restrict__ x, const uint16_t* __restrict__ wqkv, const uint16_t* __restrict__ wo,
                                               const uint16_t* __restrict__ u, const uint16_t* __restrict__ lar, const uint16_t* __restrict__ aim,
                                               const uint16_t* __restrict__ cre, const uint16_t* __restrict__ cim, const uint16_t* __restrict__ ldt,
                                               const uint16_t* __restrict__ dskip, uint16_t* __restrict__ out, char* __restrict__ ws) {
    extern __shared__ __attribute__((aligned(16))) char smem[];
    LAS char* lds = (LAS char*)smem;
    const int lane = threadIdx.x & 63, w = __builtin_amdgcn_readfirstlane(threadIdx.x >> 6);
    STAMP(0);
    const u64 code0 = __builtin_amdgcn_s_getpc() & ~255ull;
    if (w >= 4) __builtin_amdgcn_s_setprio(3);
    if (w == 4) ((LAS unsigned*)(lds + LSY))[lane] = 0;
    asm volatile("" :: "s"(x), "s"(wqkv), "s"(wo), "s"(u), "s"(lar), "s"(aim), "s"(cre), "s"(cim), "s"(ldt), "s"(dskip), "s"(out), "s"(ws));
    asm volatile("s_waitcnt lgkmcnt(0)\n\ts_barrier" ::: "memory");
    if (w < 4) role_a(x, wqkv, out, ws, lds, w, lane);
    else role_b(wo, u, lar, aim, cre, cim, ldt, dskip, out, ws, lds, w - 4, lane, code0);
}
}

extern "C" void kernel_launch(void* const* d_in, const int* in_sizes, int n_in, void* d_out, int out_size, void* d_ws, size_t ws_size, hipStream_t stream) {
    using namespace k;
    (void)hipFuncSetAttribute((const void*)fused, hipFuncAttributeMaxDynamicSharedMemorySize, LDS_BYTES);
    const uint16_t* const* in = (const uint16_t* const*)d_in;
    fused<<<256, 512, LDS_BYTES, stream>>>(in[0], in[1], in[2], in[3], in[4], in[5], in[6], in[7], in[8], in[9], (uint16_t*)d_out, (char*)d_ws);
}
```

```cpp
#include <hip/hip_runtime.h>
#include <hip/hip_bf16.h>
#include <stdint.h>

typedef short bf16x8 __attribute__((ext_vector_type(8)));
typedef _Float16 f16x8 __attribute__((ext_vector_type(8)));
typedef _Float16 f16x2 __attribute__((ext_vector_type(2)));
typedef __bf16 bf16x2 __attribute__((ext_vector_type(2)));
typedef short s16x4 __attribute__((ext_vector_type(4)));
typedef float f32x16 __attribute__((ext_vector_type(16)));
typedef float f32x4 __attribute__((ext_vector_type(4)));
typedef float f32x2 __attribute__((ext_vector_type(2)));
typedef unsigned u32x4 __attribute__((ext_vector_type(4)));
typedef unsigned u32x2 __attribute__((ext_vector_type(2)));
typedef unsigned long long u64;
#define LAS __attribute__((address_space(3)))
#define GAS __attribute__((address_space(1)))
#define DI __device__ __forceinline__
#define MFMA_BF(a, b, c) __builtin_amdgcn_mfma_f32_32x32x16_bf16((a), (b), (c), 0, 0, 0)
#define MFMA_F16(a, b, c) __builtin_amdgcn_mfma_f32_32x32x16_f16((a), (b), (c), 0, 0, 0)
#define SBAR() __builtin_amdgcn_sched_barrier(0)
#define CFENCE() asm volatile("" ::: "memory")
#define PIN(x) asm volatile("" : "+v"(x))
#define BC8(x) __builtin_bit_cast(bf16x8, (x))
#define EX(x) __builtin_amdgcn_exp2f(x)
#define PRIO_A1 0
#define PRIO_B 0
#define PRIO_B2 1
#define ROT 1
#define KVM 6u
#define HOLDB 0
#define PRIO_B3 1
#define PRIO_BC 0
#define FLJ 13
#define OSL 2
#define LATE2 1
#define CODEPF 1
#define EARLYT 1
#define WOSL 2

namespace k {
#define STAMP(i)
constexpr int S = 2048, D = 448, DH = 28, NS = 256;
constexpr unsigned FS = 128;
constexpr unsigned FL_KV = FS, FL_O = FL_KV + 1024 * FS, FL_UT = FL_O + 1024 * FS, FL_BT = FL_UT + 896 * FS, FL_WO = FL_BT + 896 * FS;
constexpr unsigned WS_KV = 512u << 10;
constexpr unsigned WS_O = WS_KV + (4u << 20);
constexpr unsigned WS_WO = WS_O + (2u << 20);
constexpr unsigned WS_UT = WS_WO + (448u << 10);
constexpr unsigned WS_BT = WS_UT + D * S * 2;
constexpr unsigned WS_END = WS_BT + D * S * 2;
constexpr int WSL = 8192, LWQ = 4 * WSL, LXR = LWQ + 16384, XSL = 16384, LB = LXR + 3 * XSL;
constexpr int LQ = LXR, LX = 0;
constexpr int TROW = 272, TABW = 53 * TROW, CBW = 2 * TABW, CPY = 4160, UOF = 4 * CPY, KOF = CBW - 1312;
constexpr int LSY = LB + 2 * CBW, LDS_BYTES = LSY + 256;
static_assert(LDS_BYTES <= 163840 && (LSY & 15) == 0 && UOF + 7680 <= KOF, "lds");
enum { SY_TAG = 0, SY_F = 2, SY_P = 10, SY_WO = 20, SY_T1 = 21, SY_BG = 22, SY_B2 = 23, SY_Q = 24, SY_X = 25, SY_G = 26, SY_T2 = 27, SY_H = 28 };
constexpr int SPIN = 1 << 18;
constexpr unsigned CODE_LINES = 272;

struct IdxTab { uint16_t v[256][12]; };
constexpr IdxTab make_idx() {
    IdxTab t = {};
    for (int y = 0; y < 256; ++y) for (int e = 0; e < 12; ++e) {
        const int j = 2047 - 8 * y - e; int ix = 644;
        if (j >= 0) { const int sh = j >= 1024 ? 3 : (j >= 512 ? 2 : (j >= 256 ? 1 : 0));
            ix = sh ? 128 * sh + ((j + (1 << (sh - 1)) - 1 + ((j >> sh) & 1)) >> sh) : j; }
        t.v[y][e] = (uint16_t)(2 * ix);
    }
    return t;
}
__device__ const IdxTab g_idx __attribute__((aligned(16))) = make_idx();

DI unsigned pk_bf16(float a, float b) { f32x2 v = {a, b}; return __builtin_bit_cast(unsigned, __builtin_convertvector(v, bf16x2)); }
DI unsigned pk_f16(f32x2 v) { return __builtin_bit_cast(unsigned, __builtin_convertvector(v, f16x2)); }
DI float bf_lo(unsigned w) { return __uint_as_float(w << 16); }
DI float bf_hi(unsigned w) { return __uint_as_float(w & 0xffff0000u); }
DI float rbf(float x) { return bf_lo(pk_bf16(x, x)); }
#define GL0 "s_mov_b32 %0, m0\n\ts_mov_b32 m0, %2\n\ts_nop 0\n\t"
#define GL1(V, O, C) "global_load_lds_dwordx4 " V ", %1 offset:" #O C "\n\t"
#define GLE "s_mov_b32 m0, %0"
DI void glds4(const void* sb, unsigned v0, unsigned v1, unsigned v2, unsigned v3, unsigned lb) { unsigned sv;
    asm volatile(GL0 GL1("%3", 0, "") GL1("%4", 1024, "") GL1("%5", 2048, "") GL1("%6", 3072, "") GLE : "=&s"(sv) : "s"(sb), "s"(lb), "v"(v0), "v"(v1), "v"(v2), "v"(v3) : "memory"); }
DI void glds3(const void* sb, unsigned v0, unsigned v1, unsigned v2, unsigned lb) { unsigned sv;
    asm volatile(GL0 GL1("%3", 0, "") GL1("%4", 1024, "") GL1("%5", 2048, "") GLE : "=&s"(sv) : "s"(sb), "s"(lb), "v"(v0), "v"(v1), "v"(v2) : "memory"); }
DI void glds2(const void* sb, unsigned v0, unsigned v1, unsigned lb) { unsigned sv;
    asm volatile(GL0 GL1("%3", 0, "") GL1("%4", 1024, "") GLE : "=&s"(sv) : "s"(sb), "s"(lb), "v"(v0), "v"(v1) : "memory"); }
DI void glds1(const void* sb, unsigned v0, unsigned lb) { unsigned sv;
    asm volatile(GL0 GL1("%3", 0, "") GLE : "=&s"(sv) : "s"(sb), "s"(lb), "v"(v0) : "memory"); }
DI void glds2c(const void* sb, unsigned v0, unsigned v1, unsigned lb) { unsigned sv;
    asm volatile(GL0 GL1("%3", 0, " sc1") GL1("%4", 1024, " sc1") GLE : "=&s"(sv) : "s"(sb), "s"(lb), "v"(v0), "v"(v1) : "memory"); }
DI float wave_max(float v) {
#define DPPMAX(ctrl, rm) v = fmaxf(v, __uint_as_float(__builtin_amdgcn_update_dpp(0, __float_as_uint(v), ctrl, rm, 0xf, true)))
    DPPMAX(0x111, 0xf); DPPMAX(0x112, 0xf); DPPMAX(0x114, 0xf); DPPMAX(0x118, 0xf); DPPMAX(0x142, 0xa); DPPMAX(0x143, 0xc);
    return __uint_as_float(__builtin_amdgcn_readlane(__float_as_uint(v), 63));
}
DI s16x4 trread(LAS const char* p) { return __builtin_bit_cast(s16x4, __builtin_amdgcn_ds_read_tr16_b64_v4i16((LAS s16x4*)p)); }
DI bf16x8 cat8(s16x4 a, s16x4 b) { return __builtin_shufflevector(a, b, 0, 1, 2, 3, 4, 5, 6, 7); }
DI f32x2 pfma(f32x2 a, f32x2 b, f32x2 c) { return __builtin_elementwise_fma(a, b, c); }
DI f32x2 cmul(f32x2 a, f32x2 b, f32x2 bs) { return pfma(a.yy, bs, a.xx * b); }
DI f32x2 rot(f32x2 b) { return (f32x2){-b.y, b.x}; }
DI f32x2 anchor(float a2, float rev, float s) {
    const float mag = EX(a2 * s);
    float r = rev * s; r -= __builtin_rintf(r);
    return (f32x2){mag * __builtin_amdgcn_cosf(r), mag * __builtin_amdgcn_sinf(r)};
}
DI void arrive(LAS unsigned* c) { asm volatile("s_mov_b64 exec, 1\n\tds_add_u32 %0, %1\n\ts_mov_b64 exec, -1" :: "v"((unsigned)(uintptr_t)c), "v"(1u) : "memory"); }
DI void await(LAS unsigned* c, unsigned n) {
    unsigned t, i = SPIN;
    asm volatile("1:\n\tds_read_b32 %0, %2\n\ts_waitcnt lgkmcnt(0)\n\tv_cmp_gt_u32 vcc, %3, %0\n\ts_cbranch_vccz 2f\n\ts_sleep 1\n\ts_sub_u32 %1, %1, 1\n\ts_cbranch_scc0 1b\n2:"
                 : "=&v"(t), "+s"(i) : "v"((unsigned)(uintptr_t)c), "s"(n) : "vcc", "scc", "memory");
}
DI void await_slow(LAS unsigned* c, unsigned n) {
    unsigned t, i = SPIN;
    asm volatile("1:\n\tds_read_b32 %0, %2\n\ts_waitcnt lgkmcnt(0)\n\tv_cmp_gt_u32 vcc, %3, %0\n\ts_cbranch_vccz 2f\n\ts_sleep 3\n\ts_sub_u32 %1, %1, 1\n\ts_cbranch_scc0 1b\n2:"
                 : "=&v"(t), "+s"(i) : "v"((unsigned)(uintptr_t)c), "s"(n) : "vcc", "scc", "memory");
}
DI u64 get_tag(LAS unsigned* sy) {
    await(sy, 1);
    const u64 v = __hip_atomic_load((LAS u64*)sy, __ATOMIC_RELAXED, __HIP_MEMORY_SCOPE_WORKGROUP);
    return ((u64)(unsigned)__builtin_amdgcn_readfirstlane((unsigned)(v >> 32)) << 32) | (unsigned)__builtin_amdgcn_readfirstlane((unsigned)v);
}
DI void drain() { asm volatile("s_waitcnt vmcnt(0)" ::: "memory"); }
DI void put_flag(char* ws, unsigned off, u64 tag, int lane) { if (lane == 0) __hip_atomic_store((GAS u64*)(ws + off), tag, __ATOMIC_RELAXED, __HIP_MEMORY_SCOPE_AGENT); }
template <int SLP> DI void wait_flags(char* ws, unsigned off, u64 tag, bool pend) {
    GAS u64* p = (GAS u64*)(ws + off);
    CFENCE();
#pragma unroll 1
    for (int i = 0; i < SPIN; ++i) {
        if (pend) pend = __hip_atomic_load(p, __ATOMIC_RELAXED, __HIP_MEMORY_SCOPE_AGENT) != tag;
        if (!__any(pend)) break;
        __builtin_amdgcn_s_sleep(SLP);
    }
    CFENCE();
}
#define BST(v, off) __builtin_amdgcn_raw_buffer_store_b128((v), rs, (off), 0, 16)
#define BLD(off) __builtin_amdgcn_raw_buffer_load_b128(rs, (off), 0, 16)
#define FR(p, off) (*(LAS const bf16x8*)((p) + (off)))
DI float swapmax(float m) { const auto r = __builtin_amdgcn_permlane32_swap(__float_as_uint(m), __float_as_uint(m), false, false); return fmaxf(__uint_as_float(r[0]), __uint_as_float(r[1])); }
DI u32x4 put_ref(u32x4 q1, float m, int hi) {
    const float m1 = rbf(m), m2 = rbf(m - m1), m3 = m - m1 - m2;
    if (hi) { q1[2] = pk_bf16(-m1, -m2); q1[3] = pk_bf16(-m3, 0.f); }
    return q1;
}
DI u64 mix(u64 v) {
    const u64 e = ((u64)(unsigned)__builtin_amdgcn_readfirstlane((unsigned)(v >> 32)) << 32) | (unsigned)__builtin_amdgcn_readfirstlane((unsigned)v);
    u64 z = e + 0x9E3779B97F4A7C15ull; z = (z ^ (z >> 30)) * 0xBF58476D1CE4E5B9ull; z = (z ^ (z >> 27)) * 0x94D049BB133111EBull; return (z ^ (z >> 31)) | 1ull; }
DI void transpose_tile(LAS const char* stage, int pst, LAS char* scratch, LAS unsigned* done, int w, int lane, u32x4& o0, u32x4& o1) {
    LAS const char* p = stage + (lane >> 4) * pst + ((lane & 15) >> 2) * 128 + w * 32 + (lane & 3) * 8;
    const bf16x8 t0 = cat8(trread(p), trread(p + 512)), t1 = cat8(trread(p + 1024), trread(p + 1536));
    if (done) arrive(done);
    LAS char* sc = scratch + (lane & 15) * 144 + (lane >> 4) * 32;
    *(LAS bf16x8*)sc = t0; *(LAS bf16x8*)(sc + 16) = t1;
    LAS const char* sr = scratch + (lane >> 3) * 144 + (lane & 7) * 16;
    o0 = *(LAS const u32x4*)sr; o1 = *(LAS const u32x4*)(sr + 8 * 144);
}

DI void role_a(const uint16_t* __restrict__ x, const uint16_t* __restrict__ wqkv, uint16_t* __restrict__ out, char* __restrict__ ws, LAS char* lds, int w, int lane, unsigned tc) {
    const int head = 2 * (blockIdx.x & 7) + ((blockIdx.x >> 3) & 1), grp = blockIdx.x >> 4;
    const unsigned lds0 = (unsigned)(uintptr_t)lds;
    const int r32 = lane & 31, hi = lane >> 5;
    const int wsh = (head & 7) == 7 ? 4 : 0;
    const unsigned wv = (unsigned)((lane >> 2) * 3 * D + (lane & 3) * 8) * 2;
    const char* wb = (const char*)(wqkv + DH * head - wsh + (16 * w) * 3 * D);
    const unsigned xv0 = (unsigned)((lane >> 3) * D + (((lane & 7) ^ (lane >> 4)) * 8)) * 2, xv1 = xv0 ^ 64;
    const char* xb = (const char*)(x + (128 * grp + 32 * w) * D);
    const int rho = ROT ? ((blockIdx.x >> 3) + ((blockIdx.x >> 1) & 3)) % 7 : 0;
#define SP(st) ((st) + rho >= 7 ? (st) + rho - 7 : (st) + rho)
#define WQO(st) ((st) < 4 ? LWQ + (st) * 4096 : ((st) < 6 ? 3 * WSL + ((st) - 4) * 4096 : 0))
#define LOADW(st) glds2(wb + SP(st) * (64 * 3 * D * 2), wv + 2 * D, wv + 4 * D - 1024, lds0 + ((st) & 3) * WSL + w * 2048)
#define LOADQ(st) glds1(wb + SP(st) * (64 * 3 * D * 2), wv, lds0 + WQO(st) + w * 1024)
#define LOADX(st) glds4(xb + SP(st) * 128, xv0, xv1 + 16 * D - 1024, xv0 + 32 * D - 2048, xv1 + 48 * D - 3072, lds0 + LXR + ((st) % 3) * XSL + w * 4096)
    LOADW(0); LOADX(0);
    STAMP(1);
    LOADW(1); LOADX(1); if (!LATE2) { LOADW(2); LOADX(2); }
    __builtin_amdgcn_s_setprio(PRIO_A1);
    LAS unsigned* sy = (LAS unsigned*)(lds + LSY);
    const __amdgpu_buffer_rsrc_t rs = __builtin_amdgcn_make_buffer_rsrc(ws, 0, WS_END, 0x00020000);
    const int xs = (r32 >> 1) & 7;
    LAS const char* xp[4];
#pragma unroll
    for (int ks = 0; ks < 4; ++ks) xp[ks] = lds + LXR + w * 4096 + r32 * 128 + (((2 * ks + hi) ^ xs) * 16);
    LAS const char* wp = lds + (8 * hi + ((lane & 15) >> 2)) * 64 + ((lane >> 4) & 1) * 32 + (lane & 3) * 8 + wsh * 2;
    f32x16 aq = {}, ak = {}, av = {};
    bf16x8 xf[7][4]; s16x4 fr[4][4];
#define RX(st, ks) xf[st][ks] = FR(xp[ks], ((st) % 3) * XSL)
#define RW(st, ks, j) fr[ks][j] = trread(wp + ((st) & 3) * WSL + (ks) * 2048 + ((j) >> 1) * 1024 + ((j) & 1) * 256)
#define MK(st, ks) ak = MFMA_BF(cat8(fr[ks][0], fr[ks][1]), xf[st][ks], ak); SBAR()
#define MV(st, ks) av = MFMA_BF(xf[st][ks], cat8(fr[ks][2], fr[ks][3]), av); SBAR()
#define QSTEP(st, ks) MK(st, ks); RW(st, ks + 1, 0); RW(st, ks + 1, 1); SBAR(); MV(st, ks); RW(st, ks + 1, 2); RW(st, ks + 1, 3); SBAR()
    u64 tag = 0;
    if (LATE2) { asm volatile("s_waitcnt vmcnt(6)" :: "v"(tc) : "memory"); arrive(sy + SY_F); LOADW(2); LOADX(2); await(sy + SY_F, 4); }
    else { asm volatile("s_waitcnt vmcnt(12)" ::: "memory"); arrive(sy + SY_F); await(sy + SY_F, 4); }
    STAMP(16);
    RX(0, 0); RW(0, 0, 0); RW(0, 0, 1); RW(0, 0, 2); RW(0, 0, 3); RX(0, 1); RX(0, 2); RX(0, 3); SBAR();
#pragma unroll
    for (int st = 0; st < 7; ++st) {
        QSTEP(st, 0); if (st < 4) LOADW(st + 3); if (st == 4) { LOADQ(4); LOADQ(5); } if (st == 5) LOADQ(6);
        QSTEP(st, 1); if (st < 4) LOADX(st + 3);
        QSTEP(st, 2); if (st == 3) { LOADQ(0); LOADQ(1); LOADQ(2); LOADQ(3); }
        if (st < 6) {
            if (st == 3) asm volatile("s_waitcnt vmcnt(16)" ::: "memory"); else if (st == 5) asm volatile("s_waitcnt vmcnt(7)" ::: "memory"); else asm volatile("s_waitcnt vmcnt(12)" ::: "memory");
            arrive(sy + SY_F + 1 + st); SBAR();
        }
        MK(st, 3);
        if (st < 6) { await(sy + SY_F + 1 + st, 4); RX(st + 1, 0); RW(st + 1, 0, 0); RW(st + 1, 0, 1); RW(st + 1, 0, 2); RW(st + 1, 0, 3); } SBAR();
        MV(st, 3); if (st < 6) { RX(st + 1, 1); RX(st + 1, 2); RX(st + 1, 3); } SBAR();
        if (st == 2) tag = get_tag(sy);
    }
    STAMP(3);
    const float cq = 0.18898223650461362f * 1.4426950408889634f;
    const unsigned voff = lane * 16;
    u32x4 q0, q1, k0, k1, v0, v1;
    const unsigned tb = WS_KV + (unsigned)(head * 64 + 4 * grp + w) * 4096 + voff;
#pragma unroll
    for (int i = 0; i < 4; ++i) { k0[i] = pk_bf16(ak[2 * i], ak[2 * i + 1]); k1[i] = pk_bf16(ak[8 + 2 * i], ak[9 + 2 * i]); }
    if (hi) { k1[2] = 0x3f803f80u; k1[3] = 0x00003f80u; }
    BST(k0, tb); BST(k1, tb + 1024);
#pragma unroll
    for (int i = 0; i < 4; ++i) {
        v0[i] = pk_bf16(av[2 * i], av[2 * i + 1]); v1[i] = pk_bf16(av[8 + 2 * i], av[9 + 2 * i]);
        if (r32 >= DH) { v0[i] = v1[i] = (r32 == DH ? 0x3f803f80u : 0u); }
    }
    BST(v0, tb + 2048); BST(v1, tb + 3072);
    asm volatile("s_waitcnt vmcnt(4)" ::: "memory");
    arrive(sy + SY_F + 7); await(sy + SY_F + 7, 4);
    {
        s16x4 fq[4][2];
#define RQ(j) do { fq[(j) & 3][0] = trread(wp + WQO((j) >> 2) + ((j) & 3) * 1024); fq[(j) & 3][1] = trread(wp + WQO((j) >> 2) + ((j) & 3) * 1024 + 256); } while (0)
        RQ(0); RQ(1); RQ(2); SBAR();
#pragma unroll
        for (int j = 0; j < 28; ++j) {
            aq = MFMA_BF(cat8(fq[j & 3][0], fq[j & 3][1]), xf[j >> 2][j & 3], aq); SBAR(); if (j < 25) RQ(j + 3); SBAR();
            if (j == FLJ) { drain(); put_flag(ws, FL_KV + (unsigned)((head * 4 + w) * 16 + grp) * FS, tag, lane); STAMP(4); }
        }
    }
    STAMP(17);
#pragma unroll
    for (int i = 0; i < 4; ++i) { q0[i] = pk_bf16(aq[2 * i] * cq, aq[2 * i + 1] * cq); q1[i] = pk_bf16(aq[8 + 2 * i] * cq, aq[9 + 2 * i] * cq); }
    if (hi) { q1[2] = 0; q1[3] = 0; }
    GAS u64* kvf = (GAS u64*)(ws + FL_KV + (unsigned)((head * 4 + w) * 16 + ((grp + lane) & 15)) * FS);
    const f32x16 zero = {};
    {
        f32x16 s = MFMA_BF(BC8(k0), BC8(q0), zero); s = MFMA_BF(BC8(k1), BC8(q1), s);
        float m = s[0];
#pragma unroll
        for (int i = 1; i < 16; ++i) m = fmaxf(m, s[i]);
        q1 = put_ref(q1, swapmax(m), hi);
    }
    { LAS u32x4* qo = (LAS u32x4*)(lds + LQ + w * 2048) + lane; qo[0] = q0; qo[64] = q1; }
    arrive(sy + SY_Q);
    const u64 kv1 = __hip_atomic_load(kvf, __ATOMIC_RELAXED, __HIP_MEMORY_SCOPE_AGENT); SBAR();
    await(sy + SY_Q, 4);
    __builtin_amdgcn_s_setprio(0);
    bf16x8 Q[4][2];
    Q[0][0] = BC8(q0); Q[0][1] = BC8(q1);
#pragma unroll
    for (int i = 1; i < 4; ++i) { LAS const char* p = lds + LQ + ((w + i) & 3) * 2048 + voff; Q[i][0] = FR(p, 0); Q[i][1] = FR(p, 1024); }
    u32x4 kv[4][4] = {{k0, k1, v0, v1}, {}, {}, {}};
    f32x16 O[4] = {}, sa, sb;
    u32x4 p0, p1 = {};
#define GRP(SC, i, P, j) do { float e0 = EX(SC[i]); SBAR(); float e1 = EX(SC[i + 1]); SBAR(); float e2 = EX(SC[i + 2]); SBAR(); float e3 = EX(SC[i + 3]); SBAR(); \
        P[j] = pk_bf16(e0, e1); SBAR(); P[j + 1] = pk_bf16(e2, e3); PIN(P); SBAR(); } while (0)
#define SLOT(SC, SN, cb, i, QK, PV1, LD) do { constexpr int nb_ = (i) < 3 ? (cb) : ((cb) + 1) % 4, pb_ = (i) > 0 ? (cb) : ((cb) + 3) % 4; \
        if (QK) SN = MFMA_BF(BC8(kv[nb_][0]), Q[((i) + 1) & 3][0], zero); SBAR(); GRP(SC, 0, p0, 0); \
        if (PV1) O[((i) + 3) & 3] = MFMA_BF(BC8(kv[pb_][3]), BC8(p1), O[((i) + 3) & 3]); SBAR(); \
        if (LD) { kv[((cb) + 3) % 4][i] = __builtin_amdgcn_raw_buffer_load_b128(rs, voff + (i) * 1024, kvo, 16); SBAR(); } GRP(SC, 4, p0, 2); \
        if (QK) SN = MFMA_BF(BC8(kv[nb_][1]), Q[((i) + 1) & 3][1], SN); SBAR(); GRP(SC, 8, p1, 0); \
        O[i] = MFMA_BF(BC8(kv[cb][2]), BC8(p0), O[i]); SBAR(); GRP(SC, 12, p1, 2); } while (0)
    const unsigned kvb = WS_KV + (unsigned)(head * 64 + w) * 4096;
    unsigned kvo = 0;
    STAMP(5);
    bool pend = lane > 0 && lane < 16 && kv1 != tag;
#define NEED(msk) do { CFENCE(); _Pragma("unroll 1") for (int i_ = 0; i_ < SPIN; ++i_) { \
        if (pend) pend = __hip_atomic_load(kvf, __ATOMIC_RELAXED, __HIP_MEMORY_SCOPE_AGENT) != tag; \
        miss = (unsigned)__ballot(pend); if (!(miss & (msk))) break; __builtin_amdgcn_s_sleep(2); } CFENCE(); } while (0)
    unsigned miss = (unsigned)__ballot(pend);
    if (miss & KVM) NEED(KVM);
    CFENCE();
    STAMP(6);
#pragma unroll
    for (int j = 1; j < 3; ++j)
#pragma unroll
        for (int f = 0; f < 4; ++f) kv[j][f] = __builtin_amdgcn_raw_buffer_load_b128(rs, voff + f * 1024, kvb + ((grp + j) & 15) * 16384, 16);
    arrive(sy + SY_G);
    sa = MFMA_BF(BC8(k0), Q[0][0], zero); sa = MFMA_BF(BC8(k1), Q[0][1], sa);
    STAMP(7);
    int nx = grp + 3;
#define TILE(cb) do { kvo = kvb + (nx & 15) * 16384; ++nx; SBAR(); \
        SLOT(sa, sb, cb, 0, true, true, true); SLOT(sb, sa, cb, 1, true, true, true); SLOT(sa, sb, cb, 2, true, true, true); SLOT(sb, sa, cb, 3, true, true, true); } while (0)
#pragma unroll 1
    for (unsigned msk = 0x78; msk < 0x80000; msk <<= 4) { if (__builtin_expect(miss & msk, 0)) NEED(msk); TILE(0); TILE(1); TILE(2); TILE(3); }
    O[3] = MFMA_BF(BC8(kv[3][3]), BC8(p1), O[3]);
    STAMP(8);
    f32x16 o = O[0];
    {
#pragma unroll
        for (int i = 1; i < 4; ++i) {
            LAS char* ex = lds + LX + (((w + i) & 3) * 3 + i - 1) * 4096 + voff;
#pragma unroll
            for (int g = 0; g < 4; ++g) *(LAS f32x4*)(ex + g * 1024) = (f32x4){O[i][4 * g], O[i][4 * g + 1], O[i][4 * g + 2], O[i][4 * g + 3]};
        }
        arrive(sy + SY_X); await(sy + SY_X, 4);
        LAS const char* er = lds + LX + w * 3 * 4096 + voff;
#pragma unroll
        for (int i = 0; i < 3; ++i)
#pragma unroll
            for (int g = 0; g < 4; ++g) { const f32x4 r = *(LAS const f32x4*)(er + i * 4096 + g * 1024);
#pragma unroll
                for (int e = 0; e < 4; ++e) o[4 * g + e] += r[e]; }
        f32x2 chk = {0.f, 0.f};
#pragma unroll
        for (int i = 0; i < 8; ++i) chk = pfma((f32x2){o[2 * i], o[2 * i + 1]}, (f32x2){0.f, 0.f}, chk);
        if (__builtin_expect(__any(!(chk[0] + chk[1] == 0.f) || !(o[12] < 1e30f)), 0)) {
            const unsigned hb = WS_KV + (unsigned)head * 64 * 4096 + voff;
            u32x4 qr = __builtin_bit_cast(u32x4, Q[0][1]); if (hi) { qr[2] = 0; qr[3] = 0; }
            float mm = -INFINITY;
            wait_flags<4>(ws, FL_KV + (unsigned)(head * 64 + lane) * FS, tag, true);
#pragma unroll 1
            for (int t = 0; t < 64; ++t) {
                f32x16 s = MFMA_BF(BC8(BLD(hb + t * 4096)), Q[0][0], zero); s = MFMA_BF(BC8(BLD(hb + t * 4096 + 1024)), BC8(qr), s);
#pragma unroll
                for (int i = 0; i < 16; ++i) mm = fmaxf(mm, s[i]);
            }
            qr = put_ref(qr, swapmax(mm), hi);
            o = zero;
#pragma unroll 1
            for (int t = 0; t < 64; ++t) {
                f32x16 s = MFMA_BF(BC8(BLD(hb + t * 4096)), Q[0][0], zero); s = MFMA_BF(BC8(BLD(hb + t * 4096 + 1024)), BC8(qr), s);
#pragma unroll
                for (int i = 0; i < 4; ++i) { p0[i] = pk_bf16(EX(s[2 * i]), EX(s[2 * i + 1])); p1[i] = pk_bf16(EX(s[8 + 2 * i]), EX(s[9 + 2 * i])); }
                o = MFMA_BF(BC8(BLD(hb + t * 4096 + 2048)), BC8(p0), o); o = MFMA_BF(BC8(BLD(hb + t * 4096 + 3072)), BC8(p1), o);
            }
        }
    }
    {
        const auto r = __builtin_amdgcn_permlane32_swap(__float_as_uint(o[12]), __float_as_uint(o[12]), false, false);
        const float rl = __builtin_amdgcn_rcpf(__uint_as_float(r[1]));
        u32x4 o0, o1;
#pragma unroll
        for (int i = 0; i < 4; ++i) { o0[i] = pk_bf16(o[2 * i] * rl, o[2 * i + 1] * rl); o1[i] = pk_bf16(o[8 + 2 * i] * rl, o[9 + 2 * i] * rl); }
        if (hi) { o1[2] = 0; o1[3] = 0; }
        const unsigned fi = (unsigned)((4 * grp + w) * 16 + head);
        BST(o0, WS_O + fi * 2048 + voff); BST(o1, WS_O + fi * 2048 + 1024 + voff);
        STAMP(9);
        drain();
        put_flag(ws, FL_O + fi * FS, tag, lane);
    }
    STAMP(10);
    const int xcd = blockIdx.x & 7, bi = blockIdx.x >> 3;
    if (xcd < 7) {
        wait_flags<2>(ws, FL_BT + (unsigned)(xcd * 128 + w * 32 + r32) * FS, tag, !hi);
        STAMP(11);
        const unsigned bo = WS_BT + (unsigned)((64 * xcd + 16 * w + (lane >> 3)) * S + 64 * bi + (lane & 7) * 8) * 2;
        glds2c(ws, bo, bo + 8 * S * 2 - 1024, lds0 + 65536 + w * 2048);
        drain();
        arrive(sy + SY_T2); await(sy + SY_T2, 4);
        u32x4 o0, o1; transpose_tile(lds + 65536, 2048, lds + 73728 + w * 2304, nullptr, w, lane, o0, o1);
        const __amdgpu_buffer_rsrc_t ro = __builtin_amdgcn_make_buffer_rsrc(out, 0, S * 2 * D * 2, 0x00020000);
        const unsigned oo = (unsigned)((64 * bi + 16 * w + (lane >> 3)) * 2 * D + D + 64 * xcd) * 2 + (lane & 7) * 16;
        __builtin_amdgcn_raw_buffer_store_b128(o0, ro, oo, 0, 16); __builtin_amdgcn_raw_buffer_store_b128(o1, ro, oo + 8 * 2 * D * 2, 0, 16);
    }
    STAMP(12);
}

DI void role_b(const uint16_t* __restrict__ wo, const uint16_t* __restrict__ u, const uint16_t* __restrict__ lar, const uint16_t* __restrict__ aim,
               const uint16_t* __restrict__ cre, const uint16_t* __restrict__ cim, const uint16_t* __restrict__ ldt, const uint16_t* __restrict__ dskip,
               uint16_t* __restrict__ out, char* __restrict__ ws, LAS char* lds, int w, int lane, u64 code0) {
    u64 tag;
    const unsigned lds0 = (unsigned)(uintptr_t)lds;
    LAS unsigned* sy = (LAS unsigned*)(lds + LSY);
    const __amdgpu_buffer_rsrc_t rs = __builtin_amdgcn_make_buffer_rsrc(ws, 0, WS_END, 0x00020000);
    const int b = blockIdx.x, xcd = b & 7, bi = b >> 3, r32 = lane & 31, hi = lane >> 5;
    const unsigned voff = lane * 16;
    LAS char* tab = lds + LB + w * TABW;
    unsigned pf = 0;
#define CODE_PF() asm volatile("global_load_dword %0, %1, %2" : "+v"(pf) : "v"(min((unsigned)((bi * 4 + w) * 3 + (lane < 3 ? lane : 0)), CODE_LINES - 1) * 128), "s"(code0) : "memory")
    const int qt = 8 * xcd + (bi >> 2), ct = 4 * (bi & 3) + w;
    bf16x8 wf[32];
#define LOADWF(i) wf[i] = BC8(__builtin_amdgcn_raw_buffer_load_b128(rs, voff, WS_WO + (unsigned)(((8 * w + (i)) & 31) * 14 + ct) * 1024, 16))
    if (xcd < 7) {
        const int cc = w >> 1, hh = w & 1, pair = 32 * xcd + bi, ch = 2 * pair + cc;
        LAS char* cb = lds + LB + cc * CBW;
        const unsigned po = (unsigned)(ch * NS + 128 * hh) / 2 + lane;
        const unsigned pl = ((const unsigned*)lar)[po], pa = ((const unsigned*)aim)[po], pr = ((const unsigned*)cre)[po], pi = ((const unsigned*)cim)[po];
        const unsigned uo = (unsigned)((64 * bi + 16 * w + (lane >> 3)) * D + 64 * xcd + (lane & 7) * 8) * 2;
        glds2(u, uo, uo + 8 * D * 2 - 1024, lds0 + LB + w * TABW + 4096);
        const u64 e0 = __hip_atomic_load((GAS u64*)ws, __ATOMIC_RELAXED, __HIP_MEMORY_SCOPE_AGENT);
        u32x2 gi[2][3];
#pragma unroll
        for (int ps = 0; ps < 2; ++ps)
#pragma unroll
            for (int i = 0; i < 3; ++i) gi[ps][i] = ((const u32x2*)g_idx.v[128 * hh + 64 * ps + lane])[i];
        if (CODEPF) CODE_PF();
        const unsigned lw = ((const unsigned*)ldt)[pair], dw = ((const unsigned*)dskip)[pair];
        const float dt = rbf(expf(cc ? bf_hi(lw) : bf_lo(lw)));
        drain(); asm volatile("" :: "v"(pf));
        STAMP(1);
        arrive(sy + SY_WO);
        tag = mix(e0);
        const float cm = wave_max(fmaxf(fmaxf(fabsf(bf_lo(pr)), fabsf(bf_hi(pr))), fmaxf(fabsf(bf_lo(pi)), fabsf(bf_hi(pi)))));
        unsigned ce = __float_as_uint(cm) & 0x7f800000u; ce = ce < 0x01000000u ? 0x01000000u : (ce > 0x7e800000u ? 0x7e800000u : ce);
        const float cscale = __uint_as_float(0x7f000000u - ce), oscale = 2.0f * dt * __uint_as_float(ce);
        LAS unsigned* tw = (LAS unsigned*)(tab + lane * 4);
        LAS const char* trd = tab + r32 * TROW + hi * 16;
        f32x16 dacc = {};
        f32x2 z1, z8, z256, x0;
#define ANCH(vl, va, vr, vi) do { const float a2 = (-rbf(expf(vl)) * dt) * 1.4426950408889634f, rev = ((va) * dt) * 0.15915494309189535f; \
            z1 = anchor(a2, -rev, 1.f); z8 = anchor(a2, rev, 8.f); z256 = anchor(a2, -rev, 256.f); x0 = (f32x2){(vr) * cscale, -(vi) * cscale}; \
            PIN(z1); PIN(z8); PIN(z256); SBAR(); } while (0)
#define TW(row) tw[(row) * (TROW / 4)]
#define CHAIN() do { \
            const f32x2 z1s = rot(z1), zs = rot(z256), z8s = rot(z8); \
            f32x2 y[4], X[8]; y[1] = z8; y[2] = cmul(z8, z8, z8s); X[0] = x0; X[1] = cmul(x0, z1, z1s); f32x2 a = cmul(x0, z256, zs); \
            y[3] = cmul(y[2], z8, z8s); const f32x2 z32 = cmul(y[2], y[2], rot(y[2])), z32s = rot(z32); X[2] = cmul(X[1], z1, z1s); y[0] = z32; \
            f32x2 c4 = cmul(X[2], z256, zs); \
            unsigned c[2][7], d0 = pk_f16(X[0]), d1 = pk_f16(X[1]), d2 = pk_f16(y[1]); \
            c[0][0] = pk_f16(y[0]); c[0][1] = 0x3c00u; c[0][2] = pk_f16(y[2]); c[0][3] = pk_f16(y[3]); c[0][4] = pk_f16(X[2]); c[0][5] = pk_f16(a); c[0][6] = pk_f16(c4); \
            TW(0) = d0; TW(1) = d1; TW(22) = d2; SBAR(); \
            _Pragma("unroll") for (int i = 1; i < 9; ++i) { \
                f32x2 t0, t1, t2, t3, t4, t5, t6; const int j = i - 1; unsigned* pc = c[j & 1]; unsigned* nc = c[i & 1]; \
                if (i < 8) { if (i > 1) t0 = y[0].xx * z32; t1 = y[1].xx * z32; t2 = y[2].xx * z32; t3 = y[3].xx * z32; \
                    if (i < 6) t4 = X[i + 1].xx * z1; t5 = a.xx * z256; \
                    if (i == 3) t6 = X[4].xx * z256; if (i == 4 || i == 5) t6 = c4.xx * z256; if (i == 6) t6 = X[6].xx * z256; } SBAR(); \
                if (j != 1) TW(21 + (j ? 4 * j : 4)) = pc[0]; TW(21 + (j ? 4 * j + 1 : 0)) = pc[1]; TW(23 + 4 * j) = pc[2]; TW(24 + 4 * j) = pc[3]; \
                if (j < 6) TW(j + 2) = pc[4]; TW(j == 0 ? 8 : (j == 1 ? 12 : (j == 2 ? 14 : 13 + j))) = pc[5]; \
                if (j == 0) TW(9) = pc[6]; if (j == 3) TW(10) = pc[6]; if (j == 4) TW(13) = pc[6]; if (j == 5) TW(15) = pc[6]; if (j == 6) TW(11) = pc[6]; SBAR(); \
                if (i < 8) { if (i > 1) y[0] = pfma(y[0].yy, z32s, t0); y[1] = pfma(y[1].yy, z32s, t1); y[2] = pfma(y[2].yy, z32s, t2); y[3] = pfma(y[3].yy, z32s, t3); \
                    if (i < 6) X[i + 2] = pfma(X[i + 1].yy, z1s, t4); a = pfma(a.yy, zs, t5); \
                    if (i == 3) c4 = pfma(X[4].yy, zs, t6); if (i == 4 || i == 5) c4 = pfma(c4.yy, zs, t6); if (i == 6) c4 = pfma(X[6].yy, zs, t6); SBAR(); \
                    if (i > 1) nc[0] = pk_f16(y[0]); nc[1] = pk_f16(y[1]); nc[2] = pk_f16(y[2]); nc[3] = pk_f16(y[3]); \
                    if (i < 6) nc[4] = pk_f16(X[i + 2]); nc[5] = pk_f16(a); if (i >= 3 && i <= 6) nc[6] = pk_f16(c4); \
                    asm volatile("" :: "v"(pc[0]), "v"(pc[1]), "v"(pc[2]), "v"(pc[3]), "v"(pc[4]), "v"(pc[5]), "v"(pc[6])); SBAR(); } \
            } } while (0)
        f16x8 fa[8], fb[8];
#define RDT() do { _Pragma("unroll") for (int kk = 0; kk < 8; ++kk) { fa[kk] = *(LAS const f16x8*)(trd + kk * 32); fb[kk] = *(LAS const f16x8*)(trd + 21 * TROW + kk * 32); } SBAR(); } while (0)
#define MMT() do { _Pragma("unroll") for (int kk = 0; kk < 8; ++kk) dacc = MFMA_F16(fa[kk], fb[kk], dacc); } while (0)
        ANCH(bf_lo(pl), bf_lo(pa), bf_lo(pr), bf_lo(pi));
        await(sy + SY_WO, 4);
        if (w == 0 && lane == 0) __hip_atomic_store((LAS u64*)sy, tag, __ATOMIC_RELAXED, __HIP_MEMORY_SCOPE_WORKGROUP);
        {
            u32x4 o0, o1; transpose_tile(lds + LB + 4096, TABW, tab, sy + SY_T1, w, lane, o0, o1);
            const unsigned to = WS_UT + (unsigned)((64 * xcd + 16 * w + (lane >> 3)) * S + 64 * bi) * 2 + (lane & 7) * 16;
            __builtin_amdgcn_s_setprio(3); BST(o0, to); BST(o1, to + 8 * S * 2); __builtin_amdgcn_s_setprio(PRIO_B);
        }
        await(sy + SY_T1, 4);
        STAMP(13);
        CHAIN(); RDT();
        drain(); __builtin_amdgcn_s_setprio(3); put_flag(ws, FL_UT + (unsigned)(xcd * 128 + w * 32 + bi) * FS, tag, lane); __builtin_amdgcn_s_setprio(PRIO_B);
        STAMP(14);
        ANCH(bf_hi(pl), bf_hi(pa), bf_hi(pr), bf_hi(pi));
        MMT(); SBAR();
        CHAIN(); RDT();
        const unsigned utf = hi ? FL_WO + (unsigned)(w * 32 + r32) * FS : FL_UT + (unsigned)(xcd * 128 + (bi >> 3) * 32 + r32) * FS;
        const u64 utv = __hip_atomic_load((GAS u64*)(ws + utf), __ATOMIC_RELAXED, __HIP_MEMORY_SCOPE_AGENT); SBAR();
        MMT();
        STAMP(2);
        {
            LAS float* pt = (LAS float*)tab;
            *(LAS f32x4*)(pt + 8 * r32 + 4 * hi) = (f32x4){dacc[0] * oscale, dacc[1] * oscale, dacc[2] * oscale, dacc[3] * oscale};
            *(LAS f32x2*)(pt + (hi ? 384 + 2 * r32 : 256 + 4 * r32)) = (f32x2){dacc[4] * oscale, dacc[5] * oscale};
            *(LAS f32x2*)(pt + (hi ? 448 + 2 * r32 : 258 + 4 * r32)) = (f32x2){dacc[6] * oscale, dacc[7] * oscale};
            pt[hi ? 640 + r32 : 512 + r32] = dacc[8] * oscale;
            pt[hi ? 672 + r32 : 544 + r32] = dacc[9] * oscale;
            pt[hi ? 672 + r32 : 576 + r32] = dacc[10] * oscale;
            pt[hi ? 672 + r32 : 608 + r32] = dacc[11] * oscale;
        }
        LAS unsigned* ps_ = sy + SY_P + 5 * cc;
        arrive(ps_ + 0);
        LAS char* ui = cb + UOF;
        LAS char* kt = cb + KOF;
        await(ps_ + 0, 2);
#pragma unroll
        for (int ps = 0; ps < 2; ++ps) {
            const int v = 81 * hh + 64 * ps + lane;
            if (ps == 0 || lane < 17) {
                const f32x4 p0 = *(LAS const f32x4*)(cb + v * 16), p1 = *(LAS const f32x4*)(cb + TABW + v * 16);
                u32x2 r = {pk_bf16(p0[0] + p1[0], p0[1] + p1[1]), pk_bf16(p0[2] + p1[2], p0[3] + p1[3])};
                if (v == 161) r = (u32x2){0u, 0u};
                *(LAS u32x2*)(kt + v * 8) = r;
            }
        }
        arrive(ps_ + 1);
        if (__any(utv != tag)) wait_flags<8>(ws, utf, tag, utv != tag);
        CFENCE();
        const unsigned ub = WS_UT + (unsigned)(ch * S + 1024 * hh) * 2 + voff;
        const u32x4 u0 = BLD(ub), u1 = BLD(ub + 1024); SBAR();
        await(ps_ + 1, 2);
        STAMP(3);
        __builtin_amdgcn_s_setprio(PRIO_B2);
        const u32x4 z4 = {};
        {
            unsigned W[2][6];
#pragma unroll
            for (int ps = 0; ps < 2; ++ps)
#pragma unroll
                for (int i = 0; i < 6; ++i) { const unsigned gv = gi[ps][i >> 1][i & 1];
                    W[ps][i] = *(LAS const uint16_t*)(kt + (gv & 0xffffu));
                    if (i < 5) W[ps][i] |= (unsigned)*(LAS const uint16_t*)(kt + (gv >> 16)) << 16; }
            *(LAS u32x4*)(ui + (64 * hh + lane) * 16) = z4;
            if (lane < 16) *(LAS u32x4*)(ui + (128 + 16 * hh + lane) * 16) = z4;
            if (lane < 8) *(LAS u32x4*)(cb + (2 * hh + (lane >> 2)) * CPY + (256 + (lane & 3)) * 16) = z4;
#pragma unroll
            for (int ps = 0; ps < 2; ++ps) {
                unsigned V[5];
#pragma unroll
                for (int i = 0; i < 5; ++i) V[i] = __builtin_amdgcn_alignbit(W[ps][i + 1], W[ps][i], 16);
                LAS char* cp = cb + (128 * hh + 64 * ps + lane) * 16;
                *(LAS u32x4*)(cp) = (u32x4){W[ps][0], W[ps][1], W[ps][2], W[ps][3]};
                *(LAS u32x4*)(cp + CPY) = (u32x4){V[0], V[1], V[2], V[3]};
                *(LAS u32x4*)(cp + 2 * CPY) = (u32x4){W[ps][1], W[ps][2], W[ps][3], W[ps][4]};
                *(LAS u32x4*)(cp + 3 * CPY) = (u32x4){V[1], V[2], V[3], V[4]};
            }
        }
        *(LAS u32x4*)(ui + (32 + 32 * hh + (lane >> 2)) * 80 + (lane & 3) * 16) = u0;
        *(LAS u32x4*)(ui + (48 + 32 * hh + (lane >> 2)) * 80 + (lane & 3) * 16) = u1;
        arrive(ps_ + 2); await(ps_ + 2, 2);
        STAMP(4);
        __builtin_amdgcn_s_setprio(PRIO_BC);
        f32x16 a0 = {}, a1 = {};
        {
            const int sb = 7 - (r32 & 7);
            LAS const char* pa = cb + (sb & 3) * CPY + (255 - (r32 >> 3) + hi) * 16 + (sb >> 2) * 8 - 64 * (16 * hh + 15) - 2048;
            LAS const char* pa2 = pa + 8; asm volatile("" : "+v"(pa2));
            LAS const char* pb = ui + (32 + r32 - 16 * hh - 15) * 80 + 16 * hi;
            s16x4 ta[2][2], tb[2][2]; bf16x8 b0[2], b1[2];
#define T4(q, off) (*(LAS const s16x4*)((q) + (off)))
#define CSTEP(i, j, RD) do { const int o_ = 64 * (15 - ((j) >> 1)) + 32 * ((j) & 1), u_ = 80 * (15 - ((j) >> 1)) + 32 * ((j) & 1); \
                a1 = MFMA_BF(cat8(ta[i][0], ta[i][1]), b1[i], a1); SBAR(); if (RD) { ta[1 - i][0] = T4(pa, o_ + 2048); ta[1 - i][1] = T4(pa2, o_ + 2048); } SBAR(); \
                a0 = MFMA_BF(cat8(ta[i][0], ta[i][1]), b0[i], a0); SBAR(); if (RD) { b1[1 - i] = FR(pb, u_ + 2560); b0[1 - i] = FR(pb, u_); } SBAR(); \
                a1 = MFMA_BF(cat8(tb[i][0], tb[i][1]), b0[i], a1); SBAR(); if (RD) { tb[1 - i][0] = T4(pa, o_); tb[1 - i][1] = T4(pa2, o_); } SBAR(); } while (0)
            ta[0][0] = T4(pa, 960 + 2048); ta[0][1] = T4(pa2, 960 + 2048); b1[0] = FR(pb, 1200 + 2560); b0[0] = FR(pb, 1200); tb[0][0] = T4(pa, 960); tb[0][1] = T4(pa2, 960);
#pragma unroll
            for (int j = 0; j < 32; ++j) {
                if (j == 31) arrive(ps_ + 3);
                CSTEP(j & 1, j + 1, j < 31);
                if (ct < 14) LOADWF(j);
                SBAR();
            }
        }
        STAMP(5);
        __builtin_amdgcn_s_setprio(PRIO_B3);
        {
            u32x2 uu[4];
#pragma unroll
            for (int g = 0; g < 4; ++g) uu[g] = *(LAS const u32x2*)(ui + (32 + 32 * hh + r32) * 80 + 16 * g + 8 * hi);
            LAS char* ex = cb + r32 * 144 + hi * 16;
            const f32x16 snd = hh ? a0 : a1; const f32x16 y = hh ? a1 : a0;
            await(ps_ + 3, 2);
#pragma unroll
            for (int g = 0; g < 4; ++g) *(LAS f32x4*)(ex + hh * 4608 + g * 32) = (f32x4){snd[4 * g], snd[4 * g + 1], snd[4 * g + 2], snd[4 * g + 3]};
            arrive(ps_ + 4);
            const float dsk = cc ? bf_hi(dw) : bf_lo(dw);
            unsigned sk[8];
#pragma unroll
            for (int g = 0; g < 4; ++g)
#pragma unroll
                for (int e = 0; e < 2; ++e) sk[2 * g + e] = pk_bf16(bf_lo(uu[g][e]) * dsk, bf_hi(uu[g][e]) * dsk);
            await(ps_ + 4, 2);
            LAS char* sc = cb + 9216 + hh * 2048;
#pragma unroll
            for (int g = 0; g < 4; ++g) {
                const f32x4 p = *(LAS const f32x4*)(ex + (1 - hh) * 4608 + g * 32);
                u32x2 r;
#pragma unroll
                for (int e = 0; e < 2; ++e) {
                    const unsigned yb = pk_bf16(y[4 * g + 2 * e] + p[2 * e], y[4 * g + 2 * e + 1] + p[2 * e + 1]);
                    r[e] = pk_bf16(bf_lo(yb) + bf_lo(sk[2 * g + e]), bf_hi(yb) + bf_hi(sk[2 * g + e]));
                }
                *(LAS u32x2*)(sc + 64 * r32 + 16 * g + 8 * hi) = r;
            }
            const u32x4 o0 = *(LAS const u32x4*)(sc + voff), o1 = *(LAS const u32x4*)(sc + 1024 + voff);
            const unsigned bo = WS_BT + (unsigned)(ch * S + 1024 * hh) * 2 + voff;
            BST(o0, bo); BST(o1, bo + 1024);
        }
        arrive(sy + SY_BG);
        STAMP(6);
        drain();
        put_flag(ws, FL_BT + (unsigned)(xcd * 128 + bi * 4 + w) * FS, tag, lane);
    } else {
        {
            const unsigned nb = (bi & 1) ? 12 * 2 * D : 16 * 2 * D;
            unsigned v4[4];
#pragma unroll
            for (int i = 0; i < 4; ++i) { const unsigned o = (unsigned)(w * 4096 + i * 1024) + voff; v4[i] = (o < nb ? o : 0u) + 4096 - i * 1024; }
            glds4((const char*)(wo + (DH * (bi >> 1) + 16 * (bi & 1)) * D) - 4096, v4[0], v4[1], v4[2], v4[3], lds0 + LB + w * 4096);
        }
        const u64 e0 = __hip_atomic_load((GAS u64*)ws, __ATOMIC_RELAXED, __HIP_MEMORY_SCOPE_AGENT);
        if (CODEPF) CODE_PF();
        drain(); asm volatile("" :: "v"(pf));
        tag = mix(e0);
        arrive(sy + SY_WO); await(sy + SY_WO, 4);
        if (w == 0 && lane == 0) __hip_atomic_store((LAS u64*)sy, tag, __ATOMIC_RELAXED, __HIP_MEMORY_SCOPE_WORKGROUP);
        LAS const char* p = lds + LB + (4 * hi + ((lane & 15) >> 2)) * (2 * D) + ((lane >> 4) & 1) * 32 + (lane & 3) * 8;
#pragma unroll
        for (int i = 0; i < 4; ++i) {
            const int ct = w + 4 * i;
            if (ct < 14) {
                u32x4 o = __builtin_bit_cast(u32x4, cat8(trread(p + ct * 64), trread(p + 8 * 2 * D + ct * 64)));
                if (hi && (bi & 1)) { o[2] = 0; o[3] = 0; }
                BST(o, WS_WO + (unsigned)(bi * 14 + ct) * 1024 + voff);
            }
        }
        drain();
        put_flag(ws, FL_WO + (unsigned)(w * 32 + bi) * FS, tag, lane);
        __builtin_amdgcn_s_setprio(0);
        arrive(sy + SY_BG);
        await_slow(sy + SY_G, 4);
        wait_flags<8>(ws, FL_WO + (unsigned)(w * 32 + r32) * FS, tag, !hi);
        if (ct < 14) {
#pragma unroll
            for (int i = 0; i < 32; ++i) { LOADWF(i); __builtin_amdgcn_s_sleep(WOSL); SBAR(); }
        }
    }
    STAMP(7);
    const __amdgpu_buffer_rsrc_t ro = __builtin_amdgcn_make_buffer_rsrc(out, 0, S * 2 * D * 2, 0x00020000);
    await(sy + SY_BG, 4);
    STAMP(8);
    await_slow(sy + SY_X, 4);
    wait_flags<OSL>(ws, FL_O + (unsigned)(qt * 16 + 4 * w + lane) * FS, tag, lane < 4);
    STAMP(9);
    u32x4 of[8];
#pragma unroll
    for (int i = 0; i < 8; ++i) of[i] = BLD(WS_O + (unsigned)(qt * 32 + 8 * w + i) * 1024 + voff);
#pragma unroll
    for (int i = 0; i < 8; ++i) *(LAS u32x4*)(lds + LB + (8 * w + i) * 1024 + voff) = of[i];
    arrive(sy + SY_B2);
    STAMP(10);
    f32x16 acc = {};
    if (ct < 14) {
#pragma unroll
        for (int i = 0; i < 8; ++i) acc = MFMA_BF(wf[i], BC8(of[i]), acc);
    }
    SBAR();
    await(sy + SY_B2, 4);
    STAMP(11);
    if (ct < 14) {
        LAS const char* po[3];
#pragma unroll
        for (int j = 0; j < 3; ++j) po[j] = lds + LB + ((w + 1 + j) & 3) * 8192 + voff;
        bf16x8 og[4];
#pragma unroll
        for (int i = 0; i < 3; ++i) og[i] = FR(po[0], i * 1024);
#pragma unroll
        for (int i = 0; i < 24; ++i) { if (i < 21) og[(i + 3) & 3] = FR(po[(i + 3) >> 3], ((i + 3) & 7) * 1024); SBAR(); acc = MFMA_BF(wf[8 + i], og[i & 3], acc); SBAR(); }
        LAS char* sc = lds + LB + 40960 + w * 2304;
#pragma unroll
        for (int g = 0; g < 4; ++g) *(LAS u32x2*)(sc + r32 * 72 + 16 * g + 8 * hi) = (u32x2){pk_bf16(acc[4 * g], acc[4 * g + 1]), pk_bf16(acc[4 * g + 2], acc[4 * g + 3])};
        LAS const char* sr = sc + (lane >> 2) * 72 + (lane & 3) * 16;
        const u32x2 a0 = *(LAS const u32x2*)sr, a1 = *(LAS const u32x2*)(sr + 8), b0 = *(LAS const u32x2*)(sr + 16 * 72), b1 = *(LAS const u32x2*)(sr + 16 * 72 + 8);
        const unsigned oo = (unsigned)((32 * qt + (lane >> 2)) * 2 * D + 32 * ct) * 2 + (lane & 3) * 16;
        __builtin_amdgcn_raw_buffer_store_b128((u32x4){a0[0], a0[1], a1[0], a1[1]}, ro, oo, 0, 16);
        __builtin_amdgcn_raw_buffer_store_b128((u32x4){b0[0], b0[1], b1[0], b1[1]}, ro, oo + 16 * 2 * D * 2, 0, 16);
    }
    if (b == 0 && w == 0) put_flag(ws, 0, tag, lane);
    STAMP(12);
}

__global__ __launch_bounds__(512, 2) void fused(const uint16_t* __restrict__ x, const uint16_t* __restrict__ wqkv, const uint16_t* __restrict__ wo,
                                               const uint16_t* __restrict__ u, const uint16_t* __restrict__ lar, const uint16_t* __restrict__ aim,
                                               const uint16_t* __restrict__ cre, const uint16_t* __restrict__ cim, const uint16_t* __restrict__ ldt,
                                               const uint16_t* __restrict__ dskip, uint16_t* __restrict__ out, char* __restrict__ ws) {
    extern __shared__ __attribute__((aligned(16))) char smem[];
    LAS char* lds = (LAS char*)smem;
    const int lane = threadIdx.x & 63, w = __builtin_amdgcn_readfirstlane(threadIdx.x >> 6);
    STAMP(0);
    const u64 code0 = __builtin_amdgcn_s_getpc() & ~255ull;
    if (w >= 4) __builtin_amdgcn_s_setprio(3);
    if (w == 4) ((LAS unsigned*)(lds + LSY))[lane] = 0;
    asm volatile("" :: "s"(x), "s"(wqkv), "s"(wo), "s"(u), "s"(lar), "s"(aim), "s"(cre), "s"(cim), "s"(ldt), "s"(dskip), "s"(out), "s"(ws));
    unsigned tc = 0;
    if (w < 4) asm volatile("global_load_dword %0, %1, %2\n\tglobal_load_dword %0, %1, %3" : "+v"(tc) : "v"(lane * 4), "s"(x + (blockIdx.x >> 4) * 128 * D), "s"(wqkv) : "memory");
    asm volatile("s_waitcnt lgkmcnt(0)\n\ts_barrier" ::: "memory");
    if (w < 4) role_a(x, wqkv, out, ws, lds, w, lane, tc);
    else role_b(wo, u, lar, aim, cre, cim, ldt, dskip, out, ws, lds, w - 4, lane, code0);
}
}

extern "C" void kernel_launch(void* const* d_in, const int* in_sizes, int n_in, void* d_out, int out_size, void* d_ws, size_t ws_size, hipStream_t stream) {
    using namespace k;
    (void)hipFuncSetAttribute((const void*)fused, hipFuncAttributeMaxDynamicSharedMemorySize, LDS_BYTES);
    const uint16_t* const* in = (const uint16_t* const*)d_in;
    fused<<<256, 512, LDS_BYTES, stream>>>(in[0], in[1], in[2], in[3], in[4], in[5], in[6], in[7], in[8], in[9], (uint16_t*)d_out, (char*)d_ws);
}
```

```cpp
#include <hip/hip_runtime.h>
#include <hip/hip_bf16.h>
#include <stdint.h>

typedef short bf16x8 __attribute__((ext_vector_type(8)));
typedef _Float16 f16x8 __attribute__((ext_vector_type(8)));
typedef _Float16 f16x2 __attribute__((ext_vector_type(2)));
typedef __bf16 bf16x2 __attribute__((ext_vector_type(2)));
typedef short s16x4 __attribute__((ext_vector_type(4)));
typedef float f32x16 __attribute__((ext_vector_type(16)));
typedef float f32x4 __attribute__((ext_vector_type(4)));
typedef float f32x2 __attribute__((ext_vector_type(2)));
typedef unsigned u32x4 __attribute__((ext_vector_type(4)));
typedef unsigned u32x2 __attribute__((ext_vector_type(2)));
typedef unsigned long long u64;
#define LAS __attribute__((address_space(3)))
#define GAS __attribute__((address_space(1)))
#define DI __device__ __forceinline__
#define MFMA_BF(a, b, c) __builtin_amdgcn_mfma_f32_32x32x16_bf16((a), (b), (c), 0, 0, 0)
#define MFMA_F16(a, b, c) __builtin_amdgcn_mfma_f32_32x32x16_f16((a), (b), (c), 0, 0, 0)
#define SBAR() __builtin_amdgcn_sched_barrier(0)
#define CFENCE() asm volatile("" ::: "memory")
#define PIN(x) asm volatile("" : "+v"(x))
#define BC8(x) __builtin_bit_cast(bf16x8, (x))
#define EX(x) __builtin_amdgcn_exp2f(x)

namespace k {
constexpr int S = 2048, D = 448, DH = 28, NS = 256;
constexpr unsigned FS = 128;
constexpr unsigned FL_KV = FS, FL_O = FL_KV + 1024 * FS, FL_UT = FL_O + 1024 * FS, FL_BT = FL_UT + 896 * FS, FL_WO = FL_BT + 896 * FS;
constexpr unsigned WS_KV = 512u << 10;
constexpr unsigned WS_O = WS_KV + (4u << 20);
constexpr unsigned WS_WO = WS_O + (2u << 20);
constexpr unsigned WS_UT = WS_WO + (448u << 10);
constexpr unsigned WS_BT = WS_UT + D * S * 2;
constexpr unsigned WS_END = WS_BT + D * S * 2;
constexpr int WSL = 8192, LWQ = 4 * WSL, LXR = LWQ + 16384, XSL = 16384, LB = LXR + 3 * XSL;
constexpr int LQ = LXR, LX = 0;
constexpr int TROW = 272, TABW = 53 * TROW, CBW = 2 * TABW, CPY = 4160, UOF = 4 * CPY, KOF = CBW - 1312;
constexpr int LSY = LB + 2 * CBW, LDS_BYTES = LSY + 256;
static_assert(LDS_BYTES <= 163840 && (LSY & 15) == 0 && UOF + 7680 <= KOF, "lds");
enum { SY_F = 2, SY_P = 10, SY_WO = 20, SY_T1 = 21, SY_BG = 22, SY_B2 = 23, SY_Q = 24, SY_X = 25, SY_G = 26, SY_T2 = 27 };
constexpr int SPIN = 1 << 18;
constexpr unsigned CODE_LINES = 264;

struct IdxTab { uint16_t v[256][12]; };
constexpr IdxTab make_idx() {
    IdxTab t = {};
    for (int y = 0; y < 256; ++y) for (int e = 0; e < 12; ++e) {
        const int j = 2047 - 8 * y - e; int ix = 644;
        if (j >= 0) { const int sh = j >= 1024 ? 3 : (j >= 512 ? 2 : (j >= 256 ? 1 : 0));
            ix = sh ? 128 * sh + ((j + (1 << (sh - 1)) - 1 + ((j >> sh) & 1)) >> sh) : j; }
        t.v[y][e] = (uint16_t)(2 * ix);
    }
    return t;
}
__device__ const IdxTab g_idx __attribute__((aligned(16))) = make_idx();

DI unsigned pk_bf16(float a, float b) { f32x2 v = {a, b}; return __builtin_bit_cast(unsigned, __builtin_convertvector(v, bf16x2)); }
DI unsigned pk_f16(f32x2 v) { return __builtin_bit_cast(unsigned, __builtin_convertvector(v, f16x2)); }
DI float bf_lo(unsigned w) { return __uint_as_float(w << 16); }
DI float bf_hi(unsigned w) { return __uint_as_float(w & 0xffff0000u); }
DI float rbf(float x) { return bf_lo(pk_bf16(x, x)); }
#define GL0 "s_mov_b32 %0, m0\n\ts_mov_b32 m0, %2\n\ts_nop 0\n\t"
#define GL1(V, O, C) "global_load_lds_dwordx4 " V ", %1 offset:" #O C "\n\t"
#define GLE "s_mov_b32 m0, %0"
DI void glds4(const void* sb, unsigned v0, unsigned v1, unsigned v2, unsigned v3, unsigned lb) { unsigned sv;
    asm volatile(GL0 GL1("%3", 0, "") GL1("%4", 1024, "") GL1("%5", 2048, "") GL1("%6", 3072, "") GLE : "=&s"(sv) : "s"(sb), "s"(lb), "v"(v0), "v"(v1), "v"(v2), "v"(v3) : "memory"); }
DI void glds2(const void* sb, unsigned v0, unsigned v1, unsigned lb) { unsigned sv;
    asm volatile(GL0 GL1("%3", 0, "") GL1("%4", 1024, "") GLE : "=&s"(sv) : "s"(sb), "s"(lb), "v"(v0), "v"(v1) : "memory"); }
DI void glds1(const void* sb, unsigned v0, unsigned lb) { unsigned sv;
    asm volatile(GL0 GL1("%3", 0, "") GLE : "=&s"(sv) : "s"(sb), "s"(lb), "v"(v0) : "memory"); }
DI float wave_max(float v) {
#define DPPMAX(ctrl, rm) v = fmaxf(v, __uint_as_float(__builtin_amdgcn_update_dpp(0, __float_as_uint(v), ctrl, rm, 0xf, true)))
    DPPMAX(0x111, 0xf); DPPMAX(0x112, 0xf); DPPMAX(0x114, 0xf); DPPMAX(0x118, 0xf); DPPMAX(0x142, 0xa); DPPMAX(0x143, 0xc);
    return __uint_as_float(__builtin_amdgcn_readlane(__float_as_uint(v), 63));
}
DI s16x4 trread(LAS const char* p) { return __builtin_bit_cast(s16x4, __builtin_amdgcn_ds_read_tr16_b64_v4i16((LAS s16x4*)p)); }
DI bf16x8 cat8(s16x4 a, s16x4 b) { return __builtin_shufflevector(a, b, 0, 1, 2, 3, 4, 5, 6, 7); }
DI f32x2 pfma(f32x2 a, f32x2 b, f32x2 c) { return __builtin_elementwise_fma(a, b, c); }
DI f32x2 cmul(f32x2 a, f32x2 b, f32x2 bs) { return pfma(a.yy, bs, a.xx * b); }
DI f32x2 rot(f32x2 b) { return (f32x2){-b.y, b.x}; }
DI f32x2 anchor(float a2, float rev, float s) {
    const float mag = EX(a2 * s);
    float r = rev * s; r -= __builtin_rintf(r);
    return (f32x2){mag * __builtin_amdgcn_cosf(r), mag * __builtin_amdgcn_sinf(r)};
}
DI void arrive(LAS unsigned* c) { asm volatile("s_mov_b64 exec, 1\n\tds_add_u32 %0, %1\n\ts_mov_b64 exec, -1" :: "v"((unsigned)(uintptr_t)c), "v"(1u) : "memory"); }
DI void await(LAS unsigned* c, unsigned n) {
    unsigned t, i = SPIN;
    asm volatile("1:\n\tds_read_b32 %0, %2\n\ts_waitcnt lgkmcnt(0)\n\tv_cmp_gt_u32 vcc, %3, %0\n\ts_cbranch_vccz 2f\n\ts_sleep 1\n\ts_sub_u32 %1, %1, 1\n\ts_cbranch_scc0 1b\n2:"
                 : "=&v"(t), "+s"(i) : "v"((unsigned)(uintptr_t)c), "s"(n) : "vcc", "scc", "memory");
}
DI void await_slow(LAS unsigned* c, unsigned n) {
    unsigned t, i = SPIN;
    asm volatile("1:\n\tds_read_b32 %0, %2\n\ts_waitcnt lgkmcnt(0)\n\tv_cmp_gt_u32 vcc, %3, %0\n\ts_cbranch_vccz 2f\n\ts_sleep 3\n\ts_sub_u32 %1, %1, 1\n\ts_cbranch_scc0 1b\n2:"
                 : "=&v"(t), "+s"(i) : "v"((unsigned)(uintptr_t)c), "s"(n) : "vcc", "scc", "memory");
}
DI u64 get_tag(LAS unsigned* sy) {
    await(sy, 1);
    const u64 v = __hip_atomic_load((LAS u64*)sy, __ATOMIC_RELAXED, __HIP_MEMORY_SCOPE_WORKGROUP);
    return ((u64)(unsigned)__builtin_amdgcn_readfirstlane((unsigned)(v >> 32)) << 32) | (unsigned)__builtin_amdgcn_readfirstlane((unsigned)v);
}
DI void set_tag(LAS unsigned* sy, u64 tag) { asm volatile("s_mov_b64 exec, 1\n\tds_write_b64 %0, %1\n\ts_mov_b64 exec, -1" :: "v"((unsigned)(uintptr_t)sy), "v"(tag) : "memory"); }
DI void drain() { asm volatile("s_waitcnt vmcnt(0)" ::: "memory"); }
DI void put_flag(char* ws, unsigned off, u64 tag) {
    asm volatile("s_mov_b64 exec, 1\n\tglobal_store_dwordx2 %0, %1, %2 sc1\n\ts_mov_b64 exec, -1" :: "v"(off), "v"(tag), "s"(ws) : "memory");
}
template <int SLP> DI u64 wait_flags(char* ws, unsigned off, u64 tag, u64 pend, u64 need) {
    u64 t, left; unsigned i = SPIN;
    asm volatile("s_mov_b64 exec, %5\n1:\n\tglobal_load_dwordx2 %0, %3, %4 sc1\n\ts_waitcnt vmcnt(0)\n\tv_cmpx_ne_u64 vcc, %6, %0\n\ts_and_b64 %2, exec, %7\n\t"
                 "s_cbranch_scc0 2f\n\ts_sleep %8\n\ts_sub_u32 %1, %1, 1\n\ts_cbranch_scc0 1b\n2:\n\ts_mov_b64 %2, exec\n\ts_mov_b64 exec, -1"
                 : "=&v"(t), "+s"(i), "=&s"(left) : "v"(off), "s"(ws), "s"(pend), "s"(tag), "s"(need), "i"(SLP) : "vcc", "scc", "memory");
    return left;
}
#define BST(v, off) __builtin_amdgcn_raw_buffer_store_b128((v), rs, (off), 0, 16)
#define BLD(off) __builtin_amdgcn_raw_buffer_load_b128(rs, (off), 0, 16)
#define FR(p, off) (*(LAS const bf16x8*)((p) + (off)))
DI float swapmax(float m) { const auto r = __builtin_amdgcn_permlane32_swap(__float_as_uint(m), __float_as_uint(m), false, false); return fmaxf(__uint_as_float(r[0]), __uint_as_float(r[1])); }
DI u32x4 put_ref(u32x4 q1, float m, int hi) {
    const float m1 = rbf(m), m2 = rbf(m - m1), m3 = m - m1 - m2;
    if (hi) { q1[2] = pk_bf16(-m1, -m2); q1[3] = pk_bf16(-m3, 0.f); }
    return q1;
}
DI u64 mix(u64 v) {
    const u64 e = ((u64)(unsigned)__builtin_amdgcn_readfirstlane((unsigned)(v >> 32)) << 32) | (unsigned)__builtin_amdgcn_readfirstlane((unsigned)v);
    u64 z = e + 0x9E3779B97F4A7C15ull; z = (z ^ (z >> 30)) * 0xBF58476D1CE4E5B9ull; z = (z ^ (z >> 27)) * 0x94D049BB133111EBull; return (z ^ (z >> 31)) | 1ull; }
DI void transpose_tile(LAS const char* stage, int pst, LAS char* scratch, LAS unsigned* done, int w, int lane, u32x4& o0, u32x4& o1) {
    LAS const char* p = stage + (lane >> 4) * pst + ((lane & 15) >> 2) * 128 + w * 32 + (lane & 3) * 8;
    const bf16x8 t0 = cat8(trread(p), trread(p + 512)), t1 = cat8(trread(p + 1024), trread(p + 1536));
    if (done) arrive(done);
    LAS char* sc = scratch + (lane & 15) * 144 + (lane >> 4) * 32;
    *(LAS bf16x8*)sc = t0; *(LAS bf16x8*)(sc + 16) = t1;
    LAS const char* sr = scratch + (lane >> 3) * 144 + (lane & 7) * 16;
    o0 = *(LAS const u32x4*)sr; o1 = *(LAS const u32x4*)(sr + 8 * 144);
}

DI void role_a(const uint16_t* __restrict__ x, const uint16_t* __restrict__ wqkv, uint16_t* __restrict__ out, char* __restrict__ ws, LAS char* lds, int w, int lane) {
    const int head = 2 * (blockIdx.x & 7) + ((blockIdx.x >> 3) & 1), grp = blockIdx.x >> 4;
    const unsigned lds0 = (unsigned)(uintptr_t)lds;
    const int r32 = lane & 31, hi = lane >> 5;
    const int wsh = (head & 7) == 7 ? 4 : 0;
    const unsigned wv = (unsigned)((lane >> 2) * 3 * D + (lane & 3) * 8) * 2;
    const char* wb = (const char*)(wqkv + DH * head - wsh + (16 * w) * 3 * D);
    const unsigned xv0 = (unsigned)((lane >> 3) * D + (((lane & 7) ^ (lane >> 4)) * 8)) * 2, xv1 = xv0 ^ 64;
    const char* xb = (const char*)(x + (128 * grp + 32 * w) * D);
    const int rho = ((blockIdx.x >> 3) + ((blockIdx.x >> 1) & 3)) % 7;
#define SP(st) ((st) + rho >= 7 ? (st) + rho - 7 : (st) + rho)
#define WQO(st) ((st) < 4 ? LWQ + (st) * 4096 : ((st) < 6 ? 3 * WSL + ((st) - 4) * 4096 : 0))
#define LOADW(st) glds2(wb + SP(st) * (64 * 3 * D * 2), wv + 2 * D, wv + 4 * D - 1024, lds0 + ((st) & 3) * WSL + w * 2048)
#define LOADQ(st) glds1(wb + SP(st) * (64 * 3 * D * 2), wv, lds0 + WQO(st) + w * 1024)
#define LOADX(st) glds4(xb + SP(st) * 128, xv0, xv1 + 16 * D - 1024, xv0 + 32 * D - 2048, xv1 + 48 * D - 3072, lds0 + LXR + ((st) % 3) * XSL + w * 4096)
    LOADW(0); LOADX(0);
    LOADW(1); LOADX(1);
    LAS unsigned* sy = (LAS unsigned*)(lds + LSY);
    const __amdgpu_buffer_rsrc_t rs = __builtin_amdgcn_make_buffer_rsrc(ws, 0, WS_END, 0x00020000);
    const int xs = (r32 >> 1) & 7;
    LAS const char* xp[4];
#pragma unroll
    for (int ks = 0; ks < 4; ++ks) xp[ks] = lds + LXR + w * 4096 + r32 * 128 + (((2 * ks + hi) ^ xs) * 16);
    LAS const char* wp = lds + (8 * hi + ((lane & 15) >> 2)) * 64 + ((lane >> 4) & 1) * 32 + (lane & 3) * 8 + wsh * 2;
    f32x16 aq = {}, ak = {}, av = {};
    bf16x8 xf[7][4]; s16x4 fr[4][4];
#define RX(st, ks) xf[st][ks] = FR(xp[ks], ((st) % 3) * XSL)
#define RW(st, ks, j) fr[ks][j] = trread(wp + ((st) & 3) * WSL + (ks) * 2048 + ((j) >> 1) * 1024 + ((j) & 1) * 256)
#define MK(st, ks) ak = MFMA_BF(cat8(fr[ks][0], fr[ks][1]), xf[st][ks], ak); SBAR()
#define MV(st, ks) av = MFMA_BF(xf[st][ks], cat8(fr[ks][2], fr[ks][3]), av); SBAR()
#define QSTEP(st, ks) MK(st, ks); RW(st, ks + 1, 0); RW(st, ks + 1, 1); SBAR(); MV(st, ks); RW(st, ks + 1, 2); RW(st, ks + 1, 3); SBAR()
    u64 tag = 0;
    asm volatile("s_waitcnt vmcnt(6)" ::: "memory");
    arrive(sy + SY_F); LOADW(2); LOADX(2); await(sy + SY_F, 4);
    RX(0, 0); RW(0, 0, 0); RW(0, 0, 1); RW(0, 0, 2); RW(0, 0, 3); RX(0, 1); RX(0, 2); RX(0, 3); SBAR();
#pragma unroll
    for (int st = 0; st < 7; ++st) {
        QSTEP(st, 0); if (st < 4) LOADW(st + 3); if (st == 4) { LOADQ(4); LOADQ(5); } if (st == 5) LOADQ(6);
        QSTEP(st, 1); if (st < 4) LOADX(st + 3);
        QSTEP(st, 2); if (st == 3) { LOADQ(0); LOADQ(1); LOADQ(2); LOADQ(3); }
        if (st < 6) {
            if (st == 3) asm volatile("s_waitcnt vmcnt(16)" ::: "memory"); else if (st == 5) asm volatile("s_waitcnt vmcnt(7)" ::: "memory"); else asm volatile("s_waitcnt vmcnt(12)" ::: "memory");
            arrive(sy + SY_F + 1 + st); SBAR();
        }
        MK(st, 3);
        if (st < 6) { await(sy + SY_F + 1 + st, 4); RX(st + 1, 0); RW(st + 1, 0, 0); RW(st + 1, 0, 1); RW(st + 1, 0, 2); RW(st + 1, 0, 3); } SBAR();
        MV(st, 3); if (st < 6) { RX(st + 1, 1); RX(st + 1, 2); RX(st + 1, 3); } SBAR();
        if (st == 2) tag = get_tag(sy);
    }
    const float cq = 0.18898223650461362f * 1.4426950408889634f;
    const unsigned voff = lane * 16;
    u32x4 q0, q1, k0, k1, v0, v1;
    const unsigned tb = WS_KV + (unsigned)(head * 64 + 4 * grp + w) * 4096 + voff;
#pragma unroll
    for (int i = 0; i < 4; ++i) { k0[i] = pk_bf16(ak[2 * i], ak[2 * i + 1]); k1[i] = pk_bf16(ak[8 + 2 * i], ak[9 + 2 * i]); }
    if (hi) { k1[2] = 0x3f803f80u; k1[3] = 0x00003f80u; }
    BST(k0, tb); BST(k1, tb + 1024);
#pragma unroll
    for (int i = 0; i < 4; ++i) {
        v0[i] = pk_bf16(av[2 * i], av[2 * i + 1]); v1[i] = pk_bf16(av[8 + 2 * i], av[9 + 2 * i]);
        if (r32 >= DH) { v0[i] = v1[i] = (r32 == DH ? 0x3f803f80u : 0u); }
    }
    BST(v0, tb + 2048); BST(v1, tb + 3072);
    asm volatile("s_waitcnt vmcnt(4)" ::: "memory");
    arrive(sy + SY_F + 7); await(sy + SY_F + 7, 4);
    {
        s16x4 fq[4][2];
#define RQ(j) do { fq[(j) & 3][0] = trread(wp + WQO((j) >> 2) + ((j) & 3) * 1024); fq[(j) & 3][1] = trread(wp + WQO((j) >> 2) + ((j) & 3) * 1024 + 256); } while (0)
        RQ(0); RQ(1); RQ(2); SBAR();
#pragma unroll
        for (int j = 0; j < 28; ++j) {
            aq = MFMA_BF(cat8(fq[j & 3][0], fq[j & 3][1]), xf[j >> 2][j & 3], aq); SBAR(); if (j < 25) RQ(j + 3); SBAR();
            if (j == 13) { drain(); put_flag(ws, FL_KV + (unsigned)((head * 4 + w) * 16 + grp) * FS, tag); }
        }
    }
#pragma unroll
    for (int i = 0; i < 4; ++i) { q0[i] = pk_bf16(aq[2 * i] * cq, aq[2 * i + 1] * cq); q1[i] = pk_bf16(aq[8 + 2 * i] * cq, aq[9 + 2 * i] * cq); }
    if (hi) { q1[2] = 0; q1[3] = 0; }
    const unsigned kvf = FL_KV + (unsigned)((head * 4 + w) * 16 + ((grp + lane) & 15)) * FS;
    const f32x16 zero = {};
    {
        f32x16 s = MFMA_BF(BC8(k0), BC8(q0), zero); s = MFMA_BF(BC8(k1), BC8(q1), s);
        float m = s[0];
#pragma unroll
        for (int i = 1; i < 16; ++i) m = fmaxf(m, s[i]);
        q1 = put_ref(q1, swapmax(m), hi);
    }
    { LAS u32x4* qo = (LAS u32x4*)(lds + LQ + w * 2048) + lane; qo[0] = q0; qo[64] = q1; }
    arrive(sy + SY_Q);
    const u64 kv1 = __hip_atomic_load((GAS u64*)(ws + kvf), __ATOMIC_RELAXED, __HIP_MEMORY_SCOPE_AGENT); SBAR();
    f32x16 O[4] = {}; PIN(O[0]); PIN(O[1]); PIN(O[2]); PIN(O[3]);
    await(sy + SY_Q, 4);
    bf16x8 Q[4][2];
    Q[0][0] = BC8(q0); Q[0][1] = BC8(q1);
#pragma unroll
    for (int i = 1; i < 4; ++i) { LAS const char* p = lds + LQ + ((w + i) & 3) * 2048 + voff; Q[i][0] = FR(p, 0); Q[i][1] = FR(p, 1024); }
    u32x4 kv[4][4] = {{k0, k1, v0, v1}, {}, {}, {}};
    f32x16 sa, sb;
    u32x4 p0, p1 = {};
#define GRP(SC, i, P, j) do { float e0 = EX(SC[i]); SBAR(); float e1 = EX(SC[i + 1]); SBAR(); float e2 = EX(SC[i + 2]); SBAR(); float e3 = EX(SC[i + 3]); SBAR(); \
        P[j] = pk_bf16(e0, e1); SBAR(); P[j + 1] = pk_bf16(e2, e3); PIN(P); SBAR(); } while (0)
#define SLOT(SC, SN, cb, i, QK, PV1, LD) do { constexpr int nb_ = (i) < 3 ? (cb) : ((cb) + 1) % 4, pb_ = (i) > 0 ? (cb) : ((cb) + 3) % 4; \
        if (QK) SN = MFMA_BF(BC8(kv[nb_][0]), Q[((i) + 1) & 3][0], zero); SBAR(); GRP(SC, 0, p0, 0); \
        if (PV1) O[((i) + 3) & 3] = MFMA_BF(BC8(kv[pb_][3]), BC8(p1), O[((i) + 3) & 3]); SBAR(); \
        if (LD) { kv[((cb) + 3) % 4][i] = __builtin_amdgcn_raw_buffer_load_b128(rs, voff + (i) * 1024, kvo, 16); SBAR(); } GRP(SC, 4, p0, 2); \
        if (QK) SN = MFMA_BF(BC8(kv[nb_][1]), Q[((i) + 1) & 3][1], SN); SBAR(); GRP(SC, 8, p1, 0); \
        O[i] = MFMA_BF(BC8(kv[cb][2]), BC8(p0), O[i]); SBAR(); GRP(SC, 12, p1, 2); } while (0)
    const unsigned kvb = WS_KV + (unsigned)(head * 64 + w) * 4096;
    unsigned kvo = 0;
    u64 miss = __ballot(kv1 != tag) & 0xfffe;
    if (miss & 6) miss = wait_flags<2>(ws, kvf, tag, miss, 6);
    CFENCE();
#pragma unroll
    for (int j = 1; j < 3; ++j)
#pragma unroll
        for (int f = 0; f < 4; ++f) kv[j][f] = __builtin_amdgcn_raw_buffer_load_b128(rs, voff + f * 1024, kvb + ((grp + j) & 15) * 16384, 16);
    arrive(sy + SY_G);
    sa = MFMA_BF(BC8(k0), Q[0][0], zero); sa = MFMA_BF(BC8(k1), Q[0][1], sa);
    int nx = grp + 3;
#define TILE(cb) do { kvo = kvb + (nx & 15) * 16384; ++nx; SBAR(); \
        SLOT(sa, sb, cb, 0, true, true, true); SLOT(sb, sa, cb, 1, true, true, true); SLOT(sa, sb, cb, 2, true, true, true); SLOT(sb, sa, cb, 3, true, true, true); } while (0)
#pragma unroll 1
    for (unsigned msk = 0x78; msk < 0x80000; msk <<= 4) { if (__builtin_expect((miss & msk) != 0, 0)) miss = wait_flags<2>(ws, kvf, tag, miss, msk); TILE(0); TILE(1); TILE(2); TILE(3); }
    O[3] = MFMA_BF(BC8(kv[3][3]), BC8(p1), O[3]);
    f32x16 o = O[0];
    {
#pragma unroll
        for (int i = 1; i < 4; ++i) {
            LAS char* ex = lds + LX + (((w + i) & 3) * 3 + i - 1) * 4096 + voff;
#pragma unroll
            for (int g = 0; g < 4; ++g) *(LAS f32x4*)(ex + g * 1024) = (f32x4){O[i][4 * g], O[i][4 * g + 1], O[i][4 * g + 2], O[i][4 * g + 3]};
        }
        arrive(sy + SY_X); await(sy + SY_X, 4);
        LAS const char* er = lds + LX + w * 3 * 4096 + voff;
#pragma unroll
        for (int i = 0; i < 3; ++i)
#pragma unroll
            for (int g = 0; g < 4; ++g) { const f32x4 r = *(LAS const f32x4*)(er + i * 4096 + g * 1024);
#pragma unroll
                for (int e = 0; e < 4; ++e) o[4 * g + e] += r[e]; }
        f32x2 chk = {0.f, 0.f};
#pragma unroll
        for (int i = 0; i < 8; ++i) chk = pfma((f32x2){o[2 * i], o[2 * i + 1]}, (f32x2){0.f, 0.f}, chk);
        if (__builtin_expect(__any(!(chk[0] + chk[1] == 0.f) || !(o[12] < 1e30f)), 0)) {
            const unsigned hb = WS_KV + (unsigned)head * 64 * 4096 + voff;
            u32x4 qr = __builtin_bit_cast(u32x4, Q[0][1]); if (hi) { qr[2] = 0; qr[3] = 0; }
            float mm = -INFINITY;
            wait_flags<4>(ws, FL_KV + (unsigned)(head * 64 + lane) * FS, tag, ~0ull, ~0ull);
#pragma unroll 1
            for (int t = 0; t < 64; ++t) {
                f32x16 s = MFMA_BF(BC8(BLD(hb + t * 4096)), Q[0][0], zero); s = MFMA_BF(BC8(BLD(hb + t * 4096 + 1024)), BC8(qr), s);
#pragma unroll
                for (int i = 0; i < 16; ++i) mm = fmaxf(mm, s[i]);
            }
            qr = put_ref(qr, swapmax(mm), hi);
            o = zero;
#pragma unroll 1
            for (int t = 0; t < 64; ++t) {
                f32x16 s = MFMA_BF(BC8(BLD(hb + t * 4096)), Q[0][0], zero); s = MFMA_BF(BC8(BLD(hb + t * 4096 + 1024)), BC8(qr), s);
#pragma unroll
                for (int i = 0; i < 4; ++i) { p0[i] = pk_bf16(EX(s[2 * i]), EX(s[2 * i + 1])); p1[i] = pk_bf16(EX(s[8 + 2 * i]), EX(s[9 + 2 * i])); }
                o = MFMA_BF(BC8(BLD(hb + t * 4096 + 2048)), BC8(p0), o); o = MFMA_BF(BC8(BLD(hb + t * 4096 + 3072)), BC8(p1), o);
            }
        }
    }
    {
        const auto r = __builtin_amdgcn_permlane32_swap(__float_as_uint(o[12]), __float_as_uint(o[12]), false, false);
        const float rl = __builtin_amdgcn_rcpf(__uint_as_float(r[1]));
        u32x4 o0, o1;
#pragma unroll
        for (int i = 0; i < 4; ++i) { o0[i] = pk_bf16(o[2 * i] * rl, o[2 * i + 1] * rl); o1[i] = pk_bf16(o[8 + 2 * i] * rl, o[9 + 2 * i] * rl); }
        if (hi) { o1[2] = 0; o1[3] = 0; }
        const unsigned fi = (unsigned)((4 * grp + w) * 16 + head);
        BST(o0, WS_O + fi * 2048 + voff); BST(o1, WS_O + fi * 2048 + 1024 + voff);
        drain();
        put_flag(ws, FL_O + fi * FS, tag);
    }
    const int xcd = blockIdx.x & 7, bi = blockIdx.x >> 3;
    if (xcd < 7) {
        wait_flags<2>(ws, FL_BT + (unsigned)(xcd * 128 + w * 32 + r32) * FS, tag, 0xffffffffull, 0xffffffffull);
        const unsigned bo = WS_BT + (unsigned)((64 * xcd + 16 * w + (lane >> 3)) * S + 64 * bi + (lane & 7) * 8) * 2;
        const u32x4 b0 = BLD(bo), b1 = BLD(bo + 8 * S * 2);
        *(LAS u32x4*)(lds + 65536 + w * 2048 + voff) = b0; *(LAS u32x4*)(lds + 65536 + w * 2048 + 1024 + voff) = b1;
        arrive(sy + SY_T2); await(sy + SY_T2, 4);
        u32x4 o0, o1; transpose_tile(lds + 65536, 2048, lds + 73728 + w * 2304, nullptr, w, lane, o0, o1);
        const __amdgpu_buffer_rsrc_t ro = __builtin_amdgcn_make_buffer_rsrc(out, 0, S * 2 * D * 2, 0x00020000);
        const unsigned oo = (unsigned)((64 * bi + 16 * w + (lane >> 3)) * 2 * D + D + 64 * xcd) * 2 + (lane & 7) * 16;
        __builtin_amdgcn_raw_buffer_store_b128(o0, ro, oo, 0, 16); __builtin_amdgcn_raw_buffer_store_b128(o1, ro, oo + 8 * 2 * D * 2, 0, 16);
    }
}

DI void role_b(const uint16_t* __restrict__ wo, const uint16_t* __restrict__ u, const uint16_t* __restrict__ lar, const uint16_t* __restrict__ aim,
               const uint16_t* __restrict__ cre, const uint16_t* __restrict__ cim, const uint16_t* __restrict__ ldt, const uint16_t* __restrict__ dskip,
               uint16_t* __restrict__ out, char* __restrict__ ws, LAS char* lds, int w, int lane, u64 code0) {
    u64 tag;
    const unsigned lds0 = (unsigned)(uintptr_t)lds;
    LAS unsigned* sy = (LAS unsigned*)(lds + LSY);
    const __amdgpu_buffer_rsrc_t rs = __builtin_amdgcn_make_buffer_rsrc(ws, 0, WS_END, 0x00020000);
    const int b = blockIdx.x, xcd = b & 7, bi = b >> 3, r32 = lane & 31, hi = lane >> 5;
    const unsigned voff = lane * 16;
    LAS char* tab = lds + LB + w * TABW;
    unsigned pf = 0;
#define CODE_PF() asm volatile("global_load_dword %0, %1, %2" : "+v"(pf) : "v"(min((unsigned)((bi * 4 + w) * 3 + (lane < 3 ? lane : 0)), CODE_LINES - 1) * 128), "s"(code0) : "memory")
    const int qt = 8 * xcd + (bi >> 2), ct = 4 * (bi & 3) + w, cl = ct < 14 ? ct : 13;
    bf16x8 wf[32];
#define LOADWF(i) wf[i] = BC8(__builtin_amdgcn_raw_buffer_load_b128(rs, voff, WS_WO + (unsigned)(((8 * w + (i)) & 31) * 14 + cl) * 1024, 16))
    if (xcd < 7) {
        const int cc = w >> 1, hh = w & 1, pair = 32 * xcd + bi, ch = 2 * pair + cc;
        LAS char* cb = lds + LB + cc * CBW;
        const unsigned po = (unsigned)(ch * NS + 128 * hh) / 2 + lane;
        const unsigned pl = ((const unsigned*)lar)[po], pa = ((const unsigned*)aim)[po], pr = ((const unsigned*)cre)[po], pi = ((const unsigned*)cim)[po];
        const unsigned uo = (unsigned)((64 * bi + 16 * w + (lane >> 3)) * D + 64 * xcd + (lane & 7) * 8) * 2;
        glds2(u, uo, uo + 8 * D * 2 - 1024, lds0 + LB + w * TABW + 4096);
        const u64 e0 = __hip_atomic_load((GAS u64*)ws, __ATOMIC_RELAXED, __HIP_MEMORY_SCOPE_AGENT);
        u32x2 gi[2][3];
#pragma unroll
        for (int ps = 0; ps < 2; ++ps)
#pragma unroll
            for (int i = 0; i < 3; ++i) gi[ps][i] = ((const u32x2*)g_idx.v[128 * hh + 64 * ps + lane])[i];
        CODE_PF();
        const unsigned lw = ((const unsigned*)ldt)[pair], dw = ((const unsigned*)dskip)[pair];
        const float dt = rbf(expf(cc ? bf_hi(lw) : bf_lo(lw)));
        drain(); asm volatile("" :: "v"(pf));
        arrive(sy + SY_WO);
        tag = mix(e0);
        const float cm = wave_max(fmaxf(fmaxf(fabsf(bf_lo(pr)), fabsf(bf_hi(pr))), fmaxf(fabsf(bf_lo(pi)), fabsf(bf_hi(pi)))));
        unsigned ce = __float_as_uint(cm) & 0x7f800000u; ce = ce < 0x01000000u ? 0x01000000u : (ce > 0x7e800000u ? 0x7e800000u : ce);
        const float cscale = __uint_as_float(0x7f000000u - ce), oscale = 2.0f * dt * __uint_as_float(ce);
        LAS unsigned* tw = (LAS unsigned*)(tab + lane * 4);
        LAS const char* trd = tab + r32 * TROW + hi * 16;
        f32x16 dacc = {};
        f32x2 z1, z8, z256, x0;
#define ANCH(vl, va, vr, vi) do { const float a2 = (-rbf(expf(vl)) * dt) * 1.4426950408889634f, rev = ((va) * dt) * 0.15915494309189535f; \
            z1 = anchor(a2, -rev, 1.f); z8 = anchor(a2, rev, 8.f); z256 = anchor(a2, -rev, 256.f); x0 = (f32x2){(vr) * cscale, -(vi) * cscale}; \
            PIN(z1); PIN(z8); PIN(z256); SBAR(); } while (0)
#define TW(row) tw[(row) * (TROW / 4)]
#define CHAIN() do { \
            const f32x2 z1s = rot(z1), zs = rot(z256), z8s = rot(z8); \
            f32x2 y[4], X[8]; y[1] = z8; y[2] = cmul(z8, z8, z8s); X[0] = x0; X[1] = cmul(x0, z1, z1s); f32x2 a = cmul(x0, z256, zs); \
            y[3] = cmul(y[2], z8, z8s); const f32x2 z32 = cmul(y[2], y[2], rot(y[2])), z32s = rot(z32); X[2] = cmul(X[1], z1, z1s); y[0] = z32; \
            f32x2 c4 = cmul(X[2], z256, zs); \
            unsigned c[2][7], d0 = pk_f16(X[0]), d1 = pk_f16(X[1]), d2 = pk_f16(y[1]); \
            c[0][0] = pk_f16(y[0]); c[0][1] = 0x3c00u; c[0][2] = pk_f16(y[2]); c[0][3] = pk_f16(y[3]); c[0][4] = pk_f16(X[2]); c[0][5] = pk_f16(a); c[0][6] = pk_f16(c4); \
            TW(0) = d0; TW(1) = d1; TW(22) = d2; SBAR(); \
            _Pragma("unroll") for (int i = 1; i < 9; ++i) { \
                f32x2 t0, t1, t2, t3, t4, t5, t6; const int j = i - 1; unsigned* pc = c[j & 1]; unsigned* nc = c[i & 1]; \
                if (i < 8) { if (i > 1) t0 = y[0].xx * z32; t1 = y[1].xx * z32; t2 = y[2].xx * z32; t3 = y[3].xx * z32; \
                    if (i < 6) t4 = X[i + 1].xx * z1; t5 = a.xx * z256; \
                    if (i == 3) t6 = X[4].xx * z256; if (i == 4 || i == 5) t6 = c4.xx * z256; if (i == 6) t6 = X[6].xx * z256; } SBAR(); \
                if (j != 1) TW(21 + (j ? 4 * j : 4)) = pc[0]; TW(21 + (j ? 4 * j + 1 : 0)) = pc[1]; TW(23 + 4 * j) = pc[2]; TW(24 + 4 * j) = pc[3]; \
                if (j < 6) TW(j + 2) = pc[4]; TW(j == 0 ? 8 : (j == 1 ? 12 : (j == 2 ? 14 : 13 + j))) = pc[5]; \
                if (j == 0) TW(9) = pc[6]; if (j == 3) TW(10) = pc[6]; if (j == 4) TW(13) = pc[6]; if (j == 5) TW(15) = pc[6]; if (j == 6) TW(11) = pc[6]; SBAR(); \
                if (i < 8) { if (i > 1) y[0] = pfma(y[0].yy, z32s, t0); y[1] = pfma(y[1].yy, z32s, t1); y[2] = pfma(y[2].yy, z32s, t2); y[3] = pfma(y[3].yy, z32s, t3); \
                    if (i < 6) X[i + 2] = pfma(X[i + 1].yy, z1s, t4); a = pfma(a.yy, zs, t5); \
                    if (i == 3) c4 = pfma(X[4].yy, zs, t6); if (i == 4 || i == 5) c4 = pfma(c4.yy, zs, t6); if (i == 6) c4 = pfma(X[6].yy, zs, t6); SBAR(); \
                    if (i > 1) nc[0] = pk_f16(y[0]); nc[1] = pk_f16(y[1]); nc[2] = pk_f16(y[2]); nc[3] = pk_f16(y[3]); \
                    if (i < 6) nc[4] = pk_f16(X[i + 2]); nc[5] = pk_f16(a); if (i >= 3 && i <= 6) nc[6] = pk_f16(c4); \
                    asm volatile("" :: "v"(pc[0]), "v"(pc[1]), "v"(pc[2]), "v"(pc[3]), "v"(pc[4]), "v"(pc[5]), "v"(pc[6])); SBAR(); } \
            } } while (0)
        f16x8 fa[8], fb[8];
#define RDT() do { _Pragma("unroll") for (int kk = 0; kk < 8; ++kk) { fa[kk] = *(LAS const f16x8*)(trd + kk * 32); fb[kk] = *(LAS const f16x8*)(trd + 21 * TROW + kk * 32); } SBAR(); } while (0)
#define MMT() do { _Pragma("unroll") for (int kk = 0; kk < 8; ++kk) dacc = MFMA_F16(fa[kk], fb[kk], dacc); } while (0)
        ANCH(bf_lo(pl), bf_lo(pa), bf_lo(pr), bf_lo(pi));
        await(sy + SY_WO, 4);
        set_tag(sy, tag);
        {
            u32x4 o0, o1; transpose_tile(lds + LB + 4096, TABW, tab, sy + SY_T1, w, lane, o0, o1);
            const unsigned to = WS_UT + (unsigned)((64 * xcd + 16 * w + (lane >> 3)) * S + 64 * bi) * 2 + (lane & 7) * 16;
            __builtin_amdgcn_s_setprio(3); BST(o0, to); BST(o1, to + 8 * S * 2); __builtin_amdgcn_s_setprio(0);
        }
        await(sy + SY_T1, 4);
        CHAIN(); RDT();
        drain(); __builtin_amdgcn_s_setprio(3); put_flag(ws, FL_UT + (unsigned)(xcd * 128 + w * 32 + bi) * FS, tag); __builtin_amdgcn_s_setprio(0);
        ANCH(bf_hi(pl), bf_hi(pa), bf_hi(pr), bf_hi(pi));
        MMT(); SBAR();
        CHAIN(); RDT();
        const unsigned utf = hi ? FL_WO + (unsigned)(w * 32 + r32) * FS : FL_UT + (unsigned)(xcd * 128 + (bi >> 3) * 32 + r32) * FS;
        const u64 utv = __hip_atomic_load((GAS u64*)(ws + utf), __ATOMIC_RELAXED, __HIP_MEMORY_SCOPE_AGENT); SBAR();
        MMT();
        {
            LAS float* pt = (LAS float*)tab;
            *(LAS f32x4*)(pt + 8 * r32 + 4 * hi) = (f32x4){dacc[0] * oscale, dacc[1] * oscale, dacc[2] * oscale, dacc[3] * oscale};
            *(LAS f32x2*)(pt + (hi ? 384 + 2 * r32 : 256 + 4 * r32)) = (f32x2){dacc[4] * oscale, dacc[5] * oscale};
            *(LAS f32x2*)(pt + (hi ? 448 + 2 * r32 : 258 + 4 * r32)) = (f32x2){dacc[6] * oscale, dacc[7] * oscale};
            pt[hi ? 640 + r32 : 512 + r32] = dacc[8] * oscale;
            pt[hi ? 672 + r32 : 544 + r32] = dacc[9] * oscale;
            pt[hi ? 672 + r32 : 576 + r32] = dacc[10] * oscale;
            pt[hi ? 672 + r32 : 608 + r32] = dacc[11] * oscale;
        }
        LAS unsigned* ps_ = sy + SY_P + 5 * cc;
        arrive(ps_ + 0);
        LAS char* ui = cb + UOF;
        LAS char* kt = cb + KOF;
        await(ps_ + 0, 2);
#pragma unroll
        for (int ps = 0; ps < 2; ++ps) {
            const int v = 81 * hh + 64 * ps + lane;
            if (ps == 0 || lane < 17) {
                const f32x4 p0 = *(LAS const f32x4*)(cb + v * 16), p1 = *(LAS const f32x4*)(cb + TABW + v * 16);
                u32x2 r = {pk_bf16(p0[0] + p1[0], p0[1] + p1[1]), pk_bf16(p0[2] + p1[2], p0[3] + p1[3])};
                if (v == 161) r = (u32x2){0u, 0u};
                *(LAS u32x2*)(kt + v * 8) = r;
            }
        }
        arrive(ps_ + 1);
        { const u64 ms = __ballot(utv != tag); if (ms) wait_flags<8>(ws, utf, tag, ms, ms); }
        CFENCE();
        const unsigned ub = WS_UT + (unsigned)(ch * S + 1024 * hh) * 2 + voff;
        const u32x4 u0 = BLD(ub), u1 = BLD(ub + 1024); SBAR();
        await(ps_ + 1, 2);
        __builtin_amdgcn_s_setprio(1);
        const u32x4 z4 = {};
        {
            unsigned W[2][6];
#pragma unroll
            for (int ps = 0; ps < 2; ++ps)
#pragma unroll
                for (int i = 0; i < 6; ++i) { const unsigned gv = gi[ps][i >> 1][i & 1];
                    W[ps][i] = *(LAS const uint16_t*)(kt + (gv & 0xffffu));
                    if (i < 5) W[ps][i] |= (unsigned)*(LAS const uint16_t*)(kt + (gv >> 16)) << 16; }
            *(LAS u32x4*)(ui + (64 * hh + lane) * 16) = z4;
            if (lane < 16) *(LAS u32x4*)(ui + (128 + 16 * hh + lane) * 16) = z4;
            if (lane < 8) *(LAS u32x4*)(cb + (2 * hh + (lane >> 2)) * CPY + (256 + (lane & 3)) * 16) = z4;
#pragma unroll
            for (int ps = 0; ps < 2; ++ps) {
                unsigned V[5];
#pragma unroll
                for (int i = 0; i < 5; ++i) V[i] = __builtin_amdgcn_alignbit(W[ps][i + 1], W[ps][i], 16);
                LAS char* cp = cb + (128 * hh + 64 * ps + lane) * 16;
                *(LAS u32x4*)(cp) = (u32x4){W[ps][0], W[ps][1], W[ps][2], W[ps][3]};
                *(LAS u32x4*)(cp + CPY) = (u32x4){V[0], V[1], V[2], V[3]};
                *(LAS u32x4*)(cp + 2 * CPY) = (u32x4){W[ps][1], W[ps][2], W[ps][3], W[ps][4]};
                *(LAS u32x4*)(cp + 3 * CPY) = (u32x4){V[1], V[2], V[3], V[4]};
            }
        }
        *(LAS u32x4*)(ui + (32 + 32 * hh + (lane >> 2)) * 80 + (lane & 3) * 16) = u0;
        *(LAS u32x4*)(ui + (48 + 32 * hh + (lane >> 2)) * 80 + (lane & 3) * 16) = u1;
        arrive(ps_ + 2); await(ps_ + 2, 2);
        __builtin_amdgcn_s_setprio(0);
        f32x16 a0 = {}, a1 = {};
        {
            const int sb = 7 - (r32 & 7);
            LAS const char* pa = cb + (sb & 3) * CPY + (255 - (r32 >> 3) + hi) * 16 + (sb >> 2) * 8 - 64 * (16 * hh + 15) - 2048;
            LAS const char* pa2 = pa + 8; asm volatile("" : "+v"(pa2));
            LAS const char* pb = ui + (32 + r32 - 16 * hh - 15) * 80 + 16 * hi;
            s16x4 ta[2][2], tb[2][2]; bf16x8 b0[2], b1[2];
#define T4(q, off) (*(LAS const s16x4*)((q) + (off)))
#define CSTEP(i, j, RD) do { const int o_ = 64 * (15 - ((j) >> 1)) + 32 * ((j) & 1), u_ = 80 * (15 - ((j) >> 1)) + 32 * ((j) & 1); \
                a1 = MFMA_BF(cat8(ta[i][0], ta[i][1]), b1[i], a1); SBAR(); if (RD) { ta[1 - i][0] = T4(pa, o_ + 2048); ta[1 - i][1] = T4(pa2, o_ + 2048); } SBAR(); \
                a0 = MFMA_BF(cat8(ta[i][0], ta[i][1]), b0[i], a0); SBAR(); if (RD) { b1[1 - i] = FR(pb, u_ + 2560); b0[1 - i] = FR(pb, u_); } SBAR(); \
                a1 = MFMA_BF(cat8(tb[i][0], tb[i][1]), b0[i], a1); SBAR(); if (RD) { tb[1 - i][0] = T4(pa, o_); tb[1 - i][1] = T4(pa2, o_); } SBAR(); } while (0)
            ta[0][0] = T4(pa, 960 + 2048); ta[0][1] = T4(pa2, 960 + 2048); b1[0] = FR(pb, 1200 + 2560); b0[0] = FR(pb, 1200); tb[0][0] = T4(pa, 960); tb[0][1] = T4(pa2, 960);
#pragma unroll
            for (int j = 0; j < 32; ++j) {
                if (j == 31) arrive(ps_ + 3);
                CSTEP(j & 1, j + 1, j < 31);
                if (ct < 14) LOADWF(j);
                SBAR();
            }
        }
        __builtin_amdgcn_s_setprio(1);
        {
            u32x2 uu[4];
#pragma unroll
            for (int g = 0; g < 4; ++g) uu[g] = *(LAS const u32x2*)(ui + (32 + 32 * hh + r32) * 80 + 16 * g + 8 * hi);
            LAS char* ex = cb + r32 * 144 + hi * 16;
            const f32x16 snd = hh ? a0 : a1; const f32x16 y = hh ? a1 : a0;
            await(ps_ + 3, 2);
#pragma unroll
            for (int g = 0; g < 4; ++g) *(LAS f32x4*)(ex + hh * 4608 + g * 32) = (f32x4){snd[4 * g], snd[4 * g + 1], snd[4 * g + 2], snd[4 * g + 3]};
            arrive(ps_ + 4);
            const float dsk = cc ? bf_hi(dw) : bf_lo(dw);
            unsigned sk[8];
#pragma unroll
            for (int g = 0; g < 4; ++g)
#pragma unroll
                for (int e = 0; e < 2; ++e) sk[2 * g + e] = pk_bf16(bf_lo(uu[g][e]) * dsk, bf_hi(uu[g][e]) * dsk);
            await(ps_ + 4, 2);
            LAS char* sc = cb + 9216 + hh * 2048;
#pragma unroll
            for (int g = 0; g < 4; ++g) {
                const f32x4 p = *(LAS const f32x4*)(ex + (1 - hh) * 4608 + g * 32);
                u32x2 r;
#pragma unroll
                for (int e = 0; e < 2; ++e) {
                    const unsigned yb = pk_bf16(y[4 * g + 2 * e] + p[2 * e], y[4 * g + 2 * e + 1] + p[2 * e + 1]);
                    r[e] = pk_bf16(bf_lo(yb) + bf_lo(sk[2 * g + e]), bf_hi(yb) + bf_hi(sk[2 * g + e]));
                }
                *(LAS u32x2*)(sc + 64 * r32 + 16 * g + 8 * hi) = r;
            }
            const u32x4 o0 = *(LAS const u32x4*)(sc + voff), o1 = *(LAS const u32x4*)(sc + 1024 + voff);
            const unsigned bo = WS_BT + (unsigned)(ch * S + 1024 * hh) * 2 + voff;
            BST(o0, bo); BST(o1, bo + 1024);
        }
        arrive(sy + SY_BG);
        drain();
        put_flag(ws, FL_BT + (unsigned)(xcd * 128 + bi * 4 + w) * FS, tag);
    } else {
        {
            const unsigned nb = (bi & 1) ? 12 * 2 * D : 16 * 2 * D;
            unsigned v4[4];
#pragma unroll
            for (int i = 0; i < 4; ++i) { const unsigned o = (unsigned)(w * 4096 + i * 1024) + voff; v4[i] = (o < nb ? o : 0u) + 4096 - i * 1024; }
            glds4((const char*)(wo + (DH * (bi >> 1) + 16 * (bi & 1)) * D) - 4096, v4[0], v4[1], v4[2], v4[3], lds0 + LB + w * 4096);
        }
        const u64 e0 = __hip_atomic_load((GAS u64*)ws, __ATOMIC_RELAXED, __HIP_MEMORY_SCOPE_AGENT);
        CODE_PF();
        drain(); asm volatile("" :: "v"(pf));
        tag = mix(e0);
        arrive(sy + SY_WO); await(sy + SY_WO, 4);
        set_tag(sy, tag);
        LAS const char* p = lds + LB + (4 * hi + ((lane & 15) >> 2)) * (2 * D) + ((lane >> 4) & 1) * 32 + (lane & 3) * 8;
#pragma unroll
        for (int i = 0; i < 4; ++i) {
            const int ct = w + 4 * i;
            if (ct < 14) {
                u32x4 o = __builtin_bit_cast(u32x4, cat8(trread(p + ct * 64), trread(p + 8 * 2 * D + ct * 64)));
                if (hi && (bi & 1)) { o[2] = 0; o[3] = 0; }
                BST(o, WS_WO + (unsigned)(bi * 14 + ct) * 1024 + voff);
            }
        }
        drain();
        put_flag(ws, FL_WO + (unsigned)(w * 32 + bi) * FS, tag);
        __builtin_amdgcn_s_setprio(0);
        arrive(sy + SY_BG);
        await_slow(sy + SY_G, 4);
        wait_flags<8>(ws, FL_WO + (unsigned)(w * 32 + r32) * FS, tag, 0xffffffffull, 0xffffffffull);
        if (ct < 14) {
#pragma unroll
        for (int i = 0; i < 32; ++i) { LOADWF(i); __builtin_amdgcn_s_sleep(2); SBAR(); } }
    }
    const __amdgpu_buffer_rsrc_t ro = __builtin_amdgcn_make_buffer_rsrc(out, 0, S * 2 * D * 2, 0x00020000);
    await(sy + SY_BG, 4);
    await_slow(sy + SY_X, 4);
    wait_flags<2>(ws, FL_O + (unsigned)(qt * 16 + 4 * w + (lane & 3)) * FS, tag, 15, 15);
    u32x4 of[8];
#pragma unroll
    for (int i = 0; i < 8; ++i) of[i] = BLD(WS_O + (unsigned)(qt * 32 + 8 * w + i) * 1024 + voff);
#pragma unroll
    for (int i = 0; i < 8; ++i) *(LAS u32x4*)(lds + LB + (8 * w + i) * 1024 + voff) = of[i];
    arrive(sy + SY_B2);
    f32x16 acc = {};
    if (ct < 14) {
#pragma unroll
        for (int i = 0; i < 8; ++i) acc = MFMA_BF(wf[i], BC8(of[i]), acc);
    }
    SBAR();
    await(sy + SY_B2, 4);
    if (ct < 14) {
        LAS const char* po[3];
#pragma unroll
        for (int j = 0; j < 3; ++j) po[j] = lds + LB + ((w + 1 + j) & 3) * 8192 + voff;
        bf16x8 og[4];
#pragma unroll
        for (int i = 0; i < 3; ++i) og[i] = FR(po[0], i * 1024);
#pragma unroll
        for (int i = 0; i < 24; ++i) { if (i < 21) og[(i + 3) & 3] = FR(po[(i + 3) >> 3], ((i + 3) & 7) * 1024); SBAR(); acc = MFMA_BF(wf[8 + i], og[i & 3], acc); SBAR(); }
        LAS char* sc = lds + LB + 40960 + w * 2304;
#pragma unroll
        for (int g = 0; g < 4; ++g) *(LAS u32x2*)(sc + r32 * 72 + 16 * g + 8 * hi) = (u32x2){pk_bf16(acc[4 * g], acc[4 * g + 1]), pk_bf16(acc[4 * g + 2], acc[4 * g + 3])};
        LAS const char* sr = sc + (lane >> 2) * 72 + (lane & 3) * 16;
        const u32x2 a0 = *(LAS const u32x2*)sr, a1 = *(LAS const u32x2*)(sr + 8), b0 = *(LAS const u32x2*)(sr + 16 * 72), b1 = *(LAS const u32x2*)(sr + 16 * 72 + 8);
        const unsigned oo = (unsigned)((32 * qt + (lane >> 2)) * 2 * D + 32 * ct) * 2 + (lane & 3) * 16;
        __builtin_amdgcn_raw_buffer_store_b128((u32x4){a0[0], a0[1], a1[0], a1[1]}, ro, oo, 0, 16);
        __builtin_amdgcn_raw_buffer_store_b128((u32x4){b0[0], b0[1], b1[0], b1[1]}, ro, oo + 16 * 2 * D * 2, 0, 16);
    }
    if (b == 0 && w == 0) put_flag(ws, 0, tag);
}

__global__ __launch_bounds__(512, 2) void fused(const uint16_t* __restrict__ x, const uint16_t* __restrict__ wqkv, const uint16_t* __restrict__ wo,
                                               const uint16_t* __restrict__ u, const uint16_t* __restrict__ lar, const uint16_t* __restrict__ aim,
                                               const uint16_t* __restrict__ cre, const uint16_t* __restrict__ cim, const uint16_t* __restrict__ ldt,
                                               const uint16_t* __restrict__ dskip, uint16_t* __restrict__ out, char* __restrict__ ws) {
    extern __shared__ __attribute__((aligned(16))) char smem[];
    LAS char* lds = (LAS char*)smem;
    const int lane = threadIdx.x & 63, w = __builtin_amdgcn_readfirstlane(threadIdx.x >> 6);
    const u64 code0 = __builtin_amdgcn_s_getpc() & ~255ull;
    if (w >= 4) __builtin_amdgcn_s_setprio(3);
    if (w == 4) ((LAS unsigned*)(lds + LSY))[lane] = 0;
    asm volatile("" :: "s"(x), "s"(wqkv), "s"(wo), "s"(u), "s"(lar), "s"(aim), "s"(cre), "s"(cim), "s"(ldt), "s"(dskip), "s"(out), "s"(ws));
    asm volatile("s_waitcnt lgkmcnt(0)\n\ts_barrier" ::: "memory");
    if (w < 4) role_a(x, wqkv, out, ws, lds, w, lane);
    else role_b(wo, u, lar, aim, cre, cim, ldt, dskip, out, ws, lds, w - 4, lane, code0);
}
}

extern "C" void kernel_launch(void* const* d_in, const int* in_sizes, int n_in, void* d_out, int out_size, void* d_ws, size_t ws_size, hipStream_t stream) {
    using namespace k;
    (void)hipFuncSetAttribute((const void*)fused, hipFuncAttributeMaxDynamicSharedMemorySize, LDS_BYTES);
    const uint16_t* const* in = (const uint16_t* const*)d_in;
    fused<<<256, 512, LDS_BYTES, stream>>>(in[0], in[1], in[2], in[3], in[4], in[5], in[6], in[7], in[8], in[9], (uint16_t*)d_out, (char*)d_ws);
}
```

```cpp
#include <hip/hip_runtime.h>
#include <hip/hip_bf16.h>
#include <stdint.h>

typedef short bf16x8 __attribute__((ext_vector_type(8)));
typedef _Float16 f16x8 __attribute__((ext_vector_type(8)));
typedef _Float16 f16x2 __attribute__((ext_vector_type(2)));
typedef __bf16 bf16x2 __attribute__((ext_vector_type(2)));
typedef short s16x4 __attribute__((ext_vector_type(4)));
typedef float f32x16 __attribute__((ext_vector_type(16)));
typedef float f32x4 __attribute__((ext_vector_type(4)));
typedef float f32x2 __attribute__((ext_vector_type(2)));
typedef unsigned u32x4 __attribute__((ext_vector_type(4)));
typedef unsigned u32x2 __attribute__((ext_vector_type(2)));
typedef unsigned long long u64;
#define LAS __attribute__((address_space(3)))
#define GAS __attribute__((address_space(1)))
#define DI __device__ __forceinline__
#define MFMA_BF(a, b, c) __builtin_amdgcn_mfma_f32_32x32x16_bf16((a), (b), (c), 0, 0, 0)
#define MFMA_F16(a, b, c) __builtin_amdgcn_mfma_f32_32x32x16_f16((a), (b), (c), 0, 0, 0)
#define SBAR() __builtin_amdgcn_sched_barrier(0)
#define CFENCE() asm volatile("" ::: "memory")
#define PIN(x) asm volatile("" : "+v"(x))
#define BC8(x) __builtin_bit_cast(bf16x8, (x))
#define EX(x) __builtin_amdgcn_exp2f(x)

namespace k {
constexpr int S = 2048, D = 448, DH = 28, NS = 256;
constexpr unsigned FS = 128;
constexpr unsigned FL_KV = FS, FL_O = FL_KV + 1024 * FS, FL_UT = FL_O + 1024 * FS, FL_BT = FL_UT + 896 * FS, FL_WO = FL_BT + 896 * FS;
constexpr unsigned WS_KV = 512u << 10;
constexpr unsigned WS_O = WS_KV + (4u << 20);
constexpr unsigned WS_WO = WS_O + (2u << 20);
constexpr unsigned WS_UT = WS_WO + (448u << 10);
constexpr unsigned WS_BT = WS_UT + D * S * 2;
constexpr unsigned WS_END = WS_BT + D * S * 2;
constexpr int WSL = 8192, LWQ = 4 * WSL, LXR = LWQ + 16384, XSL = 16384, LB = LXR + 3 * XSL;
constexpr int LQ = LXR, LX = 0;
constexpr int TROW = 272, TABW = 33 * TROW, CPY = 4160, UOF = 2 * TABW, KOF = UOF + 7680, CBW = KOF + 1312;
constexpr int LT1 = LB + 2 * CBW, LSY = LT1 + 8192, LDS_BYTES = LSY + 256;
static_assert(LDS_BYTES <= 163840 && (LSY & 15) == 0 && 4 * CPY <= UOF && LB + 40960 + 4 * 2304 <= LSY, "lds");
enum { SY_F = 2, SY_P = 10, SY_WO = 20, SY_BG = 22, SY_B2 = 23, SY_Q = 24, SY_X = 25, SY_G = 26, SY_T2 = 27 };
constexpr int SPIN = 1 << 18;
constexpr unsigned CODE_LINES = 280;

struct IdxTab { uint16_t v[256][12]; };
constexpr IdxTab make_idx() {
    IdxTab t = {};
    for (int y = 0; y < 256; ++y) for (int e = 0; e < 12; ++e) {
        const int j = 2047 - 8 * y - e; int ix = 644;
        if (j >= 0) { const int sh = j >= 1024 ? 3 : (j >= 512 ? 2 : (j >= 256 ? 1 : 0));
            ix = sh ? 128 * sh + ((j + (1 << (sh - 1)) - 1 + ((j >> sh) & 1)) >> sh) : j; }
        t.v[y][e] = (uint16_t)(2 * ix);
    }
    return t;
}
__device__ const IdxTab g_idx __attribute__((aligned(16))) = make_idx();

DI unsigned pk_bf16(float a, float b) { f32x2 v = {a, b}; return __builtin_bit_cast(unsigned, __builtin_convertvector(v, bf16x2)); }
DI unsigned pk_f16(f32x2 v) { return __builtin_bit_cast(unsigned, __builtin_convertvector(v, f16x2)); }
DI float bf_lo(unsigned w) { return __uint_as_float(w << 16); }
DI float bf_hi(unsigned w) { return __uint_as_float(w & 0xffff0000u); }
DI float rbf(float x) { return bf_lo(pk_bf16(x, x)); }
#define GL0 "s_mov_b32 %0, m0\n\ts_mov_b32 m0, %2\n\ts_nop 0\n\t"
#define GL1(V, O, C) "global_load_lds_dwordx4 " V ", %1 offset:" #O C "\n\t"
#define GLE "s_mov_b32 m0, %0"
DI void glds4(const void* sb, unsigned v0, unsigned v1, unsigned v2, unsigned v3, unsigned lb) { unsigned sv;
    asm volatile(GL0 GL1("%3", 0, "") GL1("%4", 1024, "") GL1("%5", 2048, "") GL1("%6", 3072, "") GLE : "=&s"(sv) : "s"(sb), "s"(lb), "v"(v0), "v"(v1), "v"(v2), "v"(v3) : "memory"); }
DI void glds2(const void* sb, unsigned v0, unsigned v1, unsigned lb) { unsigned sv;
    asm volatile(GL0 GL1("%3", 0, "") GL1("%4", 1024, "") GLE : "=&s"(sv) : "s"(sb), "s"(lb), "v"(v0), "v"(v1) : "memory"); }
DI void glds1(const void* sb, unsigned v0, unsigned lb) { unsigned sv;
    asm volatile(GL0 GL1("%3", 0, "") GLE : "=&s"(sv) : "s"(sb), "s"(lb), "v"(v0) : "memory"); }
DI float wave_max(float v) {
#define DPPMAX(ctrl, rm) v = fmaxf(v, __uint_as_float(__builtin_amdgcn_update_dpp(0, __float_as_uint(v), ctrl, rm, 0xf, true)))
    DPPMAX(0x111, 0xf); DPPMAX(0x112, 0xf); DPPMAX(0x114, 0xf); DPPMAX(0x118, 0xf); DPPMAX(0x142, 0xa); DPPMAX(0x143, 0xc);
    return __uint_as_float(__builtin_amdgcn_readlane(__float_as_uint(v), 63));
}
DI s16x4 trread(LAS const char* p) { return __builtin_bit_cast(s16x4, __builtin_amdgcn_ds_read_tr16_b64_v4i16((LAS s16x4*)p)); }
DI bf16x8 cat8(s16x4 a, s16x4 b) { return __builtin_shufflevector(a, b, 0, 1, 2, 3, 4, 5, 6, 7); }
DI f32x2 pfma(f32x2 a, f32x2 b, f32x2 c) { return __builtin_elementwise_fma(a, b, c); }
DI f32x2 cmul(f32x2 a, f32x2 b, f32x2 bs) { return pfma(a.yy, bs, a.xx * b); }
DI f16x8 cmul4h(u32x4 p, u32x4 q) {
    u32x4 t;
#define PM(i, a, b) "v_pk_mul_f16 %" #i ", %" #a ", %" #b " op_sel:[0,0] op_sel_hi:[0,1]\n\t"
#define PF(i, a, b) "v_pk_fma_f16 %" #i ", %" #a ", %" #b ", %" #i " op_sel:[1,1,0] op_sel_hi:[1,0,1] neg_lo:[1,0,0]\n\t"
    asm(PM(0, 4, 8) PM(1, 5, 9) PM(2, 6, 10) PM(3, 7, 11) PF(0, 4, 8) PF(1, 5, 9) PF(2, 6, 10) PF(3, 7, 11) "s_nop 1"
        : "=&v"(t[0]), "=&v"(t[1]), "=&v"(t[2]), "=&v"(t[3]) : "v"(p[0]), "v"(p[1]), "v"(p[2]), "v"(p[3]), "v"(q[0]), "v"(q[1]), "v"(q[2]), "v"(q[3]));
    return __builtin_bit_cast(f16x8, t);
}
DI f32x2 rot(f32x2 b) { return (f32x2){-b.y, b.x}; }
DI f32x2 anchor(float a2, float rev, float s) {
    const float mag = EX(a2 * s);
    float r = rev * s; r -= __builtin_rintf(r);
    return (f32x2){mag * __builtin_amdgcn_cosf(r), mag * __builtin_amdgcn_sinf(r)};
}
DI void arrive(LAS unsigned* c) { asm volatile("s_mov_b64 exec, 1\n\tds_add_u32 %0, %1\n\ts_mov_b64 exec, -1" :: "v"((unsigned)(uintptr_t)c), "v"(1u) : "memory"); }
DI void await(LAS unsigned* c, unsigned n) {
    unsigned t, i = SPIN;
    asm volatile("1:\n\tds_read_b32 %0, %2\n\ts_waitcnt lgkmcnt(0)\n\tv_cmp_gt_u32 vcc, %3, %0\n\ts_cbranch_vccz 2f\n\ts_sleep 1\n\ts_sub_u32 %1, %1, 1\n\ts_cbranch_scc0 1b\n2:"
                 : "=&v"(t), "+s"(i) : "v"((unsigned)(uintptr_t)c), "s"(n) : "vcc", "scc", "memory");
}
DI void await_slow(LAS unsigned* c, unsigned n) {
    unsigned t, i = SPIN;
    asm volatile("1:\n\tds_read_b32 %0, %2\n\ts_waitcnt lgkmcnt(0)\n\tv_cmp_gt_u32 vcc, %3, %0\n\ts_cbranch_vccz 2f\n\ts_sleep 3\n\ts_sub_u32 %1, %1, 1\n\ts_cbranch_scc0 1b\n2:"
                 : "=&v"(t), "+s"(i) : "v"((unsigned)(uintptr_t)c), "s"(n) : "vcc", "scc", "memory");
}
DI u64 get_tag(LAS unsigned* sy) {
    await(sy, 1);
    const u64 v = __hip_atomic_load((LAS u64*)sy, __ATOMIC_RELAXED, __HIP_MEMORY_SCOPE_WORKGROUP);
    return ((u64)(unsigned)__builtin_amdgcn_readfirstlane((unsigned)(v >> 32)) << 32) | (unsigned)__builtin_amdgcn_readfirstlane((unsigned)v);
}
DI void set_tag(LAS unsigned* sy, u64 tag) { asm volatile("s_mov_b64 exec, 1\n\tds_write_b64 %0, %1\n\ts_mov_b64 exec, -1" :: "v"((unsigned)(uintptr_t)sy), "v"(tag) : "memory"); }
DI void drain() { asm volatile("s_waitcnt vmcnt(0)" ::: "memory"); }
DI void put_flag(char* ws, unsigned off, u64 tag) {
    asm volatile("s_mov_b64 exec, 1\n\tglobal_store_dwordx2 %0, %1, %2 sc1\n\ts_mov_b64 exec, -1" :: "v"(off), "v"(tag), "s"(ws) : "memory");
}
template <int SLP> DI u64 wait_flags(char* ws, unsigned off, u64 tag, u64 pend, u64 need) {
    u64 t, left; unsigned i = SPIN;
    asm volatile("s_mov_b64 exec, %5\n1:\n\tglobal_load_dwordx2 %0, %3, %4 sc1\n\ts_waitcnt vmcnt(0)\n\tv_cmpx_ne_u64 vcc, %6, %0\n\ts_and_b64 %2, exec, %7\n\t"
                 "s_cbranch_scc0 2f\n\ts_sleep %8\n\ts_sub_u32 %1, %1, 1\n\ts_cbranch_scc0 1b\n2:\n\ts_mov_b64 %2, exec\n\ts_mov_b64 exec, -1"
                 : "=&v"(t), "+s"(i), "=&s"(left) : "v"(off), "s"(ws), "s"(pend), "s"(tag), "s"(need), "i"(SLP) : "vcc", "scc", "memory");
    return left;
}
#define BST(v, off) __builtin_amdgcn_raw_buffer_store_b128((v), rs, (off), 0, 16)
#define BLD(off) __builtin_amdgcn_raw_buffer_load_b128(rs, (off), 0, 16)
#define FR(p, off) (*(LAS const bf16x8*)((p) + (off)))
DI float swapmax(float m) { const auto r = __builtin_amdgcn_permlane32_swap(__float_as_uint(m), __float_as_uint(m), false, false); return fmaxf(__uint_as_float(r[0]), __uint_as_float(r[1])); }
DI u32x4 put_ref(u32x4 q1, float m, int hi) {
    const float m1 = rbf(m), m2 = rbf(m - m1), m3 = m - m1 - m2;
    if (hi) { q1[2] = pk_bf16(-m1, -m2); q1[3] = pk_bf16(-m3, 0.f); }
    return q1;
}
DI u64 mix(u64 v) {
    const u64 e = ((u64)(unsigned)__builtin_amdgcn_readfirstlane((unsigned)(v >> 32)) << 32) | (unsigned)__builtin_amdgcn_readfirstlane((unsigned)v);
    u64 z = e + 0x9E3779B97F4A7C15ull; z = (z ^ (z >> 30)) * 0xBF58476D1CE4E5B9ull; z = (z ^ (z >> 27)) * 0x94D049BB133111EBull; return (z ^ (z >> 31)) | 1ull; }
DI void transpose_tile(LAS const char* stage, LAS char* scratch, int w, int lane, u32x4& o0, u32x4& o1) {
    LAS const char* p = stage + (lane >> 4) * 2048 + ((lane & 15) >> 2) * 128 + w * 32 + (lane & 3) * 8;
    const bf16x8 t0 = cat8(trread(p), trread(p + 512)), t1 = cat8(trread(p + 1024), trread(p + 1536));
    LAS char* sc = scratch + (lane & 15) * 144 + (lane >> 4) * 32;
    *(LAS bf16x8*)sc = t0; *(LAS bf16x8*)(sc + 16) = t1;
    LAS const char* sr = scratch + (lane >> 3) * 144 + (lane & 7) * 16;
    o0 = *(LAS const u32x4*)sr; o1 = *(LAS const u32x4*)(sr + 8 * 144);
}

DI void role_a(const uint16_t* __restrict__ x, const uint16_t* __restrict__ wqkv, uint16_t* __restrict__ out, char* __restrict__ ws, LAS char* lds, int w, int lane) {
    const int head = 2 * (blockIdx.x & 7) + ((blockIdx.x >> 3) & 1), grp = blockIdx.x >> 4;
    const unsigned lds0 = (unsigned)(uintptr_t)lds;
    const int r32 = lane & 31, hi = lane >> 5;
    const int wsh = (head & 7) == 7 ? 4 : 0;
    const unsigned wv = (unsigned)((lane >> 2) * 3 * D + (lane & 3) * 8) * 2;
    const char* wb = (const char*)(wqkv + DH * head - wsh + (16 * w) * 3 * D);
    const unsigned xv0 = (unsigned)((lane >> 3) * D + (((lane & 7) ^ (lane >> 4)) * 8)) * 2, xv1 = xv0 ^ 64;
    const char* xb = (const char*)(x + (128 * grp + 32 * w) * D);
    const int rho = ((blockIdx.x >> 3) + ((blockIdx.x >> 1) & 3)) % 7;
#define SP(st) ((st) + rho >= 7 ? (st) + rho - 7 : (st) + rho)
#define WQO(st) ((st) < 4 ? LWQ + (st) * 4096 : ((st) < 6 ? 3 * WSL + ((st) - 4) * 4096 : 0))
#define LOADW(st) glds2(wb + SP(st) * (64 * 3 * D * 2), wv + 2 * D, wv + 4 * D - 1024, lds0 + ((st) & 3) * WSL + w * 2048)
#define LOADQ(st) glds1(wb + SP(st) * (64 * 3 * D * 2), wv, lds0 + WQO(st) + w * 1024)
#define LOADX(st) glds4(xb + SP(st) * 128, xv0, xv1 + 16 * D - 1024, xv0 + 32 * D - 2048, xv1 + 48 * D - 3072, lds0 + LXR + ((st) % 3) * XSL + w * 4096)
    LOADW(0); LOADX(0);
    LOADW(1); LOADX(1);
    LAS unsigned* sy = (LAS unsigned*)(lds + LSY);
    const __amdgpu_buffer_rsrc_t rs = __builtin_amdgcn_make_buffer_rsrc(ws, 0, WS_END, 0x00020000);
    const int xs = (r32 >> 1) & 7;
    LAS const char* xp[4];
#pragma unroll
    for (int ks = 0; ks < 4; ++ks) xp[ks] = lds + LXR + w * 4096 + r32 * 128 + (((2 * ks + hi) ^ xs) * 16);
    LAS const char* wp = lds + (8 * hi + ((lane & 15) >> 2)) * 64 + ((lane >> 4) & 1) * 32 + (lane & 3) * 8 + wsh * 2;
    f32x16 aq = {}, ak = {}, av = {};
    bf16x8 xf[7][4]; s16x4 fr[4][4];
#define RX(st, ks) xf[st][ks] = FR(xp[ks], ((st) % 3) * XSL)
#define RW(st, ks, j) fr[ks][j] = trread(wp + ((st) & 3) * WSL + (ks) * 2048 + ((j) >> 1) * 1024 + ((j) & 1) * 256)
#define MK(st, ks) ak = MFMA_BF(cat8(fr[ks][0], fr[ks][1]), xf[st][ks], ak); SBAR()
#define MV(st, ks) av = MFMA_BF(xf[st][ks], cat8(fr[ks][2], fr[ks][3]), av); SBAR()
#define QSTEP(st, ks) MK(st, ks); RW(st, ks + 1, 0); RW(st, ks + 1, 1); SBAR(); MV(st, ks); RW(st, ks + 1, 2); RW(st, ks + 1, 3); SBAR()
    u64 tag = 0;
    asm volatile("s_waitcnt vmcnt(6)" ::: "memory");
    arrive(sy + SY_F); LOADW(2); LOADX(2); await(sy + SY_F, 4);
    RX(0, 0); RW(0, 0, 0); RW(0, 0, 1); RW(0, 0, 2); RW(0, 0, 3); RX(0, 1); RX(0, 2); RX(0, 3); SBAR();
#pragma unroll
    for (int st = 0; st < 7; ++st) {
        QSTEP(st, 0); if (st < 4) LOADW(st + 3); if (st == 4) { LOADQ(4); LOADQ(5); } if (st == 5) LOADQ(6);
        QSTEP(st, 1); if (st < 4) LOADX(st + 3);
        QSTEP(st, 2); if (st == 3) { LOADQ(0); LOADQ(1); LOADQ(2); LOADQ(3); }
        if (st < 6) {
            if (st == 3) asm volatile("s_waitcnt vmcnt(16)" ::: "memory"); else if (st == 5) asm volatile("s_waitcnt vmcnt(7)" ::: "memory"); else asm volatile("s_waitcnt vmcnt(12)" ::: "memory");
            arrive(sy + SY_F + 1 + st); SBAR();
        }
        MK(st, 3);
        if (st < 6) { await(sy + SY_F + 1 + st, 4); RX(st + 1, 0); RW(st + 1, 0, 0); RW(st + 1, 0, 1); RW(st + 1, 0, 2); RW(st + 1, 0, 3); } SBAR();
        MV(st, 3); if (st < 6) { RX(st + 1, 1); RX(st + 1, 2); RX(st + 1, 3); } SBAR();
        if (st == 2) tag = get_tag(sy);
    }
    const float cq = 0.18898223650461362f * 1.4426950408889634f;
    const unsigned voff = lane * 16;
    u32x4 q0, q1, k0, k1, v0, v1;
    const unsigned tb = WS_KV + (unsigned)(head * 64 + 4 * grp + w) * 4096 + voff;
#pragma unroll
    for (int i = 0; i < 4; ++i) { k0[i] = pk_bf16(ak[2 * i], ak[2 * i + 1]); k1[i] = pk_bf16(ak[8 + 2 * i], ak[9 + 2 * i]); }
    if (hi) { k1[2] = 0x3f803f80u; k1[3] = 0x00003f80u; }
    BST(k0, tb); BST(k1, tb + 1024);
#pragma unroll
    for (int i = 0; i < 4; ++i) {
        v0[i] = pk_bf16(av[2 * i], av[2 * i + 1]); v1[i] = pk_bf16(av[8 + 2 * i], av[9 + 2 * i]);
        if (r32 >= DH) { v0[i] = v1[i] = (r32 == DH ? 0x3f803f80u : 0u); }
    }
    BST(v0, tb + 2048); BST(v1, tb + 3072);
    asm volatile("s_waitcnt vmcnt(4)" ::: "memory");
    arrive(sy + SY_F + 7); await(sy + SY_F + 7, 4);
    {
        s16x4 fq[4][2];
#define RQ(j) do { fq[(j) & 3][0] = trread(wp + WQO((j) >> 2) + ((j) & 3) * 1024); fq[(j) & 3][1] = trread(wp + WQO((j) >> 2) + ((j) & 3) * 1024 + 256); } while (0)
        RQ(0); RQ(1); RQ(2); SBAR();
#pragma unroll
        for (int j = 0; j < 28; ++j) {
            aq = MFMA_BF(cat8(fq[j & 3][0], fq[j & 3][1]), xf[j >> 2][j & 3], aq); SBAR(); if (j < 25) RQ(j + 3); SBAR();
            if (j == 13) { drain(); put_flag(ws, FL_KV + (unsigned)((head * 4 + w) * 16 + grp) * FS, tag); }
        }
    }
#pragma unroll
    for (int i = 0; i < 4; ++i) { q0[i] = pk_bf16(aq[2 * i] * cq, aq[2 * i + 1] * cq); q1[i] = pk_bf16(aq[8 + 2 * i] * cq, aq[9 + 2 * i] * cq); }
    if (hi) { q1[2] = 0; q1[3] = 0; }
    const unsigned kvf = FL_KV + (unsigned)((head * 4 + w) * 16 + ((grp + lane) & 15)) * FS;
    const f32x16 zero = {};
    {
        f32x16 s = MFMA_BF(BC8(k0), BC8(q0), zero); s = MFMA_BF(BC8(k1), BC8(q1), s);
        float m = s[0];
#pragma unroll
        for (int i = 1; i < 16; ++i) m = fmaxf(m, s[i]);
        q1 = put_ref(q1, swapmax(m), hi);
    }
    { LAS u32x4* qo = (LAS u32x4*)(lds + LQ + w * 2048) + lane; qo[0] = q0; qo[64] = q1; }
    arrive(sy + SY_Q);
    const u64 kv1 = __hip_atomic_load((GAS u64*)(ws + kvf), __ATOMIC_RELAXED, __HIP_MEMORY_SCOPE_AGENT); SBAR();
    f32x16 O[4] = {}; PIN(O[0]); PIN(O[1]); PIN(O[2]); PIN(O[3]);
    await(sy + SY_Q, 4);
    bf16x8 Q[4][2];
    Q[0][0] = BC8(q0); Q[0][1] = BC8(q1);
#pragma unroll
    for (int i = 1; i < 4; ++i) { LAS const char* p = lds + LQ + ((w + i) & 3) * 2048 + voff; Q[i][0] = FR(p, 0); Q[i][1] = FR(p, 1024); }
    u32x4 kv[4][4] = {{k0, k1, v0, v1}, {}, {}, {}};
    f32x16 sa, sb;
    u32x4 p0, p1 = {};
#define GRP(SC, i, P, j) do { float e0 = EX(SC[i]); SBAR(); float e1 = EX(SC[i + 1]); SBAR(); float e2 = EX(SC[i + 2]); SBAR(); float e3 = EX(SC[i + 3]); SBAR(); \
        P[j] = pk_bf16(e0, e1); SBAR(); P[j + 1] = pk_bf16(e2, e3); PIN(P); SBAR(); } while (0)
#define SLOT(SC, SN, cb, i, QK, PV1, LD) do { constexpr int nb_ = (i) < 3 ? (cb) : ((cb) + 1) % 4, pb_ = (i) > 0 ? (cb) : ((cb) + 3) % 4; \
        if (QK) SN = MFMA_BF(BC8(kv[nb_][0]), Q[((i) + 1) & 3][0], zero); SBAR(); GRP(SC, 0, p0, 0); \
        if (PV1) O[((i) + 3) & 3] = MFMA_BF(BC8(kv[pb_][3]), BC8(p1), O[((i) + 3) & 3]); SBAR(); \
        if (LD) { kv[((cb) + 3) % 4][i] = __builtin_amdgcn_raw_buffer_load_b128(rs, voff + (i) * 1024, kvo, 16); SBAR(); } GRP(SC, 4, p0, 2); \
        if (QK) SN = MFMA_BF(BC8(kv[nb_][1]), Q[((i) + 1) & 3][1], SN); SBAR(); GRP(SC, 8, p1, 0); \
        O[i] = MFMA_BF(BC8(kv[cb][2]), BC8(p0), O[i]); SBAR(); GRP(SC, 12, p1, 2); } while (0)
    const unsigned kvb = WS_KV + (unsigned)(head * 64 + w) * 4096;
    unsigned kvo = 0;
    u64 miss = __ballot(kv1 != tag) & 0xfffe;
    if (miss & 6) miss = wait_flags<2>(ws, kvf, tag, miss, 6);
    CFENCE();
#pragma unroll
    for (int j = 1; j < 3; ++j)
#pragma unroll
        for (int f = 0; f < 4; ++f) kv[j][f] = __builtin_amdgcn_raw_buffer_load_b128(rs, voff + f * 1024, kvb + ((grp + j) & 15) * 16384, 16);
    arrive(sy + SY_G);
    sa = MFMA_BF(BC8(k0), Q[0][0], zero); sa = MFMA_BF(BC8(k1), Q[0][1], sa);
    int nx = grp + 3;
#define TILE(cb) do { kvo = kvb + (nx & 15) * 16384; ++nx; SBAR(); \
        SLOT(sa, sb, cb, 0, true, true, true); SLOT(sb, sa, cb, 1, true, true, true); SLOT(sa, sb, cb, 2, true, true, true); SLOT(sb, sa, cb, 3, true, true, true); } while (0)
#pragma unroll 1
    for (unsigned msk = 0x78; msk < 0x80000; msk <<= 4) { if (__builtin_expect((miss & msk) != 0, 0)) miss = wait_flags<2>(ws, kvf, tag, miss, msk); TILE(0); TILE(1); TILE(2); TILE(3); }
    O[3] = MFMA_BF(BC8(kv[3][3]), BC8(p1), O[3]);
    f32x16 o = O[0];
    {
#pragma unroll
        for (int i = 1; i < 4; ++i) {
            LAS char* ex = lds + LX + (((w + i) & 3) * 3 + i - 1) * 4096 + voff;
#pragma unroll
            for (int g = 0; g < 4; ++g) *(LAS f32x4*)(ex + g * 1024) = (f32x4){O[i][4 * g], O[i][4 * g + 1], O[i][4 * g + 2], O[i][4 * g + 3]};
        }
        arrive(sy + SY_X); await(sy + SY_X, 4);
        LAS const char* er = lds + LX + w * 3 * 4096 + voff;
#pragma unroll
        for (int i = 0; i < 3; ++i)
#pragma unroll
            for (int g = 0; g < 4; ++g) { const f32x4 r = *(LAS const f32x4*)(er + i * 4096 + g * 1024);
#pragma unroll
                for (int e = 0; e < 4; ++e) o[4 * g + e] += r[e]; }
        f32x2 chk = {0.f, 0.f};
#pragma unroll
        for (int i = 0; i < 8; ++i) chk = pfma((f32x2){o[2 * i], o[2 * i + 1]}, (f32x2){0.f, 0.f}, chk);
        if (__builtin_expect(__any(!(chk[0] + chk[1] == 0.f) || !(o[12] < 1e30f)), 0)) {
            const unsigned hb = WS_KV + (unsigned)head * 64 * 4096 + voff;
            u32x4 qr = __builtin_bit_cast(u32x4, Q[0][1]); if (hi) { qr[2] = 0; qr[3] = 0; }
            float mm = -INFINITY;
            wait_flags<4>(ws, FL_KV + (unsigned)(head * 64 + lane) * FS, tag, ~0ull, ~0ull);
#pragma unroll 1
            for (int t = 0; t < 64; ++t) {
                f32x16 s = MFMA_BF(BC8(BLD(hb + t * 4096)), Q[0][0], zero); s = MFMA_BF(BC8(BLD(hb + t * 4096 + 1024)), BC8(qr), s);
#pragma unroll
                for (int i = 0; i < 16; ++i) mm = fmaxf(mm, s[i]);
            }
            qr = put_ref(qr, swapmax(mm), hi);
            o = zero;
#pragma unroll 1
            for (int t = 0; t < 64; ++t) {
                f32x16 s = MFMA_BF(BC8(BLD(hb + t * 4096)), Q[0][0], zero); s = MFMA_BF(BC8(BLD(hb + t * 4096 + 1024)), BC8(qr), s);
#pragma unroll
                for (int i = 0; i < 4; ++i) { p0[i] = pk_bf16(EX(s[2 * i]), EX(s[2 * i + 1])); p1[i] = pk_bf16(EX(s[8 + 2 * i]), EX(s[9 + 2 * i])); }
                o = MFMA_BF(BC8(BLD(hb + t * 4096 + 2048)), BC8(p0), o); o = MFMA_BF(BC8(BLD(hb + t * 4096 + 3072)), BC8(p1), o);
            }
        }
    }
    {
        const auto r = __builtin_amdgcn_permlane32_swap(__float_as_uint(o[12]), __float_as_uint(o[12]), false, false);
        const float rl = __builtin_amdgcn_rcpf(__uint_as_float(r[1]));
        u32x4 o0, o1;
#pragma unroll
        for (int i = 0; i < 4; ++i) { o0[i] = pk_bf16(o[2 * i] * rl, o[2 * i + 1] * rl); o1[i] = pk_bf16(o[8 + 2 * i] * rl, o[9 + 2 * i] * rl); }
        if (hi) { o1[2] = 0; o1[3] = 0; }
        const unsigned fi = (unsigned)((4 * grp + w) * 16 + head);
        BST(o0, WS_O + fi * 2048 + voff); BST(o1, WS_O + fi * 2048 + 1024 + voff);
        drain();
        put_flag(ws, FL_O + fi * FS, tag);
    }
    const int xcd = blockIdx.x & 7, bi = blockIdx.x >> 3;
    if (xcd < 7) {
        wait_flags<2>(ws, FL_BT + (unsigned)(xcd * 128 + w * 32 + r32) * FS, tag, 0xffffffffull, 0xffffffffull);
        const unsigned bo = WS_BT + (unsigned)((64 * xcd + 16 * w + (lane >> 3)) * S + 64 * bi + (lane & 7) * 8) * 2;
        const u32x4 b0 = BLD(bo), b1 = BLD(bo + 8 * S * 2);
        *(LAS u32x4*)(lds + 65536 + w * 2048 + voff) = b0; *(LAS u32x4*)(lds + 65536 + w * 2048 + 1024 + voff) = b1;
        arrive(sy + SY_T2); await(sy + SY_T2, 4);
        u32x4 o0, o1; transpose_tile(lds + 65536, lds + 73728 + w * 2304, w, lane, o0, o1);
        const __amdgpu_buffer_rsrc_t ro = __builtin_amdgcn_make_buffer_rsrc(out, 0, S * 2 * D * 2, 0x00020000);
        const unsigned oo = (unsigned)((64 * bi + 16 * w + (lane >> 3)) * 2 * D + D + 64 * xcd) * 2 + (lane & 7) * 16;
        __builtin_amdgcn_raw_buffer_store_b128(o0, ro, oo, 0, 16); __builtin_amdgcn_raw_buffer_store_b128(o1, ro, oo + 8 * 2 * D * 2, 0, 16);
    }
}

DI void role_b(const uint16_t* __restrict__ wo, const uint16_t* __restrict__ u, const uint16_t* __restrict__ lar, const uint16_t* __restrict__ aim,
               const uint16_t* __restrict__ cre, const uint16_t* __restrict__ cim, const uint16_t* __restrict__ ldt, const uint16_t* __restrict__ dskip,
               uint16_t* __restrict__ out, char* __restrict__ ws, LAS char* lds, int w, int lane, u64 code0) {
    u64 tag;
    const unsigned lds0 = (unsigned)(uintptr_t)lds;
    LAS unsigned* sy = (LAS unsigned*)(lds + LSY);
    const __amdgpu_buffer_rsrc_t rs = __builtin_amdgcn_make_buffer_rsrc(ws, 0, WS_END, 0x00020000);
    const int b = blockIdx.x, xcd = b & 7, bi = b >> 3, r32 = lane & 31, hi = lane >> 5;
    const unsigned voff = lane * 16;
    LAS char* tab = lds + LB + (w >> 1) * CBW + (w & 1) * TABW;
    unsigned pf = 0;
#define CODE_PF() asm volatile("global_load_dword %0, %1, %2" : "+v"(pf) : "v"(min((unsigned)((bi * 4 + w) * 3 + (lane < 3 ? lane : 0)), CODE_LINES - 1) * 128), "s"(code0) : "memory")
    const int qt = 8 * xcd + (bi >> 2), ct = 4 * (bi & 3) + w, cl = ct < 14 ? ct : 13;
    bf16x8 wf[32];
#define LOADWF(i) wf[i] = BC8(__builtin_amdgcn_raw_buffer_load_b128(rs, voff, WS_WO + (unsigned)(((8 * w + (i)) & 31) * 14 + cl) * 1024, 16))
    if (xcd < 7) {
        const int cc = w >> 1, hh = w & 1, pair = 32 * xcd + bi, ch = 2 * pair + cc;
        LAS char* cb = lds + LB + cc * CBW;
        const unsigned po = (unsigned)(ch * NS + 128 * hh) / 2 + lane;
        const unsigned pl = ((const unsigned*)lar)[po], pa = ((const unsigned*)aim)[po], pr = ((const unsigned*)cre)[po], pi = ((const unsigned*)cim)[po];
        const unsigned uo = (unsigned)((64 * bi + 16 * w + (lane >> 3)) * D + 64 * xcd + (lane & 7) * 8) * 2;
        glds2(u, uo, uo + 8 * D * 2 - 1024, lds0 + LT1 + w * 2048);
        const u64 e0 = __hip_atomic_load((GAS u64*)ws, __ATOMIC_RELAXED, __HIP_MEMORY_SCOPE_AGENT);
        u32x2 gi[2][3];
#pragma unroll
        for (int ps = 0; ps < 2; ++ps)
#pragma unroll
            for (int i = 0; i < 3; ++i) gi[ps][i] = ((const u32x2*)g_idx.v[128 * hh + 64 * ps + lane])[i];
        CODE_PF();
        const unsigned lw = ((const unsigned*)ldt)[pair], dw = ((const unsigned*)dskip)[pair];
        LAS char* ui = cb + UOF;
        const u32x4 z4 = {};
        *(LAS u32x4*)(ui + (64 * hh + lane) * 16) = z4; *(LAS u32x4*)(ui + (96 + lane) * 16) = z4;
        const float dt = rbf(expf(cc ? bf_hi(lw) : bf_lo(lw)));
        drain(); asm volatile("" :: "v"(pf));
        arrive(sy + SY_WO);
        tag = mix(e0);
        const float cm = wave_max(fmaxf(fmaxf(fabsf(bf_lo(pr)), fabsf(bf_hi(pr))), fmaxf(fabsf(bf_lo(pi)), fabsf(bf_hi(pi)))));
        unsigned ce = __float_as_uint(cm) & 0x7f800000u; ce = ce < 0x01000000u ? 0x01000000u : (ce > 0x7e800000u ? 0x7e800000u : ce);
        const float cscale = __uint_as_float(0x7f000000u - ce), oscale = 2.0f * dt * __uint_as_float(ce);
        LAS unsigned* tw = (LAS unsigned*)(tab + lane * 4);
        LAS const char* trd = tab + r32 * TROW + hi * 16;
        f32x16 dacc = {};
        f32x2 z1, z8, z256, x0;
#define ANCH(vl, va, vr, vi) do { const float a2 = (-rbf(expf(vl)) * dt) * 1.4426950408889634f, rev = ((va) * dt) * 0.15915494309189535f; \
            z1 = anchor(a2, -rev, 1.f); z8 = anchor(a2, rev, 8.f); z256 = anchor(a2, -rev, 256.f); x0 = (f32x2){(vr) * cscale, -(vi) * cscale}; \
            PIN(z1); PIN(z8); PIN(z256); SBAR(); } while (0)
#define TW(row) tw[(row) * (TROW / 4)]
#define CHAIN() do { \
            const f32x2 z1s = rot(z1), zs = rot(z256), z8s = rot(z8); \
            f32x2 X[8]; const f32x2 z16 = cmul(z8, z8, z8s); X[0] = x0; X[1] = cmul(x0, z1, z1s); f32x2 a = cmul(x0, z256, zs); \
            const f32x2 z24 = cmul(z16, z8, z8s), z32 = cmul(z16, z16, rot(z16)), z32s = rot(z32); X[2] = cmul(X[1], z1, z1s); f32x2 y = z32; \
            f32x2 c4 = cmul(X[2], z256, zs); \
            unsigned c[2][4], d0 = pk_f16(X[0]), d1 = pk_f16(X[1]), d2 = pk_f16(z8), d3 = pk_f16(z16), d4 = pk_f16(z24), one = 0x3c00u; \
            c[0][0] = pk_f16(y); c[0][1] = pk_f16(X[2]); c[0][2] = pk_f16(a); c[0][3] = pk_f16(c4); \
            TW(0) = d0; TW(1) = d1; TW(30) = d2; TW(31) = d3; TW(32) = d4; TW(21) = one; TW(29) = one; SBAR(); \
            _Pragma("unroll") for (int i = 1; i < 9; ++i) { \
                f32x2 t0, t4, t5, t6; const int j = i - 1; unsigned* pc = c[j & 1]; unsigned* nc = c[i & 1]; \
                if (i < 8) { if (i < 7) t0 = y.xx * z32; if (i < 6) t4 = X[i + 1].xx * z1; t5 = a.xx * z256; \
                    if (i == 3) t6 = X[4].xx * z256; if (i == 4 || i == 5) t6 = c4.xx * z256; if (i == 6) t6 = X[6].xx * z256; } SBAR(); \
                if (j < 7) TW(22 + j) = pc[0]; if (j < 6) TW(j + 2) = pc[1]; TW(j == 0 ? 8 : (j == 1 ? 12 : (j == 2 ? 14 : 13 + j))) = pc[2]; \
                if (j == 0) TW(9) = pc[3]; if (j == 3) TW(10) = pc[3]; if (j == 4) TW(13) = pc[3]; if (j == 5) TW(15) = pc[3]; if (j == 6) TW(11) = pc[3]; SBAR(); \
                if (i < 8) { if (i < 7) y = pfma(y.yy, z32s, t0); if (i < 6) X[i + 2] = pfma(X[i + 1].yy, z1s, t4); a = pfma(a.yy, zs, t5); \
                    if (i == 3) c4 = pfma(X[4].yy, zs, t6); if (i == 4 || i == 5) c4 = pfma(c4.yy, zs, t6); if (i == 6) c4 = pfma(X[6].yy, zs, t6); SBAR(); \
                    if (i < 7) nc[0] = pk_f16(y); if (i < 6) nc[1] = pk_f16(X[i + 2]); nc[2] = pk_f16(a); if (i >= 3 && i <= 6) nc[3] = pk_f16(c4); \
                    asm volatile("" :: "v"(pc[0]), "v"(pc[1]), "v"(pc[2]), "v"(pc[3])); SBAR(); } \
            } } while (0)
        f16x8 fa[8]; u32x4 fp[8], fq[8];
        LAS const char* trp = tab + (21 + (r32 >> 2)) * TROW + hi * 16;
        LAS const char* trq = tab + (29 + (r32 & 3)) * TROW + hi * 16;
#define RDT() do { _Pragma("unroll") for (int kk = 0; kk < 8; ++kk) { fp[kk] = *(LAS const u32x4*)(trp + kk * 32); fq[kk] = *(LAS const u32x4*)(trq + kk * 32); fa[kk] = *(LAS const f16x8*)(trd + kk * 32); } SBAR(); } while (0)
#define MMT() do { _Pragma("unroll") for (int kk = 0; kk < 8; ++kk) dacc = MFMA_F16(fa[kk], cmul4h(fp[kk], fq[kk]), dacc); } while (0)
        ANCH(bf_lo(pl), bf_lo(pa), bf_lo(pr), bf_lo(pi));
        await(sy + SY_WO, 4);
        set_tag(sy, tag);
        {
            u32x4 o0, o1; transpose_tile(lds + LT1, tab, w, lane, o0, o1);
            const unsigned to = WS_UT + (unsigned)((64 * xcd + 16 * w + (lane >> 3)) * S + 64 * bi) * 2 + (lane & 7) * 16;
            __builtin_amdgcn_s_setprio(3); BST(o0, to); BST(o1, to + 8 * S * 2); __builtin_amdgcn_s_setprio(0);
        }
        CHAIN(); RDT();
        drain(); __builtin_amdgcn_s_setprio(3); put_flag(ws, FL_UT + (unsigned)(xcd * 128 + w * 32 + bi) * FS, tag); __builtin_amdgcn_s_setprio(0);
        ANCH(bf_hi(pl), bf_hi(pa), bf_hi(pr), bf_hi(pi));
        MMT(); SBAR();
        CHAIN(); RDT();
        const unsigned utf = hi ? FL_WO + (unsigned)(w * 32 + r32) * FS : FL_UT + (unsigned)(xcd * 128 + (bi >> 3) * 32 + r32) * FS;
        __builtin_amdgcn_s_setprio(3);
        const u64 utv = __hip_atomic_load((GAS u64*)(ws + utf), __ATOMIC_RELAXED, __HIP_MEMORY_SCOPE_AGENT); SBAR();
        __builtin_amdgcn_s_setprio(1);
        MMT();
        {
            LAS float* pt = (LAS float*)tab;
            *(LAS f32x4*)(pt + 8 * r32 + 4 * hi) = (f32x4){dacc[0] * oscale, dacc[1] * oscale, dacc[2] * oscale, dacc[3] * oscale};
            *(LAS f32x2*)(pt + (hi ? 384 + 2 * r32 : 256 + 4 * r32)) = (f32x2){dacc[4] * oscale, dacc[5] * oscale};
            *(LAS f32x2*)(pt + (hi ? 448 + 2 * r32 : 258 + 4 * r32)) = (f32x2){dacc[6] * oscale, dacc[7] * oscale};
            pt[hi ? 640 + r32 : 512 + r32] = dacc[8] * oscale;
            pt[hi ? 672 + r32 : 544 + r32] = dacc[9] * oscale;
            pt[hi ? 672 + r32 : 576 + r32] = dacc[10] * oscale;
            pt[hi ? 672 + r32 : 608 + r32] = dacc[11] * oscale;
        }
        LAS unsigned* ps_ = sy + SY_P + 5 * cc;
        arrive(ps_ + 0);
        LAS char* kt = cb + KOF;
        await(ps_ + 0, 2);
#pragma unroll
        for (int ps = 0; ps < 2; ++ps) {
            const int v = 81 * hh + 64 * ps + lane;
            if (ps == 0 || lane < 17) {
                const f32x4 p0 = *(LAS const f32x4*)(cb + v * 16), p1 = *(LAS const f32x4*)(cb + TABW + v * 16);
                u32x2 r = {pk_bf16(p0[0] + p1[0], p0[1] + p1[1]), pk_bf16(p0[2] + p1[2], p0[3] + p1[3])};
                if (v == 161) r = (u32x2){0u, 0u};
                *(LAS u32x2*)(kt + v * 8) = r;
            }
        }
        arrive(ps_ + 1);
        { const u64 ms = __ballot(utv != tag); if (ms) wait_flags<8>(ws, utf, tag, ms, ms); }
        CFENCE();
        const unsigned ub = WS_UT + (unsigned)(ch * S + 1024 * hh) * 2 + voff;
        const u32x4 u0 = BLD(ub), u1 = BLD(ub + 1024); SBAR();
        await(ps_ + 1, 2);
        __builtin_amdgcn_s_setprio(1);
        {
            unsigned W[2][6];
#pragma unroll
            for (int ps = 0; ps < 2; ++ps)
#pragma unroll
                for (int i = 0; i < 6; ++i) { const unsigned gv = gi[ps][i >> 1][i & 1];
                    W[ps][i] = *(LAS const uint16_t*)(kt + (gv & 0xffffu));
                    if (i < 5) W[ps][i] |= (unsigned)*(LAS const uint16_t*)(kt + (gv >> 16)) << 16; }
            if (lane < 8) *(LAS u32x4*)(cb + (2 * hh + (lane >> 2)) * CPY + (256 + (lane & 3)) * 16) = z4;
#pragma unroll
            for (int ps = 0; ps < 2; ++ps) {
                unsigned V[5];
#pragma unroll
                for (int i = 0; i < 5; ++i) V[i] = __builtin_amdgcn_alignbit(W[ps][i + 1], W[ps][i], 16);
                LAS char* cp = cb + (128 * hh + 64 * ps + lane) * 16;
                *(LAS u32x4*)(cp) = (u32x4){W[ps][0], W[ps][1], W[ps][2], W[ps][3]};
                *(LAS u32x4*)(cp + CPY) = (u32x4){V[0], V[1], V[2], V[3]};
                *(LAS u32x4*)(cp + 2 * CPY) = (u32x4){W[ps][1], W[ps][2], W[ps][3], W[ps][4]};
                *(LAS u32x4*)(cp + 3 * CPY) = (u32x4){V[1], V[2], V[3], V[4]};
            }
        }
        *(LAS u32x4*)(ui + (32 + 32 * hh + (lane >> 2)) * 80 + (lane & 3) * 16) = u0;
        *(LAS u32x4*)(ui + (48 + 32 * hh + (lane >> 2)) * 80 + (lane & 3) * 16) = u1;
        arrive(ps_ + 2); await(ps_ + 2, 2);
        __builtin_amdgcn_s_setprio(0);
        f32x16 a0 = {}, a1 = {};
        {
            const int sb = 7 - (r32 & 7);
            LAS const char* pa = cb + (sb & 3) * CPY + (255 - (r32 >> 3) + hi) * 16 + (sb >> 2) * 8 - 64 * (16 * hh + 15) - 2048;
            LAS const char* pa2 = pa + 8; asm volatile("" : "+v"(pa2));
            LAS const char* pb = ui + (32 + r32 - 16 * hh - 15) * 80 + 16 * hi;
            s16x4 ta[2][2], tb[2][2]; bf16x8 b0[2], b1[2];
#define T4(q, off) (*(LAS const s16x4*)((q) + (off)))
#define CSTEP(i, j, RD) do { const int o_ = 64 * (15 - ((j) >> 1)) + 32 * ((j) & 1), u_ = 80 * (15 - ((j) >> 1)) + 32 * ((j) & 1); \
                a1 = MFMA_BF(cat8(ta[i][0], ta[i][1]), b1[i], a1); SBAR(); if (RD) { ta[1 - i][0] = T4(pa, o_ + 2048); ta[1 - i][1] = T4(pa2, o_ + 2048); } SBAR(); \
                a0 = MFMA_BF(cat8(ta[i][0], ta[i][1]), b0[i], a0); SBAR(); if (RD) { b1[1 - i] = FR(pb, u_ + 2560); b0[1 - i] = FR(pb, u_); } SBAR(); \
                a1 = MFMA_BF(cat8(tb[i][0], tb[i][1]), b0[i], a1); SBAR(); if (RD) { tb[1 - i][0] = T4(pa, o_); tb[1 - i][1] = T4(pa2, o_); } SBAR(); } while (0)
            ta[0][0] = T4(pa, 960 + 2048); ta[0][1] = T4(pa2, 960 + 2048); b1[0] = FR(pb, 1200 + 2560); b0[0] = FR(pb, 1200); tb[0][0] = T4(pa, 960); tb[0][1] = T4(pa2, 960);
#pragma unroll
            for (int j = 0; j < 32; ++j) {
                if (j == 31) arrive(ps_ + 3);
                CSTEP(j & 1, j + 1, j < 31);
                if (ct < 14) LOADWF(j);
                SBAR();
            }
        }
        __builtin_amdgcn_s_setprio(1);
        {
            u32x2 uu[4];
#pragma unroll
            for (int g = 0; g < 4; ++g) uu[g] = *(LAS const u32x2*)(ui + (32 + 32 * hh + r32) * 80 + 16 * g + 8 * hi);
            LAS char* ex = cb + r32 * 144 + hi * 16;
            const f32x16 snd = hh ? a0 : a1; const f32x16 y = hh ? a1 : a0;
            await(ps_ + 3, 2);
#pragma unroll
            for (int g = 0; g < 4; ++g) *(LAS f32x4*)(ex + hh * 4608 + g * 32) = (f32x4){snd[4 * g], snd[4 * g + 1], snd[4 * g + 2], snd[4 * g + 3]};
            arrive(ps_ + 4);
            const float dsk = cc ? bf_hi(dw) : bf_lo(dw);
            unsigned sk[8];
#pragma unroll
            for (int g = 0; g < 4; ++g)
#pragma unroll
                for (int e = 0; e < 2; ++e) sk[2 * g + e] = pk_bf16(bf_lo(uu[g][e]) * dsk, bf_hi(uu[g][e]) * dsk);
            await(ps_ + 4, 2);
            LAS char* sc = cb + 9216 + hh * 2048;
#pragma unroll
            for (int g = 0; g < 4; ++g) {
                const f32x4 p = *(LAS const f32x4*)(ex + (1 - hh) * 4608 + g * 32);
                u32x2 r;
#pragma unroll
                for (int e = 0; e < 2; ++e) {
                    const unsigned yb = pk_bf16(y[4 * g + 2 * e] + p[2 * e], y[4 * g + 2 * e + 1] + p[2 * e + 1]);
                    r[e] = pk_bf16(bf_lo(yb) + bf_lo(sk[2 * g + e]), bf_hi(yb) + bf_hi(sk[2 * g + e]));
                }
                *(LAS u32x2*)(sc + 64 * r32 + 16 * g + 8 * hi) = r;
            }
            const u32x4 o0 = *(LAS const u32x4*)(sc + voff), o1 = *(LAS const u32x4*)(sc + 1024 + voff);
            const unsigned bo = WS_BT + (unsigned)(ch * S + 1024 * hh) * 2 + voff;
            BST(o0, bo); BST(o1, bo + 1024);
        }
        arrive(sy + SY_BG);
        drain();
        put_flag(ws, FL_BT + (unsigned)(xcd * 128 + bi * 4 + w) * FS, tag);
    } else {
        {
            const unsigned nb = (bi & 1) ? 12 * 2 * D : 16 * 2 * D;
            unsigned v4[4];
#pragma unroll
            for (int i = 0; i < 4; ++i) { const unsigned o = (unsigned)(w * 4096 + i * 1024) + voff; v4[i] = (o < nb ? o : 0u) + 4096 - i * 1024; }
            glds4((const char*)(wo + (DH * (bi >> 1) + 16 * (bi & 1)) * D) - 4096, v4[0], v4[1], v4[2], v4[3], lds0 + LB + w * 4096);
        }
        const u64 e0 = __hip_atomic_load((GAS u64*)ws, __ATOMIC_RELAXED, __HIP_MEMORY_SCOPE_AGENT);
        CODE_PF();
        drain(); asm volatile("" :: "v"(pf));
        tag = mix(e0);
        arrive(sy + SY_WO); await(sy + SY_WO, 4);
        set_tag(sy, tag);
        LAS const char* p = lds + LB + (4 * hi + ((lane & 15) >> 2)) * (2 * D) + ((lane >> 4) & 1) * 32 + (lane & 3) * 8;
#pragma unroll
        for (int i = 0; i < 4; ++i) {
            const int ct = w + 4 * i;
            if (ct < 14) {
                u32x4 o = __builtin_bit_cast(u32x4, cat8(trread(p + ct * 64), trread(p + 8 * 2 * D + ct * 64)));
                if (hi && (bi & 1)) { o[2] = 0; o[3] = 0; }
                BST(o, WS_WO + (unsigned)(bi * 14 + ct) * 1024 + voff);
            }
        }
        drain();
        put_flag(ws, FL_WO + (unsigned)(w * 32 + bi) * FS, tag);
        __builtin_amdgcn_s_setprio(0);
        arrive(sy + SY_BG);
        await_slow(sy + SY_G, 4);
        wait_flags<8>(ws, FL_WO + (unsigned)(w * 32 + r32) * FS, tag, 0xffffffffull, 0xffffffffull);
        if (ct < 14) {
#pragma unroll
        for (int i = 0; i < 32; ++i) { LOADWF(i); __builtin_amdgcn_s_sleep(2); SBAR(); } }
    }
    const __amdgpu_buffer_rsrc_t ro = __builtin_amdgcn_make_buffer_rsrc(out, 0, S * 2 * D * 2, 0x00020000);
    await(sy + SY_BG, 4);
    await_slow(sy + SY_X, 4);
    wait_flags<2>(ws, FL_O + (unsigned)(qt * 16 + 4 * w + (lane & 3)) * FS, tag, 15, 15);
    u32x4 of[8];
#pragma unroll
    for (int i = 0; i < 8; ++i) of[i] = BLD(WS_O + (unsigned)(qt * 32 + 8 * w + i) * 1024 + voff);
#pragma unroll
    for (int i = 0; i < 8; ++i) *(LAS u32x4*)(lds + LB + (8 * w + i) * 1024 + voff) = of[i];
    arrive(sy + SY_B2);
    f32x16 acc = {};
    if (ct < 14) {
#pragma unroll
        for (int i = 0; i < 8; ++i) acc = MFMA_BF(wf[i], BC8(of[i]), acc);
    }
    SBAR();
    await(sy + SY_B2, 4);
    if (ct < 14) {
        LAS const char* po[3];
#pragma unroll
        for (int j = 0; j < 3; ++j) po[j] = lds + LB + ((w + 1 + j) & 3) * 8192 + voff;
        bf16x8 og[4];
#pragma unroll
        for (int i = 0; i < 3; ++i) og[i] = FR(po[0], i * 1024);
#pragma unroll
        for (int i = 0; i < 24; ++i) { if (i < 21) og[(i + 3) & 3] = FR(po[(i + 3) >> 3], ((i + 3) & 7) * 1024); SBAR(); acc = MFMA_BF(wf[8 + i], og[i & 3], acc); SBAR(); }
        LAS char* sc = lds + LB + 40960 + w * 2304;
#pragma unroll
        for (int g = 0; g < 4; ++g) *(LAS u32x2*)(sc + r32 * 72 + 16 * g + 8 * hi) = (u32x2){pk_bf16(acc[4 * g], acc[4 * g + 1]), pk_bf16(acc[4 * g + 2], acc[4 * g + 3])};
        LAS const char* sr = sc + (lane >> 2) * 72 + (lane & 3) * 16;
        const u32x2 a0 = *(LAS const u32x2*)sr, a1 = *(LAS const u32x2*)(sr + 8), b0 = *(LAS const u32x2*)(sr + 16 * 72), b1 = *(LAS const u32x2*)(sr + 16 * 72 + 8);
        const unsigned oo = (unsigned)((32 * qt + (lane >> 2)) * 2 * D + 32 * ct) * 2 + (lane & 3) * 16;
        __builtin_amdgcn_raw_buffer_store_b128((u32x4){a0[0], a0[1], a1[0], a1[1]}, ro, oo, 0, 16);
        __builtin_amdgcn_raw_buffer_store_b128((u32x4){b0[0], b0[1], b1[0], b1[1]}, ro, oo + 16 * 2 * D * 2, 0, 16);
    }
    if (b == 0 && w == 0) put_flag(ws, 0, tag);
}

__global__ __launch_bounds__(512, 2) void fused(const uint16_t* __restrict__ x, const uint16_t* __restrict__ wqkv, const uint16_t* __restrict__ wo,
                                               const uint16_t* __restrict__ u, const uint16_t* __restrict__ lar, const uint16_t* __restrict__ aim,
                                               const uint16_t* __restrict__ cre, const uint16_t* __restrict__ cim, const uint16_t* __restrict__ ldt,
                                               const uint16_t* __restrict__ dskip, uint16_t* __restrict__ out, char* __restrict__ ws) {
    extern __shared__ __attribute__((aligned(16))) char smem[];
    LAS char* lds = (LAS char*)smem;
    const int lane = threadIdx.x & 63, w = __builtin_amdgcn_readfirstlane(threadIdx.x >> 6);
    const u64 code0 = __builtin_amdgcn_s_getpc() & ~255ull;
    if (w >= 4) __builtin_amdgcn_s_setprio(3);
    if (w == 4) ((LAS unsigned*)(lds + LSY))[lane] = 0;
    asm volatile("" :: "s"(x), "s"(wqkv), "s"(wo), "s"(u), "s"(lar), "s"(aim), "s"(cre), "s"(cim), "s"(ldt), "s"(dskip), "s"(out), "s"(ws));
    asm volatile("s_waitcnt lgkmcnt(0)\n\ts_barrier" ::: "memory");
    if (w < 4) role_a(x, wqkv, out, ws, lds, w, lane);
    else role_b(wo, u, lar, aim, cre, cim, ldt, dskip, out, ws, lds, w - 4, lane, code0);
}
}

extern "C" void kernel_launch(void* const* d_in, const int* in_sizes, int n_in, void* d_out, int out_size, void* d_ws, size_t ws_size, hipStream_t stream) {
    using namespace k;
    (void)hipFuncSetAttribute((const void*)fused, hipFuncAttributeMaxDynamicSharedMemorySize, LDS_BYTES);
    const uint16_t* const* in = (const uint16_t* const*)d_in;
    fused<<<256, 512, LDS_BYTES, stream>>>(in[0], in[1], in[2], in[3], in[4], in[5], in[6], in[7], in[8], in[9], (uint16_t*)d_out, (char*)d_ws);
}
```

```cpp
#include <hip/hip_runtime.h>
#include <hip/hip_bf16.h>
#include <stdint.h>

typedef short bf16x8 __attribute__((ext_vector_type(8)));
typedef _Float16 f16x8 __attribute__((ext_vector_type(8)));
typedef _Float16 f16x2 __attribute__((ext_vector_type(2)));
typedef __bf16 bf16x2 __attribute__((ext_vector_type(2)));
typedef short s16x4 __attribute__((ext_vector_type(4)));
typedef float f32x16 __attribute__((ext_vector_type(16)));
typedef float f32x4 __attribute__((ext_vector_type(4)));
typedef float f32x2 __attribute__((ext_vector_type(2)));
typedef unsigned u32x4 __attribute__((ext_vector_type(4)));
typedef unsigned u32x2 __attribute__((ext_vector_type(2)));
typedef unsigned long long u64;
#define LAS __attribute__((address_space(3)))
#define GAS __attribute__((address_space(1)))
#define DI __device__ __forceinline__
#define MFMA_BF(a, b, c) __builtin_amdgcn_mfma_f32_32x32x16_bf16((a), (b), (c), 0, 0, 0)
#define MFMA_F16(a, b, c) __builtin_amdgcn_mfma_f32_32x32x16_f16((a), (b), (c), 0, 0, 0)
#define SBAR() __builtin_amdgcn_sched_barrier(0)
#define CFENCE() asm volatile("" ::: "memory")
#define PIN(x) asm volatile("" : "+v"(x))
#define BC8(x) __builtin_bit_cast(bf16x8, (x))
#define EX(x) __builtin_amdgcn_exp2f(x)

namespace k {
constexpr int S = 2048, D = 448, DH = 28, NS = 256;
constexpr unsigned FS = 128;
constexpr unsigned FL_KV = FS, FL_O = FL_KV + 1024 * FS, FL_UT = FL_O + 1024 * FS, FL_BT = FL_UT + 896 * FS, FL_WO = FL_BT + 896 * FS;
constexpr unsigned WS_KV = 512u << 10;
constexpr unsigned WS_O = WS_KV + (4u << 20);
constexpr unsigned WS_WO = WS_O + (2u << 20);
constexpr unsigned WS_UT = WS_WO + (448u << 10);
constexpr unsigned WS_BT = WS_UT + D * S * 2;
constexpr unsigned WS_END = WS_BT + D * S * 2;
constexpr int WSL = 8192, LWQ = 4 * WSL, LXR = LWQ + 16384, XSL = 16384, LB = LXR + 3 * XSL;
constexpr int LQ = LXR, LX = 0;
constexpr int TROW = 272, TABW = 33 * TROW, CPY = 4160, UOF = 2 * TABW, KOF = UOF + 7680, CBW = KOF + 1312;
constexpr int LT1 = LB + 2 * CBW, LSY = LT1 + 8192, LDS_BYTES = LSY + 256;
static_assert(LDS_BYTES <= 163840 && (LSY & 15) == 0 && 4 * CPY <= UOF && LB + 40960 + 4 * 2304 <= LSY, "lds");
enum { SY_F = 2, SY_P = 10, SY_WO = 20, SY_BG = 22, SY_B2 = 23, SY_Q = 24, SY_X = 25, SY_G = 26, SY_T2 = 27 };
constexpr int SPIN = 1 << 18;
constexpr unsigned CODE_LINES = 216;

struct IdxTab { uint16_t v[256][12]; };
constexpr IdxTab make_idx() {
    IdxTab t = {};
    for (int y = 0; y < 256; ++y) for (int e = 0; e < 12; ++e) {
        const int j = 2047 - 8 * y - e; int ix = 644;
        if (j >= 0) { const int sh = j >= 1024 ? 3 : (j >= 512 ? 2 : (j >= 256 ? 1 : 0));
            ix = sh ? 128 * sh + ((j + (1 << (sh - 1)) - 1 + ((j >> sh) & 1)) >> sh) : j; }
        t.v[y][e] = (uint16_t)(2 * ix);
    }
    return t;
}
__device__ const IdxTab g_idx __attribute__((aligned(16))) = make_idx();

DI unsigned pk_bf16(float a, float b) { f32x2 v = {a, b}; return __builtin_bit_cast(unsigned, __builtin_convertvector(v, bf16x2)); }
DI unsigned pk_f16(f32x2 v) { return __builtin_bit_cast(unsigned, __builtin_convertvector(v, f16x2)); }
DI float bf_lo(unsigned w) { return __uint_as_float(w << 16); }
DI float bf_hi(unsigned w) { return __uint_as_float(w & 0xffff0000u); }
DI float rbf(float x) { return bf_lo(pk_bf16(x, x)); }
#define GL0 "s_mov_b32 %0, m0\n\ts_mov_b32 m0, %2\n\ts_nop 0\n\t"
#define GL1(V, O, C) "global_load_lds_dwordx4 " V ", %1 offset:" #O C "\n\t"
#define GLE "s_mov_b32 m0, %0"
DI void glds4(const void* sb, unsigned v0, unsigned v1, unsigned v2, unsigned v3, unsigned lb) { unsigned sv;
    asm volatile(GL0 GL1("%3", 0, "") GL1("%4", 1024, "") GL1("%5", 2048, "") GL1("%6", 3072, "") GLE : "=&s"(sv) : "s"(sb), "s"(lb), "v"(v0), "v"(v1), "v"(v2), "v"(v3) : "memory"); }
DI void glds2(const void* sb, unsigned v0, unsigned v1, unsigned lb) { unsigned sv;
    asm volatile(GL0 GL1("%3", 0, "") GL1("%4", 1024, "") GLE : "=&s"(sv) : "s"(sb), "s"(lb), "v"(v0), "v"(v1) : "memory"); }
DI void glds1(const void* sb, unsigned v0, unsigned lb) { unsigned sv;
    asm volatile(GL0 GL1("%3", 0, "") GLE : "=&s"(sv) : "s"(sb), "s"(lb), "v"(v0) : "memory"); }
DI float wave_max(float v) {
#define DPPMAX(ctrl, rm) v = fmaxf(v, __uint_as_float(__builtin_amdgcn_update_dpp(0, __float_as_uint(v), ctrl, rm, 0xf, true)))
    DPPMAX(0x111, 0xf); DPPMAX(0x112, 0xf); DPPMAX(0x114, 0xf); DPPMAX(0x118, 0xf); DPPMAX(0x142, 0xa); DPPMAX(0x143, 0xc);
    return __uint_as_float(__builtin_amdgcn_readlane(__float_as_uint(v), 63));
}
DI s16x4 trread(LAS const char* p) { return __builtin_bit_cast(s16x4, __builtin_amdgcn_ds_read_tr16_b64_v4i16((LAS s16x4*)p)); }
DI bf16x8 cat8(s16x4 a, s16x4 b) { return __builtin_shufflevector(a, b, 0, 1, 2, 3, 4, 5, 6, 7); }
DI f32x2 pfma(f32x2 a, f32x2 b, f32x2 c) { return __builtin_elementwise_fma(a, b, c); }
DI f32x2 cmul(f32x2 a, f32x2 b, f32x2 bs) { return pfma(a.yy, bs, a.xx * b); }
DI f16x8 cmul4h(u32x4 p, u32x4 q) {
    u32x4 t;
#define PM(i, a, b) "v_pk_mul_f16 %" #i ", %" #a ", %" #b " op_sel:[0,0] op_sel_hi:[0,1]\n\t"
#define PF(i, a, b) "v_pk_fma_f16 %" #i ", %" #a ", %" #b ", %" #i " op_sel:[1,1,0] op_sel_hi:[1,0,1] neg_lo:[1,0,0]\n\t"
    asm(PM(0, 4, 8) PM(1, 5, 9) PM(2, 6, 10) PM(3, 7, 11) PF(0, 4, 8) PF(1, 5, 9) PF(2, 6, 10) PF(3, 7, 11) "s_nop 1"
        : "=&v"(t[0]), "=&v"(t[1]), "=&v"(t[2]), "=&v"(t[3]) : "v"(p[0]), "v"(p[1]), "v"(p[2]), "v"(p[3]), "v"(q[0]), "v"(q[1]), "v"(q[2]), "v"(q[3]));
    return __builtin_bit_cast(f16x8, t);
}
DI f32x2 rot(f32x2 b) { return (f32x2){-b.y, b.x}; }
DI f32x2 anchor(float a2, float rev, float s) {
    const float mag = EX(a2 * s);
    float r = rev * s; r -= __builtin_rintf(r);
    return (f32x2){mag * __builtin_amdgcn_cosf(r), mag * __builtin_amdgcn_sinf(r)};
}
DI void arrive(LAS unsigned* c) { asm volatile("s_mov_b64 exec, 1\n\tds_add_u32 %0, %1\n\ts_mov_b64 exec, -1" :: "v"((unsigned)(uintptr_t)c), "v"(1u) : "memory"); }
DI void await(LAS unsigned* c, unsigned n) {
    unsigned t, i = SPIN;
    asm volatile("1:\n\tds_read_b32 %0, %2\n\ts_waitcnt lgkmcnt(0)\n\tv_cmp_gt_u32 vcc, %3, %0\n\ts_cbranch_vccz 2f\n\ts_sleep 1\n\ts_sub_u32 %1, %1, 1\n\ts_cbranch_scc0 1b\n2:"
                 : "=&v"(t), "+s"(i) : "v"((unsigned)(uintptr_t)c), "s"(n) : "vcc", "scc", "memory");
}
DI void await_slow(LAS unsigned* c, unsigned n) {
    unsigned t, i = SPIN;
    asm volatile("1:\n\tds_read_b32 %0, %2\n\ts_waitcnt lgkmcnt(0)\n\tv_cmp_gt_u32 vcc, %3, %0\n\ts_cbranch_vccz 2f\n\ts_sleep 3\n\ts_sub_u32 %1, %1, 1\n\ts_cbranch_scc0 1b\n2:"
                 : "=&v"(t), "+s"(i) : "v"((unsigned)(uintptr_t)c), "s"(n) : "vcc", "scc", "memory");
}
DI u64 get_tag(LAS unsigned* sy) {
    await(sy, 1);
    const u64 v = __hip_atomic_load((LAS u64*)sy, __ATOMIC_RELAXED, __HIP_MEMORY_SCOPE_WORKGROUP);
    return ((u64)(unsigned)__builtin_amdgcn_readfirstlane((unsigned)(v >> 32)) << 32) | (unsigned)__builtin_amdgcn_readfirstlane((unsigned)v);
}
DI void set_tag(LAS unsigned* sy, u64 tag) { asm volatile("s_mov_b64 exec, 1\n\tds_write_b64 %0, %1\n\ts_mov_b64 exec, -1" :: "v"((unsigned)(uintptr_t)sy), "v"(tag) : "memory"); }
DI void drain() { asm volatile("s_waitcnt vmcnt(0)" ::: "memory"); }
DI void put_flag(char* ws, unsigned off, u64 tag) {
    asm volatile("s_mov_b64 exec, 1\n\tglobal_store_dwordx2 %0, %1, %2 sc1\n\ts_mov_b64 exec, -1" :: "v"(off), "v"(tag), "s"(ws) : "memory");
}
template <int SLP> DI u64 wait_flags(char* ws, unsigned off, u64 tag, u64 pend, u64 need) {
    u64 t, left; unsigned i = SPIN;
    asm volatile("s_mov_b64 exec, %5\n1:\n\tglobal_load_dwordx2 %0, %3, %4 sc1\n\ts_waitcnt vmcnt(0)\n\tv_cmpx_ne_u64 vcc, %6, %0\n\ts_and_b64 %2, exec, %7\n\t"
                 "s_cbranch_scc0 2f\n\ts_sleep %8\n\ts_sub_u32 %1, %1, 1\n\ts_cbranch_scc0 1b\n2:\n\ts_mov_b64 %2, exec\n\ts_mov_b64 exec, -1"
                 : "=&v"(t), "+s"(i), "=&s"(left) : "v"(off), "s"(ws), "s"(pend), "s"(tag), "s"(need), "i"(SLP) : "vcc", "scc", "memory");
    return left;
}
#define BST(v, off) __builtin_amdgcn_raw_buffer_store_b128((v), rs, (off), 0, 16)
#define BLD(off) __builtin_amdgcn_raw_buffer_load_b128(rs, (off), 0, 16)
#define FR(p, off) (*(LAS const bf16x8*)((p) + (off)))
DI float swapmax(float m) { const auto r = __builtin_amdgcn_permlane32_swap(__float_as_uint(m), __float_as_uint(m), false, false); return fmaxf(__uint_as_float(r[0]), __uint_as_float(r[1])); }
DI u32x4 put_ref(u32x4 q1, float m, int hi) {
    const float m1 = rbf(m), m2 = rbf(m - m1), m3 = m - m1 - m2;
    if (hi) { q1[2] = pk_bf16(-m1, -m2); q1[3] = pk_bf16(-m3, 0.f); }
    return q1;
}
DI u64 mix(u64 v) {
    const u64 e = ((u64)(unsigned)__builtin_amdgcn_readfirstlane((unsigned)(v >> 32)) << 32) | (unsigned)__builtin_amdgcn_readfirstlane((unsigned)v);
    u64 z = e + 0x9E3779B97F4A7C15ull; z = (z ^ (z >> 30)) * 0xBF58476D1CE4E5B9ull; z = (z ^ (z >> 27)) * 0x94D049BB133111EBull; return (z ^ (z >> 31)) | 1ull; }
DI void transpose_tile(LAS const char* stage, LAS char* scratch, int w, int lane, u32x4& o0, u32x4& o1) {
    LAS const char* p = stage + (lane >> 4) * 2048 + ((lane & 15) >> 2) * 128 + w * 32 + (lane & 3) * 8;
    const bf16x8 t0 = cat8(trread(p), trread(p + 512)), t1 = cat8(trread(p + 1024), trread(p + 1536));
    LAS char* sc = scratch + (lane & 15) * 144 + (lane >> 4) * 32;
    *(LAS bf16x8*)sc = t0; *(LAS bf16x8*)(sc + 16) = t1;
    LAS const char* sr = scratch + (lane >> 3) * 144 + (lane & 7) * 16;
    o0 = *(LAS const u32x4*)sr; o1 = *(LAS const u32x4*)(sr + 8 * 144);
}

DI void role_a(const uint16_t* __restrict__ x, const uint16_t* __restrict__ wqkv, uint16_t* __restrict__ out, char* __restrict__ ws, LAS char* lds, int w, int lane) {
    const int head = 2 * (blockIdx.x & 7) + ((blockIdx.x >> 3) & 1), grp = blockIdx.x >> 4;
    const unsigned lds0 = (unsigned)(uintptr_t)lds;
    const int r32 = lane & 31, hi = lane >> 5;
    const int wsh = (head & 7) == 7 ? 4 : 0;
    const unsigned wv = (unsigned)((lane >> 2) * 3 * D + (lane & 3) * 8) * 2;
    const char* wb = (const char*)(wqkv + DH * head - wsh + (16 * w) * 3 * D);
    const unsigned xv0 = (unsigned)((lane >> 3) * D + (((lane & 7) ^ (lane >> 4)) * 8)) * 2, xv1 = xv0 ^ 64;
    const char* xb = (const char*)(x + (128 * grp + 32 * w) * D);
    const int rho = ((blockIdx.x >> 3) + ((blockIdx.x >> 1) & 3)) % 7;
#define SP(st) ((st) + rho >= 7 ? (st) + rho - 7 : (st) + rho)
#define WQO(st) ((st) < 4 ? LWQ + (st) * 4096 : ((st) < 6 ? 3 * WSL + ((st) - 4) * 4096 : 0))
#define LOADW(st) glds2(wb + SP(st) * (64 * 3 * D * 2), wv + 2 * D, wv + 4 * D - 1024, lds0 + ((st) & 3) * WSL + w * 2048)
#define LOADQ(st) glds1(wb + SP(st) * (64 * 3 * D * 2), wv, lds0 + WQO(st) + w * 1024)
#define LOADX(st) glds4(xb + SP(st) * 128, xv0, xv1 + 16 * D - 1024, xv0 + 32 * D - 2048, xv1 + 48 * D - 3072, lds0 + LXR + ((st) % 3) * XSL + w * 4096)
    LOADW(0); LOADX(0);
    LOADW(1); LOADX(1);
    LAS unsigned* sy = (LAS unsigned*)(lds + LSY);
    const __amdgpu_buffer_rsrc_t rs = __builtin_amdgcn_make_buffer_rsrc(ws, 0, WS_END, 0x00020000);
    const int xs = (r32 >> 1) & 7;
    LAS const char* xp[4];
#pragma unroll
    for (int ks = 0; ks < 4; ++ks) xp[ks] = lds + LXR + w * 4096 + r32 * 128 + (((2 * ks + hi) ^ xs) * 16);
    LAS const char* wp = lds + (8 * hi + ((lane & 15) >> 2)) * 64 + ((lane >> 4) & 1) * 32 + (lane & 3) * 8 + wsh * 2;
    f32x16 aq = {}, ak = {}, av = {};
    bf16x8 xf[7][4]; s16x4 fr[4][4];
#define RX(st, ks) xf[st][ks] = FR(xp[ks], ((st) % 3) * XSL)
#define RW(st, ks, j) fr[ks][j] = trread(wp + ((st) & 3) * WSL + (ks) * 2048 + ((j) >> 1) * 1024 + ((j) & 1) * 256)
#define MK(st, ks) ak = MFMA_BF(cat8(fr[ks][0], fr[ks][1]), xf[st][ks], ak); SBAR()
#define MV(st, ks) av = MFMA_BF(xf[st][ks], cat8(fr[ks][2], fr[ks][3]), av); SBAR()
#define QSTEP(st, ks) MK(st, ks); RW(st, ks + 1, 0); RW(st, ks + 1, 1); SBAR(); MV(st, ks); RW(st, ks + 1, 2); RW(st, ks + 1, 3); SBAR()
    u64 tag = 0;
    asm volatile("s_waitcnt vmcnt(6)" ::: "memory");
    arrive(sy + SY_F); LOADW(2); LOADX(2); await(sy + SY_F, 4);
    RX(0, 0); RW(0, 0, 0); RW(0, 0, 1); RW(0, 0, 2); RW(0, 0, 3); RX(0, 1); RX(0, 2); RX(0, 3); SBAR();
#pragma unroll
    for (int st = 0; st < 7; ++st) {
        QSTEP(st, 0); if (st < 4) LOADW(st + 3); if (st == 4) { LOADQ(4); LOADQ(5); } if (st == 5) LOADQ(6);
        QSTEP(st, 1); if (st < 4) LOADX(st + 3);
        QSTEP(st, 2); if (st == 3) { LOADQ(0); LOADQ(1); LOADQ(2); LOADQ(3); }
        if (st < 6) {
            if (st == 3) asm volatile("s_waitcnt vmcnt(16)" ::: "memory"); else if (st == 5) asm volatile("s_waitcnt vmcnt(7)" ::: "memory"); else asm volatile("s_waitcnt vmcnt(12)" ::: "memory");
            arrive(sy + SY_F + 1 + st); SBAR();
        }
        MK(st, 3);
        if (st < 6) { await(sy + SY_F + 1 + st, 4); RX(st + 1, 0); RW(st + 1, 0, 0); RW(st + 1, 0, 1); RW(st + 1, 0, 2); RW(st + 1, 0, 3); } SBAR();
        MV(st, 3); if (st < 6) { RX(st + 1, 1); RX(st + 1, 2); RX(st + 1, 3); } SBAR();
        if (st == 2) tag = get_tag(sy);
    }
    const float cq = 0.18898223650461362f * 1.4426950408889634f;
    const unsigned voff = lane * 16;
    u32x4 q0, q1, k0, k1, v0, v1;
    const unsigned tb = WS_KV + (unsigned)(head * 64 + 4 * grp + w) * 4096 + voff;
#pragma unroll
    for (int i = 0; i < 4; ++i) { k0[i] = pk_bf16(ak[2 * i], ak[2 * i + 1]); k1[i] = pk_bf16(ak[8 + 2 * i], ak[9 + 2 * i]); }
    if (hi) { k1[2] = 0x3f803f80u; k1[3] = 0x00003f80u; }
    BST(k0, tb); BST(k1, tb + 1024);
#pragma unroll
    for (int i = 0; i < 4; ++i) {
        v0[i] = pk_bf16(av[2 * i], av[2 * i + 1]); v1[i] = pk_bf16(av[8 + 2 * i], av[9 + 2 * i]);
        if (r32 >= DH) { v0[i] = v1[i] = (r32 == DH ? 0x3f803f80u : 0u); }
    }
    BST(v0, tb + 2048); BST(v1, tb + 3072);
    asm volatile("s_waitcnt vmcnt(4)" ::: "memory");
    arrive(sy + SY_F + 7); await(sy + SY_F + 7, 4);
    {
        s16x4 fq[4][2];
#define RQ(j) do { fq[(j) & 3][0] = trread(wp + WQO((j) >> 2) + ((j) & 3) * 1024); fq[(j) & 3][1] = trread(wp + WQO((j) >> 2) + ((j) & 3) * 1024 + 256); } while (0)
        RQ(0); RQ(1); RQ(2); SBAR();
#pragma unroll
        for (int j = 0; j < 28; ++j) {
            aq = MFMA_BF(cat8(fq[j & 3][0], fq[j & 3][1]), xf[j >> 2][j & 3], aq); SBAR(); if (j < 25) RQ(j + 3); SBAR();
            if (j == 13) { drain(); put_flag(ws, FL_KV + (unsigned)((head * 4 + w) * 16 + grp) * FS, tag); }
        }
    }
#pragma unroll
    for (int i = 0; i < 4; ++i) { q0[i] = pk_bf16(aq[2 * i] * cq, aq[2 * i + 1] * cq); q1[i] = pk_bf16(aq[8 + 2 * i] * cq, aq[9 + 2 * i] * cq); }
    if (hi) { q1[2] = 0; q1[3] = 0; }
    const unsigned kvf = FL_KV + (unsigned)((head * 4 + w) * 16 + ((grp + lane) & 15)) * FS;
    const f32x16 zero = {};
    {
        f32x16 s = MFMA_BF(BC8(k0), BC8(q0), zero); s = MFMA_BF(BC8(k1), BC8(q1), s);
        float m = s[0];
#pragma unroll
        for (int i = 1; i < 16; ++i) m = fmaxf(m, s[i]);
        q1 = put_ref(q1, swapmax(m), hi);
    }
    { LAS u32x4* qo = (LAS u32x4*)(lds + LQ + w * 2048) + lane; qo[0] = q0; qo[64] = q1; }
    arrive(sy + SY_Q);
    const u64 kv1 = __hip_atomic_load((GAS u64*)(ws + kvf), __ATOMIC_RELAXED, __HIP_MEMORY_SCOPE_AGENT); SBAR();
    f32x16 O[4] = {}; PIN(O[0]); PIN(O[1]); PIN(O[2]); PIN(O[3]);
    await(sy + SY_Q, 4);
    bf16x8 Q[4][2];
    Q[0][0] = BC8(q0); Q[0][1] = BC8(q1);
#pragma unroll
    for (int i = 1; i < 4; ++i) { LAS const char* p = lds + LQ + ((w + i) & 3) * 2048 + voff; Q[i][0] = FR(p, 0); Q[i][1] = FR(p, 1024); }
    u32x4 kv[4][4] = {{k0, k1, v0, v1}, {}, {}, {}};
    f32x16 sa, sb;
    u32x4 p0, p1 = {};
#define GRP(SC, i, P, j) do { float e0 = EX(SC[i]); SBAR(); float e1 = EX(SC[i + 1]); SBAR(); float e2 = EX(SC[i + 2]); SBAR(); float e3 = EX(SC[i + 3]); SBAR(); \
        P[j] = pk_bf16(e0, e1); SBAR(); P[j + 1] = pk_bf16(e2, e3); PIN(P); SBAR(); } while (0)
#define SLOT(SC, SN, cb, i, QK, PV1, LD) do { constexpr int nb_ = (i) < 3 ? (cb) : ((cb) + 1) % 4, pb_ = (i) > 0 ? (cb) : ((cb) + 3) % 4; \
        if (QK) SN = MFMA_BF(BC8(kv[nb_][0]), Q[((i) + 1) & 3][0], zero); SBAR(); GRP(SC, 0, p0, 0); \
        if (PV1) O[((i) + 3) & 3] = MFMA_BF(BC8(kv[pb_][3]), BC8(p1), O[((i) + 3) & 3]); SBAR(); \
        if (LD) { kv[((cb) + 3) % 4][i] = __builtin_amdgcn_raw_buffer_load_b128(rs, voff + (i) * 1024, kvo, 16); SBAR(); } GRP(SC, 4, p0, 2); \
        if (QK) SN = MFMA_BF(BC8(kv[nb_][1]), Q[((i) + 1) & 3][1], SN); SBAR(); GRP(SC, 8, p1, 0); \
        O[i] = MFMA_BF(BC8(kv[cb][2]), BC8(p0), O[i]); SBAR(); GRP(SC, 12, p1, 2); } while (0)
    const unsigned kvb = WS_KV + (unsigned)(head * 64 + w) * 4096;
    unsigned kvo = 0;
    u64 miss = __ballot(kv1 != tag) & 0xfffe;
    if (miss & 6) miss = wait_flags<2>(ws, kvf, tag, miss, 6);
    CFENCE();
#pragma unroll
    for (int j = 1; j < 3; ++j)
#pragma unroll
        for (int f = 0; f < 4; ++f) kv[j][f] = __builtin_amdgcn_raw_buffer_load_b128(rs, voff + f * 1024, kvb + ((grp + j) & 15) * 16384, 16);
    arrive(sy + SY_G);
    sa = MFMA_BF(BC8(k0), Q[0][0], zero); sa = MFMA_BF(BC8(k1), Q[0][1], sa);
    int nx = grp + 3;
#define TILE(cb) do { kvo = kvb + (nx & 15) * 16384; ++nx; SBAR(); \
        SLOT(sa, sb, cb, 0, true, true, true); SLOT(sb, sa, cb, 1, true, true, true); SLOT(sa, sb, cb, 2, true, true, true); SLOT(sb, sa, cb, 3, true, true, true); } while (0)
#pragma unroll 1
    for (unsigned msk = 0x78; msk < 0x80000; msk <<= 4) { if (__builtin_expect((miss & msk) != 0, 0)) miss = wait_flags<2>(ws, kvf, tag, miss, msk); TILE(0); TILE(1); TILE(2); TILE(3); }
    O[3] = MFMA_BF(BC8(kv[3][3]), BC8(p1), O[3]);
    f32x16 o = O[0];
    {
#pragma unroll
        for (int i = 1; i < 4; ++i) {
            LAS char* ex = lds + LX + (((w + i) & 3) * 3 + i - 1) * 4096 + voff;
#pragma unroll
            for (int g = 0; g < 4; ++g) *(LAS f32x4*)(ex + g * 1024) = (f32x4){O[i][4 * g], O[i][4 * g + 1], O[i][4 * g + 2], O[i][4 * g + 3]};
        }
        arrive(sy + SY_X); await(sy + SY_X, 4);
        LAS const char* er = lds + LX + w * 3 * 4096 + voff;
#pragma unroll
        for (int i = 0; i < 3; ++i)
#pragma unroll
            for (int g = 0; g < 4; ++g) { const f32x4 r = *(LAS const f32x4*)(er + i * 4096 + g * 1024);
#pragma unroll
                for (int e = 0; e < 4; ++e) o[4 * g + e] += r[e]; }
        f32x2 chk = {0.f, 0.f};
#pragma unroll
        for (int i = 0; i < 8; ++i) chk = pfma((f32x2){o[2 * i], o[2 * i + 1]}, (f32x2){0.f, 0.f}, chk);
        if (__builtin_expect(__any(!(chk[0] + chk[1] == 0.f) || !(o[12] < 1e30f)), 0)) {
            const unsigned hb = WS_KV + (unsigned)head * 64 * 4096 + voff;
            u32x4 qr = __builtin_bit_cast(u32x4, Q[0][1]); if (hi) { qr[2] = 0; qr[3] = 0; }
            float mm = -INFINITY;
            wait_flags<4>(ws, FL_KV + (unsigned)(head * 64 + lane) * FS, tag, ~0ull, ~0ull);
#pragma unroll 1
            for (int t = 0; t < 64; ++t) {
                f32x16 s = MFMA_BF(BC8(BLD(hb + t * 4096)), Q[0][0], zero); s = MFMA_BF(BC8(BLD(hb + t * 4096 + 1024)), BC8(qr), s);
#pragma unroll
                for (int i = 0; i < 16; ++i) mm = fmaxf(mm, s[i]);
            }
            qr = put_ref(qr, swapmax(mm), hi);
            o = zero;
#pragma unroll 1
            for (int t = 0; t < 64; ++t) {
                f32x16 s = MFMA_BF(BC8(BLD(hb + t * 4096)), Q[0][0], zero); s = MFMA_BF(BC8(BLD(hb + t * 4096 + 1024)), BC8(qr), s);
#pragma unroll
                for (int i = 0; i < 4; ++i) { p0[i] = pk_bf16(EX(s[2 * i]), EX(s[2 * i + 1])); p1[i] = pk_bf16(EX(s[8 + 2 * i]), EX(s[9 + 2 * i])); }
                o = MFMA_BF(BC8(BLD(hb + t * 4096 + 2048)), BC8(p0), o); o = MFMA_BF(BC8(BLD(hb + t * 4096 + 3072)), BC8(p1), o);
            }
        }
    }
    {
        const auto r = __builtin_amdgcn_permlane32_swap(__float_as_uint(o[12]), __float_as_uint(o[12]), false, false);
        const float rl = __builtin_amdgcn_rcpf(__uint_as_float(r[1]));
        u32x4 o0, o1;
#pragma unroll
        for (int i = 0; i < 4; ++i) { o0[i] = pk_bf16(o[2 * i] * rl, o[2 * i + 1] * rl); o1[i] = pk_bf16(o[8 + 2 * i] * rl, o[9 + 2 * i] * rl); }
        if (hi) { o1[2] = 0; o1[3] = 0; }
        const unsigned fi = (unsigned)((4 * grp + w) * 16 + head);
        BST(o0, WS_O + fi * 2048 + voff); BST(o1, WS_O + fi * 2048 + 1024 + voff);
        drain();
        put_flag(ws, FL_O + fi * FS, tag);
    }
    const int xcd = blockIdx.x & 7, bi = blockIdx.x >> 3;
    if (xcd < 7) {
        wait_flags<2>(ws, FL_BT + (unsigned)(xcd * 128 + w * 32 + r32) * FS, tag, 0xffffffffull, 0xffffffffull);
        const unsigned bo = WS_BT + (unsigned)((64 * xcd + 16 * w + (lane >> 3)) * S + 64 * bi + (lane & 7) * 8) * 2;
        const u32x4 b0 = BLD(bo), b1 = BLD(bo + 8 * S * 2);
        *(LAS u32x4*)(lds + 65536 + w * 2048 + voff) = b0; *(LAS u32x4*)(lds + 65536 + w * 2048 + 1024 + voff) = b1;
        arrive(sy + SY_T2); await(sy + SY_T2, 4);
        u32x4 o0, o1; transpose_tile(lds + 65536, lds + 73728 + w * 2304, w, lane, o0, o1);
        const __amdgpu_buffer_rsrc_t ro = __builtin_amdgcn_make_buffer_rsrc(out, 0, S * 2 * D * 2, 0x00020000);
        const unsigned oo = (unsigned)((64 * bi + 16 * w + (lane >> 3)) * 2 * D + D + 64 * xcd) * 2 + (lane & 7) * 16;
        __builtin_amdgcn_raw_buffer_store_b128(o0, ro, oo, 0, 16); __builtin_amdgcn_raw_buffer_store_b128(o1, ro, oo + 8 * 2 * D * 2, 0, 16);
    }
}

DI void role_b(const uint16_t* __restrict__ wo, const uint16_t* __restrict__ u, const uint16_t* __restrict__ lar, const uint16_t* __restrict__ aim,
               const uint16_t* __restrict__ cre, const uint16_t* __restrict__ cim, const uint16_t* __restrict__ ldt, const uint16_t* __restrict__ dskip,
               uint16_t* __restrict__ out, char* __restrict__ ws, LAS char* lds, int w, int lane, u64 code0) {
    u64 tag;
    const unsigned lds0 = (unsigned)(uintptr_t)lds;
    LAS unsigned* sy = (LAS unsigned*)(lds + LSY);
    const __amdgpu_buffer_rsrc_t rs = __builtin_amdgcn_make_buffer_rsrc(ws, 0, WS_END, 0x00020000);
    const int b = blockIdx.x, xcd = b & 7, bi = b >> 3, r32 = lane & 31, hi = lane >> 5;
    const unsigned voff = lane * 16;
    LAS char* tab = lds + LB + (w >> 1) * CBW + (w & 1) * TABW;
    unsigned pf = 0;
#define CODE_PF() asm volatile("global_load_dword %0, %1, %2" : "+v"(pf) : "v"(min((unsigned)((bi * 4 + w) * 3 + (lane < 3 ? lane : 0)), CODE_LINES - 1) * 128), "s"(code0) : "memory")
    const int qt = 8 * xcd + (bi >> 2), ct = 4 * (bi & 3) + w;
    bf16x8 wf[32];
    const unsigned wvo = ct < 14 ? voff : 0x80000000u;
    unsigned wgb[4];
#pragma unroll
    for (int g = 0; g < 4; ++g) wgb[g] = WS_WO + (unsigned)(((w + g) & 3) * 8 * 14 + ct) * 1024;
#define LOADWF(i) wf[i] = BC8(__builtin_amdgcn_raw_buffer_load_b128(rs, wvo, wgb[(i) >> 3] + ((i) & 7) * 14336, 16))
    if (xcd < 7) {
        const int cc = w >> 1, hh = w & 1, pair = 32 * xcd + bi, ch = 2 * pair + cc;
        LAS char* cb = lds + LB + cc * CBW;
        const unsigned po = (unsigned)(ch * NS + 128 * hh) / 2 + lane;
        const unsigned pl = ((const unsigned*)lar)[po], pa = ((const unsigned*)aim)[po], pr = ((const unsigned*)cre)[po], pi = ((const unsigned*)cim)[po];
        const unsigned uo = (unsigned)((64 * bi + 16 * w + (lane >> 3)) * D + 64 * xcd + (lane & 7) * 8) * 2;
        glds2(u, uo, uo + 8 * D * 2 - 1024, lds0 + LT1 + w * 2048);
        const u64 e0 = __hip_atomic_load((GAS u64*)ws, __ATOMIC_RELAXED, __HIP_MEMORY_SCOPE_AGENT);
        u32x2 gi[2][3];
#pragma unroll
        for (int ps = 0; ps < 2; ++ps)
#pragma unroll
            for (int i = 0; i < 3; ++i) gi[ps][i] = ((const u32x2*)g_idx.v[128 * hh + 64 * ps + lane])[i];
        CODE_PF();
        const unsigned lw = ((const unsigned*)ldt)[pair], dw = ((const unsigned*)dskip)[pair];
        LAS char* ui = cb + UOF;
        const u32x4 z4 = {};
        *(LAS u32x4*)(ui + (64 * hh + lane) * 16) = z4; *(LAS u32x4*)(ui + (96 + lane) * 16) = z4;
        const float dt = rbf(expf(cc ? bf_hi(lw) : bf_lo(lw)));
        drain(); asm volatile("" :: "v"(pf));
        arrive(sy + SY_WO);
        tag = mix(e0);
        const float cm = wave_max(fmaxf(fmaxf(fabsf(bf_lo(pr)), fabsf(bf_hi(pr))), fmaxf(fabsf(bf_lo(pi)), fabsf(bf_hi(pi)))));
        unsigned ce = __float_as_uint(cm) & 0x7f800000u; ce = ce < 0x01000000u ? 0x01000000u : (ce > 0x7e800000u ? 0x7e800000u : ce);
        const float cscale = __uint_as_float(0x7f000000u - ce), oscale = 2.0f * dt * __uint_as_float(ce);
        LAS unsigned* tw = (LAS unsigned*)(tab + lane * 4);
        LAS const char* trd = tab + r32 * TROW + hi * 16;
        f32x16 dacc = {};
        f32x2 z1, z8, z256, x0;
#define ANCH(vl, va, vr, vi) do { const float a2 = (-rbf(expf(vl)) * dt) * 1.4426950408889634f, rev = ((va) * dt) * 0.15915494309189535f; \
            z1 = anchor(a2, -rev, 1.f); z8 = anchor(a2, rev, 8.f); z256 = anchor(a2, -rev, 256.f); x0 = (f32x2){(vr) * cscale, -(vi) * cscale}; \
            PIN(z1); PIN(z8); PIN(z256); SBAR(); } while (0)
#define TW(row) tw[(row) * (TROW / 4)]
#define CHAIN() do { \
            const f32x2 z1s = rot(z1), zs = rot(z256), z8s = rot(z8); \
            f32x2 X[8]; const f32x2 z16 = cmul(z8, z8, z8s); X[0] = x0; X[1] = cmul(x0, z1, z1s); f32x2 a = cmul(x0, z256, zs); \
            const f32x2 z24 = cmul(z16, z8, z8s), z32 = cmul(z16, z16, rot(z16)), z32s = rot(z32); X[2] = cmul(X[1], z1, z1s); f32x2 y = z32; \
            f32x2 c4 = cmul(X[2], z256, zs); \
            unsigned c[2][4], d0 = pk_f16(X[0]), d1 = pk_f16(X[1]), d2 = pk_f16(z8), d3 = pk_f16(z16), d4 = pk_f16(z24), one = 0x3c00u; \
            c[0][0] = pk_f16(y); c[0][1] = pk_f16(X[2]); c[0][2] = pk_f16(a); c[0][3] = pk_f16(c4); \
            TW(0) = d0; TW(1) = d1; TW(30) = d2; TW(31) = d3; TW(32) = d4; TW(21) = one; TW(29) = one; SBAR(); \
            _Pragma("unroll") for (int i = 1; i < 9; ++i) { \
                f32x2 t0, t4, t5, t6; const int j = i - 1; unsigned* pc = c[j & 1]; unsigned* nc = c[i & 1]; \
                if (i < 8) { if (i < 7) t0 = y.xx * z32; if (i < 6) t4 = X[i + 1].xx * z1; t5 = a.xx * z256; \
                    if (i == 3) t6 = X[4].xx * z256; if (i == 4 || i == 5) t6 = c4.xx * z256; if (i == 6) t6 = X[6].xx * z256; } SBAR(); \
                if (j < 7) TW(22 + j) = pc[0]; if (j < 6) TW(j + 2) = pc[1]; TW(j == 0 ? 8 : (j == 1 ? 12 : (j == 2 ? 14 : 13 + j))) = pc[2]; \
                if (j == 0) TW(9) = pc[3]; if (j == 3) TW(10) = pc[3]; if (j == 4) TW(13) = pc[3]; if (j == 5) TW(15) = pc[3]; if (j == 6) TW(11) = pc[3]; SBAR(); \
                if (i < 8) { if (i < 7) y = pfma(y.yy, z32s, t0); if (i < 6) X[i + 2] = pfma(X[i + 1].yy, z1s, t4); a = pfma(a.yy, zs, t5); \
                    if (i == 3) c4 = pfma(X[4].yy, zs, t6); if (i == 4 || i == 5) c4 = pfma(c4.yy, zs, t6); if (i == 6) c4 = pfma(X[6].yy, zs, t6); SBAR(); \
                    if (i < 7) nc[0] = pk_f16(y); if (i < 6) nc[1] = pk_f16(X[i + 2]); nc[2] = pk_f16(a); if (i >= 3 && i <= 6) nc[3] = pk_f16(c4); \
                    asm volatile("" :: "v"(pc[0]), "v"(pc[1]), "v"(pc[2]), "v"(pc[3])); SBAR(); } \
            } } while (0)
        f16x8 fa[8]; u32x4 fp[8], fq[8];
        LAS const char* trp = tab + (21 + (r32 >> 2)) * TROW + hi * 16;
        LAS const char* trq = tab + (29 + (r32 & 3)) * TROW + hi * 16;
#define RDT() do { _Pragma("unroll") for (int kk = 0; kk < 8; ++kk) { fp[kk] = *(LAS const u32x4*)(trp + kk * 32); fq[kk] = *(LAS const u32x4*)(trq + kk * 32); fa[kk] = *(LAS const f16x8*)(trd + kk * 32); } SBAR(); } while (0)
#define MMT() do { _Pragma("unroll") for (int kk = 0; kk < 8; ++kk) dacc = MFMA_F16(fa[kk], cmul4h(fp[kk], fq[kk]), dacc); } while (0)
        ANCH(bf_lo(pl), bf_lo(pa), bf_lo(pr), bf_lo(pi));
        await(sy + SY_WO, 4);
        set_tag(sy, tag);
        {
            u32x4 o0, o1; transpose_tile(lds + LT1, tab, w, lane, o0, o1);
            const unsigned to = WS_UT + (unsigned)((64 * xcd + 16 * w + (lane >> 3)) * S + 64 * bi) * 2 + (lane & 7) * 16;
            __builtin_amdgcn_s_setprio(3); BST(o0, to); BST(o1, to + 8 * S * 2); __builtin_amdgcn_s_setprio(0);
        }
        CHAIN(); RDT();
        drain(); __builtin_amdgcn_s_setprio(3); put_flag(ws, FL_UT + (unsigned)(xcd * 128 + w * 32 + bi) * FS, tag); __builtin_amdgcn_s_setprio(0);
        ANCH(bf_hi(pl), bf_hi(pa), bf_hi(pr), bf_hi(pi));
        MMT(); SBAR();
        CHAIN(); RDT();
        const unsigned utf = hi ? FL_WO + (unsigned)(w * 32 + r32) * FS : FL_UT + (unsigned)(xcd * 128 + (bi >> 3) * 32 + r32) * FS;
        __builtin_amdgcn_s_setprio(3);
        const u64 utv = __hip_atomic_load((GAS u64*)(ws + utf), __ATOMIC_RELAXED, __HIP_MEMORY_SCOPE_AGENT); SBAR();
        __builtin_amdgcn_s_setprio(1);
        MMT();
        {
            LAS float* pt = (LAS float*)tab;
            *(LAS f32x4*)(pt + 8 * r32 + 4 * hi) = (f32x4){dacc[0] * oscale, dacc[1] * oscale, dacc[2] * oscale, dacc[3] * oscale};
            *(LAS f32x2*)(pt + (hi ? 384 + 2 * r32 : 256 + 4 * r32)) = (f32x2){dacc[4] * oscale, dacc[5] * oscale};
            *(LAS f32x2*)(pt + (hi ? 448 + 2 * r32 : 258 + 4 * r32)) = (f32x2){dacc[6] * oscale, dacc[7] * oscale};
            pt[hi ? 640 + r32 : 512 + r32] = dacc[8] * oscale;
            pt[hi ? 672 + r32 : 544 + r32] = dacc[9] * oscale;
            pt[hi ? 672 + r32 : 576 + r32] = dacc[10] * oscale;
            pt[hi ? 672 + r32 : 608 + r32] = dacc[11] * oscale;
        }
        LAS unsigned* ps_ = sy + SY_P + 5 * cc;
        arrive(ps_ + 0);
        LAS char* kt = cb + KOF;
        await(ps_ + 0, 2);
#pragma unroll
        for (int ps = 0; ps < 2; ++ps) {
            const int v = 81 * hh + 64 * ps + lane;
            if (ps == 0 || lane < 17) {
                const f32x4 p0 = *(LAS const f32x4*)(cb + v * 16), p1 = *(LAS const f32x4*)(cb + TABW + v * 16);
                u32x2 r = {pk_bf16(p0[0] + p1[0], p0[1] + p1[1]), pk_bf16(p0[2] + p1[2], p0[3] + p1[3])};
                if (v == 161) r = (u32x2){0u, 0u};
                *(LAS u32x2*)(kt + v * 8) = r;
            }
        }
        arrive(ps_ + 1);
        { const u64 ms = __ballot(utv != tag); if (ms) wait_flags<8>(ws, utf, tag, ms, ms); }
        CFENCE();
        const unsigned ub = WS_UT + (unsigned)(ch * S + 1024 * hh) * 2 + voff;
        const u32x4 u0 = BLD(ub), u1 = BLD(ub + 1024); SBAR();
        await(ps_ + 1, 2);
        __builtin_amdgcn_s_setprio(1);
        {
            unsigned W[2][6];
#pragma unroll
            for (int ps = 0; ps < 2; ++ps)
#pragma unroll
                for (int i = 0; i < 6; ++i) { const unsigned gv = gi[ps][i >> 1][i & 1];
                    W[ps][i] = *(LAS const uint16_t*)(kt + (gv & 0xffffu));
                    if (i < 5) W[ps][i] |= (unsigned)*(LAS const uint16_t*)(kt + (gv >> 16)) << 16; }
            if (lane < 8) *(LAS u32x4*)(cb + (2 * hh + (lane >> 2)) * CPY + (256 + (lane & 3)) * 16) = z4;
#pragma unroll
            for (int ps = 0; ps < 2; ++ps) {
                unsigned V[5];
#pragma unroll
                for (int i = 0; i < 5; ++i) V[i] = __builtin_amdgcn_alignbit(W[ps][i + 1], W[ps][i], 16);
                LAS char* cp = cb + (128 * hh + 64 * ps + lane) * 16;
                *(LAS u32x4*)(cp) = (u32x4){W[ps][0], W[ps][1], W[ps][2], W[ps][3]};
                *(LAS u32x4*)(cp + CPY) = (u32x4){V[0], V[1], V[2], V[3]};
                *(LAS u32x4*)(cp + 2 * CPY) = (u32x4){W[ps][1], W[ps][2], W[ps][3], W[ps][4]};
                *(LAS u32x4*)(cp + 3 * CPY) = (u32x4){V[1], V[2], V[3], V[4]};
            }
        }
        *(LAS u32x4*)(ui + (32 + 32 * hh + (lane >> 2)) * 80 + (lane & 3) * 16) = u0;
        *(LAS u32x4*)(ui + (48 + 32 * hh + (lane >> 2)) * 80 + (lane & 3) * 16) = u1;
        arrive(ps_ + 2); await(ps_ + 2, 2);
        __builtin_amdgcn_s_setprio(0);
        f32x16 a0 = {}, a1 = {};
        {
            const int sb = 7 - (r32 & 7);
            LAS const char* pa = cb + (sb & 3) * CPY + (255 - (r32 >> 3) + hi) * 16 + (sb >> 2) * 8 - 64 * (16 * hh + 15) - 2048;
            LAS const char* pa2 = pa + 8; asm volatile("" : "+v"(pa2));
            LAS const char* pb = ui + (32 + r32 - 16 * hh - 15) * 80 + 16 * hi;
            s16x4 ta[2][2], tb[2][2]; bf16x8 b0[2], b1[2];
#define T4(q, off) (*(LAS const s16x4*)((q) + (off)))
#define CSTEP(i, j, RD) do { const int o_ = 64 * (15 - ((j) >> 1)) + 32 * ((j) & 1), u_ = 80 * (15 - ((j) >> 1)) + 32 * ((j) & 1); \
                a1 = MFMA_BF(cat8(ta[i][0], ta[i][1]), b1[i], a1); SBAR(); if (RD) { ta[1 - i][0] = T4(pa, o_ + 2048); ta[1 - i][1] = T4(pa2, o_ + 2048); } SBAR(); \
                a0 = MFMA_BF(cat8(ta[i][0], ta[i][1]), b0[i], a0); SBAR(); if (RD) { b1[1 - i] = FR(pb, u_ + 2560); b0[1 - i] = FR(pb, u_); } SBAR(); \
                a1 = MFMA_BF(cat8(tb[i][0], tb[i][1]), b0[i], a1); SBAR(); if (RD) { tb[1 - i][0] = T4(pa, o_); tb[1 - i][1] = T4(pa2, o_); } SBAR(); } while (0)
            ta[0][0] = T4(pa, 960 + 2048); ta[0][1] = T4(pa2, 960 + 2048); b1[0] = FR(pb, 1200 + 2560); b0[0] = FR(pb, 1200); tb[0][0] = T4(pa, 960); tb[0][1] = T4(pa2, 960);
#pragma unroll
            for (int j = 0; j < 32; ++j) {
                if (j == 31) arrive(ps_ + 3);
                CSTEP(j & 1, j + 1, j < 31);
                LOADWF(j);
                SBAR();
            }
        }
        __builtin_amdgcn_s_setprio(1);
        {
            LAS char* ex = cb + hh * 9216 + r32 * 144 + hi * 16;
            await(ps_ + 3, 2);
#pragma unroll
            for (int g = 0; g < 4; ++g) {
                *(LAS f32x4*)(ex + g * 32) = (f32x4){a0[4 * g], a0[4 * g + 1], a0[4 * g + 2], a0[4 * g + 3]};
                *(LAS f32x4*)(ex + 4608 + g * 32) = (f32x4){a1[4 * g], a1[4 * g + 1], a1[4 * g + 2], a1[4 * g + 3]};
            }
            arrive(ps_ + 4);
            const float dsk = cc ? bf_hi(dw) : bf_lo(dw);
            u32x4 sk[2];
#pragma unroll
            for (int e = 0; e < 4; ++e) { sk[0][e] = pk_bf16(bf_lo(u0[e]) * dsk, bf_hi(u0[e]) * dsk); sk[1][e] = pk_bf16(bf_lo(u1[e]) * dsk, bf_hi(u1[e]) * dsk); }
            PIN(sk[0]); PIN(sk[1]);
            await(ps_ + 4, 2);
            LAS const char* er = cb + (32 * hh + (lane >> 2)) * 144 + (lane & 3) * 32;
            const unsigned bo = WS_BT + (unsigned)(ch * S + 1024 * hh) * 2 + voff;
#pragma unroll
            for (int i = 0; i < 2; ++i) {
                const f32x4 y0 = *(LAS const f32x4*)(er + i * 2304) + *(LAS const f32x4*)(er + i * 2304 + 9216);
                const f32x4 y1 = *(LAS const f32x4*)(er + i * 2304 + 16) + *(LAS const f32x4*)(er + i * 2304 + 9216 + 16);
                const u32x4 yb = {pk_bf16(y0[0], y0[1]), pk_bf16(y0[2], y0[3]), pk_bf16(y1[0], y1[1]), pk_bf16(y1[2], y1[3])};
                u32x4 r;
#pragma unroll
                for (int e = 0; e < 4; ++e) r[e] = pk_bf16(bf_lo(yb[e]) + bf_lo(sk[i][e]), bf_hi(yb[e]) + bf_hi(sk[i][e]));
                BST(r, bo + i * 1024);
            }
        }
        arrive(sy + SY_BG);
        drain();
        put_flag(ws, FL_BT + (unsigned)(xcd * 128 + bi * 4 + w) * FS, tag);
    } else {
        {
            const unsigned nb = (bi & 1) ? 12 * 2 * D : 16 * 2 * D;
            unsigned v4[4];
#pragma unroll
            for (int i = 0; i < 4; ++i) { const unsigned o = (unsigned)(w * 4096 + i * 1024) + voff; v4[i] = (o < nb ? o : 0u) + 4096 - i * 1024; }
            glds4((const char*)(wo + (DH * (bi >> 1) + 16 * (bi & 1)) * D) - 4096, v4[0], v4[1], v4[2], v4[3], lds0 + LB + w * 4096);
        }
        const u64 e0 = __hip_atomic_load((GAS u64*)ws, __ATOMIC_RELAXED, __HIP_MEMORY_SCOPE_AGENT);
        CODE_PF();
        drain(); asm volatile("" :: "v"(pf));
        tag = mix(e0);
        arrive(sy + SY_WO); await(sy + SY_WO, 4);
        set_tag(sy, tag);
        LAS const char* p = lds + LB + (4 * hi + ((lane & 15) >> 2)) * (2 * D) + ((lane >> 4) & 1) * 32 + (lane & 3) * 8;
#pragma unroll
        for (int i = 0; i < 4; ++i) {
            const int ct = w + 4 * i;
            if (ct < 14) {
                u32x4 o = __builtin_bit_cast(u32x4, cat8(trread(p + ct * 64), trread(p + 8 * 2 * D + ct * 64)));
                if (hi && (bi & 1)) { o[2] = 0; o[3] = 0; }
                BST(o, WS_WO + (unsigned)(bi * 14 + ct) * 1024 + voff);
            }
        }
        drain();
        put_flag(ws, FL_WO + (unsigned)(w * 32 + bi) * FS, tag);
        __builtin_amdgcn_s_setprio(0);
        arrive(sy + SY_BG);
        await_slow(sy + SY_G, 4);
        wait_flags<8>(ws, FL_WO + (unsigned)(w * 32 + r32) * FS, tag, 0xffffffffull, 0xffffffffull);
        if (ct < 14) {
#pragma unroll
        for (int i = 0; i < 32; ++i) { LOADWF(i); __builtin_amdgcn_s_sleep(2); SBAR(); } }
    }
    const __amdgpu_buffer_rsrc_t ro = __builtin_amdgcn_make_buffer_rsrc(out, 0, S * 2 * D * 2, 0x00020000);
    await(sy + SY_BG, 4);
    await_slow(sy + SY_X, 4);
    wait_flags<2>(ws, FL_O + (unsigned)(qt * 16 + 4 * w + (lane & 3)) * FS, tag, 15, 15);
    u32x4 of[8];
#pragma unroll
    for (int i = 0; i < 8; ++i) of[i] = BLD(WS_O + (unsigned)(qt * 32 + 8 * w + i) * 1024 + voff);
#pragma unroll
    for (int i = 0; i < 8; ++i) *(LAS u32x4*)(lds + LB + (8 * w + i) * 1024 + voff) = of[i];
    arrive(sy + SY_B2);
    f32x16 acc = {};
    if (ct < 14) {
#pragma unroll
        for (int i = 0; i < 8; ++i) acc = MFMA_BF(wf[i], BC8(of[i]), acc);
    }
    SBAR();
    await(sy + SY_B2, 4);
    if (ct < 14) {
        LAS const char* po[3];
#pragma unroll
        for (int j = 0; j < 3; ++j) po[j] = lds + LB + ((w + 1 + j) & 3) * 8192 + voff;
        bf16x8 og[4];
#pragma unroll
        for (int i = 0; i < 3; ++i) og[i] = FR(po[0], i * 1024);
#pragma unroll
        for (int i = 0; i < 24; ++i) { if (i < 21) og[(i + 3) & 3] = FR(po[(i + 3) >> 3], ((i + 3) & 7) * 1024); SBAR(); acc = MFMA_BF(wf[8 + i], og[i & 3], acc); SBAR(); }
        LAS char* sc = lds + LB + 40960 + w * 2304;
#pragma unroll
        for (int g = 0; g < 4; ++g) *(LAS u32x2*)(sc + r32 * 72 + 16 * g + 8 * hi) = (u32x2){pk_bf16(acc[4 * g], acc[4 * g + 1]), pk_bf16(acc[4 * g + 2], acc[4 * g + 3])};
        LAS const char* sr = sc + (lane >> 2) * 72 + (lane & 3) * 16;
        const u32x2 a0 = *(LAS const u32x2*)sr, a1 = *(LAS const u32x2*)(sr + 8), b0 = *(LAS const u32x2*)(sr + 16 * 72), b1 = *(LAS const u32x2*)(sr + 16 * 72 + 8);
        const unsigned oo = (unsigned)((32 * qt + (lane >> 2)) * 2 * D + 32 * ct) * 2 + (lane & 3) * 16;
        __builtin_amdgcn_raw_buffer_store_b128((u32x4){a0[0], a0[1], a1[0], a1[1]}, ro, oo, 0, 16);
        __builtin_amdgcn_raw_buffer_store_b128((u32x4){b0[0], b0[1], b1[0], b1[1]}, ro, oo + 16 * 2 * D * 2, 0, 16);
    }
    if (b == 0 && w == 0) put_flag(ws, 0, tag);
}

__global__ __launch_bounds__(512, 2) void fused(const uint16_t* __restrict__ x, const uint16_t* __restrict__ wqkv, const uint16_t* __restrict__ wo,
                                               const uint16_t* __restrict__ u, const uint16_t* __restrict__ lar, const uint16_t* __restrict__ aim,
                                               const uint16_t* __restrict__ cre, const uint16_t* __restrict__ cim, const uint16_t* __restrict__ ldt,
                                               const uint16_t* __restrict__ dskip, uint16_t* __restrict__ out, char* __restrict__ ws) {
    extern __shared__ __attribute__((aligned(16))) char smem[];
    LAS char* lds = (LAS char*)smem;
    const int lane = threadIdx.x & 63, w = __builtin_amdgcn_readfirstlane(threadIdx.x >> 6);
    const u64 code0 = __builtin_amdgcn_s_getpc() & ~255ull;
    if (w >= 4) __builtin_amdgcn_s_setprio(3);
    if (w == 4) ((LAS unsigned*)(lds + LSY))[lane] = 0;
    asm volatile("" :: "s"(x), "s"(wqkv), "s"(wo), "s"(u), "s"(lar), "s"(aim), "s"(cre), "s"(cim), "s"(ldt), "s"(dskip), "s"(out), "s"(ws));
    asm volatile("s_waitcnt lgkmcnt(0)\n\ts_barrier" ::: "memory");
    if (w < 4) role_a(x, wqkv, out, ws, lds, w, lane);
    else role_b(wo, u, lar, aim, cre, cim, ldt, dskip, out, ws, lds, w - 4, lane, code0);
}
}

extern "C" void kernel_launch(void* const* d_in, const int* in_sizes, int n_in, void* d_out, int out_size, void* d_ws, size_t ws_size, hipStream_t stream) {
    using namespace k;
    (void)hipFuncSetAttribute((const void*)fused, hipFuncAttributeMaxDynamicSharedMemorySize, LDS_BYTES);
    const uint16_t* const* in = (const uint16_t* const*)d_in;
    fused<<<256, 512, LDS_BYTES, stream>>>(in[0], in[1], in[2], in[3], in[4], in[5], in[6], in[7], in[8], in[9], (uint16_t*)d_out, (char*)d_ws);
}
```

```cpp
#include <hip/hip_runtime.h>
#include <hip/hip_bf16.h>
#include <stdint.h>

typedef short bf16x8 __attribute__((ext_vector_type(8)));
typedef _Float16 f16x8 __attribute__((ext_vector_type(8)));
typedef _Float16 f16x2 __attribute__((ext_vector_type(2)));
typedef __bf16 bf16x2 __attribute__((ext_vector_type(2)));
typedef short s16x4 __attribute__((ext_vector_type(4)));
typedef float f32x16 __attribute__((ext_vector_type(16)));
typedef float f32x4 __attribute__((ext_vector_type(4)));
typedef float f32x2 __attribute__((ext_vector_type(2)));
typedef unsigned u32x4 __attribute__((ext_vector_type(4)));
typedef unsigned u32x2 __attribute__((ext_vector_type(2)));
typedef unsigned long long u64;
#define LAS __attribute__((address_space(3)))
#define GAS __attribute__((address_space(1)))
#define DI __device__ __forceinline__
#define MFMA_BF(a, b, c) __builtin_amdgcn_mfma_f32_32x32x16_bf16((a), (b), (c), 0, 0, 0)
#define MFMA_F16(a, b, c) __builtin_amdgcn_mfma_f32_32x32x16_f16((a), (b), (c), 0, 0, 0)
#define SBAR() __builtin_amdgcn_sched_barrier(0)
#define CFENCE() asm volatile("" ::: "memory")
#define PIN(x) asm volatile("" : "+v"(x))
#define BC8(x) __builtin_bit_cast(bf16x8, (x))
#define EX(x) __builtin_amdgcn_exp2f(x)

namespace k {
constexpr int S = 2048, D = 448, DH = 28, NS = 256;
constexpr unsigned FS = 128;
constexpr unsigned FL_KV = FS, FL_O = FL_KV + 1024 * FS, FL_UT = FL_O + 1024 * FS, FL_BT = FL_UT + 896 * FS, FL_WO = FL_BT + 896 * FS;
constexpr unsigned WS_KV = 512u << 10;
constexpr unsigned WS_O = WS_KV + (4u << 20);
constexpr unsigned WS_WO = WS_O + (2u << 20);
constexpr unsigned WS_UT = WS_WO + (448u << 10);
constexpr unsigned WS_BT = WS_UT + D * S * 2;
constexpr unsigned WS_END = WS_BT + D * S * 2;
constexpr int WSL = 8192, LWQ = 4 * WSL, LXR = LWQ + 16384, XSL = 16384, LB = LXR + 3 * XSL;
constexpr int LQ = LXR, LX = 0;
constexpr int TROW = 272, TABW = 33 * TROW, CPY = 4160, UOF = 2 * TABW, KOF = UOF + 7680, CBW = KOF + 1312;
constexpr int LT1 = LB + 2 * CBW, LSY = LT1 + 8192, LDS_BYTES = LSY + 256;
static_assert(LDS_BYTES <= 163840 && (LSY & 15) == 0 && 4 * CPY <= UOF && LB + 40960 + 4 * 2304 <= LSY, "lds");
enum { SY_F = 2, SY_P = 10, SY_WO = 20, SY_BG = 22, SY_B2 = 23, SY_Q = 24, SY_X = 25, SY_G = 26, SY_T2 = 27 };
constexpr int SPIN = 1 << 18;
constexpr unsigned CODE_LINES = 216;

struct IdxTab { uint16_t v[256][12]; };
constexpr IdxTab make_idx() {
    IdxTab t = {};
    for (int y = 0; y < 256; ++y) for (int e = 0; e < 12; ++e) {
        const int j = 2047 - 8 * y - e; int ix = 644;
        if (j >= 0) { const int sh = j >= 1024 ? 3 : (j >= 512 ? 2 : (j >= 256 ? 1 : 0));
            ix = sh ? 128 * sh + ((j + (1 << (sh - 1)) - 1 + ((j >> sh) & 1)) >> sh) : j; }
        t.v[y][e] = (uint16_t)(2 * ix);
    }
    return t;
}
__device__ const IdxTab g_idx __attribute__((aligned(16))) = make_idx();

DI unsigned pk_bf16(float a, float b) { f32x2 v = {a, b}; return __builtin_bit_cast(unsigned, __builtin_convertvector(v, bf16x2)); }
DI unsigned pk_f16(f32x2 v) { return __builtin_bit_cast(unsigned, __builtin_convertvector(v, f16x2)); }
DI float bf_lo(unsigned w) { return __uint_as_float(w << 16); }
DI float bf_hi(unsigned w) { return __uint_as_float(w & 0xffff0000u); }
DI float rbf(float x) { return bf_lo(pk_bf16(x, x)); }
#define GL0 "s_mov_b32 %0, m0\n\ts_mov_b32 m0, %2\n\ts_nop 0\n\t"
#define GL1(V, O, C) "global_load_lds_dwordx4 " V ", %1 offset:" #O C "\n\t"
#define GLE "s_mov_b32 m0, %0"
DI void glds4(const void* sb, unsigned v0, unsigned v1, unsigned v2, unsigned v3, unsigned lb) { unsigned sv;
    asm volatile(GL0 GL1("%3", 0, "") GL1("%4", 1024, "") GL1("%5", 2048, "") GL1("%6", 3072, "") GLE : "=&s"(sv) : "s"(sb), "s"(lb), "v"(v0), "v"(v1), "v"(v2), "v"(v3) : "memory"); }
DI void glds2(const void* sb, unsigned v0, unsigned v1, unsigned lb) { unsigned sv;
    asm volatile(GL0 GL1("%3", 0, "") GL1("%4", 1024, "") GLE : "=&s"(sv) : "s"(sb), "s"(lb), "v"(v0), "v"(v1) : "memory"); }
DI void glds1(const void* sb, unsigned v0, unsigned lb) { unsigned sv;
    asm volatile(GL0 GL1("%3", 0, "") GLE : "=&s"(sv) : "s"(sb), "s"(lb), "v"(v0) : "memory"); }
DI float wave_max(float v) {
#define DPPMAX(ctrl, rm) v = fmaxf(v, __uint_as_float(__builtin_amdgcn_update_dpp(0, __float_as_uint(v), ctrl, rm, 0xf, true)))
    DPPMAX(0x111, 0xf); DPPMAX(0x112, 0xf); DPPMAX(0x114, 0xf); DPPMAX(0x118, 0xf); DPPMAX(0x142, 0xa); DPPMAX(0x143, 0xc);
    return __uint_as_float(__builtin_amdgcn_readlane(__float_as_uint(v), 63));
}
DI s16x4 trread(LAS const char* p) { return __builtin_bit_cast(s16x4, __builtin_amdgcn_ds_read_tr16_b64_v4i16((LAS s16x4*)p)); }
DI bf16x8 cat8(s16x4 a, s16x4 b) { return __builtin_shufflevector(a, b, 0, 1, 2, 3, 4, 5, 6, 7); }
DI f32x2 pfma(f32x2 a, f32x2 b, f32x2 c) { return __builtin_elementwise_fma(a, b, c); }
DI f32x2 cmul(f32x2 a, f32x2 b, f32x2 bs) { return pfma(a.yy, bs, a.xx * b); }
DI f16x8 cmul4h(u32x4 p, u32x4 q) {
    u32x4 t;
#define PM(i, a, b) "v_pk_mul_f16 %" #i ", %" #a ", %" #b " op_sel:[0,0] op_sel_hi:[0,1]\n\t"
#define PF(i, a, b) "v_pk_fma_f16 %" #i ", %" #a ", %" #b ", %" #i " op_sel:[1,1,0] op_sel_hi:[1,0,1] neg_lo:[1,0,0]\n\t"
    asm(PM(0, 4, 8) PM(1, 5, 9) PM(2, 6, 10) PM(3, 7, 11) PF(0, 4, 8) PF(1, 5, 9) PF(2, 6, 10) PF(3, 7, 11) "s_nop 1"
        : "=&v"(t[0]), "=&v"(t[1]), "=&v"(t[2]), "=&v"(t[3]) : "v"(p[0]), "v"(p[1]), "v"(p[2]), "v"(p[3]), "v"(q[0]), "v"(q[1]), "v"(q[2]), "v"(q[3]));
    return __builtin_bit_cast(f16x8, t);
}
DI f32x2 rot(f32x2 b) { return (f32x2){-b.y, b.x}; }
DI f32x2 anchor(float a2, float rev, float s) {
    const float mag = EX(a2 * s);
    float r = rev * s; r -= __builtin_rintf(r);
    return (f32x2){mag * __builtin_amdgcn_cosf(r), mag * __builtin_amdgcn_sinf(r)};
}
DI void arrive(LAS unsigned* c) { asm volatile("s_mov_b64 exec, 1\n\tds_add_u32 %0, %1\n\ts_mov_b64 exec, -1" :: "v"((unsigned)(uintptr_t)c), "v"(1u) : "memory"); }
DI void await(LAS unsigned* c, unsigned n) {
    unsigned t, i = SPIN;
    asm volatile("1:\n\tds_read_b32 %0, %2\n\ts_waitcnt lgkmcnt(0)\n\tv_cmp_gt_u32 vcc, %3, %0\n\ts_cbranch_vccz 2f\n\ts_sleep 1\n\ts_sub_u32 %1, %1, 1\n\ts_cbranch_scc0 1b\n2:"
                 : "=&v"(t), "+s"(i) : "v"((unsigned)(uintptr_t)c), "s"(n) : "vcc", "scc", "memory");
}
DI void await_slow(LAS unsigned* c, unsigned n) {
    unsigned t, i = SPIN;
    asm volatile("1:\n\tds_read_b32 %0, %2\n\ts_waitcnt lgkmcnt(0)\n\tv_cmp_gt_u32 vcc, %3, %0\n\ts_cbranch_vccz 2f\n\ts_sleep 3\n\ts_sub_u32 %1, %1, 1\n\ts_cbranch_scc0 1b\n2:"
                 : "=&v"(t), "+s"(i) : "v"((unsigned)(uintptr_t)c), "s"(n) : "vcc", "scc", "memory");
}
DI u64 get_tag(LAS unsigned* sy) {
    await(sy, 1);
    const u64 v = __hip_atomic_load((LAS u64*)sy, __ATOMIC_RELAXED, __HIP_MEMORY_SCOPE_WORKGROUP);
    return ((u64)(unsigned)__builtin_amdgcn_readfirstlane((unsigned)(v >> 32)) << 32) | (unsigned)__builtin_amdgcn_readfirstlane((unsigned)v);
}
DI void set_tag(LAS unsigned* sy, u64 tag) { asm volatile("s_mov_b64 exec, 1\n\tds_write_b64 %0, %1\n\ts_mov_b64 exec, -1" :: "v"((unsigned)(uintptr_t)sy), "v"(tag) : "memory"); }
DI void drain() { asm volatile("s_waitcnt vmcnt(0)" ::: "memory"); }
DI void put_flag(char* ws, unsigned off, u64 tag) {
    asm volatile("s_mov_b64 exec, 1\n\tglobal_store_dwordx2 %0, %1, %2 sc1\n\ts_mov_b64 exec, -1" :: "v"(off), "v"(tag), "s"(ws) : "memory");
}
template <int SLP> DI u64 wait_flags(char* ws, unsigned off, u64 tag, u64 pend, u64 need) {
    u64 t, left; unsigned i = SPIN;
    asm volatile("s_mov_b64 exec, %5\n1:\n\tglobal_load_dwordx2 %0, %3, %4 sc1\n\ts_waitcnt vmcnt(0)\n\tv_cmpx_ne_u64 vcc, %6, %0\n\ts_and_b64 %2, exec, %7\n\t"
                 "s_cbranch_scc0 2f\n\ts_sleep %8\n\ts_sub_u32 %1, %1, 1\n\ts_cbranch_scc0 1b\n2:\n\ts_mov_b64 %2, exec\n\ts_mov_b64 exec, -1"
                 : "=&v"(t), "+s"(i), "=&s"(left) : "v"(off), "s"(ws), "s"(pend), "s"(tag), "s"(need), "i"(SLP) : "vcc", "scc", "memory");
    return left;
}
#define BST(v, off) __builtin_amdgcn_raw_buffer_store_b128((v), rs, (off), 0, 16)
#define BLD(off) __builtin_amdgcn_raw_buffer_load_b128(rs, (off), 0, 16)
#define FR(p, off) (*(LAS const bf16x8*)((p) + (off)))
DI float swapmax(float m) { const auto r = __builtin_amdgcn_permlane32_swap(__float_as_uint(m), __float_as_uint(m), false, false); return fmaxf(__uint_as_float(r[0]), __uint_as_float(r[1])); }
DI u32x4 put_ref(u32x4 q1, float m, int hi) {
    const float m1 = rbf(m), m2 = rbf(m - m1), m3 = m - m1 - m2;
    if (hi) { q1[2] = pk_bf16(-m1, -m2); q1[3] = pk_bf16(-m3, 0.f); }
    return q1;
}
DI u64 mix(u64 v) {
    const u64 e = ((u64)(unsigned)__builtin_amdgcn_readfirstlane((unsigned)(v >> 32)) << 32) | (unsigned)__builtin_amdgcn_readfirstlane((unsigned)v);
    u64 z = e + 0x9E3779B97F4A7C15ull; z = (z ^ (z >> 30)) * 0xBF58476D1CE4E5B9ull; z = (z ^ (z >> 27)) * 0x94D049BB133111EBull; return (z ^ (z >> 31)) | 1ull; }
DI void transpose_tile(LAS const char* stage, LAS char* scratch, int w, int lane, u32x4& o0, u32x4& o1) {
    LAS const char* p = stage + (lane >> 4) * 2048 + ((lane & 15) >> 2) * 128 + w * 32 + (lane & 3) * 8;
    const bf16x8 t0 = cat8(trread(p), trread(p + 512)), t1 = cat8(trread(p + 1024), trread(p + 1536));
    LAS char* sc = scratch + (lane & 15) * 144 + (lane >> 4) * 32;
    *(LAS bf16x8*)sc = t0; *(LAS bf16x8*)(sc + 16) = t1;
    LAS const char* sr = scratch + (lane >> 3) * 144 + (lane & 7) * 16;
    o0 = *(LAS const u32x4*)sr; o1 = *(LAS const u32x4*)(sr + 8 * 144);
}

DI void role_a(const uint16_t* __restrict__ x, const uint16_t* __restrict__ wqkv, uint16_t* __restrict__ out, char* __restrict__ ws, LAS char* lds, int w, int lane) {
    const int head = 2 * (blockIdx.x & 7) + ((blockIdx.x >> 3) & 1), grp = blockIdx.x >> 4;
    const unsigned lds0 = (unsigned)(uintptr_t)lds;
    const int r32 = lane & 31, hi = lane >> 5;
    const int wsh = (head & 7) == 7 ? 4 : 0;
    const unsigned wv = (unsigned)((lane >> 2) * 3 * D + (lane & 3) * 8) * 2;
    const char* wb = (const char*)(wqkv + DH * head - wsh + (16 * w) * 3 * D);
    const unsigned xv0 = (unsigned)((lane >> 3) * D + (((lane & 7) ^ (lane >> 4)) * 8)) * 2, xv1 = xv0 ^ 64;
    const char* xb = (const char*)(x + (128 * grp + 32 * w) * D);
    const int rho = ((blockIdx.x >> 3) + ((blockIdx.x >> 1) & 3)) % 7;
#define SP(st) ((st) + rho >= 7 ? (st) + rho - 7 : (st) + rho)
#define WQO(st) ((st) < 4 ? LWQ + (st) * 4096 : ((st) < 6 ? 3 * WSL + ((st) - 4) * 4096 : 0))
#define LOADW(st) glds2(wb + SP(st) * (64 * 3 * D * 2), wv + 2 * D, wv + 4 * D - 1024, lds0 + ((st) & 3) * WSL + w * 2048)
#define LOADQ(st) glds1(wb + SP(st) * (64 * 3 * D * 2), wv, lds0 + WQO(st) + w * 1024)
#define LOADX(st) glds4(xb + SP(st) * 128, xv0, xv1 + 16 * D - 1024, xv0 + 32 * D - 2048, xv1 + 48 * D - 3072, lds0 + LXR + ((st) % 3) * XSL + w * 4096)
    LOADW(0); LOADX(0);
    LOADW(1); LOADX(1);
    LAS unsigned* sy = (LAS unsigned*)(lds + LSY);
    const __amdgpu_buffer_rsrc_t rs = __builtin_amdgcn_make_buffer_rsrc(ws, 0, WS_END, 0x00020000);
    const int xs = (r32 >> 1) & 7;
    LAS const char* xp[4];
#pragma unroll
    for (int ks = 0; ks < 4; ++ks) xp[ks] = lds + LXR + w * 4096 + r32 * 128 + (((2 * ks + hi) ^ xs) * 16);
    LAS const char* wp = lds + (8 * hi + ((lane & 15) >> 2)) * 64 + ((lane >> 4) & 1) * 32 + (lane & 3) * 8 + wsh * 2;
    f32x16 aq = {}, ak = {}, av = {};
    bf16x8 xf[7][4]; s16x4 fr[4][4];
#define RX(st, ks) xf[st][ks] = FR(xp[ks], ((st) % 3) * XSL)
#define RW(st, ks, j) fr[ks][j] = trread(wp + ((st) & 3) * WSL + (ks) * 2048 + ((j) >> 1) * 1024 + ((j) & 1) * 256)
#define MK(st, ks) ak = MFMA_BF(cat8(fr[ks][0], fr[ks][1]), xf[st][ks], ak); SBAR()
#define MV(st, ks) av = MFMA_BF(xf[st][ks], cat8(fr[ks][2], fr[ks][3]), av); SBAR()
#define QSTEP(st, ks) MK(st, ks); RW(st, ks + 1, 0); RW(st, ks + 1, 1); SBAR(); MV(st, ks); RW(st, ks + 1, 2); RW(st, ks + 1, 3); SBAR()
    u64 tag = 0;
    asm volatile("s_waitcnt vmcnt(6)" ::: "memory");
    arrive(sy + SY_F); LOADW(2); LOADX(2); await(sy + SY_F, 4);
    RX(0, 0); RW(0, 0, 0); RW(0, 0, 1); RW(0, 0, 2); RW(0, 0, 3); RX(0, 1); RX(0, 2); RX(0, 3); SBAR();
#pragma unroll
    for (int st = 0; st < 7; ++st) {
        QSTEP(st, 0); if (st < 4) LOADW(st + 3); if (st == 4) { LOADQ(4); LOADQ(5); } if (st == 5) LOADQ(6);
        QSTEP(st, 1); if (st < 4) LOADX(st + 3);
        QSTEP(st, 2); if (st == 3) { LOADQ(0); LOADQ(1); LOADQ(2); LOADQ(3); }
        if (st < 6) {
            if (st == 3) asm volatile("s_waitcnt vmcnt(16)" ::: "memory"); else if (st == 5) asm volatile("s_waitcnt vmcnt(7)" ::: "memory"); else asm volatile("s_waitcnt vmcnt(12)" ::: "memory");
            arrive(sy + SY_F + 1 + st); SBAR();
        }
        MK(st, 3);
        if (st < 6) { await(sy + SY_F + 1 + st, 4); RX(st + 1, 0); RW(st + 1, 0, 0); RW(st + 1, 0, 1); RW(st + 1, 0, 2); RW(st + 1, 0, 3); } SBAR();
        MV(st, 3); if (st < 6) { RX(st + 1, 1); RX(st + 1, 2); RX(st + 1, 3); } SBAR();
        if (st == 2) tag = get_tag(sy);
    }
    const float cq = 0.18898223650461362f * 1.4426950408889634f;
    const unsigned voff = lane * 16;
    u32x4 q0, q1, k0, k1, v0, v1;
    const unsigned tb = WS_KV + (unsigned)(head * 64 + 4 * grp + w) * 4096 + voff;
#pragma unroll
    for (int i = 0; i < 4; ++i) { k0[i] = pk_bf16(ak[2 * i], ak[2 * i + 1]); k1[i] = pk_bf16(ak[8 + 2 * i], ak[9 + 2 * i]); }
    if (hi) { k1[2] = 0x3f803f80u; k1[3] = 0x00003f80u; }
    BST(k0, tb); BST(k1, tb + 1024);
#pragma unroll
    for (int i = 0; i < 4; ++i) {
        v0[i] = pk_bf16(av[2 * i], av[2 * i + 1]); v1[i] = pk_bf16(av[8 + 2 * i], av[9 + 2 * i]);
        if (r32 >= DH) { v0[i] = v1[i] = (r32 == DH ? 0x3f803f80u : 0u); }
    }
    BST(v0, tb + 2048); BST(v1, tb + 3072);
    asm volatile("s_waitcnt vmcnt(4)" ::: "memory");
    arrive(sy + SY_F + 7); await(sy + SY_F + 7, 4);
    {
        s16x4 fq[4][2];
#define RQ(j) do { fq[(j) & 3][0] = trread(wp + WQO((j) >> 2) + ((j) & 3) * 1024); fq[(j) & 3][1] = trread(wp + WQO((j) >> 2) + ((j) & 3) * 1024 + 256); } while (0)
        RQ(0); RQ(1); RQ(2); SBAR();
#pragma unroll
        for (int j = 0; j < 28; ++j) {
            aq = MFMA_BF(cat8(fq[j & 3][0], fq[j & 3][1]), xf[j >> 2][j & 3], aq); SBAR(); if (j < 25) RQ(j + 3); SBAR();
            if (j == 13) { drain(); put_flag(ws, FL_KV + (unsigned)((head * 4 + w) * 16 + grp) * FS, tag); }
        }
    }
#pragma unroll
    for (int i = 0; i < 4; ++i) { q0[i] = pk_bf16(aq[2 * i] * cq, aq[2 * i + 1] * cq); q1[i] = pk_bf16(aq[8 + 2 * i] * cq, aq[9 + 2 * i] * cq); }
    if (hi) { q1[2] = 0; q1[3] = 0; }
    const unsigned kvf = FL_KV + (unsigned)((head * 4 + w) * 16 + ((grp + lane) & 15)) * FS;
    const f32x16 zero = {};
    {
        f32x16 s = MFMA_BF(BC8(k0), BC8(q0), zero); s = MFMA_BF(BC8(k1), BC8(q1), s);
        float m = s[0];
#pragma unroll
        for (int i = 1; i < 16; ++i) m = fmaxf(m, s[i]);
        q1 = put_ref(q1, swapmax(m), hi);
    }
    { LAS u32x4* qo = (LAS u32x4*)(lds + LQ + w * 2048) + lane; qo[0] = q0; qo[64] = q1; }
    arrive(sy + SY_Q);
    const u64 kv1 = __hip_atomic_load((GAS u64*)(ws + kvf), __ATOMIC_RELAXED, __HIP_MEMORY_SCOPE_AGENT); SBAR();
    f32x16 O[4] = {}; PIN(O[0]); PIN(O[1]); PIN(O[2]); PIN(O[3]);
    await(sy + SY_Q, 4);
    bf16x8 Q[4][2];
    Q[0][0] = BC8(q0); Q[0][1] = BC8(q1);
#pragma unroll
    for (int i = 1; i < 4; ++i) { LAS const char* p = lds + LQ + ((w + i) & 3) * 2048 + voff; Q[i][0] = FR(p, 0); Q[i][1] = FR(p, 1024); }
    u32x4 kv[4][4] = {{k0, k1, v0, v1}, {}, {}, {}};
    f32x16 sa, sb;
    u32x4 p0, p1 = {};
#define GRP(SC, i, P, j) do { float e0 = EX(SC[i]); SBAR(); float e1 = EX(SC[i + 1]); SBAR(); float e2 = EX(SC[i + 2]); SBAR(); float e3 = EX(SC[i + 3]); SBAR(); \
        P[j] = pk_bf16(e0, e1); SBAR(); P[j + 1] = pk_bf16(e2, e3); PIN(P); SBAR(); } while (0)
#define SLOT(SC, SN, cb, i, QK, PV1, LD) do { constexpr int nb_ = (i) < 3 ? (cb) : ((cb) + 1) % 4, pb_ = (i) > 0 ? (cb) : ((cb) + 3) % 4; \
        if (QK) SN = MFMA_BF(BC8(kv[nb_][0]), Q[((i) + 1) & 3][0], zero); SBAR(); GRP(SC, 0, p0, 0); \
        if (PV1) O[((i) + 3) & 3] = MFMA_BF(BC8(kv[pb_][3]), BC8(p1), O[((i) + 3) & 3]); SBAR(); \
        if (LD) { kv[((cb) + 3) % 4][i] = __builtin_amdgcn_raw_buffer_load_b128(rs, voff + (i) * 1024, kvo, 16); SBAR(); } GRP(SC, 4, p0, 2); \
        if (QK) SN = MFMA_BF(BC8(kv[nb_][1]), Q[((i) + 1) & 3][1], SN); SBAR(); GRP(SC, 8, p1, 0); \
        O[i] = MFMA_BF(BC8(kv[cb][2]), BC8(p0), O[i]); SBAR(); GRP(SC, 12, p1, 2); } while (0)
    const unsigned kvb = WS_KV + (unsigned)(head * 64 + w) * 4096;
    unsigned kvo = 0;
    u64 miss = __ballot(kv1 != tag) & 0xfffe;
    if (miss & 6) miss = wait_flags<2>(ws, kvf, tag, miss, 6);
    CFENCE();
#pragma unroll
    for (int j = 1; j < 3; ++j)
#pragma unroll
        for (int f = 0; f < 4; ++f) kv[j][f] = __builtin_amdgcn_raw_buffer_load_b128(rs, voff + f * 1024, kvb + ((grp + j) & 15) * 16384, 16);
    arrive(sy + SY_G);
    sa = MFMA_BF(BC8(k0), Q[0][0], zero); sa = MFMA_BF(BC8(k1), Q[0][1], sa);
    int nx = grp + 3;
#define TILE(cb) do { kvo = kvb + (nx & 15) * 16384; ++nx; SBAR(); \
        SLOT(sa, sb, cb, 0, true, true, true); SLOT(sb, sa, cb, 1, true, true, true); SLOT(sa, sb, cb, 2, true, true, true); SLOT(sb, sa, cb, 3, true, true, true); } while (0)
#pragma unroll 1
    for (unsigned msk = 0x78; msk < 0x80000; msk <<= 4) { if (__builtin_expect((miss & msk) != 0, 0)) miss = wait_flags<2>(ws, kvf, tag, miss, msk); TILE(0); TILE(1); TILE(2); TILE(3); }
    O[3] = MFMA_BF(BC8(kv[3][3]), BC8(p1), O[3]);
    f32x16 o = O[0];
    {
#pragma unroll
        for (int i = 1; i < 4; ++i) {
            LAS char* ex = lds + LX + (((w + i) & 3) * 3 + i - 1) * 4096 + voff;
#pragma unroll
            for (int g = 0; g < 4; ++g) *(LAS f32x4*)(ex + g * 1024) = (f32x4){O[i][4 * g], O[i][4 * g + 1], O[i][4 * g + 2], O[i][4 * g + 3]};
        }
        arrive(sy + SY_X); await(sy + SY_X, 4);
        LAS const char* er = lds + LX + w * 3 * 4096 + voff;
#pragma unroll
        for (int i = 0; i < 3; ++i)
#pragma unroll
            for (int g = 0; g < 4; ++g) { const f32x4 r = *(LAS const f32x4*)(er + i * 4096 + g * 1024);
#pragma unroll
                for (int e = 0; e < 4; ++e) o[4 * g + e] += r[e]; }
        f32x2 chk = {0.f, 0.f};
#pragma unroll
        for (int i = 0; i < 8; ++i) chk = pfma((f32x2){o[2 * i], o[2 * i + 1]}, (f32x2){0.f, 0.f}, chk);
        if (__builtin_expect(__any(!(chk[0] + chk[1] == 0.f) || !(o[12] < 1e30f)), 0)) {
            const unsigned hb = WS_KV + (unsigned)head * 64 * 4096 + voff;
            u32x4 qr = __builtin_bit_cast(u32x4, Q[0][1]); if (hi) { qr[2] = 0; qr[3] = 0; }
            float mm = -INFINITY;
            wait_flags<4>(ws, FL_KV + (unsigned)(head * 64 + lane) * FS, tag, ~0ull, ~0ull);
#pragma unroll 1
            for (int t = 0; t < 64; ++t) {
                f32x16 s = MFMA_BF(BC8(BLD(hb + t * 4096)), Q[0][0], zero); s = MFMA_BF(BC8(BLD(hb + t * 4096 + 1024)), BC8(qr), s);
#pragma unroll
                for (int i = 0; i < 16; ++i) mm = fmaxf(mm, s[i]);
            }
            qr = put_ref(qr, swapmax(mm), hi);
            o = zero;
#pragma unroll 1
            for (int t = 0; t < 64; ++t) {
                f32x16 s = MFMA_BF(BC8(BLD(hb + t * 4096)), Q[0][0], zero); s = MFMA_BF(BC8(BLD(hb + t * 4096 + 1024)), BC8(qr), s);
#pragma unroll
                for (int i = 0; i < 4; ++i) { p0[i] = pk_bf16(EX(s[2 * i]), EX(s[2 * i + 1])); p1[i] = pk_bf16(EX(s[8 + 2 * i]), EX(s[9 + 2 * i])); }
                o = MFMA_BF(BC8(BLD(hb + t * 4096 + 2048)), BC8(p0), o); o = MFMA_BF(BC8(BLD(hb + t * 4096 + 3072)), BC8(p1), o);
            }
        }
    }
    {
        const auto r = __builtin_amdgcn_permlane32_swap(__float_as_uint(o[12]), __float_as_uint(o[12]), false, false);
        const float rl = __builtin_amdgcn_rcpf(__uint_as_float(r[1]));
        u32x4 o0, o1;
#pragma unroll
        for (int i = 0; i < 4; ++i) { o0[i] = pk_bf16(o[2 * i] * rl, o[2 * i + 1] * rl); o1[i] = pk_bf16(o[8 + 2 * i] * rl, o[9 + 2 * i] * rl); }
        if (hi) { o1[2] = 0; o1[3] = 0; }
        const unsigned fi = (unsigned)((4 * grp + w) * 16 + head);
        BST(o0, WS_O + fi * 2048 + voff); BST(o1, WS_O + fi * 2048 + 1024 + voff);
        drain();
        put_flag(ws, FL_O + fi * FS, tag);
    }
    const int xcd = blockIdx.x & 7, bi = blockIdx.x >> 3;
    if (xcd < 7) {
        wait_flags<2>(ws, FL_BT + (unsigned)(xcd * 128 + w * 32 + r32) * FS, tag, 0xffffffffull, 0xffffffffull);
        const unsigned bo = WS_BT + (unsigned)((64 * xcd + 16 * w + (lane >> 3)) * S + 64 * bi + (lane & 7) * 8) * 2;
        const u32x4 b0 = BLD(bo), b1 = BLD(bo + 8 * S * 2);
        *(LAS u32x4*)(lds + 65536 + w * 2048 + voff) = b0; *(LAS u32x4*)(lds + 65536 + w * 2048 + 1024 + voff) = b1;
        arrive(sy + SY_T2); await(sy + SY_T2, 4);
        u32x4 o0, o1; transpose_tile(lds + 65536, lds + 73728 + w * 2304, w, lane, o0, o1);
        const __amdgpu_buffer_rsrc_t ro = __builtin_amdgcn_make_buffer_rsrc(out, 0, S * 2 * D * 2, 0x00020000);
        const unsigned oo = (unsigned)((64 * bi + 16 * w + (lane >> 3)) * 2 * D + D + 64 * xcd) * 2 + (lane & 7) * 16;
        __builtin_amdgcn_raw_buffer_store_b128(o0, ro, oo, 0, 16); __builtin_amdgcn_raw_buffer_store_b128(o1, ro, oo + 8 * 2 * D * 2, 0, 16);
    }
}

DI void role_b(const uint16_t* __restrict__ wo, const uint16_t* __restrict__ u, const uint16_t* __restrict__ lar, const uint16_t* __restrict__ aim,
               const uint16_t* __restrict__ cre, const uint16_t* __restrict__ cim, const uint16_t* __restrict__ ldt, const uint16_t* __restrict__ dskip,
               uint16_t* __restrict__ out, char* __restrict__ ws, LAS char* lds, int w, int lane, u64 code0) {
    u64 tag;
    const unsigned lds0 = (unsigned)(uintptr_t)lds;
    LAS unsigned* sy = (LAS unsigned*)(lds + LSY);
    const __amdgpu_buffer_rsrc_t rs = __builtin_amdgcn_make_buffer_rsrc(ws, 0, WS_END, 0x00020000);
    const int b = blockIdx.x, xcd = b & 7, bi = b >> 3, r32 = lane & 31, hi = lane >> 5;
    const unsigned voff = lane * 16;
    LAS char* tab = lds + LB + (w >> 1) * CBW + (w & 1) * TABW;
    unsigned pf = 0;
#define CODE_PF() asm volatile("global_load_dword %0, %1, %2" : "+v"(pf) : "v"(min((unsigned)((bi * 4 + w) * 3 + (lane < 3 ? lane : 0)), CODE_LINES - 1) * 128), "s"(code0) : "memory")
    const int qt = 8 * xcd + (bi >> 2), ct = 4 * (bi & 3) + w;
    bf16x8 wf[32];
    const unsigned wvo = ct < 14 ? voff : 0x80000000u;
    unsigned wgb[4];
#pragma unroll
    for (int g = 0; g < 4; ++g) wgb[g] = WS_WO + (unsigned)(((w + g) & 3) * 8 * 14 + ct) * 1024;
#define LOADWF(i) wf[i] = BC8(__builtin_amdgcn_raw_buffer_load_b128(rs, wvo, wgb[(i) >> 3] + ((i) & 7) * 14336, 16))
    if (xcd < 7) {
        const int cc = w >> 1, hh = w & 1, pair = 32 * xcd + bi, ch = 2 * pair + cc;
        LAS char* cb = lds + LB + cc * CBW;
        const unsigned po = (unsigned)(ch * NS + 128 * hh) / 2 + lane;
        const unsigned pl = ((const unsigned*)lar)[po], pa = ((const unsigned*)aim)[po], pr = ((const unsigned*)cre)[po], pi = ((const unsigned*)cim)[po];
        const unsigned uo = (unsigned)((64 * bi + 16 * w + (lane >> 3)) * D + 64 * xcd + (lane & 7) * 8) * 2;
        glds2(u, uo, uo + 8 * D * 2 - 1024, lds0 + LT1 + w * 2048);
        const u64 e0 = __hip_atomic_load((GAS u64*)ws, __ATOMIC_RELAXED, __HIP_MEMORY_SCOPE_AGENT);
        u32x2 gi[2][3];
#pragma unroll
        for (int ps = 0; ps < 2; ++ps)
#pragma unroll
            for (int i = 0; i < 3; ++i) gi[ps][i] = ((const u32x2*)g_idx.v[128 * hh + 64 * ps + lane])[i];
        CODE_PF();
        const unsigned lw = ((const unsigned*)ldt)[pair], dw = ((const unsigned*)dskip)[pair];
        LAS char* ui = cb + UOF;
        const u32x4 z4 = {};
        *(LAS u32x4*)(ui + (64 * hh + lane) * 16) = z4; *(LAS u32x4*)(ui + (96 + lane) * 16) = z4;
        const float dt = rbf(expf(cc ? bf_hi(lw) : bf_lo(lw)));
        drain(); asm volatile("" :: "v"(pf));
        arrive(sy + SY_WO);
        tag = mix(e0);
        const float cm = wave_max(fmaxf(fmaxf(fabsf(bf_lo(pr)), fabsf(bf_hi(pr))), fmaxf(fabsf(bf_lo(pi)), fabsf(bf_hi(pi)))));
        unsigned ce = __float_as_uint(cm) & 0x7f800000u; ce = ce < 0x01000000u ? 0x01000000u : (ce > 0x7e800000u ? 0x7e800000u : ce);
        const float cscale = __uint_as_float(0x7f000000u - ce), oscale = 2.0f * dt * __uint_as_float(ce);
        LAS unsigned* tw = (LAS unsigned*)(tab + lane * 4);
        LAS const char* trd = tab + r32 * TROW + hi * 16;
        f32x16 dacc = {};
        f32x2 z1, z8, z256, x0;
#define ANCH(vl, va, vr, vi) do { const float a2 = (-rbf(expf(vl)) * dt) * 1.4426950408889634f, rev = ((va) * dt) * 0.15915494309189535f; \
            z1 = anchor(a2, -rev, 1.f); z8 = anchor(a2, rev, 8.f); z256 = anchor(a2, -rev, 256.f); x0 = (f32x2){(vr) * cscale, -(vi) * cscale}; \
            PIN(z1); PIN(z8); PIN(z256); SBAR(); } while (0)
#define TW(row) tw[(row) * (TROW / 4)]
#define CHAIN() do { \
            const f32x2 z1s = rot(z1), zs = rot(z256), z8s = rot(z8); \
            f32x2 X[8]; const f32x2 z16 = cmul(z8, z8, z8s); X[0] = x0; X[1] = cmul(x0, z1, z1s); f32x2 a = cmul(x0, z256, zs); \
            const f32x2 z24 = cmul(z16, z8, z8s), z32 = cmul(z16, z16, rot(z16)), z32s = rot(z32); X[2] = cmul(X[1], z1, z1s); f32x2 y = z32; \
            f32x2 c4 = cmul(X[2], z256, zs); \
            unsigned c[2][4], d0 = pk_f16(X[0]), d1 = pk_f16(X[1]), d2 = pk_f16(z8), d3 = pk_f16(z16), d4 = pk_f16(z24), one = 0x3c00u; \
            c[0][0] = pk_f16(y); c[0][1] = pk_f16(X[2]); c[0][2] = pk_f16(a); c[0][3] = pk_f16(c4); \
            TW(0) = d0; TW(1) = d1; TW(30) = d2; TW(31) = d3; TW(32) = d4; TW(21) = one; TW(29) = one; SBAR(); \
            _Pragma("unroll") for (int i = 1; i < 9; ++i) { \
                f32x2 t0, t4, t5, t6; const int j = i - 1; unsigned* pc = c[j & 1]; unsigned* nc = c[i & 1]; \
                if (i < 8) { if (i < 7) t0 = y.xx * z32; if (i < 6) t4 = X[i + 1].xx * z1; t5 = a.xx * z256; \
                    if (i == 3) t6 = X[4].xx * z256; if (i == 4 || i == 5) t6 = c4.xx * z256; if (i == 6) t6 = X[6].xx * z256; } SBAR(); \
                if (j < 7) TW(22 + j) = pc[0]; if (j < 6) TW(j + 2) = pc[1]; TW(j == 0 ? 8 : (j == 1 ? 12 : (j == 2 ? 14 : 13 + j))) = pc[2]; \
                if (j == 0) TW(9) = pc[3]; if (j == 3) TW(10) = pc[3]; if (j == 4) TW(13) = pc[3]; if (j == 5) TW(15) = pc[3]; if (j == 6) TW(11) = pc[3]; SBAR(); \
                if (i < 8) { if (i < 7) y = pfma(y.yy, z32s, t0); if (i < 6) X[i + 2] = pfma(X[i + 1].yy, z1s, t4); a = pfma(a.yy, zs, t5); \
                    if (i == 3) c4 = pfma(X[4].yy, zs, t6); if (i == 4 || i == 5) c4 = pfma(c4.yy, zs, t6); if (i == 6) c4 = pfma(X[6].yy, zs, t6); SBAR(); \
                    if (i < 7) nc[0] = pk_f16(y); if (i < 6) nc[1] = pk_f16(X[i + 2]); nc[2] = pk_f16(a); if (i >= 3 && i <= 6) nc[3] = pk_f16(c4); \
                    asm volatile("" :: "v"(pc[0]), "v"(pc[1]), "v"(pc[2]), "v"(pc[3])); SBAR(); } \
            } } while (0)
        f16x8 fa[8]; u32x4 fp[8], fq[8];
        LAS const char* trp = tab + (21 + (r32 >> 2)) * TROW + hi * 16;
        LAS const char* trq = tab + (29 + (r32 & 3)) * TROW + hi * 16;
#define RDT() do { _Pragma("unroll") for (int kk = 0; kk < 8; ++kk) { fp[kk] = *(LAS const u32x4*)(trp + kk * 32); fq[kk] = *(LAS const u32x4*)(trq + kk * 32); fa[kk] = *(LAS const f16x8*)(trd + kk * 32); } SBAR(); } while (0)
#define MMT() do { _Pragma("unroll") for (int kk = 0; kk < 8; ++kk) dacc = MFMA_F16(fa[kk], cmul4h(fp[kk], fq[kk]), dacc); } while (0)
        ANCH(bf_lo(pl), bf_lo(pa), bf_lo(pr), bf_lo(pi));
        await(sy + SY_WO, 4);
        set_tag(sy, tag);
        {
            u32x4 o0, o1; transpose_tile(lds + LT1, tab, w, lane, o0, o1);
            const unsigned to = WS_UT + (unsigned)((64 * xcd + 16 * w + (lane >> 3)) * S + 64 * bi) * 2 + (lane & 7) * 16;
            __builtin_amdgcn_s_setprio(3); BST(o0, to); BST(o1, to + 8 * S * 2); __builtin_amdgcn_s_setprio(0);
        }
        CHAIN(); RDT();
        drain(); __builtin_amdgcn_s_setprio(3); put_flag(ws, FL_UT + (unsigned)(xcd * 128 + w * 32 + bi) * FS, tag); __builtin_amdgcn_s_setprio(0);
        ANCH(bf_hi(pl), bf_hi(pa), bf_hi(pr), bf_hi(pi));
        MMT(); SBAR();
        CHAIN(); RDT();
        const unsigned utf = hi ? FL_WO + (unsigned)(w * 32 + r32) * FS : FL_UT + (unsigned)(xcd * 128 + (bi >> 3) * 32 + r32) * FS;
        __builtin_amdgcn_s_setprio(3);
        const u64 utv = __hip_atomic_load((GAS u64*)(ws + utf), __ATOMIC_RELAXED, __HIP_MEMORY_SCOPE_AGENT); SBAR();
        __builtin_amdgcn_s_setprio(1);
        MMT();
        {
            LAS float* pt = (LAS float*)tab;
            *(LAS f32x4*)(pt + 8 * r32 + 4 * hi) = (f32x4){dacc[0] * oscale, dacc[1] * oscale, dacc[2] * oscale, dacc[3] * oscale};
            *(LAS f32x2*)(pt + (hi ? 384 + 2 * r32 : 256 + 4 * r32)) = (f32x2){dacc[4] * oscale, dacc[5] * oscale};
            *(LAS f32x2*)(pt + (hi ? 448 + 2 * r32 : 258 + 4 * r32)) = (f32x2){dacc[6] * oscale, dacc[7] * oscale};
            pt[hi ? 640 + r32 : 512 + r32] = dacc[8] * oscale;
            pt[hi ? 672 + r32 : 544 + r32] = dacc[9] * oscale;
            pt[hi ? 672 + r32 : 576 + r32] = dacc[10] * oscale;
            pt[hi ? 672 + r32 : 608 + r32] = dacc[11] * oscale;
        }
        LAS unsigned* ps_ = sy + SY_P + 5 * cc;
        arrive(ps_ + 0);
        LAS char* kt = cb + KOF;
        await(ps_ + 0, 2);
#pragma unroll
        for (int ps = 0; ps < 2; ++ps) {
            const int v = 81 * hh + 64 * ps + lane;
            if (ps == 0 || lane < 17) {
                const f32x4 p0 = *(LAS const f32x4*)(cb + v * 16), p1 = *(LAS const f32x4*)(cb + TABW + v * 16);
                u32x2 r = {pk_bf16(p0[0] + p1[0], p0[1] + p1[1]), pk_bf16(p0[2] + p1[2], p0[3] + p1[3])};
                if (v == 161) r = (u32x2){0u, 0u};
                *(LAS u32x2*)(kt + v * 8) = r;
            }
        }
        arrive(ps_ + 1);
        { const u64 ms = __ballot(utv != tag); if (ms) wait_flags<8>(ws, utf, tag, ms, ms); }
        CFENCE();
        const unsigned ub = WS_UT + (unsigned)(ch * S + 1024 * hh) * 2 + voff;
        const u32x4 u0 = BLD(ub), u1 = BLD(ub + 1024); SBAR();
        await(ps_ + 1, 2);
        __builtin_amdgcn_s_setprio(1);
        {
            unsigned W[2][6];
#pragma unroll
            for (int ps = 0; ps < 2; ++ps)
#pragma unroll
                for (int i = 0; i < 6; ++i) { const unsigned gv = gi[ps][i >> 1][i & 1];
                    W[ps][i] = *(LAS const uint16_t*)(kt + (gv & 0xffffu));
                    if (i < 5) W[ps][i] |= (unsigned)*(LAS const uint16_t*)(kt + (gv >> 16)) << 16; }
            if (lane < 8) *(LAS u32x4*)(cb + (2 * hh + (lane >> 2)) * CPY + (256 + (lane & 3)) * 16) = z4;
#pragma unroll
            for (int ps = 0; ps < 2; ++ps) {
                unsigned V[5];
#pragma unroll
                for (int i = 0; i < 5; ++i) V[i] = __builtin_amdgcn_alignbit(W[ps][i + 1], W[ps][i], 16);
                LAS char* cp = cb + (128 * hh + 64 * ps + lane) * 16;
                *(LAS u32x4*)(cp) = (u32x4){W[ps][0], W[ps][1], W[ps][2], W[ps][3]};
                *(LAS u32x4*)(cp + CPY) = (u32x4){V[0], V[1], V[2], V[3]};
                *(LAS u32x4*)(cp + 2 * CPY) = (u32x4){W[ps][1], W[ps][2], W[ps][3], W[ps][4]};
                *(LAS u32x4*)(cp + 3 * CPY) = (u32x4){V[1], V[2], V[3], V[4]};
            }
        }
        *(LAS u32x4*)(ui + (32 + 32 * hh + (lane >> 2)) * 80 + (lane & 3) * 16) = u0;
        *(LAS u32x4*)(ui + (48 + 32 * hh + (lane >> 2)) * 80 + (lane & 3) * 16) = u1;
        arrive(ps_ + 2); await(ps_ + 2, 2);
        __builtin_amdgcn_s_setprio(0);
        f32x16 a0 = {}, a1 = {};
        {
            const int sb = 7 - (r32 & 7);
            LAS const char* pa = cb + (sb & 3) * CPY + (255 - (r32 >> 3) + hi) * 16 + (sb >> 2) * 8 - 64 * (16 * hh + 15) - 2048;
            LAS const char* pa2 = pa + 8; asm volatile("" : "+v"(pa2));
            LAS const char* pb = ui + (32 + r32 - 16 * hh - 15) * 80 + 16 * hi;
            s16x4 ta[2][2], tb[2][2]; bf16x8 b0[2], b1[2];
#define T4(q, off) (*(LAS const s16x4*)((q) + (off)))
#define CSTEP(i, j, RD) do { const int o_ = 64 * (15 - ((j) >> 1)) + 32 * ((j) & 1), u_ = 80 * (15 - ((j) >> 1)) + 32 * ((j) & 1); \
                a1 = MFMA_BF(cat8(ta[i][0], ta[i][1]), b1[i], a1); SBAR(); if (RD) { ta[1 - i][0] = T4(pa, o_ + 2048); ta[1 - i][1] = T4(pa2, o_ + 2048); } SBAR(); \
                a0 = MFMA_BF(cat8(ta[i][0], ta[i][1]), b0[i], a0); SBAR(); if (RD) { b1[1 - i] = FR(pb, u_ + 2560); b0[1 - i] = FR(pb, u_); } SBAR(); \
                a1 = MFMA_BF(cat8(tb[i][0], tb[i][1]), b0[i], a1); SBAR(); if (RD) { tb[1 - i][0] = T4(pa, o_); tb[1 - i][1] = T4(pa2, o_); } SBAR(); } while (0)
            ta[0][0] = T4(pa, 960 + 2048); ta[0][1] = T4(pa2, 960 + 2048); b1[0] = FR(pb, 1200 + 2560); b0[0] = FR(pb, 1200); tb[0][0] = T4(pa, 960); tb[0][1] = T4(pa2, 960);
#pragma unroll
            for (int j = 0; j < 32; ++j) {
                if (j == 31) arrive(ps_ + 3);
                CSTEP(j & 1, j + 1, j < 31);
                LOADWF(j);
                SBAR();
            }
        }
        __builtin_amdgcn_s_setprio(1);
        {
            LAS char* ex = cb + hh * 9216 + r32 * 144 + hi * 16;
            await(ps_ + 3, 2);
#pragma unroll
            for (int g = 0; g < 4; ++g) {
                *(LAS f32x4*)(ex + g * 32) = (f32x4){a0[4 * g], a0[4 * g + 1], a0[4 * g + 2], a0[4 * g + 3]};
                *(LAS f32x4*)(ex + 4608 + g * 32) = (f32x4){a1[4 * g], a1[4 * g + 1], a1[4 * g + 2], a1[4 * g + 3]};
            }
            arrive(ps_ + 4);
            const float dsk = cc ? bf_hi(dw) : bf_lo(dw);
            u32x4 sk[2];
#pragma unroll
            for (int e = 0; e < 4; ++e) { sk[0][e] = pk_bf16(bf_lo(u0[e]) * dsk, bf_hi(u0[e]) * dsk); sk[1][e] = pk_bf16(bf_lo(u1[e]) * dsk, bf_hi(u1[e]) * dsk); }
            PIN(sk[0]); PIN(sk[1]);
            await(ps_ + 4, 2);
            LAS const char* er = cb + (32 * hh + (lane >> 2)) * 144 + (lane & 3) * 32;
            const unsigned bo = WS_BT + (unsigned)(ch * S + 1024 * hh) * 2 + voff;
#pragma unroll
            for (int i = 0; i < 2; ++i) {
                const f32x4 y0 = *(LAS const f32x4*)(er + i * 2304) + *(LAS const f32x4*)(er + i * 2304 + 9216);
                const f32x4 y1 = *(LAS const f32x4*)(er + i * 2304 + 16) + *(LAS const f32x4*)(er + i * 2304 + 9216 + 16);
                const u32x4 yb = {pk_bf16(y0[0], y0[1]), pk_bf16(y0[2], y0[3]), pk_bf16(y1[0], y1[1]), pk_bf16(y1[2], y1[3])};
                u32x4 r;
#pragma unroll
                for (int e = 0; e < 4; ++e) r[e] = pk_bf16(bf_lo(yb[e]) + bf_lo(sk[i][e]), bf_hi(yb[e]) + bf_hi(sk[i][e]));
                BST(r, bo + i * 1024);
            }
        }
        arrive(sy + SY_BG);
        drain();
        put_flag(ws, FL_BT + (unsigned)(xcd * 128 + bi * 4 + w) * FS, tag);
    } else {
        {
            const unsigned nb = (bi & 1) ? 12 * 2 * D : 16 * 2 * D;
            unsigned v4[4];
#pragma unroll
            for (int i = 0; i < 4; ++i) { const unsigned o = (unsigned)(w * 4096 + i * 1024) + voff; v4[i] = (o < nb ? o : 0u) + 4096 - i * 1024; }
            glds4((const char*)(wo + (DH * (bi >> 1) + 16 * (bi & 1)) * D) - 4096, v4[0], v4[1], v4[2], v4[3], lds0 + LB + w * 4096);
        }
        const u64 e0 = __hip_atomic_load((GAS u64*)ws, __ATOMIC_RELAXED, __HIP_MEMORY_SCOPE_AGENT);
        CODE_PF();
        drain(); asm volatile("" :: "v"(pf));
        tag = mix(e0);
        arrive(sy + SY_WO); await(sy + SY_WO, 4);
        set_tag(sy, tag);
        LAS const char* p = lds + LB + (4 * hi + ((lane & 15) >> 2)) * (2 * D) + ((lane >> 4) & 1) * 32 + (lane & 3) * 8;
#pragma unroll
        for (int i = 0; i < 4; ++i) {
            const int ct = w + 4 * i;
            if (ct < 14) {
                u32x4 o = __builtin_bit_cast(u32x4, cat8(trread(p + ct * 64), trread(p + 8 * 2 * D + ct * 64)));
                if (hi && (bi & 1)) { o[2] = 0; o[3] = 0; }
                BST(o, WS_WO + (unsigned)(bi * 14 + ct) * 1024 + voff);
            }
        }
        drain();
        put_flag(ws, FL_WO + (unsigned)(w * 32 + bi) * FS, tag);
        __builtin_amdgcn_s_setprio(0);
        arrive(sy + SY_BG);
        await_slow(sy + SY_G, 4);
        wait_flags<8>(ws, FL_WO + (unsigned)(w * 32 + r32) * FS, tag, 0xffffffffull, 0xffffffffull);
        if (ct < 14) {
#pragma unroll
        for (int i = 0; i < 32; ++i) { LOADWF(i); __builtin_amdgcn_s_sleep(2); SBAR(); } }
    }
    const __amdgpu_buffer_rsrc_t ro = __builtin_amdgcn_make_buffer_rsrc(out, 0, S * 2 * D * 2, 0x00020000);
    await(sy + SY_BG, 4);
    await_slow(sy + SY_X, 4);
    wait_flags<2>(ws, FL_O + (unsigned)(qt * 16 + 4 * w + (lane & 3)) * FS, tag, 15, 15);
    u32x4 of[8];
#pragma unroll
    for (int i = 0; i < 8; ++i) of[i] = BLD(WS_O + (unsigned)(qt * 32 + 8 * w + i) * 1024 + voff);
#pragma unroll
    for (int i = 0; i < 8; ++i) *(LAS u32x4*)(lds + LB + (8 * w + i) * 1024 + voff) = of[i];
    arrive(sy + SY_B2);
    f32x16 acc = {};
    if (ct < 14) {
#pragma unroll
        for (int i = 0; i < 8; ++i) acc = MFMA_BF(wf[i], BC8(of[i]), acc);
    }
    SBAR();
    await(sy + SY_B2, 4);
    if (ct < 14) {
        LAS const char* po[3];
#pragma unroll
        for (int j = 0; j < 3; ++j) po[j] = lds + LB + ((w + 1 + j) & 3) * 8192 + voff;
        bf16x8 og[4];
#pragma unroll
        for (int i = 0; i < 3; ++i) og[i] = FR(po[0], i * 1024);
#pragma unroll
        for (int i = 0; i < 24; ++i) { if (i < 21) og[(i + 3) & 3] = FR(po[(i + 3) >> 3], ((i + 3) & 7) * 1024); SBAR(); acc = MFMA_BF(wf[8 + i], og[i & 3], acc); SBAR(); }
        LAS char* sc = lds + LB + 40960 + w * 2304;
#pragma unroll
        for (int g = 0; g < 4; ++g) *(LAS u32x2*)(sc + r32 * 72 + 16 * g + 8 * hi) = (u32x2){pk_bf16(acc[4 * g], acc[4 * g + 1]), pk_bf16(acc[4 * g + 2], acc[4 * g + 3])};
        LAS const char* sr = sc + (lane >> 2) * 72 + (lane & 3) * 16;
        const u32x2 a0 = *(LAS const u32x2*)sr, a1 = *(LAS const u32x2*)(sr + 8), b0 = *(LAS const u32x2*)(sr + 16 * 72), b1 = *(LAS const u32x2*)(sr + 16 * 72 + 8);
        const unsigned oo = (unsigned)((32 * qt + (lane >> 2)) * 2 * D + 32 * ct) * 2 + (lane & 3) * 16;
        __builtin_amdgcn_raw_buffer_store_b128((u32x4){a0[0], a0[1], a1[0], a1[1]}, ro, oo, 0, 16);
        __builtin_amdgcn_raw_buffer_store_b128((u32x4){b0[0], b0[1], b1[0], b1[1]}, ro, oo + 16 * 2 * D * 2, 0, 16);
    }
    if (b == 0 && w == 0) put_flag(ws, 0, tag);
}

__global__ __launch_bounds__(512, 2) void fused(const uint16_t* __restrict__ x, const uint16_t* __restrict__ wqkv, const uint16_t* __restrict__ wo,
                                               const uint16_t* __restrict__ u, const uint16_t* __restrict__ lar, const uint16_t* __restrict__ aim,
                                               const uint16_t* __restrict__ cre, const uint16_t* __restrict__ cim, const uint16_t* __restrict__ ldt,
                                               const uint16_t* __restrict__ dskip, uint16_t* __restrict__ out, char* __restrict__ ws) {
    extern __shared__ __attribute__((aligned(16))) char smem[];
    LAS char* lds = (LAS char*)smem;
    const int lane = threadIdx.x & 63, w = __builtin_amdgcn_readfirstlane(threadIdx.x >> 6);
    const u64 code0 = __builtin_amdgcn_s_getpc() & ~255ull;
    const u64 t_start = __builtin_amdgcn_s_memrealtime();
    if (w >= 4) __builtin_amdgcn_s_setprio(3);
    if (w == 4) ((LAS unsigned*)(lds + LSY))[lane] = 0;
    asm volatile("" :: "s"(x), "s"(wqkv), "s"(wo), "s"(u), "s"(lar), "s"(aim), "s"(cre), "s"(cim), "s"(ldt), "s"(dskip), "s"(out), "s"(ws));
    asm volatile("s_waitcnt lgkmcnt(0)\n\ts_barrier" ::: "memory");
    if (w < 4) role_a(x, wqkv, out, ws, lds, w, lane);
    else role_b(wo, u, lar, aim, cre, cim, ldt, dskip, out, ws, lds, w - 4, lane, code0);
    while (__builtin_amdgcn_s_memrealtime() - t_start < 2400) __builtin_amdgcn_s_sleep(2);
    if (w == 7) asm volatile("buffer_wbl2 sc0 sc1\n\ts_waitcnt vmcnt(0)\n\tbuffer_inv sc0 sc1\n\ts_waitcnt vmcnt(0)" ::: "memory");
    while (__builtin_amdgcn_s_memrealtime() - t_start < 3200) __builtin_amdgcn_s_sleep(2);
}
}

extern "C" void kernel_launch(void* const* d_in, const int* in_sizes, int n_in, void* d_out, int out_size, void* d_ws, size_t ws_size, hipStream_t stream) {
    using namespace k;
    (void)hipFuncSetAttribute((const void*)fused, hipFuncAttributeMaxDynamicSharedMemorySize, LDS_BYTES);
    const uint16_t* const* in = (const uint16_t* const*)d_in;
    fused<<<256, 512, LDS_BYTES, stream>>>(in[0], in[1], in[2], in[3], in[4], in[5], in[6], in[7], in[8], in[9], (uint16_t*)d_out, (char*)d_ws);
}
```

```cpp
#include <hip/hip_runtime.h>
#include <hip/hip_bf16.h>
#include <stdint.h>

typedef short bf16x8 __attribute__((ext_vector_type(8)));
typedef _Float16 f16x8 __attribute__((ext_vector_type(8)));
typedef _Float16 f16x2 __attribute__((ext_vector_type(2)));
typedef __bf16 bf16x2 __attribute__((ext_vector_type(2)));
typedef short s16x4 __attribute__((ext_vector_type(4)));
typedef float f32x16 __attribute__((ext_vector_type(16)));
typedef float f32x4 __attribute__((ext_vector_type(4)));
typedef float f32x2 __attribute__((ext_vector_type(2)));
typedef unsigned u32x4 __attribute__((ext_vector_type(4)));
typedef unsigned u32x2 __attribute__((ext_vector_type(2)));
typedef unsigned long long u64;
#define LAS __attribute__((address_space(3)))
#define GAS __attribute__((address_space(1)))
#define DI __device__ __forceinline__
#define MFMA_BF(a, b, c) __builtin_amdgcn_mfma_f32_32x32x16_bf16((a), (b), (c), 0, 0, 0)
#define MFMA_F16(a, b, c) __builtin_amdgcn_mfma_f32_32x32x16_f16((a), (b), (c), 0, 0, 0)
#define SBAR() __builtin_amdgcn_sched_barrier(0)
#define CFENCE() asm volatile("" ::: "memory")
#define PIN(x) asm volatile("" : "+v"(x))
#define BC8(x) __builtin_bit_cast(bf16x8, (x))
#define EX(x) __builtin_amdgcn_exp2f(x)

namespace k {
constexpr int S = 2048, D = 448, DH = 28, NS = 256;
constexpr unsigned FS = 128;
constexpr unsigned FL_KV = FS, FL_O = FL_KV + 1024 * FS, FL_UT = FL_O + 1024 * FS, FL_BT = FL_UT + 896 * FS, FL_WO = FL_BT + 896 * FS;
constexpr unsigned WS_KV = 512u << 10;
constexpr unsigned WS_O = WS_KV + (4u << 20);
constexpr unsigned WS_WO = WS_O + (2u << 20);
constexpr unsigned WS_UT = WS_WO + (448u << 10);
constexpr unsigned WS_BT = WS_UT + D * S * 2;
constexpr unsigned WS_END = WS_BT + D * S * 2;
constexpr int WSL = 8192, LWQ = 4 * WSL, LXR = LWQ + 16384, XSL = 16384, LB = LXR + 3 * XSL;
constexpr int LQ = LXR, LX = 0;
constexpr int TROW = 272, TABW = 33 * TROW, CPY = 4160, UOF = 2 * TABW, KOF = UOF + 7680, CBW = KOF + 1312;
constexpr int LT1 = LB + 2 * CBW, LSY = LT1 + 8192, LDS_BYTES = LSY + 256;
static_assert(LDS_BYTES <= 163840 && (LSY & 15) == 0 && 4 * CPY <= UOF && LB + 40960 + 4 * 2304 <= LSY, "lds");
enum { SY_F = 2, SY_P = 10, SY_WO = 20, SY_BG = 22, SY_B2 = 23, SY_Q = 24, SY_X = 25, SY_G = 26, SY_T2 = 27 };
constexpr int SPIN = 1 << 18;
constexpr unsigned CODE_LINES = 216;

struct IdxTab { uint16_t v[256][12]; };
constexpr IdxTab make_idx() {
    IdxTab t = {};
    for (int y = 0; y < 256; ++y) for (int e = 0; e < 12; ++e) {
        const int j = 2047 - 8 * y - e; int ix = 644;
        if (j >= 0) { const int sh = j >= 1024 ? 3 : (j >= 512 ? 2 : (j >= 256 ? 1 : 0));
            ix = sh ? 128 * sh + ((j + (1 << (sh - 1)) - 1 + ((j >> sh) & 1)) >> sh) : j; }
        t.v[y][e] = (uint16_t)(2 * ix);
    }
    return t;
}
__device__ const IdxTab g_idx __attribute__((aligned(16))) = make_idx();

DI unsigned pk_bf16(float a, float b) { f32x2 v = {a, b}; return __builtin_bit_cast(unsigned, __builtin_convertvector(v, bf16x2)); }
DI unsigned pk_f16(f32x2 v) { return __builtin_bit_cast(unsigned, __builtin_convertvector(v, f16x2)); }
DI float bf_lo(unsigned w) { return __uint_as_float(w << 16); }
DI float bf_hi(unsigned w) { return __uint_as_float(w & 0xffff0000u); }
DI float rbf(float x) { return bf_lo(pk_bf16(x, x)); }
#define GL0 "s_mov_b32 %0, m0\n\ts_mov_b32 m0, %2\n\ts_nop 0\n\t"
#define GL1(V, O, C) "global_load_lds_dwordx4 " V ", %1 offset:" #O C "\n\t"
#define GLE "s_mov_b32 m0, %0"
DI void glds4(const void* sb, unsigned v0, unsigned v1, unsigned v2, unsigned v3, unsigned lb) { unsigned sv;
    asm volatile(GL0 GL1("%3", 0, "") GL1("%4", 1024, "") GL1("%5", 2048, "") GL1("%6", 3072, "") GLE : "=&s"(sv) : "s"(sb), "s"(lb), "v"(v0), "v"(v1), "v"(v2), "v"(v3) : "memory"); }
DI void glds2(const void* sb, unsigned v0, unsigned v1, unsigned lb) { unsigned sv;
    asm volatile(GL0 GL1("%3", 0, "") GL1("%4", 1024, "") GLE : "=&s"(sv) : "s"(sb), "s"(lb), "v"(v0), "v"(v1) : "memory"); }
DI void glds1(const void* sb, unsigned v0, unsigned lb) { unsigned sv;
    asm volatile(GL0 GL1("%3", 0, "") GLE : "=&s"(sv) : "s"(sb), "s"(lb), "v"(v0) : "memory"); }
DI float wave_max(float v) {
#define DPPMAX(ctrl, rm) v = fmaxf(v, __uint_as_float(__builtin_amdgcn_update_dpp(0, __float_as_uint(v), ctrl, rm, 0xf, true)))
    DPPMAX(0x111, 0xf); DPPMAX(0x112, 0xf); DPPMAX(0x114, 0xf); DPPMAX(0x118, 0xf); DPPMAX(0x142, 0xa); DPPMAX(0x143, 0xc);
    return __uint_as_float(__builtin_amdgcn_readlane(__float_as_uint(v), 63));
}
DI s16x4 trread(LAS const char* p) { return __builtin_bit_cast(s16x4, __builtin_amdgcn_ds_read_tr16_b64_v4i16((LAS s16x4*)p)); }
DI bf16x8 cat8(s16x4 a, s16x4 b) { return __builtin_shufflevector(a, b, 0, 1, 2, 3, 4, 5, 6, 7); }
DI f32x2 pfma(f32x2 a, f32x2 b, f32x2 c) { return __builtin_elementwise_fma(a, b, c); }
DI f32x2 cmul(f32x2 a, f32x2 b, f32x2 bs) { return pfma(a.yy, bs, a.xx * b); }
DI f16x8 cmul4h(u32x4 p, u32x4 q) {
    u32x4 t;
#define PM(i, a, b) "v_pk_mul_f16 %" #i ", %" #a ", %" #b " op_sel:[0,0] op_sel_hi:[0,1]\n\t"
#define PF(i, a, b) "v_pk_fma_f16 %" #i ", %" #a ", %" #b ", %" #i " op_sel:[1,1,0] op_sel_hi:[1,0,1] neg_lo:[1,0,0]\n\t"
    asm(PM(0, 4, 8) PM(1, 5, 9) PM(2, 6, 10) PM(3, 7, 11) PF(0, 4, 8) PF(1, 5, 9) PF(2, 6, 10) PF(3, 7, 11) "s_nop 1"
        : "=&v"(t[0]), "=&v"(t[1]), "=&v"(t[2]), "=&v"(t[3]) : "v"(p[0]), "v"(p[1]), "v"(p[2]), "v"(p[3]), "v"(q[0]), "v"(q[1]), "v"(q[2]), "v"(q[3]));
    return __builtin_bit_cast(f16x8, t);
}
DI f32x2 rot(f32x2 b) { return (f32x2){-b.y, b.x}; }
DI f32x2 anchor(float a2, float rev, float s) {
    const float mag = EX(a2 * s);
    float r = rev * s; r -= __builtin_rintf(r);
    return (f32x2){mag * __builtin_amdgcn_cosf(r), mag * __builtin_amdgcn_sinf(r)};
}
DI void arrive(LAS unsigned* c) { asm volatile("s_mov_b64 exec, 1\n\tds_add_u32 %0, %1\n\ts_mov_b64 exec, -1" :: "v"((unsigned)(uintptr_t)c), "v"(1u) : "memory"); }
DI void await(LAS unsigned* c, unsigned n) {
    unsigned t, i = SPIN;
    asm volatile("1:\n\tds_read_b32 %0, %2\n\ts_waitcnt lgkmcnt(0)\n\tv_cmp_gt_u32 vcc, %3, %0\n\ts_cbranch_vccz 2f\n\ts_sleep 1\n\ts_sub_u32 %1, %1, 1\n\ts_cbranch_scc0 1b\n2:"
                 : "=&v"(t), "+s"(i) : "v"((unsigned)(uintptr_t)c), "s"(n) : "vcc", "scc", "memory");
}
DI void await_slow(LAS unsigned* c, unsigned n) {
    unsigned t, i = SPIN;
    asm volatile("1:\n\tds_read_b32 %0, %2\n\ts_waitcnt lgkmcnt(0)\n\tv_cmp_gt_u32 vcc, %3, %0\n\ts_cbranch_vccz 2f\n\ts_sleep 3\n\ts_sub_u32 %1, %1, 1\n\ts_cbranch_scc0 1b\n2:"
                 : "=&v"(t), "+s"(i) : "v"((unsigned)(uintptr_t)c), "s"(n) : "vcc", "scc", "memory");
}
DI u64 get_tag(LAS unsigned* sy) {
    await(sy, 1);
    const u64 v = __hip_atomic_load((LAS u64*)sy, __ATOMIC_RELAXED, __HIP_MEMORY_SCOPE_WORKGROUP);
    return ((u64)(unsigned)__builtin_amdgcn_readfirstlane((unsigned)(v >> 32)) << 32) | (unsigned)__builtin_amdgcn_readfirstlane((unsigned)v);
}
DI void set_tag(LAS unsigned* sy, u64 tag) { asm volatile("s_mov_b64 exec, 1\n\tds_write_b64 %0, %1\n\ts_mov_b64 exec, -1" :: "v"((unsigned)(uintptr_t)sy), "v"(tag) : "memory"); }
DI void drain() { asm volatile("s_waitcnt vmcnt(0)" ::: "memory"); }
DI void put_flag(char* ws, unsigned off, u64 tag) {
    asm volatile("s_mov_b64 exec, 1\n\tglobal_store_dwordx2 %0, %1, %2 sc1\n\ts_mov_b64 exec, -1" :: "v"(off), "v"(tag), "s"(ws) : "memory");
}
template <int SLP> DI u64 wait_flags(char* ws, unsigned off, u64 tag, u64 pend, u64 need) {
    u64 t, left; unsigned i = SPIN;
    asm volatile("s_mov_b64 exec, %5\n1:\n\tglobal_load_dwordx2 %0, %3, %4 sc1\n\ts_waitcnt vmcnt(0)\n\tv_cmpx_ne_u64 vcc, %6, %0\n\ts_and_b64 %2, exec, %7\n\t"
                 "s_cbranch_scc0 2f\n\ts_sleep %8\n\ts_sub_u32 %1, %1, 1\n\ts_cbranch_scc0 1b\n2:\n\ts_mov_b64 %2, exec\n\ts_mov_b64 exec, -1"
                 : "=&v"(t), "+s"(i), "=&s"(left) : "v"(off), "s"(ws), "s"(pend), "s"(tag), "s"(need), "i"(SLP) : "vcc", "scc", "memory");
    return left;
}
#define BST(v, off) __builtin_amdgcn_raw_buffer_store_b128((v), rs, (off), 0, 16)
#define BLD(off) __builtin_amdgcn_raw_buffer_load_b128(rs, (off), 0, 16)
#define FR(p, off) (*(LAS const bf16x8*)((p) + (off)))
DI float swapmax(float m) { const auto r = __builtin_amdgcn_permlane32_swap(__float_as_uint(m), __float_as_uint(m), false, false); return fmaxf(__uint_as_float(r[0]), __uint_as_float(r[1])); }
DI u32x4 put_ref(u32x4 q1, float m, int hi) {
    const float m1 = rbf(m), m2 = rbf(m - m1), m3 = m - m1 - m2;
    if (hi) { q1[2] = pk_bf16(-m1, -m2); q1[3] = pk_bf16(-m3, 0.f); }
    return q1;
}
DI u64 mix(u64 v) {
    const u64 e = ((u64)(unsigned)__builtin_amdgcn_readfirstlane((unsigned)(v >> 32)) << 32) | (unsigned)__builtin_amdgcn_readfirstlane((unsigned)v);
    u64 z = e + 0x9E3779B97F4A7C15ull; z = (z ^ (z >> 30)) * 0xBF58476D1CE4E5B9ull; z = (z ^ (z >> 27)) * 0x94D049BB133111EBull; return (z ^ (z >> 31)) | 1ull; }
DI void transpose_tile(LAS const char* stage, LAS char* scratch, int w, int lane, u32x4& o0, u32x4& o1) {
    LAS const char* p = stage + (lane >> 4) * 2048 + ((lane & 15) >> 2) * 128 + w * 32 + (lane & 3) * 8;
    const bf16x8 t0 = cat8(trread(p), trread(p + 512)), t1 = cat8(trread(p + 1024), trread(p + 1536));
    LAS char* sc = scratch + (lane & 15) * 144 + (lane >> 4) * 32;
    *(LAS bf16x8*)sc = t0; *(LAS bf16x8*)(sc + 16) = t1;
    LAS const char* sr = scratch + (lane >> 3) * 144 + (lane & 7) * 16;
    o0 = *(LAS const u32x4*)sr; o1 = *(LAS const u32x4*)(sr + 8 * 144);
}

DI void role_a(const uint16_t* __restrict__ x, const uint16_t* __restrict__ wqkv, uint16_t* __restrict__ out, char* __restrict__ ws, LAS char* lds, int w, int lane) {
    const int head = 2 * (blockIdx.x & 7) + ((blockIdx.x >> 3) & 1), grp = blockIdx.x >> 4;
    const unsigned lds0 = (unsigned)(uintptr_t)lds;
    const int r32 = lane & 31, hi = lane >> 5;
    const int wsh = (head & 7) == 7 ? 4 : 0;
    const unsigned wv = (unsigned)((lane >> 2) * 3 * D + (lane & 3) * 8) * 2;
    const char* wb = (const char*)(wqkv + DH * head - wsh + (16 * w) * 3 * D);
    const unsigned xv0 = (unsigned)((lane >> 3) * D + (((lane & 7) ^ (lane >> 4)) * 8)) * 2, xv1 = xv0 ^ 64;
    const char* xb = (const char*)(x + (128 * grp + 32 * w) * D);
    const int rho = ((blockIdx.x >> 3) + ((blockIdx.x >> 1) & 3)) % 7;
#define SP(st) ((st) + rho >= 7 ? (st) + rho - 7 : (st) + rho)
#define WQO(st) ((st) < 4 ? LWQ + (st) * 4096 : ((st) < 6 ? 3 * WSL + ((st) - 4) * 4096 : 0))
#define LOADW(st) glds2(wb + SP(st) * (64 * 3 * D * 2), wv + 2 * D, wv + 4 * D - 1024, lds0 + ((st) & 3) * WSL + w * 2048)
#define LOADQ(st) glds1(wb + SP(st) * (64 * 3 * D * 2), wv, lds0 + WQO(st) + w * 1024)
#define LOADX(st) glds4(xb + SP(st) * 128, xv0, xv1 + 16 * D - 1024, xv0 + 32 * D - 2048, xv1 + 48 * D - 3072, lds0 + LXR + ((st) % 3) * XSL + w * 4096)
    LOADX(0); LOADW(0);
    LOADX(1); LOADW(1);
    LAS unsigned* sy = (LAS unsigned*)(lds + LSY);
    const __amdgpu_buffer_rsrc_t rs = __builtin_amdgcn_make_buffer_rsrc(ws, 0, WS_END, 0x00020000);
    const int xs = (r32 >> 1) & 7;
    LAS const char* xp[4];
#pragma unroll
    for (int ks = 0; ks < 4; ++ks) xp[ks] = lds + LXR + w * 4096 + r32 * 128 + (((2 * ks + hi) ^ xs) * 16);
    LAS const char* wp = lds + (8 * hi + ((lane & 15) >> 2)) * 64 + ((lane >> 4) & 1) * 32 + (lane & 3) * 8 + wsh * 2;
    f32x16 aq = {}, ak = {}, av = {};
    bf16x8 xf[7][4]; s16x4 fr[4][4];
#define RX(st, ks) xf[st][ks] = FR(xp[ks], ((st) % 3) * XSL)
#define RW(st, ks, j) fr[ks][j] = trread(wp + ((st) & 3) * WSL + (ks) * 2048 + ((j) >> 1) * 1024 + ((j) & 1) * 256)
#define MK(st, ks) ak = MFMA_BF(cat8(fr[ks][0], fr[ks][1]), xf[st][ks], ak); SBAR()
#define MV(st, ks) av = MFMA_BF(xf[st][ks], cat8(fr[ks][2], fr[ks][3]), av); SBAR()
#define QSTEP(st, ks) MK(st, ks); RW(st, ks + 1, 0); RW(st, ks + 1, 1); SBAR(); MV(st, ks); RW(st, ks + 1, 2); RW(st, ks + 1, 3); SBAR()
    u64 tag = 0;
    asm volatile("s_waitcnt vmcnt(6)" ::: "memory");
    arrive(sy + SY_F); LOADW(2); LOADX(2); await(sy + SY_F, 4);
    RX(0, 0); RW(0, 0, 0); RW(0, 0, 1); RW(0, 0, 2); RW(0, 0, 3); RX(0, 1); RX(0, 2); RX(0, 3); SBAR();
#pragma unroll
    for (int st = 0; st < 7; ++st) {
        QSTEP(st, 0); if (st < 4) LOADW(st + 3); if (st == 4) { LOADQ(4); LOADQ(5); } if (st == 5) LOADQ(6);
        QSTEP(st, 1); if (st < 4) LOADX(st + 3);
        QSTEP(st, 2); if (st == 3) { LOADQ(0); LOADQ(1); LOADQ(2); LOADQ(3); }
        if (st < 6) {
            if (st == 3) asm volatile("s_waitcnt vmcnt(16)" ::: "memory"); else if (st == 5) asm volatile("s_waitcnt vmcnt(7)" ::: "memory"); else asm volatile("s_waitcnt vmcnt(12)" ::: "memory");
            arrive(sy + SY_F + 1 + st); SBAR();
        }
        MK(st, 3);
        if (st < 6) { await(sy + SY_F + 1 + st, 4); RX(st + 1, 0); RW(st + 1, 0, 0); RW(st + 1, 0, 1); RW(st + 1, 0, 2); RW(st + 1, 0, 3); } SBAR();
        MV(st, 3); if (st < 6) { RX(st + 1, 1); RX(st + 1, 2); RX(st + 1, 3); } SBAR();
        if (st == 2) tag = get_tag(sy);
    }
    const float cq = 0.18898223650461362f * 1.4426950408889634f;
    const unsigned voff = lane * 16;
    u32x4 q0, q1, k0, k1, v0, v1;
    const unsigned tb = WS_KV + (unsigned)(head * 64 + 4 * grp + w) * 4096 + voff;
#pragma unroll
    for (int i = 0; i < 4; ++i) { k0[i] = pk_bf16(ak[2 * i], ak[2 * i + 1]); k1[i] = pk_bf16(ak[8 + 2 * i], ak[9 + 2 * i]); }
    if (hi) { k1[2] = 0x3f803f80u; k1[3] = 0x00003f80u; }
    BST(k0, tb); BST(k1, tb + 1024);
#pragma unroll
    for (int i = 0; i < 4; ++i) {
        v0[i] = pk_bf16(av[2 * i], av[2 * i + 1]); v1[i] = pk_bf16(av[8 + 2 * i], av[9 + 2 * i]);
        if (r32 >= DH) { v0[i] = v1[i] = (r32 == DH ? 0x3f803f80u : 0u); }
    }
    BST(v0, tb + 2048); BST(v1, tb + 3072);
    asm volatile("s_waitcnt vmcnt(4)" ::: "memory");
    arrive(sy + SY_F + 7); await(sy + SY_F + 7, 4);
    {
        s16x4 fq[4][2];
#define RQ(j) do { fq[(j) & 3][0] = trread(wp + WQO((j) >> 2) + ((j) & 3) * 1024); fq[(j) & 3][1] = trread(wp + WQO((j) >> 2) + ((j) & 3) * 1024 + 256); } while (0)
        RQ(0); RQ(1); RQ(2); SBAR();
#pragma unroll
        for (int j = 0; j < 28; ++j) {
            aq = MFMA_BF(cat8(fq[j & 3][0], fq[j & 3][1]), xf[j >> 2][j & 3], aq); SBAR(); if (j < 25) RQ(j + 3); SBAR();
            if (j == 13) { drain(); put_flag(ws, FL_KV + (unsigned)((head * 4 + w) * 16 + grp) * FS, tag); }
        }
    }
#pragma unroll
    for (int i = 0; i < 4; ++i) { q0[i] = pk_bf16(aq[2 * i] * cq, aq[2 * i + 1] * cq); q1[i] = pk_bf16(aq[8 + 2 * i] * cq, aq[9 + 2 * i] * cq); }
    if (hi) { q1[2] = 0; q1[3] = 0; }
    const unsigned kvf = FL_KV + (unsigned)((head * 4 + w) * 16 + ((grp + lane) & 15)) * FS;
    const f32x16 zero = {};
    {
        f32x16 s = MFMA_BF(BC8(k0), BC8(q0), zero); s = MFMA_BF(BC8(k1), BC8(q1), s);
        float m = s[0];
#pragma unroll
        for (int i = 1; i < 16; ++i) m = fmaxf(m, s[i]);
        q1 = put_ref(q1, swapmax(m), hi);
    }
    { LAS u32x4* qo = (LAS u32x4*)(lds + LQ + w * 2048) + lane; qo[0] = q0; qo[64] = q1; }
    arrive(sy + SY_Q);
    const u64 kv1 = __hip_atomic_load((GAS u64*)(ws + kvf), __ATOMIC_RELAXED, __HIP_MEMORY_SCOPE_AGENT); SBAR();
    f32x16 O[4] = {}; PIN(O[0]); PIN(O[1]); PIN(O[2]); PIN(O[3]);
    await(sy + SY_Q, 4);
    bf16x8 Q[4][2];
    Q[0][0] = BC8(q0); Q[0][1] = BC8(q1);
#pragma unroll
    for (int i = 1; i < 4; ++i) { LAS const char* p = lds + LQ + ((w + i) & 3) * 2048 + voff; Q[i][0] = FR(p, 0); Q[i][1] = FR(p, 1024); }
    u32x4 kv[4][4] = {{k0, k1, v0, v1}, {}, {}, {}};
    f32x16 sa, sb;
    u32x4 p0, p1 = {};
#define GRP(SC, i, P, j) do { float e0 = EX(SC[i]); SBAR(); float e1 = EX(SC[i + 1]); SBAR(); float e2 = EX(SC[i + 2]); SBAR(); float e3 = EX(SC[i + 3]); SBAR(); \
        P[j] = pk_bf16(e0, e1); SBAR(); P[j + 1] = pk_bf16(e2, e3); PIN(P); SBAR(); } while (0)
#define SLOT(SC, SN, cb, i, QK, PV1, LD) do { constexpr int nb_ = (i) < 3 ? (cb) : ((cb) + 1) % 4, pb_ = (i) > 0 ? (cb) : ((cb) + 3) % 4; \
        if (QK) SN = MFMA_BF(BC8(kv[nb_][0]), Q[((i) + 1) & 3][0], zero); SBAR(); GRP(SC, 0, p0, 0); \
        if (PV1) O[((i) + 3) & 3] = MFMA_BF(BC8(kv[pb_][3]), BC8(p1), O[((i) + 3) & 3]); SBAR(); \
        if (LD) { kv[((cb) + 3) % 4][i] = __builtin_amdgcn_raw_buffer_load_b128(rs, voff + (i) * 1024, kvo, 16); SBAR(); } GRP(SC, 4, p0, 2); \
        if (QK) SN = MFMA_BF(BC8(kv[nb_][1]), Q[((i) + 1) & 3][1], SN); SBAR(); GRP(SC, 8, p1, 0); \
        O[i] = MFMA_BF(BC8(kv[cb][2]), BC8(p0), O[i]); SBAR(); GRP(SC, 12, p1, 2); } while (0)
    const unsigned kvb = WS_KV + (unsigned)(head * 64 + w) * 4096;
    unsigned kvo = 0;
    u64 miss = __ballot(kv1 != tag) & 0xfffe;
    if (miss & 6) miss = wait_flags<2>(ws, kvf, tag, miss, 6);
    CFENCE();
#pragma unroll
    for (int j = 1; j < 3; ++j)
#pragma unroll
        for (int f = 0; f < 4; ++f) kv[j][f] = __builtin_amdgcn_raw_buffer_load_b128(rs, voff + f * 1024, kvb + ((grp + j) & 15) * 16384, 16);
    arrive(sy + SY_G);
    sa = MFMA_BF(BC8(k0), Q[0][0], zero); sa = MFMA_BF(BC8(k1), Q[0][1], sa);
    int nx = grp + 3;
#define TILE(cb) do { kvo = kvb + (nx & 15) * 16384; ++nx; SBAR(); \
        SLOT(sa, sb, cb, 0, true, true, true); SLOT(sb, sa, cb, 1, true, true, true); SLOT(sa, sb, cb, 2, true, true, true); SLOT(sb, sa, cb, 3, true, true, true); } while (0)
#pragma unroll 1
    for (unsigned msk = 0x78; msk < 0x80000; msk <<= 4) { if (__builtin_expect((miss & msk) != 0, 0)) miss = wait_flags<2>(ws, kvf, tag, miss, msk); TILE(0); TILE(1); TILE(2); TILE(3); }
    O[3] = MFMA_BF(BC8(kv[3][3]), BC8(p1), O[3]);
    f32x16 o = O[0];
    {
#pragma unroll
        for (int i = 1; i < 4; ++i) {
            LAS char* ex = lds + LX + (((w + i) & 3) * 3 + i - 1) * 4096 + voff;
#pragma unroll
            for (int g = 0; g < 4; ++g) *(LAS f32x4*)(ex + g * 1024) = (f32x4){O[i][4 * g], O[i][4 * g + 1], O[i][4 * g + 2], O[i][4 * g + 3]};
        }
        arrive(sy + SY_X); await(sy + SY_X, 4);
        LAS const char* er = lds + LX + w * 3 * 4096 + voff;
#pragma unroll
        for (int i = 0; i < 3; ++i)
#pragma unroll
            for (int g = 0; g < 4; ++g) { const f32x4 r = *(LAS const f32x4*)(er + i * 4096 + g * 1024);
#pragma unroll
                for (int e = 0; e < 4; ++e) o[4 * g + e] += r[e]; }
        f32x2 chk = {0.f, 0.f};
#pragma unroll
        for (int i = 0; i < 8; ++i) chk = pfma((f32x2){o[2 * i], o[2 * i + 1]}, (f32x2){0.f, 0.f}, chk);
        if (__builtin_expect(__any(!(chk[0] + chk[1] == 0.f) || !(o[12] < 1e30f)), 0)) {
            const unsigned hb = WS_KV + (unsigned)head * 64 * 4096 + voff;
            u32x4 qr = __builtin_bit_cast(u32x4, Q[0][1]); if (hi) { qr[2] = 0; qr[3] = 0; }
            float mm = -INFINITY;
            wait_flags<4>(ws, FL_KV + (unsigned)(head * 64 + lane) * FS, tag, ~0ull, ~0ull);
#pragma unroll 1
            for (int t = 0; t < 64; ++t) {
                f32x16 s = MFMA_BF(BC8(BLD(hb + t * 4096)), Q[0][0], zero); s = MFMA_BF(BC8(BLD(hb + t * 4096 + 1024)), BC8(qr), s);
#pragma unroll
                for (int i = 0; i < 16; ++i) mm = fmaxf(mm, s[i]);
            }
            qr = put_ref(qr, swapmax(mm), hi);
            o = zero;
#pragma unroll 1
            for (int t = 0; t < 64; ++t) {
                f32x16 s = MFMA_BF(BC8(BLD(hb + t * 4096)), Q[0][0], zero); s = MFMA_BF(BC8(BLD(hb + t * 4096 + 1024)), BC8(qr), s);
#pragma unroll
                for (int i = 0; i < 4; ++i) { p0[i] = pk_bf16(EX(s[2 * i]), EX(s[2 * i + 1])); p1[i] = pk_bf16(EX(s[8 + 2 * i]), EX(s[9 + 2 * i])); }
                o = MFMA_BF(BC8(BLD(hb + t * 4096 + 2048)), BC8(p0), o); o = MFMA_BF(BC8(BLD(hb + t * 4096 + 3072)), BC8(p1), o);
            }
        }
    }
    {
        const auto r = __builtin_amdgcn_permlane32_swap(__float_as_uint(o[12]), __float_as_uint(o[12]), false, false);
        const float rl = __builtin_amdgcn_rcpf(__uint_as_float(r[1]));
        u32x4 o0, o1;
#pragma unroll
        for (int i = 0; i < 4; ++i) { o0[i] = pk_bf16(o[2 * i] * rl, o[2 * i + 1] * rl); o1[i] = pk_bf16(o[8 + 2 * i] * rl, o[9 + 2 * i] * rl); }
        if (hi) { o1[2] = 0; o1[3] = 0; }
        const unsigned fi = (unsigned)((4 * grp + w) * 16 + head);
        BST(o0, WS_O + fi * 2048 + voff); BST(o1, WS_O + fi * 2048 + 1024 + voff);
        drain();
        put_flag(ws, FL_O + fi * FS, tag);
    }
    const int xcd = blockIdx.x & 7, bi = blockIdx.x >> 3;
    if (xcd < 7) {
        wait_flags<2>(ws, FL_BT + (unsigned)(xcd * 128 + w * 32 + r32) * FS, tag, 0xffffffffull, 0xffffffffull);
        const unsigned bo = WS_BT + (unsigned)((64 * xcd + 16 * w + (lane >> 3)) * S + 64 * bi + (lane & 7) * 8) * 2;
        const u32x4 b0 = BLD(bo), b1 = BLD(bo + 8 * S * 2);
        *(LAS u32x4*)(lds + 65536 + w * 2048 + voff) = b0; *(LAS u32x4*)(lds + 65536 + w * 2048 + 1024 + voff) = b1;
        arrive(sy + SY_T2); await(sy + SY_T2, 4);
        u32x4 o0, o1; transpose_tile(lds + 65536, lds + 73728 + w * 2304, w, lane, o0, o1);
        const __amdgpu_buffer_rsrc_t ro = __builtin_amdgcn_make_buffer_rsrc(out, 0, S * 2 * D * 2, 0x00020000);
        const unsigned oo = (unsigned)((64 * bi + 16 * w + (lane >> 3)) * 2 * D + D + 64 * xcd) * 2 + (lane & 7) * 16;
        __builtin_amdgcn_raw_buffer_store_b128(o0, ro, oo, 0, 16); __builtin_amdgcn_raw_buffer_store_b128(o1, ro, oo + 8 * 2 * D * 2, 0, 16);
    }
}

DI void role_b(const uint16_t* __restrict__ wo, const uint16_t* __restrict__ u, const uint16_t* __restrict__ lar, const uint16_t* __restrict__ aim,
               const uint16_t* __restrict__ cre, const uint16_t* __restrict__ cim, const uint16_t* __restrict__ ldt, const uint16_t* __restrict__ dskip,
               uint16_t* __restrict__ out, char* __restrict__ ws, LAS char* lds, int w, int lane, u64 code0) {
    u64 tag;
    const unsigned lds0 = (unsigned)(uintptr_t)lds;
    LAS unsigned* sy = (LAS unsigned*)(lds + LSY);
    const __amdgpu_buffer_rsrc_t rs = __builtin_amdgcn_make_buffer_rsrc(ws, 0, WS_END, 0x00020000);
    const int b = blockIdx.x, xcd = b & 7, bi = b >> 3, r32 = lane & 31, hi = lane >> 5;
    const unsigned voff = lane * 16;
    LAS char* tab = lds + LB + (w >> 1) * CBW + (w & 1) * TABW;
    unsigned pf = 0;
#define CODE_PF() asm volatile("global_load_dword %0, %1, %2" : "+v"(pf) : "v"(min((unsigned)((bi * 4 + w) * 3 + (lane < 3 ? lane : 0)), CODE_LINES - 1) * 128), "s"(code0) : "memory")
    const int qt = 8 * xcd + (bi >> 2), ct = 4 * (bi & 3) + w;
    bf16x8 wf[32];
    const unsigned wvo = ct < 14 ? voff : 0x80000000u;
    unsigned wgb[4];
#pragma unroll
    for (int g = 0; g < 4; ++g) wgb[g] = WS_WO + (unsigned)(((w + g) & 3) * 8 * 14 + ct) * 1024;
#define LOADWF(i) wf[i] = BC8(__builtin_amdgcn_raw_buffer_load_b128(rs, wvo, wgb[(i) >> 3] + ((i) & 7) * 14336, 16))
    if (xcd < 7) {
        const int cc = w >> 1, hh = w & 1, pair = 32 * xcd + bi, ch = 2 * pair + cc;
        LAS char* cb = lds + LB + cc * CBW;
        const unsigned po = (unsigned)(ch * NS + 128 * hh) / 2 + lane;
        const unsigned pl = ((const unsigned*)lar)[po], pa = ((const unsigned*)aim)[po], pr = ((const unsigned*)cre)[po], pi = ((const unsigned*)cim)[po];
        const unsigned uo = (unsigned)((64 * bi + 16 * w + (lane >> 3)) * D + 64 * xcd + (lane & 7) * 8) * 2;
        glds2(u, uo, uo + 8 * D * 2 - 1024, lds0 + LT1 + w * 2048);
        const u64 e0 = __hip_atomic_load((GAS u64*)ws, __ATOMIC_RELAXED, __HIP_MEMORY_SCOPE_AGENT);
        u32x2 gi[2][3];
        CODE_PF();
        const unsigned lw = ((const unsigned*)ldt)[pair], dw = ((const unsigned*)dskip)[pair];
        LAS char* ui = cb + UOF;
        const u32x4 z4 = {};
        *(LAS u32x4*)(ui + (64 * hh + lane) * 16) = z4; *(LAS u32x4*)(ui + (96 + lane) * 16) = z4;
        const float dt = rbf(expf(cc ? bf_hi(lw) : bf_lo(lw)));
        drain(); asm volatile("" :: "v"(pf));
        arrive(sy + SY_WO);
        tag = mix(e0);
        const float cm = wave_max(fmaxf(fmaxf(fabsf(bf_lo(pr)), fabsf(bf_hi(pr))), fmaxf(fabsf(bf_lo(pi)), fabsf(bf_hi(pi)))));
        unsigned ce = __float_as_uint(cm) & 0x7f800000u; ce = ce < 0x01000000u ? 0x01000000u : (ce > 0x7e800000u ? 0x7e800000u : ce);
        const float cscale = __uint_as_float(0x7f000000u - ce), oscale = 2.0f * dt * __uint_as_float(ce);
        LAS unsigned* tw = (LAS unsigned*)(tab + lane * 4);
        LAS const char* trd = tab + r32 * TROW + hi * 16;
        f32x16 dacc = {};
        f32x2 z1, z8, z256, x0;
#define ANCH(vl, va, vr, vi) do { const float a2 = (-rbf(expf(vl)) * dt) * 1.4426950408889634f, rev = ((va) * dt) * 0.15915494309189535f; \
            z1 = anchor(a2, -rev, 1.f); z8 = anchor(a2, rev, 8.f); z256 = anchor(a2, -rev, 256.f); x0 = (f32x2){(vr) * cscale, -(vi) * cscale}; \
            PIN(z1); PIN(z8); PIN(z256); SBAR(); } while (0)
#define TW(row) tw[(row) * (TROW / 4)]
#define CHAIN() do { \
            const f32x2 z1s = rot(z1), zs = rot(z256), z8s = rot(z8); \
            f32x2 X[8]; const f32x2 z16 = cmul(z8, z8, z8s); X[0] = x0; X[1] = cmul(x0, z1, z1s); f32x2 a = cmul(x0, z256, zs); \
            const f32x2 z24 = cmul(z16, z8, z8s), z32 = cmul(z16, z16, rot(z16)), z32s = rot(z32); X[2] = cmul(X[1], z1, z1s); f32x2 y = z32; \
            f32x2 c4 = cmul(X[2], z256, zs); \
            unsigned c[2][4], d0 = pk_f16(X[0]), d1 = pk_f16(X[1]), d2 = pk_f16(z8), d3 = pk_f16(z16), d4 = pk_f16(z24), one = 0x3c00u; \
            c[0][0] = pk_f16(y); c[0][1] = pk_f16(X[2]); c[0][2] = pk_f16(a); c[0][3] = pk_f16(c4); \
            TW(0) = d0; TW(1) = d1; TW(30) = d2; TW(31) = d3; TW(32) = d4; TW(21) = one; TW(29) = one; SBAR(); \
            _Pragma("unroll") for (int i = 1; i < 9; ++i) { \
                f32x2 t0, t4, t5, t6; const int j = i - 1; unsigned* pc = c[j & 1]; unsigned* nc = c[i & 1]; \
                if (i < 8) { if (i < 7) t0 = y.xx * z32; if (i < 6) t4 = X[i + 1].xx * z1; t5 = a.xx * z256; \
                    if (i == 3) t6 = X[4].xx * z256; if (i == 4 || i == 5) t6 = c4.xx * z256; if (i == 6) t6 = X[6].xx * z256; } SBAR(); \
                if (j < 7) TW(22 + j) = pc[0]; if (j < 6) TW(j + 2) = pc[1]; TW(j == 0 ? 8 : (j == 1 ? 12 : (j == 2 ? 14 : 13 + j))) = pc[2]; \
                if (j == 0) TW(9) = pc[3]; if (j == 3) TW(10) = pc[3]; if (j == 4) TW(13) = pc[3]; if (j == 5) TW(15) = pc[3]; if (j == 6) TW(11) = pc[3]; SBAR(); \
                if (i < 8) { if (i < 7) y = pfma(y.yy, z32s, t0); if (i < 6) X[i + 2] = pfma(X[i + 1].yy, z1s, t4); a = pfma(a.yy, zs, t5); \
                    if (i == 3) c4 = pfma(X[4].yy, zs, t6); if (i == 4 || i == 5) c4 = pfma(c4.yy, zs, t6); if (i == 6) c4 = pfma(X[6].yy, zs, t6); SBAR(); \
                    if (i < 7) nc[0] = pk_f16(y); if (i < 6) nc[1] = pk_f16(X[i + 2]); nc[2] = pk_f16(a); if (i >= 3 && i <= 6) nc[3] = pk_f16(c4); \
                    asm volatile("" :: "v"(pc[0]), "v"(pc[1]), "v"(pc[2]), "v"(pc[3])); SBAR(); } \
            } } while (0)
        f16x8 fa[8]; u32x4 fp[8], fq[8];
        LAS const char* trp = tab + (21 + (r32 >> 2)) * TROW + hi * 16;
        LAS const char* trq = tab + (29 + (r32 & 3)) * TROW + hi * 16;
#define RDT() do { _Pragma("unroll") for (int kk = 0; kk < 8; ++kk) { fp[kk] = *(LAS const u32x4*)(trp + kk * 32); fq[kk] = *(LAS const u32x4*)(trq + kk * 32); fa[kk] = *(LAS const f16x8*)(trd + kk * 32); } SBAR(); } while (0)
#define MMT() do { _Pragma("unroll") for (int kk = 0; kk < 8; ++kk) dacc = MFMA_F16(fa[kk], cmul4h(fp[kk], fq[kk]), dacc); } while (0)
        ANCH(bf_lo(pl), bf_lo(pa), bf_lo(pr), bf_lo(pi));
        await(sy + SY_WO, 4);
        set_tag(sy, tag);
        {
            u32x4 o0, o1; transpose_tile(lds + LT1, tab, w, lane, o0, o1);
            const unsigned to = WS_UT + (unsigned)((64 * xcd + 16 * w + (lane >> 3)) * S + 64 * bi) * 2 + (lane & 7) * 16;
            __builtin_amdgcn_s_setprio(3); BST(o0, to); BST(o1, to + 8 * S * 2);
#pragma unroll
        for (int ps = 0; ps < 2; ++ps)
#pragma unroll
            for (int i = 0; i < 3; ++i) gi[ps][i] = ((const u32x2*)g_idx.v[128 * hh + 64 * ps + lane])[i];
            __builtin_amdgcn_s_setprio(0);
        }
        CHAIN(); RDT();
        drain(); __builtin_amdgcn_s_setprio(3); put_flag(ws, FL_UT + (unsigned)(xcd * 128 + w * 32 + bi) * FS, tag); __builtin_amdgcn_s_setprio(0);
        ANCH(bf_hi(pl), bf_hi(pa), bf_hi(pr), bf_hi(pi));
        MMT(); SBAR();
        CHAIN(); RDT();
        const unsigned utf = hi ? FL_WO + (unsigned)(w * 32 + r32) * FS : FL_UT + (unsigned)(xcd * 128 + (bi >> 3) * 32 + r32) * FS;
        __builtin_amdgcn_s_setprio(3);
        const u64 utv = __hip_atomic_load((GAS u64*)(ws + utf), __ATOMIC_RELAXED, __HIP_MEMORY_SCOPE_AGENT); SBAR();
        __builtin_amdgcn_s_setprio(1);
        MMT();
        {
            LAS float* pt = (LAS float*)tab;
            *(LAS f32x4*)(pt + 8 * r32 + 4 * hi) = (f32x4){dacc[0] * oscale, dacc[1] * oscale, dacc[2] * oscale, dacc[3] * oscale};
            *(LAS f32x2*)(pt + (hi ? 384 + 2 * r32 : 256 + 4 * r32)) = (f32x2){dacc[4] * oscale, dacc[5] * oscale};
            *(LAS f32x2*)(pt + (hi ? 448 + 2 * r32 : 258 + 4 * r32)) = (f32x2){dacc[6] * oscale, dacc[7] * oscale};
            pt[hi ? 640 + r32 : 512 + r32] = dacc[8] * oscale;
            pt[hi ? 672 + r32 : 544 + r32] = dacc[9] * oscale;
            pt[hi ? 672 + r32 : 576 + r32] = dacc[10] * oscale;
            pt[hi ? 672 + r32 : 608 + r32] = dacc[11] * oscale;
        }
        LAS unsigned* ps_ = sy + SY_P + 5 * cc;
        arrive(ps_ + 0);
        LAS char* kt = cb + KOF;
        await(ps_ + 0, 2);
#pragma unroll
        for (int ps = 0; ps < 2; ++ps) {
            const int v = 81 * hh + 64 * ps + lane;
            if (ps == 0 || lane < 17) {
                const f32x4 p0 = *(LAS const f32x4*)(cb + v * 16), p1 = *(LAS const f32x4*)(cb + TABW + v * 16);
                u32x2 r = {pk_bf16(p0[0] + p1[0], p0[1] + p1[1]), pk_bf16(p0[2] + p1[2], p0[3] + p1[3])};
                if (v == 161) r = (u32x2){0u, 0u};
                *(LAS u32x2*)(kt + v * 8) = r;
            }
        }
        arrive(ps_ + 1);
        { const u64 ms = __ballot(utv != tag); if (ms) wait_flags<8>(ws, utf, tag, ms, ms); }
        CFENCE();
        const unsigned ub = WS_UT + (unsigned)(ch * S + 1024 * hh) * 2 + voff;
        const u32x4 u0 = BLD(ub), u1 = BLD(ub + 1024); SBAR();
        await(ps_ + 1, 2);
        __builtin_amdgcn_s_setprio(1);
        {
            unsigned W[2][6];
#pragma unroll
            for (int ps = 0; ps < 2; ++ps)
#pragma unroll
                for (int i = 0; i < 6; ++i) { const unsigned gv = gi[ps][i >> 1][i & 1];
                    W[ps][i] = *(LAS const uint16_t*)(kt + (gv & 0xffffu));
                    if (i < 5) W[ps][i] |= (unsigned)*(LAS const uint16_t*)(kt + (gv >> 16)) << 16; }
            if (lane < 8) *(LAS u32x4*)(cb + (2 * hh + (lane >> 2)) * CPY + (256 + (lane & 3)) * 16) = z4;
#pragma unroll
            for (int ps = 0; ps < 2; ++ps) {
                unsigned V[5];
#pragma unroll
                for (int i = 0; i < 5; ++i) V[i] = __builtin_amdgcn_alignbit(W[ps][i + 1], W[ps][i], 16);
                LAS char* cp = cb + (128 * hh + 64 * ps + lane) * 16;
                *(LAS u32x4*)(cp) = (u32x4){W[ps][0], W[ps][1], W[ps][2], W[ps][3]};
                *(LAS u32x4*)(cp + CPY) = (u32x4){V[0], V[1], V[2], V[3]};
                *(LAS u32x4*)(cp + 2 * CPY) = (u32x4){W[ps][1], W[ps][2], W[ps][3], W[ps][4]};
                *(LAS u32x4*)(cp + 3 * CPY) = (u32x4){V[1], V[2], V[3], V[4]};
            }
        }
        *(LAS u32x4*)(ui + (32 + 32 * hh + (lane >> 2)) * 80 + (lane & 3) * 16) = u0;
        *(LAS u32x4*)(ui + (48 + 32 * hh + (lane >> 2)) * 80 + (lane & 3) * 16) = u1;
        arrive(ps_ + 2); await(ps_ + 2, 2);
        __builtin_amdgcn_s_setprio(0);
        f32x16 a0 = {}, a1 = {};
        {
            const int sb = 7 - (r32 & 7);
            LAS const char* pa = cb + (sb & 3) * CPY + (255 - (r32 >> 3) + hi) * 16 + (sb >> 2) * 8 - 64 * (16 * hh + 15) - 2048;
            LAS const char* pa2 = pa + 8; asm volatile("" : "+v"(pa2));
            LAS const char* pb = ui + (32 + r32 - 16 * hh - 15) * 80 + 16 * hi;
            s16x4 ta[2][2], tb[2][2]; bf16x8 b0[2], b1[2];
#define T4(q, off) (*(LAS const s16x4*)((q) + (off)))
#define CSTEP(i, j, RD) do { const int o_ = 64 * (15 - ((j) >> 1)) + 32 * ((j) & 1), u_ = 80 * (15 - ((j) >> 1)) + 32 * ((j) & 1); \
                a1 = MFMA_BF(cat8(ta[i][0], ta[i][1]), b1[i], a1); SBAR(); if (RD) { ta[1 - i][0] = T4(pa, o_ + 2048); ta[1 - i][1] = T4(pa2, o_ + 2048); } SBAR(); \
                a0 = MFMA_BF(cat8(ta[i][0], ta[i][1]), b0[i], a0); SBAR(); if (RD) { b1[1 - i] = FR(pb, u_ + 2560); b0[1 - i] = FR(pb, u_); } SBAR(); \
                a1 = MFMA_BF(cat8(tb[i][0], tb[i][1]), b0[i], a1); SBAR(); if (RD) { tb[1 - i][0] = T4(pa, o_); tb[1 - i][1] = T4(pa2, o_); } SBAR(); } while (0)
            ta[0][0] = T4(pa, 960 + 2048); ta[0][1] = T4(pa2, 960 + 2048); b1[0] = FR(pb, 1200 + 2560); b0[0] = FR(pb, 1200); tb[0][0] = T4(pa, 960); tb[0][1] = T4(pa2, 960);
#pragma unroll
            for (int j = 0; j < 32; ++j) {
                if (j == 31) arrive(ps_ + 3);
                CSTEP(j & 1, j + 1, j < 31);
                LOADWF(j);
                SBAR();
            }
        }
        __builtin_amdgcn_s_setprio(1);
        {
            LAS char* ex = cb + hh * 9216 + r32 * 144 + hi * 16;
            await(ps_ + 3, 2);
#pragma unroll
            for (int g = 0; g < 4; ++g) {
                *(LAS f32x4*)(ex + g * 32) = (f32x4){a0[4 * g], a0[4 * g + 1], a0[4 * g + 2], a0[4 * g + 3]};
                *(LAS f32x4*)(ex + 4608 + g * 32) = (f32x4){a1[4 * g], a1[4 * g + 1], a1[4 * g + 2], a1[4 * g + 3]};
            }
            arrive(ps_ + 4);
            const float dsk = cc ? bf_hi(dw) : bf_lo(dw);
            u32x4 sk[2];
#pragma unroll
            for (int e = 0; e < 4; ++e) { sk[0][e] = pk_bf16(bf_lo(u0[e]) * dsk, bf_hi(u0[e]) * dsk); sk[1][e] = pk_bf16(bf_lo(u1[e]) * dsk, bf_hi(u1[e]) * dsk); }
            PIN(sk[0]); PIN(sk[1]);
            await(ps_ + 4, 2);
            LAS const char* er = cb + (32 * hh + (lane >> 2)) * 144 + (lane & 3) * 32;
            const unsigned bo = WS_BT + (unsigned)(ch * S + 1024 * hh) * 2 + voff;
#pragma unroll
            for (int i = 0; i < 2; ++i) {
                const f32x4 y0 = *(LAS const f32x4*)(er + i * 2304) + *(LAS const f32x4*)(er + i * 2304 + 9216);
                const f32x4 y1 = *(LAS const f32x4*)(er + i * 2304 + 16) + *(LAS const f32x4*)(er + i * 2304 + 9216 + 16);
                const u32x4 yb = {pk_bf16(y0[0], y0[1]), pk_bf16(y0[2], y0[3]), pk_bf16(y1[0], y1[1]), pk_bf16(y1[2], y1[3])};
                u32x4 r;
#pragma unroll
                for (int e = 0; e < 4; ++e) r[e] = pk_bf16(bf_lo(yb[e]) + bf_lo(sk[i][e]), bf_hi(yb[e]) + bf_hi(sk[i][e]));
                BST(r, bo + i * 1024);
            }
        }
        arrive(sy + SY_BG);
        drain();
        put_flag(ws, FL_BT + (unsigned)(xcd * 128 + bi * 4 + w) * FS, tag);
    } else {
        {
            const unsigned nb = (bi & 1) ? 12 * 2 * D : 16 * 2 * D;
            unsigned v4[4];
#pragma unroll
            for (int i = 0; i < 4; ++i) { const unsigned o = (unsigned)(w * 4096 + i * 1024) + voff; v4[i] = (o < nb ? o : 0u) + 4096 - i * 1024; }
            glds4((const char*)(wo + (DH * (bi >> 1) + 16 * (bi & 1)) * D) - 4096, v4[0], v4[1], v4[2], v4[3], lds0 + LB + w * 4096);
        }
        const u64 e0 = __hip_atomic_load((GAS u64*)ws, __ATOMIC_RELAXED, __HIP_MEMORY_SCOPE_AGENT);
        CODE_PF();
        drain(); asm volatile("" :: "v"(pf));
        tag = mix(e0);
        arrive(sy + SY_WO); await(sy + SY_WO, 4);
        set_tag(sy, tag);
        LAS const char* p = lds + LB + (4 * hi + ((lane & 15) >> 2)) * (2 * D) + ((lane >> 4) & 1) * 32 + (lane & 3) * 8;
#pragma unroll
        for (int i = 0; i < 4; ++i) {
            const int ct = w + 4 * i;
            if (ct < 14) {
                u32x4 o = __builtin_bit_cast(u32x4, cat8(trread(p + ct * 64), trread(p + 8 * 2 * D + ct * 64)));
                if (hi && (bi & 1)) { o[2] = 0; o[3] = 0; }
                BST(o, WS_WO + (unsigned)(bi * 14 + ct) * 1024 + voff);
            }
        }
        drain();
        put_flag(ws, FL_WO + (unsigned)(w * 32 + bi) * FS, tag);
        __builtin_amdgcn_s_setprio(0);
        arrive(sy + SY_BG);
        await_slow(sy + SY_G, 4);
        wait_flags<8>(ws, FL_WO + (unsigned)(w * 32 + r32) * FS, tag, 0xffffffffull, 0xffffffffull);
        if (ct < 14) {
#pragma unroll
        for (int i = 0; i < 32; ++i) { LOADWF(i); __builtin_amdgcn_s_sleep(2); SBAR(); } }
    }
    const __amdgpu_buffer_rsrc_t ro = __builtin_amdgcn_make_buffer_rsrc(out, 0, S * 2 * D * 2, 0x00020000);
    await(sy + SY_BG, 4);
    await_slow(sy + SY_X, 4);
    wait_flags<2>(ws, FL_O + (unsigned)(qt * 16 + 4 * w + (lane & 3)) * FS, tag, 15, 15);
    u32x4 of[8];
#pragma unroll
    for (int i = 0; i < 8; ++i) of[i] = BLD(WS_O + (unsigned)(qt * 32 + 8 * w + i) * 1024 + voff);
#pragma unroll
    for (int i = 0; i < 8; ++i) *(LAS u32x4*)(lds + LB + (8 * w + i) * 1024 + voff) = of[i];
    arrive(sy + SY_B2);
    f32x16 acc = {};
    if (ct < 14) {
#pragma unroll
        for (int i = 0; i < 8; ++i) acc = MFMA_BF(wf[i], BC8(of[i]), acc);
    }
    SBAR();
    await(sy + SY_B2, 4);
    if (ct < 14) {
        LAS const char* po[3];
#pragma unroll
        for (int j = 0; j < 3; ++j) po[j] = lds + LB + ((w + 1 + j) & 3) * 8192 + voff;
        bf16x8 og[4];
#pragma unroll
        for (int i = 0; i < 3; ++i) og[i] = FR(po[0], i * 1024);
#pragma unroll
        for (int i = 0; i < 24; ++i) { if (i < 21) og[(i + 3) & 3] = FR(po[(i + 3) >> 3], ((i + 3) & 7) * 1024); SBAR(); acc = MFMA_BF(wf[8 + i], og[i & 3], acc); SBAR(); }
        LAS char* sc = lds + LB + 40960 + w * 2304;
#pragma unroll
        for (int g = 0; g < 4; ++g) *(LAS u32x2*)(sc + r32 * 72 + 16 * g + 8 * hi) = (u32x2){pk_bf16(acc[4 * g], acc[4 * g + 1]), pk_bf16(acc[4 * g + 2], acc[4 * g + 3])};
        LAS const char* sr = sc + (lane >> 2) * 72 + (lane & 3) * 16;
        const u32x2 a0 = *(LAS const u32x2*)sr, a1 = *(LAS const u32x2*)(sr + 8), b0 = *(LAS const u32x2*)(sr + 16 * 72), b1 = *(LAS const u32x2*)(sr + 16 * 72 + 8);
        const unsigned oo = (unsigned)((32 * qt + (lane >> 2)) * 2 * D + 32 * ct) * 2 + (lane & 3) * 16;
        __builtin_amdgcn_raw_buffer_store_b128((u32x4){a0[0], a0[1], a1[0], a1[1]}, ro, oo, 0, 16);
        __builtin_amdgcn_raw_buffer_store_b128((u32x4){b0[0], b0[1], b1[0], b1[1]}, ro, oo + 16 * 2 * D * 2, 0, 16);
    }
    if (b == 0 && w == 0) put_flag(ws, 0, tag);
}

__global__ __launch_bounds__(512, 2) void fused(const uint16_t* __restrict__ x, const uint16_t* __restrict__ wqkv, const uint16_t* __restrict__ wo,
                                               const uint16_t* __restrict__ u, const uint16_t* __restrict__ lar, const uint16_t* __restrict__ aim,
                                               const uint16_t* __restrict__ cre, const uint16_t* __restrict__ cim, const uint16_t* __restrict__ ldt,
                                               const uint16_t* __restrict__ dskip, uint16_t* __restrict__ out, char* __restrict__ ws) {
    extern __shared__ __attribute__((aligned(16))) char smem[];
    LAS char* lds = (LAS char*)smem;
    const int lane = threadIdx.x & 63, w = __builtin_amdgcn_readfirstlane(threadIdx.x >> 6);
    const u64 code0 = __builtin_amdgcn_s_getpc() & ~255ull;
    if (w >= 4) __builtin_amdgcn_s_setprio(3);
    if (w == 4) ((LAS unsigned*)(lds + LSY))[lane] = 0;
    asm volatile("" :: "s"(x), "s"(wqkv), "s"(wo), "s"(u), "s"(lar), "s"(aim), "s"(cre), "s"(cim), "s"(ldt), "s"(dskip), "s"(out), "s"(ws));
    asm volatile("s_waitcnt lgkmcnt(0)\n\ts_barrier" ::: "memory");
    if (w < 4) role_a(x, wqkv, out, ws, lds, w, lane);
    else role_b(wo, u, lar, aim, cre, cim, ldt, dskip, out, ws, lds, w - 4, lane, code0);
}
}

extern "C" void kernel_launch(void* const* d_in, const int* in_sizes, int n_in, void* d_out, int out_size, void* d_ws, size_t ws_size, hipStream_t stream) {
    using namespace k;
    (void)hipFuncSetAttribute((const void*)fused, hipFuncAttributeMaxDynamicSharedMemorySize, LDS_BYTES);
    const uint16_t* const* in = (const uint16_t* const*)d_in;
    fused<<<256, 512, LDS_BYTES, stream>>>(in[0], in[1], in[2], in[3], in[4], in[5], in[6], in[7], in[8], in[9], (uint16_t*)d_out, (char*)d_ws);
}
```

```cpp
#include <hip/hip_runtime.h>
#include <hip/hip_bf16.h>
#include <stdint.h>

typedef short bf16x8 __attribute__((ext_vector_type(8)));
typedef _Float16 f16x8 __attribute__((ext_vector_type(8)));
typedef _Float16 f16x2 __attribute__((ext_vector_type(2)));
typedef __bf16 bf16x2 __attribute__((ext_vector_type(2)));
typedef short s16x4 __attribute__((ext_vector_type(4)));
typedef float f32x16 __attribute__((ext_vector_type(16)));
typedef float f32x4 __attribute__((ext_vector_type(4)));
typedef float f32x2 __attribute__((ext_vector_type(2)));
typedef unsigned u32x4 __attribute__((ext_vector_type(4)));
typedef unsigned u32x2 __attribute__((ext_vector_type(2)));
typedef unsigned long long u64;
#define LAS __attribute__((address_space(3)))
#define GAS __attribute__((address_space(1)))
#define DI __device__ __forceinline__
#define MFMA_BF(a, b, c) __builtin_amdgcn_mfma_f32_32x32x16_bf16((a), (b), (c), 0, 0, 0)
#define MFMA_F16(a, b, c) __builtin_amdgcn_mfma_f32_32x32x16_f16((a), (b), (c), 0, 0, 0)
#define SBAR() __builtin_amdgcn_sched_barrier(0)
#define CFENCE() asm volatile("" ::: "memory")
#define PIN(x) asm volatile("" : "+v"(x))
#define BC8(x) __builtin_bit_cast(bf16x8, (x))
#define EX(x) __builtin_amdgcn_exp2f(x)

namespace k {
constexpr int S = 2048, D = 448, DH = 28, NS = 256;
constexpr unsigned FS = 128;
constexpr unsigned FL_KV = FS, FL_O = FL_KV + 1024 * FS, FL_UT = FL_O + 1024 * FS, FL_BT = FL_UT + 896 * FS, FL_WO = FL_BT + 896 * FS;
constexpr unsigned WS_KV = 512u << 10;
constexpr unsigned WS_O = WS_KV + (4u << 20);
constexpr unsigned WS_WO = WS_O + (2u << 20);
constexpr unsigned WS_UT = WS_WO + (448u << 10);
constexpr unsigned WS_BT = WS_UT + D * S * 2;
constexpr unsigned WS_END = WS_BT + D * S * 2;
constexpr int WSL = 8192, LWQ = 4 * WSL, LXR = LWQ + 16384, XSL = 16384, LB = LXR + 3 * XSL;
constexpr int LQ = LXR, LX = 0;
constexpr int TROW = 272, TABW = 33 * TROW, CPY = 4160, UOF = 2 * TABW, KOF = UOF + 7680, CBW = KOF + 1312;
constexpr int LT1 = LB + 2 * CBW, LSY = LT1 + 8192, LDS_BYTES = LSY + 256;
static_assert(LDS_BYTES <= 163840 && (LSY & 15) == 0 && 4 * CPY <= UOF && LB + 40960 + 4 * 2304 <= LSY, "lds");
enum { SY_F = 2, SY_P = 10, SY_WO = 20, SY_BG = 22, SY_B2 = 23, SY_Q = 24, SY_X = 25, SY_G = 26, SY_T2 = 27 };
constexpr int SPIN = 1 << 18;
constexpr unsigned CODE_LINES = 216;

struct IdxTab { uint16_t v[256][12]; };
constexpr IdxTab make_idx() {
    IdxTab t = {};
    for (int y = 0; y < 256; ++y) for (int e = 0; e < 12; ++e) {
        const int j = 2047 - 8 * y - e; int ix = 644;
        if (j >= 0) { const int sh = j >= 1024 ? 3 : (j >= 512 ? 2 : (j >= 256 ? 1 : 0));
            ix = sh ? 128 * sh + ((j + (1 << (sh - 1)) - 1 + ((j >> sh) & 1)) >> sh) : j; }
        t.v[y][e] = (uint16_t)(2 * ix);
    }
    return t;
}
__device__ const IdxTab g_idx __attribute__((aligned(16))) = make_idx();

DI unsigned pk_bf16(float a, float b) { f32x2 v = {a, b}; return __builtin_bit_cast(unsigned, __builtin_convertvector(v, bf16x2)); }
DI unsigned pk_f16(f32x2 v) { return __builtin_bit_cast(unsigned, __builtin_convertvector(v, f16x2)); }
DI float bf_lo(unsigned w) { return __uint_as_float(w << 16); }
DI float bf_hi(unsigned w) { return __uint_as_float(w & 0xffff0000u); }
DI float rbf(float x) { return bf_lo(pk_bf16(x, x)); }
#define GL0 "s_mov_b32 %0, m0\n\ts_mov_b32 m0, %2\n\ts_nop 0\n\t"
#define GL1(V, O, C) "global_load_lds_dwordx4 " V ", %1 offset:" #O C "\n\t"
#define GLE "s_mov_b32 m0, %0"
DI void glds4(const void* sb, unsigned v0, unsigned v1, unsigned v2, unsigned v3, unsigned lb) { unsigned sv;
    asm volatile(GL0 GL1("%3", 0, "") GL1("%4", 1024, "") GL1("%5", 2048, "") GL1("%6", 3072, "") GLE : "=&s"(sv) : "s"(sb), "s"(lb), "v"(v0), "v"(v1), "v"(v2), "v"(v3) : "memory"); }
DI void glds2(const void* sb, unsigned v0, unsigned v1, unsigned lb) { unsigned sv;
    asm volatile(GL0 GL1("%3", 0, "") GL1("%4", 1024, "") GLE : "=&s"(sv) : "s"(sb), "s"(lb), "v"(v0), "v"(v1) : "memory"); }
DI void glds1(const void* sb, unsigned v0, unsigned lb) { unsigned sv;
    asm volatile(GL0 GL1("%3", 0, "") GLE : "=&s"(sv) : "s"(sb), "s"(lb), "v"(v0) : "memory"); }
DI float wave_max(float v) {
#define DPPMAX(ctrl, rm) v = fmaxf(v, __uint_as_float(__builtin_amdgcn_update_dpp(0, __float_as_uint(v), ctrl, rm, 0xf, true)))
    DPPMAX(0x111, 0xf); DPPMAX(0x112, 0xf); DPPMAX(0x114, 0xf); DPPMAX(0x118, 0xf); DPPMAX(0x142, 0xa); DPPMAX(0x143, 0xc);
    return __uint_as_float(__builtin_amdgcn_readlane(__float_as_uint(v), 63));
}
DI s16x4 trread(LAS const char* p) { return __builtin_bit_cast(s16x4, __builtin_amdgcn_ds_read_tr16_b64_v4i16((LAS s16x4*)p)); }
DI bf16x8 cat8(s16x4 a, s16x4 b) { return __builtin_shufflevector(a, b, 0, 1, 2, 3, 4, 5, 6, 7); }
DI f32x2 pfma(f32x2 a, f32x2 b, f32x2 c) { return __builtin_elementwise_fma(a, b, c); }
DI f32x2 cmul(f32x2 a, f32x2 b, f32x2 bs) { return pfma(a.yy, bs, a.xx * b); }
DI f16x8 cmul4h(u32x4 p, u32x4 q) {
    u32x4 t;
#define PM(i, a, b) "v_pk_mul_f16 %" #i ", %" #a ", %" #b " op_sel:[0,0] op_sel_hi:[0,1]\n\t"
#define PF(i, a, b) "v_pk_fma_f16 %" #i ", %" #a ", %" #b ", %" #i " op_sel:[1,1,0] op_sel_hi:[1,0,1] neg_lo:[1,0,0]\n\t"
    asm(PM(0, 4, 8) PM(1, 5, 9) PM(2, 6, 10) PM(3, 7, 11) PF(0, 4, 8) PF(1, 5, 9) PF(2, 6, 10) PF(3, 7, 11) "s_nop 1"
        : "=&v"(t[0]), "=&v"(t[1]), "=&v"(t[2]), "=&v"(t[3]) : "v"(p[0]), "v"(p[1]), "v"(p[2]), "v"(p[3]), "v"(q[0]), "v"(q[1]), "v"(q[2]), "v"(q[3]));
    return __builtin_bit_cast(f16x8, t);
}
DI f32x2 rot(f32x2 b) { return (f32x2){-b.y, b.x}; }
DI f32x2 anchor(float a2, float rev, float s) {
    const float mag = EX(a2 * s);
    float r = rev * s; r -= __builtin_rintf(r);
    return (f32x2){mag * __builtin_amdgcn_cosf(r), mag * __builtin_amdgcn_sinf(r)};
}
DI void arrive(LAS unsigned* c) { asm volatile("s_mov_b64 exec, 1\n\tds_add_u32 %0, %1\n\ts_mov_b64 exec, -1" :: "v"((unsigned)(uintptr_t)c), "v"(1u) : "memory"); }
DI void await(LAS unsigned* c, unsigned n) {
    unsigned t, i = SPIN;
    asm volatile("1:\n\tds_read_b32 %0, %2\n\ts_waitcnt lgkmcnt(0)\n\tv_cmp_gt_u32 vcc, %3, %0\n\ts_cbranch_vccz 2f\n\ts_sleep 1\n\ts_sub_u32 %1, %1, 1\n\ts_cbranch_scc0 1b\n2:"
                 : "=&v"(t), "+s"(i) : "v"((unsigned)(uintptr_t)c), "s"(n) : "vcc", "scc", "memory");
}
DI void await_slow(LAS unsigned* c, unsigned n) {
    unsigned t, i = SPIN;
    asm volatile("1:\n\tds_read_b32 %0, %2\n\ts_waitcnt lgkmcnt(0)\n\tv_cmp_gt_u32 vcc, %3, %0\n\ts_cbranch_vccz 2f\n\ts_sleep 3\n\ts_sub_u32 %1, %1, 1\n\ts_cbranch_scc0 1b\n2:"
                 : "=&v"(t), "+s"(i) : "v"((unsigned)(uintptr_t)c), "s"(n) : "vcc", "scc", "memory");
}
DI u64 get_tag(LAS unsigned* sy) {
    await(sy, 1);
    const u64 v = __hip_atomic_load((LAS u64*)sy, __ATOMIC_RELAXED, __HIP_MEMORY_SCOPE_WORKGROUP);
    return ((u64)(unsigned)__builtin_amdgcn_readfirstlane((unsigned)(v >> 32)) << 32) | (unsigned)__builtin_amdgcn_readfirstlane((unsigned)v);
}
DI void set_tag(LAS unsigned* sy, u64 tag) { asm volatile("s_mov_b64 exec, 1\n\tds_write_b64 %0, %1\n\ts_mov_b64 exec, -1" :: "v"((unsigned)(uintptr_t)sy), "v"(tag) : "memory"); }
DI void drain() { asm volatile("s_waitcnt vmcnt(0)" ::: "memory"); }
DI void put_flag(char* ws, unsigned off, u64 tag) {
    asm volatile("s_mov_b64 exec, 1\n\tglobal_store_dwordx2 %0, %1, %2 sc1\n\ts_mov_b64 exec, -1" :: "v"(off), "v"(tag), "s"(ws) : "memory");
}
template <int SLP> DI u64 wait_flags(char* ws, unsigned off, u64 tag, u64 pend, u64 need) {
    u64 t, left; unsigned i = SPIN;
    asm volatile("s_mov_b64 exec, %5\n1:\n\tglobal_load_dwordx2 %0, %3, %4 sc1\n\ts_waitcnt vmcnt(0)\n\tv_cmpx_ne_u64 vcc, %6, %0\n\ts_and_b64 %2, exec, %7\n\t"
                 "s_cbranch_scc0 2f\n\ts_sleep %8\n\ts_sub_u32 %1, %1, 1\n\ts_cbranch_scc0 1b\n2:\n\ts_mov_b64 %2, exec\n\ts_mov_b64 exec, -1"
                 : "=&v"(t), "+s"(i), "=&s"(left) : "v"(off), "s"(ws), "s"(pend), "s"(tag), "s"(need), "i"(SLP) : "vcc", "scc", "memory");
    return left;
}
#define BST(v, off) __builtin_amdgcn_raw_buffer_store_b128((v), rs, (off), 0, 16)
#define BLD(off) __builtin_amdgcn_raw_buffer_load_b128(rs, (off), 0, 16)
#define FR(p, off) (*(LAS const bf16x8*)((p) + (off)))
DI float swapmax(float m) { const auto r = __builtin_amdgcn_permlane32_swap(__float_as_uint(m), __float_as_uint(m), false, false); return fmaxf(__uint_as_float(r[0]), __uint_as_float(r[1])); }
DI u32x4 put_ref(u32x4 q1, float m, int hi) {
    const float m1 = rbf(m), m2 = rbf(m - m1), m3 = m - m1 - m2;
    if (hi) { q1[2] = pk_bf16(-m1, -m2); q1[3] = pk_bf16(-m3, 0.f); }
    return q1;
}
DI u64 mix(u64 v) {
    const u64 e = ((u64)(unsigned)__builtin_amdgcn_readfirstlane((unsigned)(v >> 32)) << 32) | (unsigned)__builtin_amdgcn_readfirstlane((unsigned)v);
    u64 z = e + 0x9E3779B97F4A7C15ull; z = (z ^ (z >> 30)) * 0xBF58476D1CE4E5B9ull; z = (z ^ (z >> 27)) * 0x94D049BB133111EBull; return (z ^ (z >> 31)) | 1ull; }
DI void transpose_tile(LAS const char* stage, LAS char* scratch, int w, int lane, u32x4& o0, u32x4& o1) {
    LAS const char* p = stage + (lane >> 4) * 2048 + ((lane & 15) >> 2) * 128 + w * 32 + (lane & 3) * 8;
    const bf16x8 t0 = cat8(trread(p), trread(p + 512)), t1 = cat8(trread(p + 1024), trread(p + 1536));
    LAS char* sc = scratch + (lane & 15) * 144 + (lane >> 4) * 32;
    *(LAS bf16x8*)sc = t0; *(LAS bf16x8*)(sc + 16) = t1;
    LAS const char* sr = scratch + (lane >> 3) * 144 + (lane & 7) * 16;
    o0 = *(LAS const u32x4*)sr; o1 = *(LAS const u32x4*)(sr + 8 * 144);
}

DI void role_a(const uint16_t* __restrict__ x, const uint16_t* __restrict__ wqkv, uint16_t* __restrict__ out, char* __restrict__ ws, LAS char* lds, int w, int lane, unsigned tc) {
    const int head = 2 * (blockIdx.x & 7) + ((blockIdx.x >> 3) & 1), grp = blockIdx.x >> 4;
    const unsigned lds0 = (unsigned)(uintptr_t)lds;
    const int r32 = lane & 31, hi = lane >> 5;
    const int wsh = (head & 7) == 7 ? 4 : 0;
    const unsigned wv = (unsigned)((lane >> 2) * 3 * D + (lane & 3) * 8) * 2;
    const char* wb = (const char*)(wqkv + DH * head - wsh + (16 * w) * 3 * D);
    const unsigned xv0 = (unsigned)((lane >> 3) * D + (((lane & 7) ^ (lane >> 4)) * 8)) * 2, xv1 = xv0 ^ 64;
    const char* xb = (const char*)(x + (128 * grp + 32 * w) * D);
    const int rho = ((blockIdx.x >> 3) + ((blockIdx.x >> 1) & 3)) % 7;
#define SP(st) ((st) + rho >= 7 ? (st) + rho - 7 : (st) + rho)
#define WQO(st) ((st) < 4 ? LWQ + (st) * 4096 : ((st) < 6 ? 3 * WSL + ((st) - 4) * 4096 : 0))
#define LOADW(st) glds2(wb + SP(st) * (64 * 3 * D * 2), wv + 2 * D, wv + 4 * D - 1024, lds0 + ((st) & 3) * WSL + w * 2048)
#define LOADQ(st) glds1(wb + SP(st) * (64 * 3 * D * 2), wv, lds0 + WQO(st) + w * 1024)
#define LOADX(st) glds4(xb + SP(st) * 128, xv0, xv1 + 16 * D - 1024, xv0 + 32 * D - 2048, xv1 + 48 * D - 3072, lds0 + LXR + ((st) % 3) * XSL + w * 4096)
    LOADX(0); LOADW(0);
    LOADX(1); LOADW(1);
    LAS unsigned* sy = (LAS unsigned*)(lds + LSY);
    const __amdgpu_buffer_rsrc_t rs = __builtin_amdgcn_make_buffer_rsrc(ws, 0, WS_END, 0x00020000);
    const int xs = (r32 >> 1) & 7;
    LAS const char* xp[4];
#pragma unroll
    for (int ks = 0; ks < 4; ++ks) xp[ks] = lds + LXR + w * 4096 + r32 * 128 + (((2 * ks + hi) ^ xs) * 16);
    LAS const char* wp = lds + (8 * hi + ((lane & 15) >> 2)) * 64 + ((lane >> 4) & 1) * 32 + (lane & 3) * 8 + wsh * 2;
    f32x16 aq = {}, ak = {}, av = {};
    bf16x8 xf[7][4]; s16x4 fr[4][4];
#define RX(st, ks) xf[st][ks] = FR(xp[ks], ((st) % 3) * XSL)
#define RW(st, ks, j) fr[ks][j] = trread(wp + ((st) & 3) * WSL + (ks) * 2048 + ((j) >> 1) * 1024 + ((j) & 1) * 256)
#define MK(st, ks) ak = MFMA_BF(cat8(fr[ks][0], fr[ks][1]), xf[st][ks], ak); SBAR()
#define MV(st, ks) av = MFMA_BF(xf[st][ks], cat8(fr[ks][2], fr[ks][3]), av); SBAR()
#define QSTEP(st, ks) MK(st, ks); RW(st, ks + 1, 0); RW(st, ks + 1, 1); SBAR(); MV(st, ks); RW(st, ks + 1, 2); RW(st, ks + 1, 3); SBAR()
    u64 tag = 0;
    asm volatile("s_waitcnt vmcnt(6)" :: "v"(tc) : "memory");
    arrive(sy + SY_F); LOADW(2); LOADX(2); await(sy + SY_F, 4);
    RX(0, 0); RW(0, 0, 0); RW(0, 0, 1); RW(0, 0, 2); RW(0, 0, 3); RX(0, 1); RX(0, 2); RX(0, 3); SBAR();
#pragma unroll
    for (int st = 0; st < 7; ++st) {
        QSTEP(st, 0); if (st < 4) LOADW(st + 3); if (st == 4) { LOADQ(4); LOADQ(5); } if (st == 5) LOADQ(6);
        QSTEP(st, 1); if (st < 4) LOADX(st + 3);
        QSTEP(st, 2); if (st == 3) { LOADQ(0); LOADQ(1); LOADQ(2); LOADQ(3); }
        if (st < 6) {
            if (st == 3) asm volatile("s_waitcnt vmcnt(16)" ::: "memory"); else if (st == 5) asm volatile("s_waitcnt vmcnt(7)" ::: "memory"); else asm volatile("s_waitcnt vmcnt(12)" ::: "memory");
            arrive(sy + SY_F + 1 + st); SBAR();
        }
        MK(st, 3);
        if (st < 6) { await(sy + SY_F + 1 + st, 4); RX(st + 1, 0); RW(st + 1, 0, 0); RW(st + 1, 0, 1); RW(st + 1, 0, 2); RW(st + 1, 0, 3); } SBAR();
        MV(st, 3); if (st < 6) { RX(st + 1, 1); RX(st + 1, 2); RX(st + 1, 3); } SBAR();
        if (st == 2) tag = get_tag(sy);
    }
    const float cq = 0.18898223650461362f * 1.4426950408889634f;
    const unsigned voff = lane * 16;
    u32x4 q0, q1, k0, k1, v0, v1;
    const unsigned tb = WS_KV + (unsigned)(head * 64 + 4 * grp + w) * 4096 + voff;
#pragma unroll
    for (int i = 0; i < 4; ++i) { k0[i] = pk_bf16(ak[2 * i], ak[2 * i + 1]); k1[i] = pk_bf16(ak[8 + 2 * i], ak[9 + 2 * i]); }
    if (hi) { k1[2] = 0x3f803f80u; k1[3] = 0x00003f80u; }
    BST(k0, tb); BST(k1, tb + 1024);
#pragma unroll
    for (int i = 0; i < 4; ++i) {
        v0[i] = pk_bf16(av[2 * i], av[2 * i + 1]); v1[i] = pk_bf16(av[8 + 2 * i], av[9 + 2 * i]);
        if (r32 >= DH) { v0[i] = v1[i] = (r32 == DH ? 0x3f803f80u : 0u); }
    }
    BST(v0, tb + 2048); BST(v1, tb + 3072);
    asm volatile("s_waitcnt vmcnt(4)" ::: "memory");
    arrive(sy + SY_F + 7); await(sy + SY_F + 7, 4);
    {
        s16x4 fq[4][2];
#define RQ(j) do { fq[(j) & 3][0] = trread(wp + WQO((j) >> 2) + ((j) & 3) * 1024); fq[(j) & 3][1] = trread(wp + WQO((j) >> 2) + ((j) & 3) * 1024 + 256); } while (0)
        RQ(0); RQ(1); RQ(2); SBAR();
#pragma unroll
        for (int j = 0; j < 28; ++j) {
            aq = MFMA_BF(cat8(fq[j & 3][0], fq[j & 3][1]), xf[j >> 2][j & 3], aq); SBAR(); if (j < 25) RQ(j + 3); SBAR();
            if (j == 13) { drain(); put_flag(ws, FL_KV + (unsigned)((head * 4 + w) * 16 + grp) * FS, tag); }
        }
    }
#pragma unroll
    for (int i = 0; i < 4; ++i) { q0[i] = pk_bf16(aq[2 * i] * cq, aq[2 * i + 1] * cq); q1[i] = pk_bf16(aq[8 + 2 * i] * cq, aq[9 + 2 * i] * cq); }
    if (hi) { q1[2] = 0; q1[3] = 0; }
    const unsigned kvf = FL_KV + (unsigned)((head * 4 + w) * 16 + ((grp + lane) & 15)) * FS;
    const f32x16 zero = {};
    {
        f32x16 s = MFMA_BF(BC8(k0), BC8(q0), zero); s = MFMA_BF(BC8(k1), BC8(q1), s);
        float m = s[0];
#pragma unroll
        for (int i = 1; i < 16; ++i) m = fmaxf(m, s[i]);
        q1 = put_ref(q1, swapmax(m), hi);
    }
    { LAS u32x4* qo = (LAS u32x4*)(lds + LQ + w * 2048) + lane; qo[0] = q0; qo[64] = q1; }
    arrive(sy + SY_Q);
    const u64 kv1 = __hip_atomic_load((GAS u64*)(ws + kvf), __ATOMIC_RELAXED, __HIP_MEMORY_SCOPE_AGENT); SBAR();
    f32x16 O[4] = {}; PIN(O[0]); PIN(O[1]); PIN(O[2]); PIN(O[3]);
    await(sy + SY_Q, 4);
    bf16x8 Q[4][2];
    Q[0][0] = BC8(q0); Q[0][1] = BC8(q1);
#pragma unroll
    for (int i = 1; i < 4; ++i) { LAS const char* p = lds + LQ + ((w + i) & 3) * 2048 + voff; Q[i][0] = FR(p, 0); Q[i][1] = FR(p, 1024); }
    u32x4 kv[4][4] = {{k0, k1, v0, v1}, {}, {}, {}};
    f32x16 sa, sb;
    u32x4 p0, p1 = {};
#define GRP(SC, i, P, j) do { float e0 = EX(SC[i]); SBAR(); float e1 = EX(SC[i + 1]); SBAR(); float e2 = EX(SC[i + 2]); SBAR(); float e3 = EX(SC[i + 3]); SBAR(); \
        P[j] = pk_bf16(e0, e1); SBAR(); P[j + 1] = pk_bf16(e2, e3); PIN(P); SBAR(); } while (0)
#define SLOT(SC, SN, cb, i, QK, PV1, LD) do { constexpr int nb_ = (i) < 3 ? (cb) : ((cb) + 1) % 4, pb_ = (i) > 0 ? (cb) : ((cb) + 3) % 4; \
        if (QK) SN = MFMA_BF(BC8(kv[nb_][0]), Q[((i) + 1) & 3][0], zero); SBAR(); GRP(SC, 0, p0, 0); \
        if (PV1) O[((i) + 3) & 3] = MFMA_BF(BC8(kv[pb_][3]), BC8(p1), O[((i) + 3) & 3]); SBAR(); \
        if (LD) { kv[((cb) + 3) % 4][i] = __builtin_amdgcn_raw_buffer_load_b128(rs, voff + (i) * 1024, kvo, 16); SBAR(); } GRP(SC, 4, p0, 2); \
        if (QK) SN = MFMA_BF(BC8(kv[nb_][1]), Q[((i) + 1) & 3][1], SN); SBAR(); GRP(SC, 8, p1, 0); \
        O[i] = MFMA_BF(BC8(kv[cb][2]), BC8(p0), O[i]); SBAR(); GRP(SC, 12, p1, 2); } while (0)
    const unsigned kvb = WS_KV + (unsigned)(head * 64 + w) * 4096;
    unsigned kvo = 0;
    u64 miss = __ballot(kv1 != tag) & 0xfffe;
    if (miss & 6) miss = wait_flags<2>(ws, kvf, tag, miss, 6);
    CFENCE();
#pragma unroll
    for (int j = 1; j < 3; ++j)
#pragma unroll
        for (int f = 0; f < 4; ++f) kv[j][f] = __builtin_amdgcn_raw_buffer_load_b128(rs, voff + f * 1024, kvb + ((grp + j) & 15) * 16384, 16);
    arrive(sy + SY_G);
    sa = MFMA_BF(BC8(k0), Q[0][0], zero); sa = MFMA_BF(BC8(k1), Q[0][1], sa);
    int nx = grp + 3;
#define TILE(cb) do { kvo = kvb + (nx & 15) * 16384; ++nx; SBAR(); \
        SLOT(sa, sb, cb, 0, true, true, true); SLOT(sb, sa, cb, 1, true, true, true); SLOT(sa, sb, cb, 2, true, true, true); SLOT(sb, sa, cb, 3, true, true, true); } while (0)
#pragma unroll 1
    for (unsigned msk = 0x78; msk < 0x80000; msk <<= 4) { if (__builtin_expect((miss & msk) != 0, 0)) miss = wait_flags<2>(ws, kvf, tag, miss, msk); TILE(0); TILE(1); TILE(2); TILE(3); }
    O[3] = MFMA_BF(BC8(kv[3][3]), BC8(p1), O[3]);
    f32x16 o = O[0];
    {
#pragma unroll
        for (int i = 1; i < 4; ++i) {
            LAS char* ex = lds + LX + (((w + i) & 3) * 3 + i - 1) * 4096 + voff;
#pragma unroll
            for (int g = 0; g < 4; ++g) *(LAS f32x4*)(ex + g * 1024) = (f32x4){O[i][4 * g], O[i][4 * g + 1], O[i][4 * g + 2], O[i][4 * g + 3]};
        }
        arrive(sy + SY_X); await(sy + SY_X, 4);
        LAS const char* er = lds + LX + w * 3 * 4096 + voff;
#pragma unroll
        for (int i = 0; i < 3; ++i)
#pragma unroll
            for (int g = 0; g < 4; ++g) { const f32x4 r = *(LAS const f32x4*)(er + i * 4096 + g * 1024);
#pragma unroll
                for (int e = 0; e < 4; ++e) o[4 * g + e] += r[e]; }
        f32x2 chk = {0.f, 0.f};
#pragma unroll
        for (int i = 0; i < 8; ++i) chk = pfma((f32x2){o[2 * i], o[2 * i + 1]}, (f32x2){0.f, 0.f}, chk);
        if (__builtin_expect(__any(!(chk[0] + chk[1] == 0.f) || !(o[12] < 1e30f)), 0)) {
            const unsigned hb = WS_KV + (unsigned)head * 64 * 4096 + voff;
            u32x4 qr = __builtin_bit_cast(u32x4, Q[0][1]); if (hi) { qr[2] = 0; qr[3] = 0; }
            float mm = -INFINITY;
            wait_flags<4>(ws, FL_KV + (unsigned)(head * 64 + lane) * FS, tag, ~0ull, ~0ull);
#pragma unroll 1
            for (int t = 0; t < 64; ++t) {
                f32x16 s = MFMA_BF(BC8(BLD(hb + t * 4096)), Q[0][0], zero); s = MFMA_BF(BC8(BLD(hb + t * 4096 + 1024)), BC8(qr), s);
#pragma unroll
                for (int i = 0; i < 16; ++i) mm = fmaxf(mm, s[i]);
            }
            qr = put_ref(qr, swapmax(mm), hi);
            o = zero;
#pragma unroll 1
            for (int t = 0; t < 64; ++t) {
                f32x16 s = MFMA_BF(BC8(BLD(hb + t * 4096)), Q[0][0], zero); s = MFMA_BF(BC8(BLD(hb + t * 4096 + 1024)), BC8(qr), s);
#pragma unroll
                for (int i = 0; i < 4; ++i) { p0[i] = pk_bf16(EX(s[2 * i]), EX(s[2 * i + 1])); p1[i] = pk_bf16(EX(s[8 + 2 * i]), EX(s[9 + 2 * i])); }
                o = MFMA_BF(BC8(BLD(hb + t * 4096 + 2048)), BC8(p0), o); o = MFMA_BF(BC8(BLD(hb + t * 4096 + 3072)), BC8(p1), o);
            }
        }
    }
    {
        const auto r = __builtin_amdgcn_permlane32_swap(__float_as_uint(o[12]), __float_as_uint(o[12]), false, false);
        const float rl = __builtin_amdgcn_rcpf(__uint_as_float(r[1]));
        u32x4 o0, o1;
#pragma unroll
        for (int i = 0; i < 4; ++i) { o0[i] = pk_bf16(o[2 * i] * rl, o[2 * i + 1] * rl); o1[i] = pk_bf16(o[8 + 2 * i] * rl, o[9 + 2 * i] * rl); }
        if (hi) { o1[2] = 0; o1[3] = 0; }
        const unsigned fi = (unsigned)((4 * grp + w) * 16 + head);
        BST(o0, WS_O + fi * 2048 + voff); BST(o1, WS_O + fi * 2048 + 1024 + voff);
        drain();
        put_flag(ws, FL_O + fi * FS, tag);
    }
    const int xcd = blockIdx.x & 7, bi = blockIdx.x >> 3;
    if (xcd < 7) {
        wait_flags<2>(ws, FL_BT + (unsigned)(xcd * 128 + w * 32 + r32) * FS, tag, 0xffffffffull, 0xffffffffull);
        const unsigned bo = WS_BT + (unsigned)((64 * xcd + 16 * w + (lane >> 3)) * S + 64 * bi + (lane & 7) * 8) * 2;
        const u32x4 b0 = BLD(bo), b1 = BLD(bo + 8 * S * 2);
        *(LAS u32x4*)(lds + 65536 + w * 2048 + voff) = b0; *(LAS u32x4*)(lds + 65536 + w * 2048 + 1024 + voff) = b1;
        arrive(sy + SY_T2); await(sy + SY_T2, 4);
        u32x4 o0, o1; transpose_tile(lds + 65536, lds + 73728 + w * 2304, w, lane, o0, o1);
        const __amdgpu_buffer_rsrc_t ro = __builtin_amdgcn_make_buffer_rsrc(out, 0, S * 2 * D * 2, 0x00020000);
        const unsigned oo = (unsigned)((64 * bi + 16 * w + (lane >> 3)) * 2 * D + D + 64 * xcd) * 2 + (lane & 7) * 16;
        __builtin_amdgcn_raw_buffer_store_b128(o0, ro, oo, 0, 16); __builtin_amdgcn_raw_buffer_store_b128(o1, ro, oo + 8 * 2 * D * 2, 0, 16);
    }
}

DI void role_b(const uint16_t* __restrict__ wo, const uint16_t* __restrict__ u, const uint16_t* __restrict__ lar, const uint16_t* __restrict__ aim,
               const uint16_t* __restrict__ cre, const uint16_t* __restrict__ cim, const uint16_t* __restrict__ ldt, const uint16_t* __restrict__ dskip,
               uint16_t* __restrict__ out, char* __restrict__ ws, LAS char* lds, int w, int lane, u64 code0, unsigned tc) {
    u64 tag;
    const unsigned lds0 = (unsigned)(uintptr_t)lds;
    LAS unsigned* sy = (LAS unsigned*)(lds + LSY);
    const __amdgpu_buffer_rsrc_t rs = __builtin_amdgcn_make_buffer_rsrc(ws, 0, WS_END, 0x00020000);
    const int b = blockIdx.x, xcd = b & 7, bi = b >> 3, r32 = lane & 31, hi = lane >> 5;
    const unsigned voff = lane * 16;
    LAS char* tab = lds + LB + (w >> 1) * CBW + (w & 1) * TABW;
    unsigned pf = 0;
#define CODE_PF() asm volatile("global_load_dword %0, %1, %2" : "+v"(pf) : "v"(min((unsigned)((bi * 4 + w) * 3 + (lane < 3 ? lane : 0)), CODE_LINES - 1) * 128), "s"(code0) : "memory")
    const int qt = 8 * xcd + (bi >> 2), ct = 4 * (bi & 3) + w;
    bf16x8 wf[32];
    const unsigned wvo = ct < 14 ? voff : 0x80000000u;
    unsigned wgb[4];
#pragma unroll
    for (int g = 0; g < 4; ++g) wgb[g] = WS_WO + (unsigned)(((w + g) & 3) * 8 * 14 + ct) * 1024;
#define LOADWF(i) wf[i] = BC8(__builtin_amdgcn_raw_buffer_load_b128(rs, wvo, wgb[(i) >> 3] + ((i) & 7) * 14336, 16))
    if (xcd < 7) {
        const int cc = w >> 1, hh = w & 1, pair = 32 * xcd + bi, ch = 2 * pair + cc;
        LAS char* cb = lds + LB + cc * CBW;
        const unsigned po = (unsigned)(ch * NS + 128 * hh) / 2 + lane;
        const unsigned pl = ((const unsigned*)lar)[po], pa = ((const unsigned*)aim)[po], pr = ((const unsigned*)cre)[po], pi = ((const unsigned*)cim)[po];
        const unsigned uo = (unsigned)((64 * bi + 16 * w + (lane >> 3)) * D + 64 * xcd + (lane & 7) * 8) * 2;
        glds2(u, uo, uo + 8 * D * 2 - 1024, lds0 + LT1 + w * 2048);
        const u64 e0 = __hip_atomic_load((GAS u64*)ws, __ATOMIC_RELAXED, __HIP_MEMORY_SCOPE_AGENT);
        u32x2 gi[2][3];
        const unsigned lw = ((const unsigned*)ldt)[pair], dw = ((const unsigned*)dskip)[pair];
        LAS char* ui = cb + UOF;
        const u32x4 z4 = {};
        *(LAS u32x4*)(ui + (64 * hh + lane) * 16) = z4; *(LAS u32x4*)(ui + (96 + lane) * 16) = z4;
        const float dt = rbf(expf(cc ? bf_hi(lw) : bf_lo(lw)));
        drain(); asm volatile("" :: "v"(tc));
        arrive(sy + SY_WO);
        tag = mix(e0);
        const float cm = wave_max(fmaxf(fmaxf(fabsf(bf_lo(pr)), fabsf(bf_hi(pr))), fmaxf(fabsf(bf_lo(pi)), fabsf(bf_hi(pi)))));
        unsigned ce = __float_as_uint(cm) & 0x7f800000u; ce = ce < 0x01000000u ? 0x01000000u : (ce > 0x7e800000u ? 0x7e800000u : ce);
        const float cscale = __uint_as_float(0x7f000000u - ce), oscale = 2.0f * dt * __uint_as_float(ce);
        LAS unsigned* tw = (LAS unsigned*)(tab + lane * 4);
        LAS const char* trd = tab + r32 * TROW + hi * 16;
        f32x16 dacc = {};
        f32x2 z1, z8, z256, x0;
#define ANCH(vl, va, vr, vi) do { const float a2 = (-rbf(expf(vl)) * dt) * 1.4426950408889634f, rev = ((va) * dt) * 0.15915494309189535f; \
            z1 = anchor(a2, -rev, 1.f); z8 = anchor(a2, rev, 8.f); z256 = anchor(a2, -rev, 256.f); x0 = (f32x2){(vr) * cscale, -(vi) * cscale}; \
            PIN(z1); PIN(z8); PIN(z256); SBAR(); } while (0)
#define TW(row) tw[(row) * (TROW / 4)]
#define CHAIN() do { \
            const f32x2 z1s = rot(z1), zs = rot(z256), z8s = rot(z8); \
            f32x2 X[8]; const f32x2 z16 = cmul(z8, z8, z8s); X[0] = x0; X[1] = cmul(x0, z1, z1s); f32x2 a = cmul(x0, z256, zs); \
            const f32x2 z24 = cmul(z16, z8, z8s), z32 = cmul(z16, z16, rot(z16)), z32s = rot(z32); X[2] = cmul(X[1], z1, z1s); f32x2 y = z32; \
            f32x2 c4 = cmul(X[2], z256, zs); \
            unsigned c[2][4], d0 = pk_f16(X[0]), d1 = pk_f16(X[1]), d2 = pk_f16(z8), d3 = pk_f16(z16), d4 = pk_f16(z24), one = 0x3c00u; \
            c[0][0] = pk_f16(y); c[0][1] = pk_f16(X[2]); c[0][2] = pk_f16(a); c[0][3] = pk_f16(c4); \
            TW(0) = d0; TW(1) = d1; TW(30) = d2; TW(31) = d3; TW(32) = d4; TW(21) = one; TW(29) = one; SBAR(); \
            _Pragma("unroll") for (int i = 1; i < 9; ++i) { \
                f32x2 t0, t4, t5, t6; const int j = i - 1; unsigned* pc = c[j & 1]; unsigned* nc = c[i & 1]; \
                if (i < 8) { if (i < 7) t0 = y.xx * z32; if (i < 6) t4 = X[i + 1].xx * z1; t5 = a.xx * z256; \
                    if (i == 3) t6 = X[4].xx * z256; if (i == 4 || i == 5) t6 = c4.xx * z256; if (i == 6) t6 = X[6].xx * z256; } SBAR(); \
                if (j < 7) TW(22 + j) = pc[0]; if (j < 6) TW(j + 2) = pc[1]; TW(j == 0 ? 8 : (j == 1 ? 12 : (j == 2 ? 14 : 13 + j))) = pc[2]; \
                if (j == 0) TW(9) = pc[3]; if (j == 3) TW(10) = pc[3]; if (j == 4) TW(13) = pc[3]; if (j == 5) TW(15) = pc[3]; if (j == 6) TW(11) = pc[3]; SBAR(); \
                if (i < 8) { if (i < 7) y = pfma(y.yy, z32s, t0); if (i < 6) X[i + 2] = pfma(X[i + 1].yy, z1s, t4); a = pfma(a.yy, zs, t5); \
                    if (i == 3) c4 = pfma(X[4].yy, zs, t6); if (i == 4 || i == 5) c4 = pfma(c4.yy, zs, t6); if (i == 6) c4 = pfma(X[6].yy, zs, t6); SBAR(); \
                    if (i < 7) nc[0] = pk_f16(y); if (i < 6) nc[1] = pk_f16(X[i + 2]); nc[2] = pk_f16(a); if (i >= 3 && i <= 6) nc[3] = pk_f16(c4); \
                    asm volatile("" :: "v"(pc[0]), "v"(pc[1]), "v"(pc[2]), "v"(pc[3])); SBAR(); } \
            } } while (0)
        f16x8 fa[8]; u32x4 fp[8], fq[8];
        LAS const char* trp = tab + (21 + (r32 >> 2)) * TROW + hi * 16;
        LAS const char* trq = tab + (29 + (r32 & 3)) * TROW + hi * 16;
#define RDT() do { _Pragma("unroll") for (int kk = 0; kk < 8; ++kk) { fp[kk] = *(LAS const u32x4*)(trp + kk * 32); fq[kk] = *(LAS const u32x4*)(trq + kk * 32); fa[kk] = *(LAS const f16x8*)(trd + kk * 32); } SBAR(); } while (0)
#define MMT() do { _Pragma("unroll") for (int kk = 0; kk < 8; ++kk) dacc = MFMA_F16(fa[kk], cmul4h(fp[kk], fq[kk]), dacc); } while (0)
        ANCH(bf_lo(pl), bf_lo(pa), bf_lo(pr), bf_lo(pi));
        await(sy + SY_WO, 4);
        set_tag(sy, tag);
        {
            u32x4 o0, o1; transpose_tile(lds + LT1, tab, w, lane, o0, o1);
            const unsigned to = WS_UT + (unsigned)((64 * xcd + 16 * w + (lane >> 3)) * S + 64 * bi) * 2 + (lane & 7) * 16;
            __builtin_amdgcn_s_setprio(3); BST(o0, to); BST(o1, to + 8 * S * 2);
#pragma unroll
        for (int ps = 0; ps < 2; ++ps)
#pragma unroll
            for (int i = 0; i < 3; ++i) gi[ps][i] = ((const u32x2*)g_idx.v[128 * hh + 64 * ps + lane])[i];
            __builtin_amdgcn_s_setprio(0);
        }
        CHAIN(); RDT();
        drain(); __builtin_amdgcn_s_setprio(3); put_flag(ws, FL_UT + (unsigned)(xcd * 128 + w * 32 + bi) * FS, tag); __builtin_amdgcn_s_setprio(0);
        ANCH(bf_hi(pl), bf_hi(pa), bf_hi(pr), bf_hi(pi));
        MMT(); SBAR();
        CHAIN(); RDT();
        const unsigned utf = hi ? FL_WO + (unsigned)(w * 32 + r32) * FS : FL_UT + (unsigned)(xcd * 128 + (bi >> 3) * 32 + r32) * FS;
        __builtin_amdgcn_s_setprio(3);
        const u64 utv = __hip_atomic_load((GAS u64*)(ws + utf), __ATOMIC_RELAXED, __HIP_MEMORY_SCOPE_AGENT); SBAR();
        __builtin_amdgcn_s_setprio(1);
        MMT();
        {
            LAS float* pt = (LAS float*)tab;
            *(LAS f32x4*)(pt + 8 * r32 + 4 * hi) = (f32x4){dacc[0] * oscale, dacc[1] * oscale, dacc[2] * oscale, dacc[3] * oscale};
            *(LAS f32x2*)(pt + (hi ? 384 + 2 * r32 : 256 + 4 * r32)) = (f32x2){dacc[4] * oscale, dacc[5] * oscale};
            *(LAS f32x2*)(pt + (hi ? 448 + 2 * r32 : 258 + 4 * r32)) = (f32x2){dacc[6] * oscale, dacc[7] * oscale};
            pt[hi ? 640 + r32 : 512 + r32] = dacc[8] * oscale;
            pt[hi ? 672 + r32 : 544 + r32] = dacc[9] * oscale;
            pt[hi ? 672 + r32 : 576 + r32] = dacc[10] * oscale;
            pt[hi ? 672 + r32 : 608 + r32] = dacc[11] * oscale;
        }
        LAS unsigned* ps_ = sy + SY_P + 5 * cc;
        arrive(ps_ + 0);
        LAS char* kt = cb + KOF;
        await(ps_ + 0, 2);
#pragma unroll
        for (int ps = 0; ps < 2; ++ps) {
            const int v = 81 * hh + 64 * ps + lane;
            if (ps == 0 || lane < 17) {
                const f32x4 p0 = *(LAS const f32x4*)(cb + v * 16), p1 = *(LAS const f32x4*)(cb + TABW + v * 16);
                u32x2 r = {pk_bf16(p0[0] + p1[0], p0[1] + p1[1]), pk_bf16(p0[2] + p1[2], p0[3] + p1[3])};
                if (v == 161) r = (u32x2){0u, 0u};
                *(LAS u32x2*)(kt + v * 8) = r;
            }
        }
        arrive(ps_ + 1);
        { const u64 ms = __ballot(utv != tag); if (ms) wait_flags<8>(ws, utf, tag, ms, ms); }
        CFENCE();
        const unsigned ub = WS_UT + (unsigned)(ch * S + 1024 * hh) * 2 + voff;
        const u32x4 u0 = BLD(ub), u1 = BLD(ub + 1024); SBAR();
        await(ps_ + 1, 2);
        __builtin_amdgcn_s_setprio(1);
        {
            unsigned W[2][6];
#pragma unroll
            for (int ps = 0; ps < 2; ++ps)
#pragma unroll
                for (int i = 0; i < 6; ++i) { const unsigned gv = gi[ps][i >> 1][i & 1];
                    W[ps][i] = *(LAS const uint16_t*)(kt + (gv & 0xffffu));
                    if (i < 5) W[ps][i] |= (unsigned)*(LAS const uint16_t*)(kt + (gv >> 16)) << 16; }
            if (lane < 8) *(LAS u32x4*)(cb + (2 * hh + (lane >> 2)) * CPY + (256 + (lane & 3)) * 16) = z4;
#pragma unroll
            for (int ps = 0; ps < 2; ++ps) {
                unsigned V[5];
#pragma unroll
                for (int i = 0; i < 5; ++i) V[i] = __builtin_amdgcn_alignbit(W[ps][i + 1], W[ps][i], 16);
                LAS char* cp = cb + (128 * hh + 64 * ps + lane) * 16;
                *(LAS u32x4*)(cp) = (u32x4){W[ps][0], W[ps][1], W[ps][2], W[ps][3]};
                *(LAS u32x4*)(cp + CPY) = (u32x4){V[0], V[1], V[2], V[3]};
                *(LAS u32x4*)(cp + 2 * CPY) = (u32x4){W[ps][1], W[ps][2], W[ps][3], W[ps][4]};
                *(LAS u32x4*)(cp + 3 * CPY) = (u32x4){V[1], V[2], V[3], V[4]};
            }
        }
        *(LAS u32x4*)(ui + (32 + 32 * hh + (lane >> 2)) * 80 + (lane & 3) * 16) = u0;
        *(LAS u32x4*)(ui + (48 + 32 * hh + (lane >> 2)) * 80 + (lane & 3) * 16) = u1;
        arrive(ps_ + 2); await(ps_ + 2, 2);
        __builtin_amdgcn_s_setprio(0);
        f32x16 a0 = {}, a1 = {};
        {
            const int sb = 7 - (r32 & 7);
            LAS const char* pa = cb + (sb & 3) * CPY + (255 - (r32 >> 3) + hi) * 16 + (sb >> 2) * 8 - 64 * (16 * hh + 15) - 2048;
            LAS const char* pa2 = pa + 8; asm volatile("" : "+v"(pa2));
            LAS const char* pb = ui + (32 + r32 - 16 * hh - 15) * 80 + 16 * hi;
            s16x4 ta[2][2], tb[2][2]; bf16x8 b0[2], b1[2];
#define T4(q, off) (*(LAS const s16x4*)((q) + (off)))
#define CSTEP(i, j, RD) do { const int o_ = 64 * (15 - ((j) >> 1)) + 32 * ((j) & 1), u_ = 80 * (15 - ((j) >> 1)) + 32 * ((j) & 1); \
                a1 = MFMA_BF(cat8(ta[i][0], ta[i][1]), b1[i], a1); SBAR(); if (RD) { ta[1 - i][0] = T4(pa, o_ + 2048); ta[1 - i][1] = T4(pa2, o_ + 2048); } SBAR(); \
                a0 = MFMA_BF(cat8(ta[i][0], ta[i][1]), b0[i], a0); SBAR(); if (RD) { b1[1 - i] = FR(pb, u_ + 2560); b0[1 - i] = FR(pb, u_); } SBAR(); \
                a1 = MFMA_BF(cat8(tb[i][0], tb[i][1]), b0[i], a1); SBAR(); if (RD) { tb[1 - i][0] = T4(pa, o_); tb[1 - i][1] = T4(pa2, o_); } SBAR(); } while (0)
            ta[0][0] = T4(pa, 960 + 2048); ta[0][1] = T4(pa2, 960 + 2048); b1[0] = FR(pb, 1200 + 2560); b0[0] = FR(pb, 1200); tb[0][0] = T4(pa, 960); tb[0][1] = T4(pa2, 960);
#pragma unroll
            for (int j = 0; j < 32; ++j) {
                if (j == 31) arrive(ps_ + 3);
                CSTEP(j & 1, j + 1, j < 31);
                LOADWF(j);
                SBAR();
            }
        }
        __builtin_amdgcn_s_setprio(1);
        {
            LAS char* ex = cb + hh * 9216 + r32 * 144 + hi * 16;
            await(ps_ + 3, 2);
#pragma unroll
            for (int g = 0; g < 4; ++g) {
                *(LAS f32x4*)(ex + g * 32) = (f32x4){a0[4 * g], a0[4 * g + 1], a0[4 * g + 2], a0[4 * g + 3]};
                *(LAS f32x4*)(ex + 4608 + g * 32) = (f32x4){a1[4 * g], a1[4 * g + 1], a1[4 * g + 2], a1[4 * g + 3]};
            }
            arrive(ps_ + 4);
            const float dsk = cc ? bf_hi(dw) : bf_lo(dw);
            u32x4 sk[2];
#pragma unroll
            for (int e = 0; e < 4; ++e) { sk[0][e] = pk_bf16(bf_lo(u0[e]) * dsk, bf_hi(u0[e]) * dsk); sk[1][e] = pk_bf16(bf_lo(u1[e]) * dsk, bf_hi(u1[e]) * dsk); }
            PIN(sk[0]); PIN(sk[1]);
            await(ps_ + 4, 2);
            LAS const char* er = cb + (32 * hh + (lane >> 2)) * 144 + (lane & 3) * 32;
            const unsigned bo = WS_BT + (unsigned)(ch * S + 1024 * hh) * 2 + voff;
#pragma unroll
            for (int i = 0; i < 2; ++i) {
                const f32x4 y0 = *(LAS const f32x4*)(er + i * 2304) + *(LAS const f32x4*)(er + i * 2304 + 9216);
                const f32x4 y1 = *(LAS const f32x4*)(er + i * 2304 + 16) + *(LAS const f32x4*)(er + i * 2304 + 9216 + 16);
                const u32x4 yb = {pk_bf16(y0[0], y0[1]), pk_bf16(y0[2], y0[3]), pk_bf16(y1[0], y1[1]), pk_bf16(y1[2], y1[3])};
                u32x4 r;
#pragma unroll
                for (int e = 0; e < 4; ++e) r[e] = pk_bf16(bf_lo(yb[e]) + bf_lo(sk[i][e]), bf_hi(yb[e]) + bf_hi(sk[i][e]));
                BST(r, bo + i * 1024);
            }
        }
        arrive(sy + SY_BG);
        drain();
        put_flag(ws, FL_BT + (unsigned)(xcd * 128 + bi * 4 + w) * FS, tag);
    } else {
        {
            const unsigned nb = (bi & 1) ? 12 * 2 * D : 16 * 2 * D;
            unsigned v4[4];
#pragma unroll
            for (int i = 0; i < 4; ++i) { const unsigned o = (unsigned)(w * 4096 + i * 1024) + voff; v4[i] = (o < nb ? o : 0u) + 4096 - i * 1024; }
            glds4((const char*)(wo + (DH * (bi >> 1) + 16 * (bi & 1)) * D) - 4096, v4[0], v4[1], v4[2], v4[3], lds0 + LB + w * 4096);
        }
        const u64 e0 = __hip_atomic_load((GAS u64*)ws, __ATOMIC_RELAXED, __HIP_MEMORY_SCOPE_AGENT);
        drain(); asm volatile("" :: "v"(tc));
        tag = mix(e0);
        arrive(sy + SY_WO); await(sy + SY_WO, 4);
        set_tag(sy, tag);
        LAS const char* p = lds + LB + (4 * hi + ((lane & 15) >> 2)) * (2 * D) + ((lane >> 4) & 1) * 32 + (lane & 3) * 8;
#pragma unroll
        for (int i = 0; i < 4; ++i) {
            const int ct = w + 4 * i;
            if (ct < 14) {
                u32x4 o = __builtin_bit_cast(u32x4, cat8(trread(p + ct * 64), trread(p + 8 * 2 * D + ct * 64)));
                if (hi && (bi & 1)) { o[2] = 0; o[3] = 0; }
                BST(o, WS_WO + (unsigned)(bi * 14 + ct) * 1024 + voff);
            }
        }
        drain();
        put_flag(ws, FL_WO + (unsigned)(w * 32 + bi) * FS, tag);
        __builtin_amdgcn_s_setprio(0);
        arrive(sy + SY_BG);
        await_slow(sy + SY_G, 4);
        wait_flags<8>(ws, FL_WO + (unsigned)(w * 32 + r32) * FS, tag, 0xffffffffull, 0xffffffffull);
        if (ct < 14) {
#pragma unroll
        for (int i = 0; i < 32; ++i) { LOADWF(i); __builtin_amdgcn_s_sleep(2); SBAR(); } }
    }
    const __amdgpu_buffer_rsrc_t ro = __builtin_amdgcn_make_buffer_rsrc(out, 0, S * 2 * D * 2, 0x00020000);
    await(sy + SY_BG, 4);
    CODE_PF();
    await_slow(sy + SY_X, 4);
    wait_flags<2>(ws, FL_O + (unsigned)(qt * 16 + 4 * w + (lane & 3)) * FS, tag, 15, 15);
    asm volatile("" :: "v"(pf));
    u32x4 of[8];
#pragma unroll
    for (int i = 0; i < 8; ++i) of[i] = BLD(WS_O + (unsigned)(qt * 32 + 8 * w + i) * 1024 + voff);
#pragma unroll
    for (int i = 0; i < 8; ++i) *(LAS u32x4*)(lds + LB + (8 * w + i) * 1024 + voff) = of[i];
    arrive(sy + SY_B2);
    f32x16 acc = {};
    if (ct < 14) {
#pragma unroll
        for (int i = 0; i < 8; ++i) acc = MFMA_BF(wf[i], BC8(of[i]), acc);
    }
    SBAR();
    await(sy + SY_B2, 4);
    if (ct < 14) {
        LAS const char* po[3];
#pragma unroll
        for (int j = 0; j < 3; ++j) po[j] = lds + LB + ((w + 1 + j) & 3) * 8192 + voff;
        bf16x8 og[4];
#pragma unroll
        for (int i = 0; i < 3; ++i) og[i] = FR(po[0], i * 1024);
#pragma unroll
        for (int i = 0; i < 24; ++i) { if (i < 21) og[(i + 3) & 3] = FR(po[(i + 3) >> 3], ((i + 3) & 7) * 1024); SBAR(); acc = MFMA_BF(wf[8 + i], og[i & 3], acc); SBAR(); }
        LAS char* sc = lds + LB + 40960 + w * 2304;
#pragma unroll
        for (int g = 0; g < 4; ++g) *(LAS u32x2*)(sc + r32 * 72 + 16 * g + 8 * hi) = (u32x2){pk_bf16(acc[4 * g], acc[4 * g + 1]), pk_bf16(acc[4 * g + 2], acc[4 * g + 3])};
        LAS const char* sr = sc + (lane >> 2) * 72 + (lane & 3) * 16;
        const u32x2 a0 = *(LAS const u32x2*)sr, a1 = *(LAS const u32x2*)(sr + 8), b0 = *(LAS const u32x2*)(sr + 16 * 72), b1 = *(LAS const u32x2*)(sr + 16 * 72 + 8);
        const unsigned oo = (unsigned)((32 * qt + (lane >> 2)) * 2 * D + 32 * ct) * 2 + (lane & 3) * 16;
        __builtin_amdgcn_raw_buffer_store_b128((u32x4){a0[0], a0[1], a1[0], a1[1]}, ro, oo, 0, 16);
        __builtin_amdgcn_raw_buffer_store_b128((u32x4){b0[0], b0[1], b1[0], b1[1]}, ro, oo + 16 * 2 * D * 2, 0, 16);
    }
    if (b == 0 && w == 0) put_flag(ws, 0, tag);
}

__global__ __launch_bounds__(512, 2) void fused(const uint16_t* __restrict__ x, const uint16_t* __restrict__ wqkv, const uint16_t* __restrict__ wo,
                                               const uint16_t* __restrict__ u, const uint16_t* __restrict__ lar, const uint16_t* __restrict__ aim,
                                               const uint16_t* __restrict__ cre, const uint16_t* __restrict__ cim, const uint16_t* __restrict__ ldt,
                                               const uint16_t* __restrict__ dskip, uint16_t* __restrict__ out, char* __restrict__ ws) {
    extern __shared__ __attribute__((aligned(16))) char smem[];
    LAS char* lds = (LAS char*)smem;
    const int lane = threadIdx.x & 63, w = __builtin_amdgcn_readfirstlane(threadIdx.x >> 6);
    const u64 code0 = __builtin_amdgcn_s_getpc() & ~255ull;
    unsigned pe = 0;
    asm volatile("global_load_dword %0, %1, %2" : "+v"(pe) : "v"(min((blockIdx.x & ~7u) + (threadIdx.x >> 6), CODE_LINES - 1) * 128), "s"(code0) : "memory");
    SBAR();
    if (w >= 4) __builtin_amdgcn_s_setprio(3);
    if (w == 4) ((LAS unsigned*)(lds + LSY))[lane] = 0;
    asm volatile("" :: "s"(x), "s"(wqkv), "s"(wo), "s"(u), "s"(lar), "s"(aim), "s"(cre), "s"(cim), "s"(ldt), "s"(dskip), "s"(out), "s"(ws));
    asm volatile("s_waitcnt lgkmcnt(0)\n\ts_barrier" ::: "memory");
    if (__builtin_expect(w < 4, 1)) role_a(x, wqkv, out, ws, lds, w, lane, pe);
    else role_b(wo, u, lar, aim, cre, cim, ldt, dskip, out, ws, lds, w - 4, lane, code0, pe);
}
}

extern "C" void kernel_launch(void* const* d_in, const int* in_sizes, int n_in, void* d_out, int out_size, void* d_ws, size_t ws_size, hipStream_t stream) {
    using namespace k;
    (void)hipFuncSetAttribute((const void*)fused, hipFuncAttributeMaxDynamicSharedMemorySize, LDS_BYTES);
    const uint16_t* const* in = (const uint16_t* const*)d_in;
    fused<<<256, 512, LDS_BYTES, stream>>>(in[0], in[1], in[2], in[3], in[4], in[5], in[6], in[7], in[8], in[9], (uint16_t*)d_out, (char*)d_ws);
}
```

```cpp
#include <hip/hip_runtime.h>
#include <hip/hip_bf16.h>
#include <stdint.h>

typedef short bf16x8 __attribute__((ext_vector_type(8)));
typedef _Float16 f16x8 __attribute__((ext_vector_type(8)));
typedef _Float16 f16x2 __attribute__((ext_vector_type(2)));
typedef __bf16 bf16x2 __attribute__((ext_vector_type(2)));
typedef short s16x4 __attribute__((ext_vector_type(4)));
typedef float f32x16 __attribute__((ext_vector_type(16)));
typedef float f32x4 __attribute__((ext_vector_type(4)));
typedef float f32x2 __attribute__((ext_vector_type(2)));
typedef unsigned u32x4 __attribute__((ext_vector_type(4)));
typedef unsigned u32x2 __attribute__((ext_vector_type(2)));
typedef unsigned long long u64;
#define LAS __attribute__((address_space(3)))
#define GAS __attribute__((address_space(1)))
#define DI __device__ __forceinline__
#define MFMA_BF(a, b, c) __builtin_amdgcn_mfma_f32_32x32x16_bf16((a), (b), (c), 0, 0, 0)
#define MFMA_F16(a, b, c) __builtin_amdgcn_mfma_f32_32x32x16_f16((a), (b), (c), 0, 0, 0)
#define SBAR() __builtin_amdgcn_sched_barrier(0)
#define CFENCE() asm volatile("" ::: "memory")
#define PIN(x) asm volatile("" : "+v"(x))
#define BC8(x) __builtin_bit_cast(bf16x8, (x))
#define EX(x) __builtin_amdgcn_exp2f(x)

namespace k {
constexpr int S = 2048, D = 448, DH = 28, NS = 256;
constexpr unsigned FS = 128;
constexpr unsigned FL_KV = FS, FL_O = FL_KV + 1024 * FS, FL_UT = FL_O + 1024 * FS, FL_BT = FL_UT + 896 * FS, FL_WO = FL_BT + 896 * FS;
constexpr unsigned WS_KV = 512u << 10;
constexpr unsigned WS_O = WS_KV + (4u << 20);
constexpr unsigned WS_WO = WS_O + (2u << 20);
constexpr unsigned WS_UT = WS_WO + (448u << 10);
constexpr unsigned WS_BT = WS_UT + D * S * 2;
constexpr unsigned WS_END = WS_BT + D * S * 2;
constexpr int WSL = 8192, LWQ = 4 * WSL, LXR = LWQ + 16384, XSL = 16384, LB = LXR + 3 * XSL;
constexpr int LQ = LXR, LX = 0;
constexpr int TROW = 272, TABW = 33 * TROW, CPY = 4160, UOF = 2 * TABW, KOF = UOF + 7680, CBW = KOF + 1312;
constexpr int LT1 = LB + 2 * CBW, LSY = LT1 + 8192, LDS_BYTES = LSY + 256;
static_assert(LDS_BYTES <= 163840 && (LSY & 15) == 0 && 4 * CPY <= UOF && LB + 40960 + 4 * 2304 <= LSY, "lds");
enum { SY_F = 2, SY_P = 10, SY_WO = 20, SY_BG = 22, SY_B2 = 23, SY_Q = 24, SY_X = 25, SY_G = 26, SY_T2 = 27 };
constexpr int SPIN = 1 << 18;
constexpr unsigned CODE_LINES = 216;

struct IdxTab { uint16_t v[256][12]; };
constexpr IdxTab make_idx() {
    IdxTab t = {};
    for (int y = 0; y < 256; ++y) for (int e = 0; e < 12; ++e) {
        const int j = 2047 - 8 * y - e; int ix = 644;
        if (j >= 0) { const int sh = j >= 1024 ? 3 : (j >= 512 ? 2 : (j >= 256 ? 1 : 0));
            ix = sh ? 128 * sh + ((j + (1 << (sh - 1)) - 1 + ((j >> sh) & 1)) >> sh) : j; }
        t.v[y][e] = (uint16_t)(2 * ix);
    }
    return t;
}
__device__ const IdxTab g_idx __attribute__((aligned(16))) = make_idx();

DI unsigned pk_bf16(float a, float b) { f32x2 v = {a, b}; return __builtin_bit_cast(unsigned, __builtin_convertvector(v, bf16x2)); }
DI unsigned pk_f16(f32x2 v) { return __builtin_bit_cast(unsigned, __builtin_convertvector(v, f16x2)); }
DI float bf_lo(unsigned w) { return __uint_as_float(w << 16); }
DI float bf_hi(unsigned w) { return __uint_as_float(w & 0xffff0000u); }
DI float rbf(float x) { return bf_lo(pk_bf16(x, x)); }
#define GL0 "s_mov_b32 %0, m0\n\ts_mov_b32 m0, %2\n\ts_nop 0\n\t"
#define GL1(V, O, C) "global_load_lds_dwordx4 " V ", %1 offset:" #O C "\n\t"
#define GLE "s_mov_b32 m0, %0"
DI void glds4(const void* sb, unsigned v0, unsigned v1, unsigned v2, unsigned v3, unsigned lb) { unsigned sv;
    asm volatile(GL0 GL1("%3", 0, "") GL1("%4", 1024, "") GL1("%5", 2048, "") GL1("%6", 3072, "") GLE : "=&s"(sv) : "s"(sb), "s"(lb), "v"(v0), "v"(v1), "v"(v2), "v"(v3) : "memory"); }
DI void glds2(const void* sb, unsigned v0, unsigned v1, unsigned lb) { unsigned sv;
    asm volatile(GL0 GL1("%3", 0, "") GL1("%4", 1024, "") GLE : "=&s"(sv) : "s"(sb), "s"(lb), "v"(v0), "v"(v1) : "memory"); }
DI void glds1(const void* sb, unsigned v0, unsigned lb) { unsigned sv;
    asm volatile(GL0 GL1("%3", 0, "") GLE : "=&s"(sv) : "s"(sb), "s"(lb), "v"(v0) : "memory"); }
DI float wave_max(float v) {
#define DPPMAX(ctrl, rm) v = fmaxf(v, __uint_as_float(__builtin_amdgcn_update_dpp(0, __float_as_uint(v), ctrl, rm, 0xf, true)))
    DPPMAX(0x111, 0xf); DPPMAX(0x112, 0xf); DPPMAX(0x114, 0xf); DPPMAX(0x118, 0xf); DPPMAX(0x142, 0xa); DPPMAX(0x143, 0xc);
    return __uint_as_float(__builtin_amdgcn_readlane(__float_as_uint(v), 63));
}
DI s16x4 trread(LAS const char* p) { return __builtin_bit_cast(s16x4, __builtin_amdgcn_ds_read_tr16_b64_v4i16((LAS s16x4*)p)); }
DI bf16x8 cat8(s16x4 a, s16x4 b) { return __builtin_shufflevector(a, b, 0, 1, 2, 3, 4, 5, 6, 7); }
DI f32x2 pfma(f32x2 a, f32x2 b, f32x2 c) { return __builtin_elementwise_fma(a, b, c); }
DI f32x2 cmul(f32x2 a, f32x2 b, f32x2 bs) { return pfma(a.yy, bs, a.xx * b); }
DI f16x8 cmul4h(u32x4 p, u32x4 q) {
    u32x4 t;
#define PM(i, a, b) "v_pk_mul_f16 %" #i ", %" #a ", %" #b " op_sel:[0,0] op_sel_hi:[0,1]\n\t"
#define PF(i, a, b) "v_pk_fma_f16 %" #i ", %" #a ", %" #b ", %" #i " op_sel:[1,1,0] op_sel_hi:[1,0,1] neg_lo:[1,0,0]\n\t"
    asm(PM(0, 4, 8) PM(1, 5, 9) PM(2, 6, 10) PM(3, 7, 11) PF(0, 4, 8) PF(1, 5, 9) PF(2, 6, 10) PF(3, 7, 11) "s_nop 1"
        : "=&v"(t[0]), "=&v"(t[1]), "=&v"(t[2]), "=&v"(t[3]) : "v"(p[0]), "v"(p[1]), "v"(p[2]), "v"(p[3]), "v"(q[0]), "v"(q[1]), "v"(q[2]), "v"(q[3]));
    return __builtin_bit_cast(f16x8, t);
}
DI f32x2 rot(f32x2 b) { return (f32x2){-b.y, b.x}; }
DI f32x2 anchor(float a2, float rev, float s) {
    const float mag = EX(a2 * s);
    float r = rev * s; r -= __builtin_rintf(r);
    return (f32x2){mag * __builtin_amdgcn_cosf(r), mag * __builtin_amdgcn_sinf(r)};
}
DI void arrive(LAS unsigned* c) { asm volatile("s_mov_b64 exec, 1\n\tds_add_u32 %0, %1\n\ts_mov_b64 exec, -1" :: "v"((unsigned)(uintptr_t)c), "v"(1u) : "memory"); }
DI void await(LAS unsigned* c, unsigned n) {
    unsigned t, i = SPIN;
    asm volatile("1:\n\tds_read_b32 %0, %2\n\ts_waitcnt lgkmcnt(0)\n\tv_cmp_gt_u32 vcc, %3, %0\n\ts_cbranch_vccz 2f\n\ts_sleep 1\n\ts_sub_u32 %1, %1, 1\n\ts_cbranch_scc0 1b\n2:"
                 : "=&v"(t), "+s"(i) : "v"((unsigned)(uintptr_t)c), "s"(n) : "vcc", "scc", "memory");
}
DI void await_slow(LAS unsigned* c, unsigned n) {
    unsigned t, i = SPIN;
    asm volatile("1:\n\tds_read_b32 %0, %2\n\ts_waitcnt lgkmcnt(0)\n\tv_cmp_gt_u32 vcc, %3, %0\n\ts_cbranch_vccz 2f\n\ts_sleep 3\n\ts_sub_u32 %1, %1, 1\n\ts_cbranch_scc0 1b\n2:"
                 : "=&v"(t), "+s"(i) : "v"((unsigned)(uintptr_t)c), "s"(n) : "vcc", "scc", "memory");
}
DI u64 get_tag(LAS unsigned* sy) {
    await(sy, 1);
    const u64 v = __hip_atomic_load((LAS u64*)sy, __ATOMIC_RELAXED, __HIP_MEMORY_SCOPE_WORKGROUP);
    return ((u64)(unsigned)__builtin_amdgcn_readfirstlane((unsigned)(v >> 32)) << 32) | (unsigned)__builtin_amdgcn_readfirstlane((unsigned)v);
}
DI void set_tag(LAS unsigned* sy, u64 tag) { asm volatile("s_mov_b64 exec, 1\n\tds_write_b64 %0, %1\n\ts_mov_b64 exec, -1" :: "v"((unsigned)(uintptr_t)sy), "v"(tag) : "memory"); }
DI void drain() { asm volatile("s_waitcnt vmcnt(0)" ::: "memory"); }
DI void put_flag(char* ws, unsigned off, u64 tag) {
    asm volatile("s_mov_b64 exec, 1\n\tglobal_store_dwordx2 %0, %1, %2 sc1\n\ts_mov_b64 exec, -1" :: "v"(off), "v"(tag), "s"(ws) : "memory");
}
template <int SLP> DI u64 wait_flags(char* ws, unsigned off, u64 tag, u64 pend, u64 need) {
    u64 t, left; unsigned i = SPIN;
    asm volatile("s_mov_b64 exec, %5\n1:\n\tglobal_load_dwordx2 %0, %3, %4 sc1\n\ts_waitcnt vmcnt(0)\n\tv_cmpx_ne_u64 vcc, %6, %0\n\ts_and_b64 %2, exec, %7\n\t"
                 "s_cbranch_scc0 2f\n\ts_sleep %8\n\ts_sub_u32 %1, %1, 1\n\ts_cbranch_scc0 1b\n2:\n\ts_mov_b64 %2, exec\n\ts_mov_b64 exec, -1"
                 : "=&v"(t), "+s"(i), "=&s"(left) : "v"(off), "s"(ws), "s"(pend), "s"(tag), "s"(need), "i"(SLP) : "vcc", "scc", "memory");
    return left;
}
#define BST(v, off) __builtin_amdgcn_raw_buffer_store_b128((v), rs, (off), 0, 16)
#define BLD(off) __builtin_amdgcn_raw_buffer_load_b128(rs, (off), 0, 16)
#define FR(p, off) (*(LAS const bf16x8*)((p) + (off)))
DI float swapmax(float m) { const auto r = __builtin_amdgcn_permlane32_swap(__float_as_uint(m), __float_as_uint(m), false, false); return fmaxf(__uint_as_float(r[0]), __uint_as_float(r[1])); }
DI u32x4 put_ref(u32x4 q1, float m, int hi) {
    const float m1 = rbf(m), m2 = rbf(m - m1), m3 = m - m1 - m2;
    if (hi) { q1[2] = pk_bf16(-m1, -m2); q1[3] = pk_bf16(-m3, 0.f); }
    return q1;
}
DI u64 mix(u64 v) {
    const u64 e = ((u64)(unsigned)__builtin_amdgcn_readfirstlane((unsigned)(v >> 32)) << 32) | (unsigned)__builtin_amdgcn_readfirstlane((unsigned)v);
    u64 z = e + 0x9E3779B97F4A7C15ull; z = (z ^ (z >> 30)) * 0xBF58476D1CE4E5B9ull; z = (z ^ (z >> 27)) * 0x94D049BB133111EBull; return (z ^ (z >> 31)) | 1ull; }
DI void transpose_tile(LAS const char* stage, LAS char* scratch, int w, int lane, u32x4& o0, u32x4& o1) {
    LAS const char* p = stage + (lane >> 4) * 2048 + ((lane & 15) >> 2) * 128 + w * 32 + (lane & 3) * 8;
    const bf16x8 t0 = cat8(trread(p), trread(p + 512)), t1 = cat8(trread(p + 1024), trread(p + 1536));
    LAS char* sc = scratch + (lane & 15) * 144 + (lane >> 4) * 32;
    *(LAS bf16x8*)sc = t0; *(LAS bf16x8*)(sc + 16) = t1;
    LAS const char* sr = scratch + (lane >> 3) * 144 + (lane & 7) * 16;
    o0 = *(LAS const u32x4*)sr; o1 = *(LAS const u32x4*)(sr + 8 * 144);
}

DI void role_a(const uint16_t* __restrict__ x, const uint16_t* __restrict__ wqkv, uint16_t* __restrict__ out, char* __restrict__ ws, LAS char* lds, int w, int lane) {
    const int head = 2 * (blockIdx.x & 7) + ((blockIdx.x >> 3) & 1), grp = blockIdx.x >> 4;
    const unsigned lds0 = (unsigned)(uintptr_t)lds;
    const int r32 = lane & 31, hi = lane >> 5;
    const int wsh = (head & 7) == 7 ? 4 : 0;
    const unsigned wv = (unsigned)((lane >> 2) * 3 * D + (lane & 3) * 8) * 2;
    const char* wb = (const char*)(wqkv + DH * head - wsh + (16 * w) * 3 * D);
    const unsigned xv0 = (unsigned)((lane >> 3) * D + (((lane & 7) ^ (lane >> 4)) * 8)) * 2, xv1 = xv0 ^ 64;
    const char* xb = (const char*)(x + (128 * grp + 32 * w) * D);
    const int rho = ((blockIdx.x >> 3) + ((blockIdx.x >> 1) & 3)) % 7;
#define SP(st) ((st) + rho >= 7 ? (st) + rho - 7 : (st) + rho)
#define WQO(st) ((st) < 4 ? LWQ + (st) * 4096 : ((st) < 6 ? 3 * WSL + ((st) - 4) * 4096 : 0))
#define LOADW(st) glds2(wb + SP(st) * (64 * 3 * D * 2), wv + 2 * D, wv + 4 * D - 1024, lds0 + ((st) & 3) * WSL + w * 2048)
#define LOADQ(st) glds1(wb + SP(st) * (64 * 3 * D * 2), wv, lds0 + WQO(st) + w * 1024)
#define LOADX(st) glds4(xb + SP(st) * 128, xv0, xv1 + 16 * D - 1024, xv0 + 32 * D - 2048, xv1 + 48 * D - 3072, lds0 + LXR + ((st) % 3) * XSL + w * 4096)
    LOADX(0); LOADW(0);
    LOADX(1); LOADW(1);
    LAS unsigned* sy = (LAS unsigned*)(lds + LSY);
    const __amdgpu_buffer_rsrc_t rs = __builtin_amdgcn_make_buffer_rsrc(ws, 0, WS_END, 0x00020000);
    const int xs = (r32 >> 1) & 7;
    LAS const char* xp[4];
#pragma unroll
    for (int ks = 0; ks < 4; ++ks) xp[ks] = lds + LXR + w * 4096 + r32 * 128 + (((2 * ks + hi) ^ xs) * 16);
    LAS const char* wp = lds + (8 * hi + ((lane & 15) >> 2)) * 64 + ((lane >> 4) & 1) * 32 + (lane & 3) * 8 + wsh * 2;
    f32x16 aq = {}, ak = {}, av = {};
    bf16x8 xf[7][4]; s16x4 fr[4][4];
#define RX(st, ks) xf[st][ks] = FR(xp[ks], ((st) % 3) * XSL)
#define RW(st, ks, j) fr[ks][j] = trread(wp + ((st) & 3) * WSL + (ks) * 2048 + ((j) >> 1) * 1024 + ((j) & 1) * 256)
#define MK(st, ks) ak = MFMA_BF(cat8(fr[ks][0], fr[ks][1]), xf[st][ks], ak); SBAR()
#define MV(st, ks) av = MFMA_BF(xf[st][ks], cat8(fr[ks][2], fr[ks][3]), av); SBAR()
#define QSTEP(st, ks) MK(st, ks); RW(st, ks + 1, 0); RW(st, ks + 1, 1); SBAR(); MV(st, ks); RW(st, ks + 1, 2); RW(st, ks + 1, 3); SBAR()
    u64 tag = 0;
    asm volatile("s_waitcnt vmcnt(6)" ::: "memory");
    arrive(sy + SY_F); LOADW(2); LOADX(2); await(sy + SY_F, 4);
    RX(0, 0); RW(0, 0, 0); RW(0, 0, 1); RW(0, 0, 2); RW(0, 0, 3); RX(0, 1); RX(0, 2); RX(0, 3); SBAR();
#pragma unroll
    for (int st = 0; st < 7; ++st) {
        QSTEP(st, 0); if (st < 4) LOADW(st + 3); if (st == 4) { LOADQ(4); LOADQ(5); } if (st == 5) LOADQ(6);
        QSTEP(st, 1); if (st < 4) LOADX(st + 3);
        QSTEP(st, 2); if (st == 3) { LOADQ(0); LOADQ(1); LOADQ(2); LOADQ(3); }
        if (st < 6) {
            if (st == 3) asm volatile("s_waitcnt vmcnt(16)" ::: "memory"); else if (st == 5) asm volatile("s_waitcnt vmcnt(7)" ::: "memory"); else asm volatile("s_waitcnt vmcnt(12)" ::: "memory");
            arrive(sy + SY_F + 1 + st); SBAR();
        }
        MK(st, 3);
        if (st < 6) { await(sy + SY_F + 1 + st, 4); RX(st + 1, 0); RW(st + 1, 0, 0); RW(st + 1, 0, 1); RW(st + 1, 0, 2); RW(st + 1, 0, 3); } SBAR();
        MV(st, 3); if (st < 6) { RX(st + 1, 1); RX(st + 1, 2); RX(st + 1, 3); } SBAR();
        if (st == 2) tag = get_tag(sy);
    }
    const float cq = 0.18898223650461362f * 1.4426950408889634f;
    const unsigned voff = lane * 16;
    u32x4 q0, q1, k0, k1, v0, v1;
    const unsigned tb = WS_KV + (unsigned)(head * 64 + 4 * grp + w) * 4096 + voff;
#pragma unroll
    for (int i = 0; i < 4; ++i) { k0[i] = pk_bf16(ak[2 * i], ak[2 * i + 1]); k1[i] = pk_bf16(ak[8 + 2 * i], ak[9 + 2 * i]); }
    if (hi) { k1[2] = 0x3f803f80u; k1[3] = 0x00003f80u; }
    BST(k0, tb); BST(k1, tb + 1024);
#pragma unroll
    for (int i = 0; i < 4; ++i) {
        v0[i] = pk_bf16(av[2 * i], av[2 * i + 1]); v1[i] = pk_bf16(av[8 + 2 * i], av[9 + 2 * i]);
        if (r32 >= DH) { v0[i] = v1[i] = (r32 == DH ? 0x3f803f80u : 0u); }
    }
    BST(v0, tb + 2048); BST(v1, tb + 3072);
    asm volatile("s_waitcnt vmcnt(4)" ::: "memory");
    arrive(sy + SY_F + 7); await(sy + SY_F + 7, 4);
    {
        s16x4 fq[4][2];
#define RQ(j) do { fq[(j) & 3][0] = trread(wp + WQO((j) >> 2) + ((j) & 3) * 1024); fq[(j) & 3][1] = trread(wp + WQO((j) >> 2) + ((j) & 3) * 1024 + 256); } while (0)
        RQ(0); RQ(1); RQ(2); SBAR();
#pragma unroll
        for (int j = 0; j < 28; ++j) {
            aq = MFMA_BF(cat8(fq[j & 3][0], fq[j & 3][1]), xf[j >> 2][j & 3], aq); SBAR(); if (j < 25) RQ(j + 3); SBAR();
            if (j == 13) { drain(); put_flag(ws, FL_KV + (unsigned)((head * 4 + w) * 16 + grp) * FS, tag); }
        }
    }
#pragma unroll
    for (int i = 0; i < 4; ++i) { q0[i] = pk_bf16(aq[2 * i] * cq, aq[2 * i + 1] * cq); q1[i] = pk_bf16(aq[8 + 2 * i] * cq, aq[9 + 2 * i] * cq); }
    if (hi) { q1[2] = 0; q1[3] = 0; }
    const unsigned kvf = FL_KV + (unsigned)((head * 4 + w) * 16 + ((grp + lane) & 15)) * FS;
    const f32x16 zero = {};
    {
        f32x16 s = MFMA_BF(BC8(k0), BC8(q0), zero); s = MFMA_BF(BC8(k1), BC8(q1), s);
        float m = s[0];
#pragma unroll
        for (int i = 1; i < 16; ++i) m = fmaxf(m, s[i]);
        q1 = put_ref(q1, swapmax(m), hi);
    }
    { LAS u32x4* qo = (LAS u32x4*)(lds + LQ + w * 2048) + lane; qo[0] = q0; qo[64] = q1; }
    arrive(sy + SY_Q);
    const u64 kv1 = __hip_atomic_load((GAS u64*)(ws + kvf), __ATOMIC_RELAXED, __HIP_MEMORY_SCOPE_AGENT); SBAR();
    f32x16 O[4] = {}; PIN(O[0]); PIN(O[1]); PIN(O[2]); PIN(O[3]);
    await(sy + SY_Q, 4);
    bf16x8 Q[4][2];
    Q[0][0] = BC8(q0); Q[0][1] = BC8(q1);
#pragma unroll
    for (int i = 1; i < 4; ++i) { LAS const char* p = lds + LQ + ((w + i) & 3) * 2048 + voff; Q[i][0] = FR(p, 0); Q[i][1] = FR(p, 1024); }
    u32x4 kv[4][4] = {{k0, k1, v0, v1}, {}, {}, {}};
    f32x16 sa, sb;
    u32x4 p0, p1 = {};
#define GRP(SC, i, P, j) do { float e0 = EX(SC[i]); SBAR(); float e1 = EX(SC[i + 1]); SBAR(); float e2 = EX(SC[i + 2]); SBAR(); float e3 = EX(SC[i + 3]); SBAR(); \
        P[j] = pk_bf16(e0, e1); SBAR(); P[j + 1] = pk_bf16(e2, e3); PIN(P); SBAR(); } while (0)
#define SLOT(SC, SN, cb, i, QK, PV1, LD) do { constexpr int nb_ = (i) < 3 ? (cb) : ((cb) + 1) % 4, pb_ = (i) > 0 ? (cb) : ((cb) + 3) % 4; \
        if (QK) SN = MFMA_BF(BC8(kv[nb_][0]), Q[((i) + 1) & 3][0], zero); SBAR(); GRP(SC, 0, p0, 0); \
        if (PV1) O[((i) + 3) & 3] = MFMA_BF(BC8(kv[pb_][3]), BC8(p1), O[((i) + 3) & 3]); SBAR(); \
        if (LD) { kv[((cb) + 3) % 4][i] = __builtin_amdgcn_raw_buffer_load_b128(rs, voff + (i) * 1024, kvo, 16); SBAR(); } GRP(SC, 4, p0, 2); \
        if (QK) SN = MFMA_BF(BC8(kv[nb_][1]), Q[((i) + 1) & 3][1], SN); SBAR(); GRP(SC, 8, p1, 0); \
        O[i] = MFMA_BF(BC8(kv[cb][2]), BC8(p0), O[i]); SBAR(); GRP(SC, 12, p1, 2); } while (0)
    const unsigned kvb = WS_KV + (unsigned)(head * 64 + w) * 4096;
    unsigned kvo = 0;
    u64 miss = __ballot(kv1 != tag) & 0xfffe;
    if (miss & 6) miss = wait_flags<2>(ws, kvf, tag, miss, 6);
    CFENCE();
#pragma unroll
    for (int j = 1; j < 3; ++j)
#pragma unroll
        for (int f = 0; f < 4; ++f) kv[j][f] = __builtin_amdgcn_raw_buffer_load_b128(rs, voff + f * 1024, kvb + ((grp + j) & 15) * 16384, 16);
    arrive(sy + SY_G);
    sa = MFMA_BF(BC8(k0), Q[0][0], zero); sa = MFMA_BF(BC8(k1), Q[0][1], sa);
    int nx = grp + 3;
#define TILE(cb) do { kvo = kvb + (nx & 15) * 16384; ++nx; SBAR(); \
        SLOT(sa, sb, cb, 0, true, true, true); SLOT(sb, sa, cb, 1, true, true, true); SLOT(sa, sb, cb, 2, true, true, true); SLOT(sb, sa, cb, 3, true, true, true); } while (0)
#pragma unroll 1
    for (unsigned msk = 0x78; msk < 0x80000; msk <<= 4) { if (__builtin_expect((miss & msk) != 0, 0)) miss = wait_flags<2>(ws, kvf, tag, miss, msk); TILE(0); TILE(1); TILE(2); TILE(3); }
    O[3] = MFMA_BF(BC8(kv[3][3]), BC8(p1), O[3]);
    f32x16 o = O[0];
    {
#pragma unroll
        for (int i = 1; i < 4; ++i) {
            LAS char* ex = lds + LX + (((w + i) & 3) * 3 + i - 1) * 4096 + voff;
#pragma unroll
            for (int g = 0; g < 4; ++g) *(LAS f32x4*)(ex + g * 1024) = (f32x4){O[i][4 * g], O[i][4 * g + 1], O[i][4 * g + 2], O[i][4 * g + 3]};
        }
        arrive(sy + SY_X); await(sy + SY_X, 4);
        LAS const char* er = lds + LX + w * 3 * 4096 + voff;
#pragma unroll
        for (int i = 0; i < 3; ++i)
#pragma unroll
            for (int g = 0; g < 4; ++g) { const f32x4 r = *(LAS const f32x4*)(er + i * 4096 + g * 1024);
#pragma unroll
                for (int e = 0; e < 4; ++e) o[4 * g + e] += r[e]; }
        f32x2 chk = {0.f, 0.f};
#pragma unroll
        for (int i = 0; i < 8; ++i) chk = pfma((f32x2){o[2 * i], o[2 * i + 1]}, (f32x2){0.f, 0.f}, chk);
        if (__builtin_expect(__any(!(chk[0] + chk[1] == 0.f) || !(o[12] < 1e30f)), 0)) {
            const unsigned hb = WS_KV + (unsigned)head * 64 * 4096 + voff;
            u32x4 qr = __builtin_bit_cast(u32x4, Q[0][1]); if (hi) { qr[2] = 0; qr[3] = 0; }
            float mm = -INFINITY;
            wait_flags<4>(ws, FL_KV + (unsigned)(head * 64 + lane) * FS, tag, ~0ull, ~0ull);
#pragma unroll 1
            for (int t = 0; t < 64; ++t) {
                f32x16 s = MFMA_BF(BC8(BLD(hb + t * 4096)), Q[0][0], zero); s = MFMA_BF(BC8(BLD(hb + t * 4096 + 1024)), BC8(qr), s);
#pragma unroll
                for (int i = 0; i < 16; ++i) mm = fmaxf(mm, s[i]);
            }
            qr = put_ref(qr, swapmax(mm), hi);
            o = zero;
#pragma unroll 1
            for (int t = 0; t < 64; ++t) {
                f32x16 s = MFMA_BF(BC8(BLD(hb + t * 4096)), Q[0][0], zero); s = MFMA_BF(BC8(BLD(hb + t * 4096 + 1024)), BC8(qr), s);
#pragma unroll
                for (int i = 0; i < 4; ++i) { p0[i] = pk_bf16(EX(s[2 * i]), EX(s[2 * i + 1])); p1[i] = pk_bf16(EX(s[8 + 2 * i]), EX(s[9 + 2 * i])); }
                o = MFMA_BF(BC8(BLD(hb + t * 4096 + 2048)), BC8(p0), o); o = MFMA_BF(BC8(BLD(hb + t * 4096 + 3072)), BC8(p1), o);
            }
        }
    }
    {
        const auto r = __builtin_amdgcn_permlane32_swap(__float_as_uint(o[12]), __float_as_uint(o[12]), false, false);
        const float rl = __builtin_amdgcn_rcpf(__uint_as_float(r[1]));
        u32x4 o0, o1;
#pragma unroll
        for (int i = 0; i < 4; ++i) { o0[i] = pk_bf16(o[2 * i] * rl, o[2 * i + 1] * rl); o1[i] = pk_bf16(o[8 + 2 * i] * rl, o[9 + 2 * i] * rl); }
        if (hi) { o1[2] = 0; o1[3] = 0; }
        const unsigned fi = (unsigned)((4 * grp + w) * 16 + head);
        BST(o0, WS_O + fi * 2048 + voff); BST(o1, WS_O + fi * 2048 + 1024 + voff);
        drain();
        put_flag(ws, FL_O + fi * FS, tag);
    }
    const int xcd = blockIdx.x & 7, bi = blockIdx.x >> 3;
    if (xcd < 7) {
        wait_flags<2>(ws, FL_BT + (unsigned)(xcd * 128 + w * 32 + r32) * FS, tag, 0xffffffffull, 0xffffffffull);
        const unsigned bo = WS_BT + (unsigned)((64 * xcd + 16 * w + (lane >> 3)) * S + 64 * bi + (lane & 7) * 8) * 2;
        const u32x4 b0 = BLD(bo), b1 = BLD(bo + 8 * S * 2);
        *(LAS u32x4*)(lds + 65536 + w * 2048 + voff) = b0; *(LAS u32x4*)(lds + 65536 + w * 2048 + 1024 + voff) = b1;
        arrive(sy + SY_T2); await(sy + SY_T2, 4);
        u32x4 o0, o1; transpose_tile(lds + 65536, lds + 73728 + w * 2304, w, lane, o0, o1);
        const __amdgpu_buffer_rsrc_t ro = __builtin_amdgcn_make_buffer_rsrc(out, 0, S * 2 * D * 2, 0x00020000);
        const unsigned oo = (unsigned)((64 * bi + 16 * w + (lane >> 3)) * 2 * D + D + 64 * xcd) * 2 + (lane & 7) * 16;
        __builtin_amdgcn_raw_buffer_store_b128(o0, ro, oo, 0, 16); __builtin_amdgcn_raw_buffer_store_b128(o1, ro, oo + 8 * 2 * D * 2, 0, 16);
    }
}

DI void role_b(const uint16_t* __restrict__ wo, const uint16_t* __restrict__ u, const uint16_t* __restrict__ lar, const uint16_t* __restrict__ aim,
               const uint16_t* __restrict__ cre, const uint16_t* __restrict__ cim, const uint16_t* __restrict__ ldt, const uint16_t* __restrict__ dskip,
               uint16_t* __restrict__ out, char* __restrict__ ws, LAS char* lds, int w, int lane, u64 code0) {
    u64 tag;
    const unsigned lds0 = (unsigned)(uintptr_t)lds;
    LAS unsigned* sy = (LAS unsigned*)(lds + LSY);
    const __amdgpu_buffer_rsrc_t rs = __builtin_amdgcn_make_buffer_rsrc(ws, 0, WS_END, 0x00020000);
    const int b = blockIdx.x, xcd = b & 7, bi = b >> 3, r32 = lane & 31, hi = lane >> 5;
    const unsigned voff = lane * 16;
    LAS char* tab = lds + LB + (w >> 1) * CBW + (w & 1) * TABW;
    unsigned pf = 0;
#define CODE_PF() asm volatile("global_load_dword %0, %1, %2" : "+v"(pf) : "v"(min((unsigned)((bi * 4 + w) * 3 + (lane < 3 ? lane : 0)), CODE_LINES - 1) * 128), "s"(code0) : "memory")
    const int qt = 8 * xcd + (bi >> 2), ct = 4 * (bi & 3) + w;
    bf16x8 wf[32];
    const unsigned wvo = ct < 14 ? voff : 0x80000000u;
    unsigned wgb[4];
#pragma unroll
    for (int g = 0; g < 4; ++g) wgb[g] = WS_WO + (unsigned)(((w + g) & 3) * 8 * 14 + ct) * 1024;
#define LOADWF(i) wf[i] = BC8(__builtin_amdgcn_raw_buffer_load_b128(rs, wvo, wgb[(i) >> 3] + ((i) & 7) * 14336, 16))
    if (xcd < 7) {
        const int cc = w >> 1, hh = w & 1, pair = 32 * xcd + bi, ch = 2 * pair + cc;
        LAS char* cb = lds + LB + cc * CBW;
        const unsigned po = (unsigned)(ch * NS + 128 * hh) / 2 + lane;
        const unsigned pl = ((const unsigned*)lar)[po], pa = ((const unsigned*)aim)[po], pr = ((const unsigned*)cre)[po], pi = ((const unsigned*)cim)[po];
        const unsigned uo = (unsigned)((64 * bi + 16 * w + (lane >> 3)) * D + 64 * xcd + (lane & 7) * 8) * 2;
        glds2(u, uo, uo + 8 * D * 2 - 1024, lds0 + LT1 + w * 2048);
        const u64 e0 = __hip_atomic_load((GAS u64*)ws, __ATOMIC_RELAXED, __HIP_MEMORY_SCOPE_AGENT);
        u32x2 gi[2][3];
        CODE_PF();
        const unsigned lw = ((const unsigned*)ldt)[pair], dw = ((const unsigned*)dskip)[pair];
        LAS char* ui = cb + UOF;
        const u32x4 z4 = {};
        *(LAS u32x4*)(ui + (64 * hh + lane) * 16) = z4; *(LAS u32x4*)(ui + (96 + lane) * 16) = z4;
        const float dt = rbf(expf(cc ? bf_hi(lw) : bf_lo(lw)));
        drain(); asm volatile("" :: "v"(pf));
        arrive(sy + SY_WO);
        tag = mix(e0);
        const float cm = wave_max(fmaxf(fmaxf(fabsf(bf_lo(pr)), fabsf(bf_hi(pr))), fmaxf(fabsf(bf_lo(pi)), fabsf(bf_hi(pi)))));
        unsigned ce = __float_as_uint(cm) & 0x7f800000u; ce = ce < 0x01000000u ? 0x01000000u : (ce > 0x7e800000u ? 0x7e800000u : ce);
        const float cscale = __uint_as_float(0x7f000000u - ce), oscale = 2.0f * dt * __uint_as_float(ce);
        LAS unsigned* tw = (LAS unsigned*)(tab + lane * 4);
        LAS const char* trd = tab + r32 * TROW + hi * 16;
        f32x16 dacc = {};
        f32x2 z1, z8, z256, x0;
#define ANCH(vl, va, vr, vi) do { const float a2 = (-rbf(expf(vl)) * dt) * 1.4426950408889634f, rev = ((va) * dt) * 0.15915494309189535f; \
            z1 = anchor(a2, -rev, 1.f); z8 = anchor(a2, rev, 8.f); z256 = anchor(a2, -rev, 256.f); x0 = (f32x2){(vr) * cscale, -(vi) * cscale}; \
            PIN(z1); PIN(z8); PIN(z256); SBAR(); } while (0)
#define TW(row) tw[(row) * (TROW / 4)]
#define CHAIN() do { \
            const f32x2 z1s = rot(z1), zs = rot(z256), z8s = rot(z8); \
            f32x2 X[8]; const f32x2 z16 = cmul(z8, z8, z8s); X[0] = x0; X[1] = cmul(x0, z1, z1s); f32x2 a = cmul(x0, z256, zs); \
            const f32x2 z24 = cmul(z16, z8, z8s), z32 = cmul(z16, z16, rot(z16)), z32s = rot(z32); X[2] = cmul(X[1], z1, z1s); f32x2 y = z32; \
            f32x2 c4 = cmul(X[2], z256, zs); \
            unsigned c[2][4], d0 = pk_f16(X[0]), d1 = pk_f16(X[1]), d2 = pk_f16(z8), d3 = pk_f16(z16), d4 = pk_f16(z24), one = 0x3c00u; \
            c[0][0] = pk_f16(y); c[0][1] = pk_f16(X[2]); c[0][2] = pk_f16(a); c[0][3] = pk_f16(c4); \
            TW(0) = d0; TW(1) = d1; TW(30) = d2; TW(31) = d3; TW(32) = d4; TW(21) = one; TW(29) = one; SBAR(); \
            _Pragma("unroll") for (int i = 1; i < 9; ++i) { \
                f32x2 t0, t4, t5, t6; const int j = i - 1; unsigned* pc = c[j & 1]; unsigned* nc = c[i & 1]; \
                if (i < 8) { if (i < 7) t0 = y.xx * z32; if (i < 6) t4 = X[i + 1].xx * z1; t5 = a.xx * z256; \
                    if (i == 3) t6 = X[4].xx * z256; if (i == 4 || i == 5) t6 = c4.xx * z256; if (i == 6) t6 = X[6].xx * z256; } SBAR(); \
                if (j < 7) TW(22 + j) = pc[0]; if (j < 6) TW(j + 2) = pc[1]; TW(j == 0 ? 8 : (j == 1 ? 12 : (j == 2 ? 14 : 13 + j))) = pc[2]; \
                if (j == 0) TW(9) = pc[3]; if (j == 3) TW(10) = pc[3]; if (j == 4) TW(13) = pc[3]; if (j == 5) TW(15) = pc[3]; if (j == 6) TW(11) = pc[3]; SBAR(); \
                if (i < 8) { if (i < 7) y = pfma(y.yy, z32s, t0); if (i < 6) X[i + 2] = pfma(X[i + 1].yy, z1s, t4); a = pfma(a.yy, zs, t5); \
                    if (i == 3) c4 = pfma(X[4].yy, zs, t6); if (i == 4 || i == 5) c4 = pfma(c4.yy, zs, t6); if (i == 6) c4 = pfma(X[6].yy, zs, t6); SBAR(); \
                    if (i < 7) nc[0] = pk_f16(y); if (i < 6) nc[1] = pk_f16(X[i + 2]); nc[2] = pk_f16(a); if (i >= 3 && i <= 6) nc[3] = pk_f16(c4); \
                    asm volatile("" :: "v"(pc[0]), "v"(pc[1]), "v"(pc[2]), "v"(pc[3])); SBAR(); } \
            } } while (0)
        f16x8 fa[8]; u32x4 fp[8], fq[8];
        LAS const char* trp = tab + (21 + (r32 >> 2)) * TROW + hi * 16;
        LAS const char* trq = tab + (29 + (r32 & 3)) * TROW + hi * 16;
#define RDT() do { _Pragma("unroll") for (int kk = 0; kk < 8; ++kk) { fp[kk] = *(LAS const u32x4*)(trp + kk * 32); fq[kk] = *(LAS const u32x4*)(trq + kk * 32); fa[kk] = *(LAS const f16x8*)(trd + kk * 32); } SBAR(); } while (0)
#define MMT() do { _Pragma("unroll") for (int kk = 0; kk < 8; ++kk) dacc = MFMA_F16(fa[kk], cmul4h(fp[kk], fq[kk]), dacc); } while (0)
        ANCH(bf_lo(pl), bf_lo(pa), bf_lo(pr), bf_lo(pi));
        await(sy + SY_WO, 4);
        set_tag(sy, tag);
        {
            u32x4 o0, o1; transpose_tile(lds + LT1, tab, w, lane, o0, o1);
            const unsigned to = WS_UT + (unsigned)((64 * xcd + 16 * w + (lane >> 3)) * S + 64 * bi) * 2 + (lane & 7) * 16;
            __builtin_amdgcn_s_setprio(3); BST(o0, to); BST(o1, to + 8 * S * 2);
#pragma unroll
        for (int ps = 0; ps < 2; ++ps)
#pragma unroll
            for (int i = 0; i < 3; ++i) gi[ps][i] = ((const u32x2*)g_idx.v[128 * hh + 64 * ps + lane])[i];
            __builtin_amdgcn_s_setprio(0);
        }
        CHAIN(); RDT();
        drain(); __builtin_amdgcn_s_setprio(3); put_flag(ws, FL_UT + (unsigned)(xcd * 128 + w * 32 + bi) * FS, tag); __builtin_amdgcn_s_setprio(0);
        ANCH(bf_hi(pl), bf_hi(pa), bf_hi(pr), bf_hi(pi));
        MMT(); SBAR();
        CHAIN(); RDT();
        const unsigned utf = hi ? FL_WO + (unsigned)(w * 32 + r32) * FS : FL_UT + (unsigned)(xcd * 128 + (bi >> 3) * 32 + r32) * FS;
        __builtin_amdgcn_s_setprio(3);
        const u64 utv = __hip_atomic_load((GAS u64*)(ws + utf), __ATOMIC_RELAXED, __HIP_MEMORY_SCOPE_AGENT); SBAR();
        __builtin_amdgcn_s_setprio(1);
        MMT();
        {
            LAS float* pt = (LAS float*)tab;
            *(LAS f32x4*)(pt + 8 * r32 + 4 * hi) = (f32x4){dacc[0] * oscale, dacc[1] * oscale, dacc[2] * oscale, dacc[3] * oscale};
            *(LAS f32x2*)(pt + (hi ? 384 + 2 * r32 : 256 + 4 * r32)) = (f32x2){dacc[4] * oscale, dacc[5] * oscale};
            *(LAS f32x2*)(pt + (hi ? 448 + 2 * r32 : 258 + 4 * r32)) = (f32x2){dacc[6] * oscale, dacc[7] * oscale};
            pt[hi ? 640 + r32 : 512 + r32] = dacc[8] * oscale;
            pt[hi ? 672 + r32 : 544 + r32] = dacc[9] * oscale;
            pt[hi ? 672 + r32 : 576 + r32] = dacc[10] * oscale;
            pt[hi ? 672 + r32 : 608 + r32] = dacc[11] * oscale;
        }
        LAS unsigned* ps_ = sy + SY_P + 5 * cc;
        arrive(ps_ + 0);
        LAS char* kt = cb + KOF;
        await(ps_ + 0, 2);
#pragma unroll
        for (int ps = 0; ps < 2; ++ps) {
            const int v = 81 * hh + 64 * ps + lane;
            if (ps == 0 || lane < 17) {
                const f32x4 p0 = *(LAS const f32x4*)(cb + v * 16), p1 = *(LAS const f32x4*)(cb + TABW + v * 16);
                u32x2 r = {pk_bf16(p0[0] + p1[0], p0[1] + p1[1]), pk_bf16(p0[2] + p1[2], p0[3] + p1[3])};
                if (v == 161) r = (u32x2){0u, 0u};
                *(LAS u32x2*)(kt + v * 8) = r;
            }
        }
        arrive(ps_ + 1);
        { const u64 ms = __ballot(utv != tag); if (ms) wait_flags<8>(ws, utf, tag, ms, ms); }
        CFENCE();
        const unsigned ub = WS_UT + (unsigned)(ch * S + 1024 * hh) * 2 + voff;
        const u32x4 u0 = BLD(ub), u1 = BLD(ub + 1024); SBAR();
        await(ps_ + 1, 2);
        __builtin_amdgcn_s_setprio(1);
        {
            unsigned W[2][6];
#pragma unroll
            for (int ps = 0; ps < 2; ++ps)
#pragma unroll
                for (int i = 0; i < 6; ++i) { const unsigned gv = gi[ps][i >> 1][i & 1];
                    W[ps][i] = *(LAS const uint16_t*)(kt + (gv & 0xffffu));
                    if (i < 5) W[ps][i] |= (unsigned)*(LAS const uint16_t*)(kt + (gv >> 16)) << 16; }
            if (lane < 8) *(LAS u32x4*)(cb + (2 * hh + (lane >> 2)) * CPY + (256 + (lane & 3)) * 16) = z4;
#pragma unroll
            for (int ps = 0; ps < 2; ++ps) {
                unsigned V[5];
#pragma unroll
                for (int i = 0; i < 5; ++i) V[i] = __builtin_amdgcn_alignbit(W[ps][i + 1], W[ps][i], 16);
                LAS char* cp = cb + (128 * hh + 64 * ps + lane) * 16;
                *(LAS u32x4*)(cp) = (u32x4){W[ps][0], W[ps][1], W[ps][2], W[ps][3]};
                *(LAS u32x4*)(cp + CPY) = (u32x4){V[0], V[1], V[2], V[3]};
                *(LAS u32x4*)(cp + 2 * CPY) = (u32x4){W[ps][1], W[ps][2], W[ps][3], W[ps][4]};
                *(LAS u32x4*)(cp + 3 * CPY) = (u32x4){V[1], V[2], V[3], V[4]};
            }
        }
        *(LAS u32x4*)(ui + (32 + 32 * hh + (lane >> 2)) * 80 + (lane & 3) * 16) = u0;
        *(LAS u32x4*)(ui + (48 + 32 * hh + (lane >> 2)) * 80 + (lane & 3) * 16) = u1;
        arrive(ps_ + 2); await(ps_ + 2, 2);
        __builtin_amdgcn_s_setprio(0);
        f32x16 a0 = {}, a1 = {};
        {
            const int sb = 7 - (r32 & 7);
            LAS const char* pa = cb + (sb & 3) * CPY + (255 - (r32 >> 3) + hi) * 16 + (sb >> 2) * 8 - 64 * (16 * hh + 15) - 2048;
            LAS const char* pa2 = pa + 8; asm volatile("" : "+v"(pa2));
            LAS const char* pb = ui + (32 + r32 - 16 * hh - 15) * 80 + 16 * hi;
            s16x4 ta[2][2], tb[2][2]; bf16x8 b0[2], b1[2];
#define T4(q, off) (*(LAS const s16x4*)((q) + (off)))
#define CSTEP(i, j, RD) do { const int o_ = 64 * (15 - ((j) >> 1)) + 32 * ((j) & 1), u_ = 80 * (15 - ((j) >> 1)) + 32 * ((j) & 1); \
                a1 = MFMA_BF(cat8(ta[i][0], ta[i][1]), b1[i], a1); SBAR(); if (RD) { ta[1 - i][0] = T4(pa, o_ + 2048); ta[1 - i][1] = T4(pa2, o_ + 2048); } SBAR(); \
                a0 = MFMA_BF(cat8(ta[i][0], ta[i][1]), b0[i], a0); SBAR(); if (RD) { b1[1 - i] = FR(pb, u_ + 2560); b0[1 - i] = FR(pb, u_); } SBAR(); \
                a1 = MFMA_BF(cat8(tb[i][0], tb[i][1]), b0[i], a1); SBAR(); if (RD) { tb[1 - i][0] = T4(pa, o_); tb[1 - i][1] = T4(pa2, o_); } SBAR(); } while (0)
            ta[0][0] = T4(pa, 960 + 2048); ta[0][1] = T4(pa2, 960 + 2048); b1[0] = FR(pb, 1200 + 2560); b0[0] = FR(pb, 1200); tb[0][0] = T4(pa, 960); tb[0][1] = T4(pa2, 960);
#pragma unroll
            for (int j = 0; j < 32; ++j) {
                if (j == 31) arrive(ps_ + 3);
                CSTEP(j & 1, j + 1, j < 31);
                LOADWF(j);
                SBAR();
            }
        }
        __builtin_amdgcn_s_setprio(1);
        {
            LAS char* ex = cb + hh * 9216 + r32 * 144 + hi * 16;
            await(ps_ + 3, 2);
#pragma unroll
            for (int g = 0; g < 4; ++g) {
                *(LAS f32x4*)(ex + g * 32) = (f32x4){a0[4 * g], a0[4 * g + 1], a0[4 * g + 2], a0[4 * g + 3]};
                *(LAS f32x4*)(ex + 4608 + g * 32) = (f32x4){a1[4 * g], a1[4 * g + 1], a1[4 * g + 2], a1[4 * g + 3]};
            }
            arrive(ps_ + 4);
            const float dsk = cc ? bf_hi(dw) : bf_lo(dw);
            u32x4 sk[2];
#pragma unroll
            for (int e = 0; e < 4; ++e) { sk[0][e] = pk_bf16(bf_lo(u0[e]) * dsk, bf_hi(u0[e]) * dsk); sk[1][e] = pk_bf16(bf_lo(u1[e]) * dsk, bf_hi(u1[e]) * dsk); }
            PIN(sk[0]); PIN(sk[1]);
            await(ps_ + 4, 2);
            LAS const char* er = cb + (32 * hh + (lane >> 2)) * 144 + (lane & 3) * 32;
            const unsigned bo = WS_BT + (unsigned)(ch * S + 1024 * hh) * 2 + voff;
#pragma unroll
            for (int i = 0; i < 2; ++i) {
                const f32x4 y0 = *(LAS const f32x4*)(er + i * 2304) + *(LAS const f32x4*)(er + i * 2304 + 9216);
                const f32x4 y1 = *(LAS const f32x4*)(er + i * 2304 + 16) + *(LAS const f32x4*)(er + i * 2304 + 9216 + 16);
                const u32x4 yb = {pk_bf16(y0[0], y0[1]), pk_bf16(y0[2], y0[3]), pk_bf16(y1[0], y1[1]), pk_bf16(y1[2], y1[3])};
                u32x4 r;
#pragma unroll
                for (int e = 0; e < 4; ++e) r[e] = pk_bf16(bf_lo(yb[e]) + bf_lo(sk[i][e]), bf_hi(yb[e]) + bf_hi(sk[i][e]));
                BST(r, bo + i * 1024);
            }
        }
        arrive(sy + SY_BG);
        drain();
        put_flag(ws, FL_BT + (unsigned)(xcd * 128 + bi * 4 + w) * FS, tag);
    } else {
        {
            const unsigned nb = (bi & 1) ? 12 * 2 * D : 16 * 2 * D;
            unsigned v4[4];
#pragma unroll
            for (int i = 0; i < 4; ++i) { const unsigned o = (unsigned)(w * 4096 + i * 1024) + voff; v4[i] = (o < nb ? o : 0u) + 4096 - i * 1024; }
            glds4((const char*)(wo + (DH * (bi >> 1) + 16 * (bi & 1)) * D) - 4096, v4[0], v4[1], v4[2], v4[3], lds0 + LB + w * 4096);
        }
        const u64 e0 = __hip_atomic_load((GAS u64*)ws, __ATOMIC_RELAXED, __HIP_MEMORY_SCOPE_AGENT);
        CODE_PF();
        drain(); asm volatile("" :: "v"(pf));
        tag = mix(e0);
        arrive(sy + SY_WO); await(sy + SY_WO, 4);
        set_tag(sy, tag);
        LAS const char* p = lds + LB + (4 * hi + ((lane & 15) >> 2)) * (2 * D) + ((lane >> 4) & 1) * 32 + (lane & 3) * 8;
#pragma unroll
        for (int i = 0; i < 4; ++i) {
            const int ct = w + 4 * i;
            if (ct < 14) {
                u32x4 o = __builtin_bit_cast(u32x4, cat8(trread(p + ct * 64), trread(p + 8 * 2 * D + ct * 64)));
                if (hi && (bi & 1)) { o[2] = 0; o[3] = 0; }
                BST(o, WS_WO + (unsigned)(bi * 14 + ct) * 1024 + voff);
            }
        }
        drain();
        put_flag(ws, FL_WO + (unsigned)(w * 32 + bi) * FS, tag);
        __builtin_amdgcn_s_setprio(0);
        arrive(sy + SY_BG);
        await_slow(sy + SY_G, 4);
        wait_flags<8>(ws, FL_WO + (unsigned)(w * 32 + r32) * FS, tag, 0xffffffffull, 0xffffffffull);
        if (ct < 14) {
#pragma unroll
        for (int i = 0; i < 32; ++i) { LOADWF(i); __builtin_amdgcn_s_sleep(2); SBAR(); } }
    }
    const __amdgpu_buffer_rsrc_t ro = __builtin_amdgcn_make_buffer_rsrc(out, 0, S * 2 * D * 2, 0x00020000);
    await(sy + SY_BG, 4);
    CODE_PF();
    await_slow(sy + SY_X, 4);
    wait_flags<2>(ws, FL_O + (unsigned)(qt * 16 + 4 * w + (lane & 3)) * FS, tag, 15, 15);
    asm volatile("" :: "v"(pf));
    u32x4 of[8];
#pragma unroll
    for (int i = 0; i < 8; ++i) of[i] = BLD(WS_O + (unsigned)(qt * 32 + 8 * w + i) * 1024 + voff);
#pragma unroll
    for (int i = 0; i < 8; ++i) *(LAS u32x4*)(lds + LB + (8 * w + i) * 1024 + voff) = of[i];
    arrive(sy + SY_B2);
    f32x16 acc = {};
    if (ct < 14) {
#pragma unroll
        for (int i = 0; i < 8; ++i) acc = MFMA_BF(wf[i], BC8(of[i]), acc);
    }
    SBAR();
    await(sy + SY_B2, 4);
    if (ct < 14) {
        LAS const char* po[3];
#pragma unroll
        for (int j = 0; j < 3; ++j) po[j] = lds + LB + ((w + 1 + j) & 3) * 8192 + voff;
        bf16x8 og[4];
#pragma unroll
        for (int i = 0; i < 3; ++i) og[i] = FR(po[0], i * 1024);
#pragma unroll
        for (int i = 0; i < 24; ++i) { if (i < 21) og[(i + 3) & 3] = FR(po[(i + 3) >> 3], ((i + 3) & 7) * 1024); SBAR(); acc = MFMA_BF(wf[8 + i], og[i & 3], acc); SBAR(); }
        LAS char* sc = lds + LB + 40960 + w * 2304;
#pragma unroll
        for (int g = 0; g < 4; ++g) *(LAS u32x2*)(sc + r32 * 72 + 16 * g + 8 * hi) = (u32x2){pk_bf16(acc[4 * g], acc[4 * g + 1]), pk_bf16(acc[4 * g + 2], acc[4 * g + 3])};
        LAS const char* sr = sc + (lane >> 2) * 72 + (lane & 3) * 16;
        const u32x2 a0 = *(LAS const u32x2*)sr, a1 = *(LAS const u32x2*)(sr + 8), b0 = *(LAS const u32x2*)(sr + 16 * 72), b1 = *(LAS const u32x2*)(sr + 16 * 72 + 8);
        const unsigned oo = (unsigned)((32 * qt + (lane >> 2)) * 2 * D + 32 * ct) * 2 + (lane & 3) * 16;
        __builtin_amdgcn_raw_buffer_store_b128((u32x4){a0[0], a0[1], a1[0], a1[1]}, ro, oo, 0, 16);
        __builtin_amdgcn_raw_buffer_store_b128((u32x4){b0[0], b0[1], b1[0], b1[1]}, ro, oo + 16 * 2 * D * 2, 0, 16);
    }
    if (b == 0 && w == 0) put_flag(ws, 0, tag);
}

__global__ __launch_bounds__(512, 2) void fused(const uint16_t* __restrict__ x, const uint16_t* __restrict__ wqkv, const uint16_t* __restrict__ wo,
                                               const uint16_t* __restrict__ u, const uint16_t* __restrict__ lar, const uint16_t* __restrict__ aim,
                                               const uint16_t* __restrict__ cre, const uint16_t* __restrict__ cim, const uint16_t* __restrict__ ldt,
                                               const uint16_t* __restrict__ dskip, uint16_t* __restrict__ out, char* __restrict__ ws) {
    extern __shared__ __attribute__((aligned(16))) char smem[];
    LAS char* lds = (LAS char*)smem;
    const int lane = threadIdx.x & 63, w = __builtin_amdgcn_readfirstlane(threadIdx.x >> 6);
    u64 code0; asm volatile("s_getpc_b64 %0" : "=s"(code0)); code0 &= ~255ull;
    if (w >= 4) __builtin_amdgcn_s_setprio(3);
    if (w == 4) ((LAS unsigned*)(lds + LSY))[lane] = 0;
    asm volatile("" :: "s"(x), "s"(wqkv), "s"(wo), "s"(u), "s"(lar), "s"(aim), "s"(cre), "s"(cim), "s"(ldt), "s"(dskip), "s"(out), "s"(ws));
    asm volatile("s_waitcnt lgkmcnt(0)\n\ts_barrier" ::: "memory");
    if (w < 4) role_a(x, wqkv, out, ws, lds, w, lane);
    else role_b(wo, u, lar, aim, cre, cim, ldt, dskip, out, ws, lds, w - 4, lane, code0);
}
}

extern "C" void kernel_launch(void* const* d_in, const int* in_sizes, int n_in, void* d_out, int out_size, void* d_ws, size_t ws_size, hipStream_t stream) {
    using namespace k;
    (void)hipFuncSetAttribute((const void*)fused, hipFuncAttributeMaxDynamicSharedMemorySize, LDS_BYTES);
    const uint16_t* const* in = (const uint16_t* const*)d_in;
    fused<<<256, 512, LDS_BYTES, stream>>>(in[0], in[1], in[2], in[3], in[4], in[5], in[6], in[7], in[8], in[9], (uint16_t*)d_out, (char*)d_ws);
}
```

```cpp
#include <hip/hip_runtime.h>
#include <hip/hip_bf16.h>
#include <stdint.h>

typedef short bf16x8 __attribute__((ext_vector_type(8)));
typedef _Float16 f16x8 __attribute__((ext_vector_type(8)));
typedef _Float16 f16x2 __attribute__((ext_vector_type(2)));
typedef __bf16 bf16x2 __attribute__((ext_vector_type(2)));
typedef short s16x4 __attribute__((ext_vector_type(4)));
typedef float f32x16 __attribute__((ext_vector_type(16)));
typedef float f32x4 __attribute__((ext_vector_type(4)));
typedef float f32x2 __attribute__((ext_vector_type(2)));
typedef unsigned u32x4 __attribute__((ext_vector_type(4)));
typedef unsigned u32x2 __attribute__((ext_vector_type(2)));
typedef unsigned long long u64;
#define LAS __attribute__((address_space(3)))
#define GAS __attribute__((address_space(1)))
#define DI __device__ __forceinline__
#define MFMA_BF(a, b, c) __builtin_amdgcn_mfma_f32_32x32x16_bf16((a), (b), (c), 0, 0, 0)
#define MFMA_F16(a, b, c) __builtin_amdgcn_mfma_f32_32x32x16_f16((a), (b), (c), 0, 0, 0)
#define SBAR() __builtin_amdgcn_sched_barrier(0)
#define CFENCE() asm volatile("" ::: "memory")
#define PIN(x) asm volatile("" : "+v"(x))
#define BC8(x) __builtin_bit_cast(bf16x8, (x))
#define EX(x) __builtin_amdgcn_exp2f(x)

namespace k {
constexpr int S = 2048, D = 448, DH = 28, NS = 256;
constexpr unsigned FS = 128;
constexpr unsigned FL_KV = FS, FL_O = FL_KV + 1024 * FS, FL_UT = FL_O + 1024 * FS, FL_BT = FL_UT + 896 * FS, FL_WO = FL_BT + 896 * FS;
constexpr unsigned WS_KV = 512u << 10;
constexpr unsigned WS_O = WS_KV + (4u << 20);
constexpr unsigned WS_WO = WS_O + (2u << 20);
constexpr unsigned WS_UT = WS_WO + (448u << 10);
constexpr unsigned WS_BT = WS_UT + D * S * 2;
constexpr unsigned WS_END = WS_BT + D * S * 2;
constexpr int WSL = 8192, LWQ = 4 * WSL, LXR = LWQ + 16384, XSL = 16384, LB = LXR + 3 * XSL;
constexpr int LQ = LXR, LX = 0;
constexpr int TROW = 272, TABW = 33 * TROW, CPY = 4160, UOF = 2 * TABW, KOF = UOF + 7680, CBW = KOF + 1312;
constexpr int LT1 = LB + 2 * CBW, LSY = LT1 + 8192, LDS_BYTES = LSY + 256;
static_assert(LDS_BYTES <= 163840 && (LSY & 15) == 0 && 4 * CPY <= UOF && LB + 40960 + 4 * 2304 <= LSY, "lds");
enum { SY_F = 2, SY_P = 10, SY_WO = 20, SY_BG = 22, SY_B2 = 23, SY_Q = 24, SY_X = 25, SY_G = 26, SY_T2 = 27 };
constexpr int SPIN = 1 << 18;
constexpr unsigned CODE_LINES = 216;

struct IdxTab { uint16_t v[256][12]; };
constexpr IdxTab make_idx() {
    IdxTab t = {};
    for (int y = 0; y < 256; ++y) for (int e = 0; e < 12; ++e) {
        const int j = 2047 - 8 * y - e; int ix = 644;
        if (j >= 0) { const int sh = j >= 1024 ? 3 : (j >= 512 ? 2 : (j >= 256 ? 1 : 0));
            ix = sh ? 128 * sh + ((j + (1 << (sh - 1)) - 1 + ((j >> sh) & 1)) >> sh) : j; }
        t.v[y][e] = (uint16_t)(2 * ix);
    }
    return t;
}
__device__ const IdxTab g_idx __attribute__((aligned(16))) = make_idx();

DI unsigned pk_bf16(float a, float b) { f32x2 v = {a, b}; return __builtin_bit_cast(unsigned, __builtin_convertvector(v, bf16x2)); }
DI unsigned pk_f16(f32x2 v) { return __builtin_bit_cast(unsigned, __builtin_convertvector(v, f16x2)); }
DI float bf_lo(unsigned w) { return __uint_as_float(w << 16); }
DI float bf_hi(unsigned w) { return __uint_as_float(w & 0xffff0000u); }
DI float rbf(float x) { return bf_lo(pk_bf16(x, x)); }
#define GL0 "s_mov_b32 %0, m0\n\ts_mov_b32 m0, %2\n\ts_nop 0\n\t"
#define GL1(V, O, C) "global_load_lds_dwordx4 " V ", %1 offset:" #O C "\n\t"
#define GLE "s_mov_b32 m0, %0"
DI void glds4(const void* sb, unsigned v0, unsigned v1, unsigned v2, unsigned v3, unsigned lb) { unsigned sv;
    asm volatile(GL0 GL1("%3", 0, "") GL1("%4", 1024, "") GL1("%5", 2048, "") GL1("%6", 3072, "") GLE : "=&s"(sv) : "s"(sb), "s"(lb), "v"(v0), "v"(v1), "v"(v2), "v"(v3) : "memory"); }
DI void glds2(const void* sb, unsigned v0, unsigned v1, unsigned lb) { unsigned sv;
    asm volatile(GL0 GL1("%3", 0, "") GL1("%4", 1024, "") GLE : "=&s"(sv) : "s"(sb), "s"(lb), "v"(v0), "v"(v1) : "memory"); }
DI void glds1(const void* sb, unsigned v0, unsigned lb) { unsigned sv;
    asm volatile(GL0 GL1("%3", 0, "") GLE : "=&s"(sv) : "s"(sb), "s"(lb), "v"(v0) : "memory"); }
DI float wave_max(float v) {
#define DPPMAX(ctrl, rm) v = fmaxf(v, __uint_as_float(__builtin_amdgcn_update_dpp(0, __float_as_uint(v), ctrl, rm, 0xf, true)))
    DPPMAX(0x111, 0xf); DPPMAX(0x112, 0xf); DPPMAX(0x114, 0xf); DPPMAX(0x118, 0xf); DPPMAX(0x142, 0xa); DPPMAX(0x143, 0xc);
    return __uint_as_float(__builtin_amdgcn_readlane(__float_as_uint(v), 63));
}
DI s16x4 trread(LAS const char* p) { return __builtin_bit_cast(s16x4, __builtin_amdgcn_ds_read_tr16_b64_v4i16((LAS s16x4*)p)); }
DI bf16x8 cat8(s16x4 a, s16x4 b) { return __builtin_shufflevector(a, b, 0, 1, 2, 3, 4, 5, 6, 7); }
DI f32x2 pfma(f32x2 a, f32x2 b, f32x2 c) { return __builtin_elementwise_fma(a, b, c); }
DI f32x2 cmul(f32x2 a, f32x2 b, f32x2 bs) { return pfma(a.yy, bs, a.xx * b); }
DI f16x8 cmul4h(u32x4 p, u32x4 q) {
    u32x4 t;
#define PM(i, a, b) "v_pk_mul_f16 %" #i ", %" #a ", %" #b " op_sel:[0,0] op_sel_hi:[0,1]\n\t"
#define PF(i, a, b) "v_pk_fma_f16 %" #i ", %" #a ", %" #b ", %" #i " op_sel:[1,1,0] op_sel_hi:[1,0,1] neg_lo:[1,0,0]\n\t"
    asm(PM(0, 4, 8) PM(1, 5, 9) PM(2, 6, 10) PM(3, 7, 11) PF(0, 4, 8) PF(1, 5, 9) PF(2, 6, 10) PF(3, 7, 11) "s_nop 1"
        : "=&v"(t[0]), "=&v"(t[1]), "=&v"(t[2]), "=&v"(t[3]) : "v"(p[0]), "v"(p[1]), "v"(p[2]), "v"(p[3]), "v"(q[0]), "v"(q[1]), "v"(q[2]), "v"(q[3]));
    return __builtin_bit_cast(f16x8, t);
}
DI f32x2 rot(f32x2 b) { return (f32x2){-b.y, b.x}; }
DI f32x2 anchor(float a2, float rev, float s) {
    const float mag = EX(a2 * s);
    float r = rev * s; r -= __builtin_rintf(r);
    return (f32x2){mag * __builtin_amdgcn_cosf(r), mag * __builtin_amdgcn_sinf(r)};
}
DI void arrive(LAS unsigned* c) { asm volatile("s_mov_b64 exec, 1\n\tds_add_u32 %0, %1\n\ts_mov_b64 exec, -1" :: "v"((unsigned)(uintptr_t)c), "v"(1u) : "memory"); }
DI void await(LAS unsigned* c, unsigned n) {
    unsigned t, i = SPIN;
    asm volatile("1:\n\tds_read_b32 %0, %2\n\ts_waitcnt lgkmcnt(0)\n\tv_cmp_gt_u32 vcc, %3, %0\n\ts_cbranch_vccz 2f\n\ts_sleep 1\n\ts_sub_u32 %1, %1, 1\n\ts_cbranch_scc0 1b\n2:"
                 : "=&v"(t), "+s"(i) : "v"((unsigned)(uintptr_t)c), "s"(n) : "vcc", "scc", "memory");
}
DI void await_slow(LAS unsigned* c, unsigned n) {
    unsigned t, i = SPIN;
    asm volatile("1:\n\tds_read_b32 %0, %2\n\ts_waitcnt lgkmcnt(0)\n\tv_cmp_gt_u32 vcc, %3, %0\n\ts_cbranch_vccz 2f\n\ts_sleep 3\n\ts_sub_u32 %1, %1, 1\n\ts_cbranch_scc0 1b\n2:"
                 : "=&v"(t), "+s"(i) : "v"((unsigned)(uintptr_t)c), "s"(n) : "vcc", "scc", "memory");
}
DI u64 get_tag(LAS unsigned* sy) {
    await(sy, 1);
    const u64 v = __hip_atomic_load((LAS u64*)sy, __ATOMIC_RELAXED, __HIP_MEMORY_SCOPE_WORKGROUP);
    return ((u64)(unsigned)__builtin_amdgcn_readfirstlane((unsigned)(v >> 32)) << 32) | (unsigned)__builtin_amdgcn_readfirstlane((unsigned)v);
}
DI void set_tag(LAS unsigned* sy, u64 tag) { asm volatile("s_mov_b64 exec, 1\n\tds_write_b64 %0, %1\n\ts_mov_b64 exec, -1" :: "v"((unsigned)(uintptr_t)sy), "v"(tag) : "memory"); }
DI void drain() { asm volatile("s_waitcnt vmcnt(0)" ::: "memory"); }
DI void put_flag(char* ws, unsigned off, u64 tag) {
    asm volatile("s_mov_b64 exec, 1\n\tglobal_store_dwordx2 %0, %1, %2 sc1\n\ts_mov_b64 exec, -1" :: "v"(off), "v"(tag), "s"(ws) : "memory");
}
template <int SLP> DI u64 wait_flags(char* ws, unsigned off, u64 tag, u64 pend, u64 need) {
    u64 t, left; unsigned i = SPIN;
    asm volatile("s_mov_b64 exec, %5\n1:\n\tglobal_load_dwordx2 %0, %3, %4 sc1\n\ts_waitcnt vmcnt(0)\n\tv_cmpx_ne_u64 vcc, %6, %0\n\ts_and_b64 %2, exec, %7\n\t"
                 "s_cbranch_scc0 2f\n\ts_sleep %8\n\ts_sub_u32 %1, %1, 1\n\ts_cbranch_scc0 1b\n2:\n\ts_mov_b64 %2, exec\n\ts_mov_b64 exec, -1"
                 : "=&v"(t), "+s"(i), "=&s"(left) : "v"(off), "s"(ws), "s"(pend), "s"(tag), "s"(need), "i"(SLP) : "vcc", "scc", "memory");
    return left;
}
#define BST(v, off) __builtin_amdgcn_raw_buffer_store_b128((v), rs, (off), 0, 16)
#define BLD(off) __builtin_amdgcn_raw_buffer_load_b128(rs, (off), 0, 16)
#define FR(p, off) (*(LAS const bf16x8*)((p) + (off)))
DI float swapmax(float m) { const auto r = __builtin_amdgcn_permlane32_swap(__float_as_uint(m), __float_as_uint(m), false, false); return fmaxf(__uint_as_float(r[0]), __uint_as_float(r[1])); }
DI u32x4 put_ref(u32x4 q1, float m, int hi) {
    const float m1 = rbf(m), m2 = rbf(m - m1), m3 = m - m1 - m2;
    if (hi) { q1[2] = pk_bf16(-m1, -m2); q1[3] = pk_bf16(-m3, 0.f); }
    return q1;
}
DI u64 mix(u64 v) {
    const u64 e = ((u64)(unsigned)__builtin_amdgcn_readfirstlane((unsigned)(v >> 32)) << 32) | (unsigned)__builtin_amdgcn_readfirstlane((unsigned)v);
    u64 z = e + 0x9E3779B97F4A7C15ull; z = (z ^ (z >> 30)) * 0xBF58476D1CE4E5B9ull; z = (z ^ (z >> 27)) * 0x94D049BB133111EBull; return (z ^ (z >> 31)) | 1ull; }
DI void transpose_tile(LAS const char* stage, LAS char* scratch, int w, int lane, u32x4& o0, u32x4& o1) {
    LAS const char* p = stage + (lane >> 4) * 2048 + ((lane & 15) >> 2) * 128 + w * 32 + (lane & 3) * 8;
    const bf16x8 t0 = cat8(trread(p), trread(p + 512)), t1 = cat8(trread(p + 1024), trread(p + 1536));
    LAS char* sc = scratch + (lane & 15) * 144 + (lane >> 4) * 32;
    *(LAS bf16x8*)sc = t0; *(LAS bf16x8*)(sc + 16) = t1;
    LAS const char* sr = scratch + (lane >> 3) * 144 + (lane & 7) * 16;
    o0 = *(LAS const u32x4*)sr; o1 = *(LAS const u32x4*)(sr + 8 * 144);
}

#define SP(st) ((st) + rho >= 7 ? (st) + rho - 7 : (st) + rho)
#define WQO(st) ((st) < 4 ? LWQ + (st) * 4096 : ((st) < 6 ? 3 * WSL + ((st) - 4) * 4096 : 0))
#define LOADW(st) glds2(wb + SP(st) * (64 * 3 * D * 2), wv + 2 * D, wv + 4 * D - 1024, lds0 + ((st) & 3) * WSL + w * 2048)
#define LOADQ(st) glds1(wb + SP(st) * (64 * 3 * D * 2), wv, lds0 + WQO(st) + w * 1024)
#define LOADX(st) glds4(xb + SP(st) * 128, xv0, xv1 + 16 * D - 1024, xv0 + 32 * D - 2048, xv1 + 48 * D - 3072, lds0 + LXR + ((st) % 3) * XSL + w * 4096)
DI void head_a(const uint16_t* __restrict__ x, const uint16_t* __restrict__ wqkv, LAS char* lds, int w, int lane) {
    const int head = 2 * (blockIdx.x & 7) + ((blockIdx.x >> 3) & 1), grp = blockIdx.x >> 4;
    const unsigned lds0 = (unsigned)(uintptr_t)lds;
    const int r32 = lane & 31, hi = lane >> 5;
    const int wsh = (head & 7) == 7 ? 4 : 0;
    const unsigned wv = (unsigned)((lane >> 2) * 3 * D + (lane & 3) * 8) * 2;
    const char* wb = (const char*)(wqkv + DH * head - wsh + (16 * w) * 3 * D);
    const unsigned xv0 = (unsigned)((lane >> 3) * D + (((lane & 7) ^ (lane >> 4)) * 8)) * 2, xv1 = xv0 ^ 64;
    const char* xb = (const char*)(x + (128 * grp + 32 * w) * D);
    const int rho = ((blockIdx.x >> 3) + ((blockIdx.x >> 1) & 3)) % 7;
    LOADX(0); LOADW(0);
    LOADX(1); LOADW(1);
}

DI void role_a(const uint16_t* __restrict__ x, const uint16_t* __restrict__ wqkv, uint16_t* __restrict__ out, char* __restrict__ ws, LAS char* lds, int w, int lane) {
    const int head = 2 * (blockIdx.x & 7) + ((blockIdx.x >> 3) & 1), grp = blockIdx.x >> 4;
    const unsigned lds0 = (unsigned)(uintptr_t)lds;
    const int r32 = lane & 31, hi = lane >> 5;
    const int wsh = (head & 7) == 7 ? 4 : 0;
    const unsigned wv = (unsigned)((lane >> 2) * 3 * D + (lane & 3) * 8) * 2;
    const char* wb = (const char*)(wqkv + DH * head - wsh + (16 * w) * 3 * D);
    const unsigned xv0 = (unsigned)((lane >> 3) * D + (((lane & 7) ^ (lane >> 4)) * 8)) * 2, xv1 = xv0 ^ 64;
    const char* xb = (const char*)(x + (128 * grp + 32 * w) * D);
    const int rho = ((blockIdx.x >> 3) + ((blockIdx.x >> 1) & 3)) % 7;
    LAS unsigned* sy = (LAS unsigned*)(lds + LSY);
    const __amdgpu_buffer_rsrc_t rs = __builtin_amdgcn_make_buffer_rsrc(ws, 0, WS_END, 0x00020000);
    const int xs = (r32 >> 1) & 7;
    LAS const char* xp[4];
#pragma unroll
    for (int ks = 0; ks < 4; ++ks) xp[ks] = lds + LXR + w * 4096 + r32 * 128 + (((2 * ks + hi) ^ xs) * 16);
    LAS const char* wp = lds + (8 * hi + ((lane & 15) >> 2)) * 64 + ((lane >> 4) & 1) * 32 + (lane & 3) * 8 + wsh * 2;
    f32x16 aq = {}, ak = {}, av = {};
    bf16x8 xf[7][4]; s16x4 fr[4][4];
#define RX(st, ks) xf[st][ks] = FR(xp[ks], ((st) % 3) * XSL)
#define RW(st, ks, j) fr[ks][j] = trread(wp + ((st) & 3) * WSL + (ks) * 2048 + ((j) >> 1) * 1024 + ((j) & 1) * 256)
#define MK(st, ks) ak = MFMA_BF(cat8(fr[ks][0], fr[ks][1]), xf[st][ks], ak); SBAR()
#define MV(st, ks) av = MFMA_BF(xf[st][ks], cat8(fr[ks][2], fr[ks][3]), av); SBAR()
#define QSTEP(st, ks) MK(st, ks); RW(st, ks + 1, 0); RW(st, ks + 1, 1); SBAR(); MV(st, ks); RW(st, ks + 1, 2); RW(st, ks + 1, 3); SBAR()
    u64 tag = 0;
    asm volatile("s_waitcnt vmcnt(6)" ::: "memory");
    arrive(sy + SY_F); LOADW(2); LOADX(2); await(sy + SY_F, 4);
    RX(0, 0); RW(0, 0, 0); RW(0, 0, 1); RW(0, 0, 2); RW(0, 0, 3); RX(0, 1); RX(0, 2); RX(0, 3); SBAR();
#pragma unroll
    for (int st = 0; st < 7; ++st) {
        QSTEP(st, 0); if (st < 4) LOADW(st + 3); if (st == 4) { LOADQ(4); LOADQ(5); } if (st == 5) LOADQ(6);
        QSTEP(st, 1); if (st < 4) LOADX(st + 3);
        QSTEP(st, 2); if (st == 3) { LOADQ(0); LOADQ(1); LOADQ(2); LOADQ(3); }
        if (st < 6) {
            if (st == 3) asm volatile("s_waitcnt vmcnt(16)" ::: "memory"); else if (st == 5) asm volatile("s_waitcnt vmcnt(7)" ::: "memory"); else asm volatile("s_waitcnt vmcnt(12)" ::: "memory");
            arrive(sy + SY_F + 1 + st); SBAR();
        }
        MK(st, 3);
        if (st < 6) { await(sy + SY_F + 1 + st, 4); RX(st + 1, 0); RW(st + 1, 0, 0); RW(st + 1, 0, 1); RW(st + 1, 0, 2); RW(st + 1, 0, 3); } SBAR();
        MV(st, 3); if (st < 6) { RX(st + 1, 1); RX(st + 1, 2); RX(st + 1, 3); } SBAR();
        if (st == 2) tag = get_tag(sy);
    }
    const float cq = 0.18898223650461362f * 1.4426950408889634f;
    const unsigned voff = lane * 16;
    u32x4 q0, q1, k0, k1, v0, v1;
    const unsigned tb = WS_KV + (unsigned)(head * 64 + 4 * grp + w) * 4096 + voff;
#pragma unroll
    for (int i = 0; i < 4; ++i) { k0[i] = pk_bf16(ak[2 * i], ak[2 * i + 1]); k1[i] = pk_bf16(ak[8 + 2 * i], ak[9 + 2 * i]); }
    if (hi) { k1[2] = 0x3f803f80u; k1[3] = 0x00003f80u; }
    BST(k0, tb); BST(k1, tb + 1024);
#pragma unroll
    for (int i = 0; i < 4; ++i) {
        v0[i] = pk_bf16(av[2 * i], av[2 * i + 1]); v1[i] = pk_bf16(av[8 + 2 * i], av[9 + 2 * i]);
        if (r32 >= DH) { v0[i] = v1[i] = (r32 == DH ? 0x3f803f80u : 0u); }
    }
    BST(v0, tb + 2048); BST(v1, tb + 3072);
    asm volatile("s_waitcnt vmcnt(4)" ::: "memory");
    arrive(sy + SY_F + 7); await(sy + SY_F + 7, 4);
    {
        s16x4 fq[4][2];
#define RQ(j) do { fq[(j) & 3][0] = trread(wp + WQO((j) >> 2) + ((j) & 3) * 1024); fq[(j) & 3][1] = trread(wp + WQO((j) >> 2) + ((j) & 3) * 1024 + 256); } while (0)
        RQ(0); RQ(1); RQ(2); SBAR();
#pragma unroll
        for (int j = 0; j < 28; ++j) {
            aq = MFMA_BF(cat8(fq[j & 3][0], fq[j & 3][1]), xf[j >> 2][j & 3], aq); SBAR(); if (j < 25) RQ(j + 3); SBAR();
            if (j == 13) { drain(); put_flag(ws, FL_KV + (unsigned)((head * 4 + w) * 16 + grp) * FS, tag); }
        }
    }
#pragma unroll
    for (int i = 0; i < 4; ++i) { q0[i] = pk_bf16(aq[2 * i] * cq, aq[2 * i + 1] * cq); q1[i] = pk_bf16(aq[8 + 2 * i] * cq, aq[9 + 2 * i] * cq); }
    if (hi) { q1[2] = 0; q1[3] = 0; }
    const unsigned kvf = FL_KV + (unsigned)((head * 4 + w) * 16 + ((grp + lane) & 15)) * FS;
    const f32x16 zero = {};
    {
        f32x16 s = MFMA_BF(BC8(k0), BC8(q0), zero); s = MFMA_BF(BC8(k1), BC8(q1), s);
        float m = s[0];
#pragma unroll
        for (int i = 1; i < 16; ++i) m = fmaxf(m, s[i]);
        q1 = put_ref(q1, swapmax(m), hi);
    }
    { LAS u32x4* qo = (LAS u32x4*)(lds + LQ + w * 2048) + lane; qo[0] = q0; qo[64] = q1; }
    arrive(sy + SY_Q);
    const u64 kv1 = __hip_atomic_load((GAS u64*)(ws + kvf), __ATOMIC_RELAXED, __HIP_MEMORY_SCOPE_AGENT); SBAR();
    f32x16 O[4] = {}; PIN(O[0]); PIN(O[1]); PIN(O[2]); PIN(O[3]);
    await(sy + SY_Q, 4);
    bf16x8 Q[4][2];
    Q[0][0] = BC8(q0); Q[0][1] = BC8(q1);
#pragma unroll
    for (int i = 1; i < 4; ++i) { LAS const char* p = lds + LQ + ((w + i) & 3) * 2048 + voff; Q[i][0] = FR(p, 0); Q[i][1] = FR(p, 1024); }
    u32x4 kv[4][4] = {{k0, k1, v0, v1}, {}, {}, {}};
    f32x16 sa, sb;
    u32x4 p0, p1 = {};
#define GRP(SC, i, P, j) do { float e0 = EX(SC[i]); SBAR(); float e1 = EX(SC[i + 1]); SBAR(); float e2 = EX(SC[i + 2]); SBAR(); float e3 = EX(SC[i + 3]); SBAR(); \
        P[j] = pk_bf16(e0, e1); SBAR(); P[j + 1] = pk_bf16(e2, e3); PIN(P); SBAR(); } while (0)
#define SLOT(SC, SN, cb, i, QK, PV1, LD) do { constexpr int nb_ = (i) < 3 ? (cb) : ((cb) + 1) % 4, pb_ = (i) > 0 ? (cb) : ((cb) + 3) % 4; \
        if (QK) SN = MFMA_BF(BC8(kv[nb_][0]), Q[((i) + 1) & 3][0], zero); SBAR(); GRP(SC, 0, p0, 0); \
        if (PV1) O[((i) + 3) & 3] = MFMA_BF(BC8(kv[pb_][3]), BC8(p1), O[((i) + 3) & 3]); SBAR(); \
        if (LD) { kv[((cb) + 3) % 4][i] = __builtin_amdgcn_raw_buffer_load_b128(rs, voff + (i) * 1024, kvo, 16); SBAR(); } GRP(SC, 4, p0, 2); \
        if (QK) SN = MFMA_BF(BC8(kv[nb_][1]), Q[((i) + 1) & 3][1], SN); SBAR(); GRP(SC, 8, p1, 0); \
        O[i] = MFMA_BF(BC8(kv[cb][2]), BC8(p0), O[i]); SBAR(); GRP(SC, 12, p1, 2); } while (0)
    const unsigned kvb = WS_KV + (unsigned)(head * 64 + w) * 4096;
    unsigned kvo = 0;
    u64 miss = __ballot(kv1 != tag) & 0xfffe;
    if (miss & 6) miss = wait_flags<2>(ws, kvf, tag, miss, 6);
    CFENCE();
#pragma unroll
    for (int j = 1; j < 3; ++j)
#pragma unroll
        for (int f = 0; f < 4; ++f) kv[j][f] = __builtin_amdgcn_raw_buffer_load_b128(rs, voff + f * 1024, kvb + ((grp + j) & 15) * 16384, 16);
    arrive(sy + SY_G);
    sa = MFMA_BF(BC8(k0), Q[0][0], zero); sa = MFMA_BF(BC8(k1), Q[0][1], sa);
    int nx = grp + 3;
#define TILE(cb) do { kvo = kvb + (nx & 15) * 16384; ++nx; SBAR(); \
        SLOT(sa, sb, cb, 0, true, true, true); SLOT(sb, sa, cb, 1, true, true, true); SLOT(sa, sb, cb, 2, true, true, true); SLOT(sb, sa, cb, 3, true, true, true); } while (0)
#pragma unroll 1
    for (unsigned msk = 0x78; msk < 0x80000; msk <<= 4) { if (__builtin_expect((miss & msk) != 0, 0)) miss = wait_flags<2>(ws, kvf, tag, miss, msk); TILE(0); TILE(1); TILE(2); TILE(3); }
    O[3] = MFMA_BF(BC8(kv[3][3]), BC8(p1), O[3]);
    f32x16 o = O[0];
    {
#pragma unroll
        for (int i = 1; i < 4; ++i) {
            LAS char* ex = lds + LX + (((w + i) & 3) * 3 + i - 1) * 4096 + voff;
#pragma unroll
            for (int g = 0; g < 4; ++g) *(LAS f32x4*)(ex + g * 1024) = (f32x4){O[i][4 * g], O[i][4 * g + 1], O[i][4 * g + 2], O[i][4 * g + 3]};
        }
        arrive(sy + SY_X); await(sy + SY_X, 4);
        LAS const char* er = lds + LX + w * 3 * 4096 + voff;
#pragma unroll
        for (int i = 0; i < 3; ++i)
#pragma unroll
            for (int g = 0; g < 4; ++g) { const f32x4 r = *(LAS const f32x4*)(er + i * 4096 + g * 1024);
#pragma unroll
                for (int e = 0; e < 4; ++e) o[4 * g + e] += r[e]; }
        f32x2 chk = {0.f, 0.f};
#pragma unroll
        for (int i = 0; i < 8; ++i) chk = pfma((f32x2){o[2 * i], o[2 * i + 1]}, (f32x2){0.f, 0.f}, chk);
        if (__builtin_expect(__any(!(chk[0] + chk[1] == 0.f) || !(o[12] < 1e30f)), 0)) {
            const unsigned hb = WS_KV + (unsigned)head * 64 * 4096 + voff;
            u32x4 qr = __builtin_bit_cast(u32x4, Q[0][1]); if (hi) { qr[2] = 0; qr[3] = 0; }
            float mm = -INFINITY;
            wait_flags<4>(ws, FL_KV + (unsigned)(head * 64 + lane) * FS, tag, ~0ull, ~0ull);
#pragma unroll 1
            for (int t = 0; t < 64; ++t) {
                f32x16 s = MFMA_BF(BC8(BLD(hb + t * 4096)), Q[0][0], zero); s = MFMA_BF(BC8(BLD(hb + t * 4096 + 1024)), BC8(qr), s);
#pragma unroll
                for (int i = 0; i < 16; ++i) mm = fmaxf(mm, s[i]);
            }
            qr = put_ref(qr, swapmax(mm), hi);
            o = zero;
#pragma unroll 1
            for (int t = 0; t < 64; ++t) {
                f32x16 s = MFMA_BF(BC8(BLD(hb + t * 4096)), Q[0][0], zero); s = MFMA_BF(BC8(BLD(hb + t * 4096 + 1024)), BC8(qr), s);
#pragma unroll
                for (int i = 0; i < 4; ++i) { p0[i] = pk_bf16(EX(s[2 * i]), EX(s[2 * i + 1])); p1[i] = pk_bf16(EX(s[8 + 2 * i]), EX(s[9 + 2 * i])); }
                o = MFMA_BF(BC8(BLD(hb + t * 4096 + 2048)), BC8(p0), o); o = MFMA_BF(BC8(BLD(hb + t * 4096 + 3072)), BC8(p1), o);
            }
        }
    }
    {
        const auto r = __builtin_amdgcn_permlane32_swap(__float_as_uint(o[12]), __float_as_uint(o[12]), false, false);
        const float rl = __builtin_amdgcn_rcpf(__uint_as_float(r[1]));
        u32x4 o0, o1;
#pragma unroll
        for (int i = 0; i < 4; ++i) { o0[i] = pk_bf16(o[2 * i] * rl, o[2 * i + 1] * rl); o1[i] = pk_bf16(o[8 + 2 * i] * rl, o[9 + 2 * i] * rl); }
        if (hi) { o1[2] = 0; o1[3] = 0; }
        const unsigned fi = (unsigned)((4 * grp + w) * 16 + head);
        BST(o0, WS_O + fi * 2048 + voff); BST(o1, WS_O + fi * 2048 + 1024 + voff);
        drain();
        put_flag(ws, FL_O + fi * FS, tag);
    }
    const int xcd = blockIdx.x & 7, bi = blockIdx.x >> 3;
    if (xcd < 7) {
        wait_flags<2>(ws, FL_BT + (unsigned)(xcd * 128 + w * 32 + r32) * FS, tag, 0xffffffffull, 0xffffffffull);
        const unsigned bo = WS_BT + (unsigned)((64 * xcd + 16 * w + (lane >> 3)) * S + 64 * bi + (lane & 7) * 8) * 2;
        const u32x4 b0 = BLD(bo), b1 = BLD(bo + 8 * S * 2);
        *(LAS u32x4*)(lds + 65536 + w * 2048 + voff) = b0; *(LAS u32x4*)(lds + 65536 + w * 2048 + 1024 + voff) = b1;
        arrive(sy + SY_T2); await(sy + SY_T2, 4);
        u32x4 o0, o1; transpose_tile(lds + 65536, lds + 73728 + w * 2304, w, lane, o0, o1);
        const __amdgpu_buffer_rsrc_t ro = __builtin_amdgcn_make_buffer_rsrc(out, 0, S * 2 * D * 2, 0x00020000);
        const unsigned oo = (unsigned)((64 * bi + 16 * w + (lane >> 3)) * 2 * D + D + 64 * xcd) * 2 + (lane & 7) * 16;
        __builtin_amdgcn_raw_buffer_store_b128(o0, ro, oo, 0, 16); __builtin_amdgcn_raw_buffer_store_b128(o1, ro, oo + 8 * 2 * D * 2, 0, 16);
    }
}

#define CODE_PF() asm volatile("global_load_dword %0, %1, %2" : "+v"(pf) : "v"(min((unsigned)((bi * 4 + w) * 3 + (lane < 3 ? lane : 0)), CODE_LINES - 1) * 128), "s"(code0) : "memory")
DI void head_b(const uint16_t* __restrict__ wo, const uint16_t* __restrict__ u, const uint16_t* __restrict__ lar, const uint16_t* __restrict__ aim,
               const uint16_t* __restrict__ cre, const uint16_t* __restrict__ cim, char* __restrict__ ws, LAS char* lds, int w, int lane, u64 code0,
               unsigned& pl, unsigned& pa, unsigned& pr, unsigned& pi, u64& e0, unsigned& pf) {
    const unsigned lds0 = (unsigned)(uintptr_t)lds, voff = lane * 16;
    const int xcd = blockIdx.x & 7, bi = blockIdx.x >> 3;
    if (xcd < 7) {
        const unsigned po = (unsigned)((2 * (32 * xcd + bi) + (w >> 1)) * NS + 128 * (w & 1)) * 2 + lane * 4;
        asm volatile("global_load_dword %0, %4, %5\n\tglobal_load_dword %1, %4, %6\n\tglobal_load_dword %2, %4, %7\n\tglobal_load_dword %3, %4, %8"
                     : "+v"(pl), "+v"(pa), "+v"(pr), "+v"(pi) : "v"(po), "s"(lar), "s"(aim), "s"(cre), "s"(cim) : "memory");
        const unsigned uo = (unsigned)((64 * bi + 16 * w + (lane >> 3)) * D + 64 * xcd + (lane & 7) * 8) * 2;
        glds2(u, uo, uo + 8 * D * 2 - 1024, lds0 + LT1 + w * 2048);
    } else
        {
            const unsigned nb = (bi & 1) ? 12 * 2 * D : 16 * 2 * D;
            unsigned v4[4];
#pragma unroll
            for (int i = 0; i < 4; ++i) { const unsigned o = (unsigned)(w * 4096 + i * 1024) + voff; v4[i] = (o < nb ? o : 0u) + 4096 - i * 1024; }
            glds4((const char*)(wo + (DH * (bi >> 1) + 16 * (bi & 1)) * D) - 4096, v4[0], v4[1], v4[2], v4[3], lds0 + LB + w * 4096);
        }
    asm volatile("global_load_dwordx2 %0, %1, %2 sc1" : "+v"(e0) : "v"(voff & 0), "s"(ws) : "memory");
    CODE_PF();
}

DI void role_b(const uint16_t* __restrict__ wo, const uint16_t* __restrict__ u, const uint16_t* __restrict__ lar, const uint16_t* __restrict__ aim,
               const uint16_t* __restrict__ cre, const uint16_t* __restrict__ cim, const uint16_t* __restrict__ ldt, const uint16_t* __restrict__ dskip,
               uint16_t* __restrict__ out, char* __restrict__ ws, LAS char* lds, int w, int lane, u64 code0, unsigned pl, unsigned pa, unsigned pr, unsigned pi, u64 e0, unsigned pf) {
    u64 tag;
    const unsigned lds0 = (unsigned)(uintptr_t)lds;
    LAS unsigned* sy = (LAS unsigned*)(lds + LSY);
    const __amdgpu_buffer_rsrc_t rs = __builtin_amdgcn_make_buffer_rsrc(ws, 0, WS_END, 0x00020000);
    const int b = blockIdx.x, xcd = b & 7, bi = b >> 3, r32 = lane & 31, hi = lane >> 5;
    const unsigned voff = lane * 16;
    LAS char* tab = lds + LB + (w >> 1) * CBW + (w & 1) * TABW;
    const int qt = 8 * xcd + (bi >> 2), ct = 4 * (bi & 3) + w;
    bf16x8 wf[32];
    const unsigned wvo = ct < 14 ? voff : 0x80000000u;
    unsigned wgb[4];
#pragma unroll
    for (int g = 0; g < 4; ++g) wgb[g] = WS_WO + (unsigned)(((w + g) & 3) * 8 * 14 + ct) * 1024;
#define LOADWF(i) wf[i] = BC8(__builtin_amdgcn_raw_buffer_load_b128(rs, wvo, wgb[(i) >> 3] + ((i) & 7) * 14336, 16))
    if (xcd < 7) {
        const int cc = w >> 1, hh = w & 1, pair = 32 * xcd + bi, ch = 2 * pair + cc;
        LAS char* cb = lds + LB + cc * CBW;
        u32x2 gi[2][3];
        const unsigned lw = ((const unsigned*)ldt)[pair], dw = ((const unsigned*)dskip)[pair];
        LAS char* ui = cb + UOF;
        const u32x4 z4 = {};
        *(LAS u32x4*)(ui + (64 * hh + lane) * 16) = z4; *(LAS u32x4*)(ui + (96 + lane) * 16) = z4;
        const float dt = rbf(expf(cc ? bf_hi(lw) : bf_lo(lw)));
        drain(); SBAR(); asm volatile("" : "+v"(pl), "+v"(pa), "+v"(pr), "+v"(pi), "+v"(e0), "+v"(pf));
        arrive(sy + SY_WO);
        tag = mix(e0);
        const float cm = wave_max(fmaxf(fmaxf(fabsf(bf_lo(pr)), fabsf(bf_hi(pr))), fmaxf(fabsf(bf_lo(pi)), fabsf(bf_hi(pi)))));
        unsigned ce = __float_as_uint(cm) & 0x7f800000u; ce = ce < 0x01000000u ? 0x01000000u : (ce > 0x7e800000u ? 0x7e800000u : ce);
        const float cscale = __uint_as_float(0x7f000000u - ce), oscale = 2.0f * dt * __uint_as_float(ce);
        LAS unsigned* tw = (LAS unsigned*)(tab + lane * 4);
        LAS const char* trd = tab + r32 * TROW + hi * 16;
        f32x16 dacc = {};
        f32x2 z1, z8, z256, x0;
#define ANCH(vl, va, vr, vi) do { const float a2 = (-rbf(expf(vl)) * dt) * 1.4426950408889634f, rev = ((va) * dt) * 0.15915494309189535f; \
            z1 = anchor(a2, -rev, 1.f); z8 = anchor(a2, rev, 8.f); z256 = anchor(a2, -rev, 256.f); x0 = (f32x2){(vr) * cscale, -(vi) * cscale}; \
            PIN(z1); PIN(z8); PIN(z256); SBAR(); } while (0)
#define TW(row) tw[(row) * (TROW / 4)]
#define CHAIN() do { \
            const f32x2 z1s = rot(z1), zs = rot(z256), z8s = rot(z8); \
            f32x2 X[8]; const f32x2 z16 = cmul(z8, z8, z8s); X[0] = x0; X[1] = cmul(x0, z1, z1s); f32x2 a = cmul(x0, z256, zs); \
            const f32x2 z24 = cmul(z16, z8, z8s), z32 = cmul(z16, z16, rot(z16)), z32s = rot(z32); X[2] = cmul(X[1], z1, z1s); f32x2 y = z32; \
            f32x2 c4 = cmul(X[2], z256, zs); \
            unsigned c[2][4], d0 = pk_f16(X[0]), d1 = pk_f16(X[1]), d2 = pk_f16(z8), d3 = pk_f16(z16), d4 = pk_f16(z24), one = 0x3c00u; \
            c[0][0] = pk_f16(y); c[0][1] = pk_f16(X[2]); c[0][2] = pk_f16(a); c[0][3] = pk_f16(c4); \
            TW(0) = d0; TW(1) = d1; TW(30) = d2; TW(31) = d3; TW(32) = d4; TW(21) = one; TW(29) = one; SBAR(); \
            _Pragma("unroll") for (int i = 1; i < 9; ++i) { \
                f32x2 t0, t4, t5, t6; const int j = i - 1; unsigned* pc = c[j & 1]; unsigned* nc = c[i & 1]; \
                if (i < 8) { if (i < 7) t0 = y.xx * z32; if (i < 6) t4 = X[i + 1].xx * z1; t5 = a.xx * z256; \
                    if (i == 3) t6 = X[4].xx * z256; if (i == 4 || i == 5) t6 = c4.xx * z256; if (i == 6) t6 = X[6].xx * z256; } SBAR(); \
                if (j < 7) TW(22 + j) = pc[0]; if (j < 6) TW(j + 2) = pc[1]; TW(j == 0 ? 8 : (j == 1 ? 12 : (j == 2 ? 14 : 13 + j))) = pc[2]; \
                if (j == 0) TW(9) = pc[3]; if (j == 3) TW(10) = pc[3]; if (j == 4) TW(13) = pc[3]; if (j == 5) TW(15) = pc[3]; if (j == 6) TW(11) = pc[3]; SBAR(); \
                if (i < 8) { if (i < 7) y = pfma(y.yy, z32s, t0); if (i < 6) X[i + 2] = pfma(X[i + 1].yy, z1s, t4); a = pfma(a.yy, zs, t5); \
                    if (i == 3) c4 = pfma(X[4].yy, zs, t6); if (i == 4 || i == 5) c4 = pfma(c4.yy, zs, t6); if (i == 6) c4 = pfma(X[6].yy, zs, t6); SBAR(); \
                    if (i < 7) nc[0] = pk_f16(y); if (i < 6) nc[1] = pk_f16(X[i + 2]); nc[2] = pk_f16(a); if (i >= 3 && i <= 6) nc[3] = pk_f16(c4); \
                    asm volatile("" :: "v"(pc[0]), "v"(pc[1]), "v"(pc[2]), "v"(pc[3])); SBAR(); } \
            } } while (0)
        f16x8 fa[8]; u32x4 fp[8], fq[8];
        LAS const char* trp = tab + (21 + (r32 >> 2)) * TROW + hi * 16;
        LAS const char* trq = tab + (29 + (r32 & 3)) * TROW + hi * 16;
#define RDT() do { _Pragma("unroll") for (int kk = 0; kk < 8; ++kk) { fp[kk] = *(LAS const u32x4*)(trp + kk * 32); fq[kk] = *(LAS const u32x4*)(trq + kk * 32); fa[kk] = *(LAS const f16x8*)(trd + kk * 32); } SBAR(); } while (0)
#define MMT() do { _Pragma("unroll") for (int kk = 0; kk < 8; ++kk) dacc = MFMA_F16(fa[kk], cmul4h(fp[kk], fq[kk]), dacc); } while (0)
        ANCH(bf_lo(pl), bf_lo(pa), bf_lo(pr), bf_lo(pi));
        await(sy + SY_WO, 4);
        set_tag(sy, tag);
        {
            u32x4 o0, o1; transpose_tile(lds + LT1, tab, w, lane, o0, o1);
            const unsigned to = WS_UT + (unsigned)((64 * xcd + 16 * w + (lane >> 3)) * S + 64 * bi) * 2 + (lane & 7) * 16;
            __builtin_amdgcn_s_setprio(3); BST(o0, to); BST(o1, to + 8 * S * 2);
#pragma unroll
        for (int ps = 0; ps < 2; ++ps)
#pragma unroll
            for (int i = 0; i < 3; ++i) gi[ps][i] = ((const u32x2*)g_idx.v[128 * hh + 64 * ps + lane])[i];
            __builtin_amdgcn_s_setprio(0);
        }
        CHAIN(); RDT();
        drain(); __builtin_amdgcn_s_setprio(3); put_flag(ws, FL_UT + (unsigned)(xcd * 128 + w * 32 + bi) * FS, tag); __builtin_amdgcn_s_setprio(0);
        ANCH(bf_hi(pl), bf_hi(pa), bf_hi(pr), bf_hi(pi));
        MMT(); SBAR();
        CHAIN(); RDT();
        const unsigned utf = hi ? FL_WO + (unsigned)(w * 32 + r32) * FS : FL_UT + (unsigned)(xcd * 128 + (bi >> 3) * 32 + r32) * FS;
        __builtin_amdgcn_s_setprio(3);
        const u64 utv = __hip_atomic_load((GAS u64*)(ws + utf), __ATOMIC_RELAXED, __HIP_MEMORY_SCOPE_AGENT); SBAR();
        __builtin_amdgcn_s_setprio(1);
        MMT();
        {
            LAS float* pt = (LAS float*)tab;
            *(LAS f32x4*)(pt + 8 * r32 + 4 * hi) = (f32x4){dacc[0] * oscale, dacc[1] * oscale, dacc[2] * oscale, dacc[3] * oscale};
            *(LAS f32x2*)(pt + (hi ? 384 + 2 * r32 : 256 + 4 * r32)) = (f32x2){dacc[4] * oscale, dacc[5] * oscale};
            *(LAS f32x2*)(pt + (hi ? 448 + 2 * r32 : 258 + 4 * r32)) = (f32x2){dacc[6] * oscale, dacc[7] * oscale};
            pt[hi ? 640 + r32 : 512 + r32] = dacc[8] * oscale;
            pt[hi ? 672 + r32 : 544 + r32] = dacc[9] * oscale;
            pt[hi ? 672 + r32 : 576 + r32] = dacc[10] * oscale;
            pt[hi ? 672 + r32 : 608 + r32] = dacc[11] * oscale;
        }
        LAS unsigned* ps_ = sy + SY_P + 5 * cc;
        arrive(ps_ + 0);
        LAS char* kt = cb + KOF;
        await(ps_ + 0, 2);
#pragma unroll
        for (int ps = 0; ps < 2; ++ps) {
            const int v = 81 * hh + 64 * ps + lane;
            if (ps == 0 || lane < 17) {
                const f32x4 p0 = *(LAS const f32x4*)(cb + v * 16), p1 = *(LAS const f32x4*)(cb + TABW + v * 16);
                u32x2 r = {pk_bf16(p0[0] + p1[0], p0[1] + p1[1]), pk_bf16(p0[2] + p1[2], p0[3] + p1[3])};
                if (v == 161) r = (u32x2){0u, 0u};
                *(LAS u32x2*)(kt + v * 8) = r;
            }
        }
        arrive(ps_ + 1);
        { const u64 ms = __ballot(utv != tag); if (ms) wait_flags<8>(ws, utf, tag, ms, ms); }
        CFENCE();
        const unsigned ub = WS_UT + (unsigned)(ch * S + 1024 * hh) * 2 + voff;
        const u32x4 u0 = BLD(ub), u1 = BLD(ub + 1024); SBAR();
        await(ps_ + 1, 2);
        __builtin_amdgcn_s_setprio(1);
        {
            unsigned W[2][6];
#pragma unroll
            for (int ps = 0; ps < 2; ++ps)
#pragma unroll
                for (int i = 0; i < 6; ++i) { const unsigned gv = gi[ps][i >> 1][i & 1];
                    W[ps][i] = *(LAS const uint16_t*)(kt + (gv & 0xffffu));
                    if (i < 5) W[ps][i] |= (unsigned)*(LAS const uint16_t*)(kt + (gv >> 16)) << 16; }
            if (lane < 8) *(LAS u32x4*)(cb + (2 * hh + (lane >> 2)) * CPY + (256 + (lane & 3)) * 16) = z4;
#pragma unroll
            for (int ps = 0; ps < 2; ++ps) {
                unsigned V[5];
#pragma unroll
                for (int i = 0; i < 5; ++i) V[i] = __builtin_amdgcn_alignbit(W[ps][i + 1], W[ps][i], 16);
                LAS char* cp = cb + (128 * hh + 64 * ps + lane) * 16;
                *(LAS u32x4*)(cp) = (u32x4){W[ps][0], W[ps][1], W[ps][2], W[ps][3]};
                *(LAS u32x4*)(cp + CPY) = (u32x4){V[0], V[1], V[2], V[3]};
                *(LAS u32x4*)(cp + 2 * CPY) = (u32x4){W[ps][1], W[ps][2], W[ps][3], W[ps][4]};
                *(LAS u32x4*)(cp + 3 * CPY) = (u32x4){V[1], V[2], V[3], V[4]};
            }
        }
        *(LAS u32x4*)(ui + (32 + 32 * hh + (lane >> 2)) * 80 + (lane & 3) * 16) = u0;
        *(LAS u32x4*)(ui + (48 + 32 * hh + (lane >> 2)) * 80 + (lane & 3) * 16) = u1;
        arrive(ps_ + 2); await(ps_ + 2, 2);
        __builtin_amdgcn_s_setprio(0);
        f32x16 a0 = {}, a1 = {};
        {
            const int sb = 7 - (r32 & 7);
            LAS const char* pa = cb + (sb & 3) * CPY + (255 - (r32 >> 3) + hi) * 16 + (sb >> 2) * 8 - 64 * (16 * hh + 15) - 2048;
            LAS const char* pa2 = pa + 8; asm volatile("" : "+v"(pa2));
            LAS const char* pb = ui + (32 + r32 - 16 * hh - 15) * 80 + 16 * hi;
            s16x4 ta[2][2], tb[2][2]; bf16x8 b0[2], b1[2];
#define T4(q, off) (*(LAS const s16x4*)((q) + (off)))
#define CSTEP(i, j, RD) do { const int o_ = 64 * (15 - ((j) >> 1)) + 32 * ((j) & 1), u_ = 80 * (15 - ((j) >> 1)) + 32 * ((j) & 1); \
                a1 = MFMA_BF(cat8(ta[i][0], ta[i][1]), b1[i], a1); SBAR(); if (RD) { ta[1 - i][0] = T4(pa, o_ + 2048); ta[1 - i][1] = T4(pa2, o_ + 2048); } SBAR(); \
                a0 = MFMA_BF(cat8(ta[i][0], ta[i][1]), b0[i], a0); SBAR(); if (RD) { b1[1 - i] = FR(pb, u_ + 2560); b0[1 - i] = FR(pb, u_); } SBAR(); \
                a1 = MFMA_BF(cat8(tb[i][0], tb[i][1]), b0[i], a1); SBAR(); if (RD) { tb[1 - i][0] = T4(pa, o_); tb[1 - i][1] = T4(pa2, o_); } SBAR(); } while (0)
            ta[0][0] = T4(pa, 960 + 2048); ta[0][1] = T4(pa2, 960 + 2048); b1[0] = FR(pb, 1200 + 2560); b0[0] = FR(pb, 1200); tb[0][0] = T4(pa, 960); tb[0][1] = T4(pa2, 960);
#pragma unroll
            for (int j = 0; j < 32; ++j) {
                if (j == 31) arrive(ps_ + 3);
                CSTEP(j & 1, j + 1, j < 31);
                LOADWF(j);
                SBAR();
            }
        }
        __builtin_amdgcn_s_setprio(1);
        {
            LAS char* ex = cb + hh * 9216 + r32 * 144 + hi * 16;
            await(ps_ + 3, 2);
#pragma unroll
            for (int g = 0; g < 4; ++g) {
                *(LAS f32x4*)(ex + g * 32) = (f32x4){a0[4 * g], a0[4 * g + 1], a0[4 * g + 2], a0[4 * g + 3]};
                *(LAS f32x4*)(ex + 4608 + g * 32) = (f32x4){a1[4 * g], a1[4 * g + 1], a1[4 * g + 2], a1[4 * g + 3]};
            }
            arrive(ps_ + 4);
            const float dsk = cc ? bf_hi(dw) : bf_lo(dw);
            u32x4 sk[2];
#pragma unroll
            for (int e = 0; e < 4; ++e) { sk[0][e] = pk_bf16(bf_lo(u0[e]) * dsk, bf_hi(u0[e]) * dsk); sk[1][e] = pk_bf16(bf_lo(u1[e]) * dsk, bf_hi(u1[e]) * dsk); }
            PIN(sk[0]); PIN(sk[1]);
            await(ps_ + 4, 2);
            LAS const char* er = cb + (32 * hh + (lane >> 2)) * 144 + (lane & 3) * 32;
            const unsigned bo = WS_BT + (unsigned)(ch * S + 1024 * hh) * 2 + voff;
#pragma unroll
            for (int i = 0; i < 2; ++i) {
                const f32x4 y0 = *(LAS const f32x4*)(er + i * 2304) + *(LAS const f32x4*)(er + i * 2304 + 9216);
                const f32x4 y1 = *(LAS const f32x4*)(er + i * 2304 + 16) + *(LAS const f32x4*)(er + i * 2304 + 9216 + 16);
                const u32x4 yb = {pk_bf16(y0[0], y0[1]), pk_bf16(y0[2], y0[3]), pk_bf16(y1[0], y1[1]), pk_bf16(y1[2], y1[3])};
                u32x4 r;
#pragma unroll
                for (int e = 0; e < 4; ++e) r[e] = pk_bf16(bf_lo(yb[e]) + bf_lo(sk[i][e]), bf_hi(yb[e]) + bf_hi(sk[i][e]));
                BST(r, bo + i * 1024);
            }
        }
        arrive(sy + SY_BG);
        drain();
        put_flag(ws, FL_BT + (unsigned)(xcd * 128 + bi * 4 + w) * FS, tag);
    } else {
        drain(); SBAR(); asm volatile("" : "+v"(e0), "+v"(pf));
        tag = mix(e0);
        arrive(sy + SY_WO); await(sy + SY_WO, 4);
        set_tag(sy, tag);
        LAS const char* p = lds + LB + (4 * hi + ((lane & 15) >> 2)) * (2 * D) + ((lane >> 4) & 1) * 32 + (lane & 3) * 8;
#pragma unroll
        for (int i = 0; i < 4; ++i) {
            const int ct = w + 4 * i;
            if (ct < 14) {
                u32x4 o = __builtin_bit_cast(u32x4, cat8(trread(p + ct * 64), trread(p + 8 * 2 * D + ct * 64)));
                if (hi && (bi & 1)) { o[2] = 0; o[3] = 0; }
                BST(o, WS_WO + (unsigned)(bi * 14 + ct) * 1024 + voff);
            }
        }
        drain();
        put_flag(ws, FL_WO + (unsigned)(w * 32 + bi) * FS, tag);
        __builtin_amdgcn_s_setprio(0);
        arrive(sy + SY_BG);
        await_slow(sy + SY_G, 4);
        wait_flags<8>(ws, FL_WO + (unsigned)(w * 32 + r32) * FS, tag, 0xffffffffull, 0xffffffffull);
        if (ct < 14) {
#pragma unroll
        for (int i = 0; i < 32; ++i) { LOADWF(i); __builtin_amdgcn_s_sleep(2); SBAR(); } }
    }
    const __amdgpu_buffer_rsrc_t ro = __builtin_amdgcn_make_buffer_rsrc(out, 0, S * 2 * D * 2, 0x00020000);
    await(sy + SY_BG, 4);
    CODE_PF();
    await_slow(sy + SY_X, 4);
    wait_flags<2>(ws, FL_O + (unsigned)(qt * 16 + 4 * w + (lane & 3)) * FS, tag, 15, 15);
    asm volatile("" :: "v"(pf));
    u32x4 of[8];
#pragma unroll
    for (int i = 0; i < 8; ++i) of[i] = BLD(WS_O + (unsigned)(qt * 32 + 8 * w + i) * 1024 + voff);
#pragma unroll
    for (int i = 0; i < 8; ++i) *(LAS u32x4*)(lds + LB + (8 * w + i) * 1024 + voff) = of[i];
    arrive(sy + SY_B2);
    f32x16 acc = {};
    if (ct < 14) {
#pragma unroll
        for (int i = 0; i < 8; ++i) acc = MFMA_BF(wf[i], BC8(of[i]), acc);
    }
    SBAR();
    await(sy + SY_B2, 4);
    if (ct < 14) {
        LAS const char* po[3];
#pragma unroll
        for (int j = 0; j < 3; ++j) po[j] = lds + LB + ((w + 1 + j) & 3) * 8192 + voff;
        bf16x8 og[4];
#pragma unroll
        for (int i = 0; i < 3; ++i) og[i] = FR(po[0], i * 1024);
#pragma unroll
        for (int i = 0; i < 24; ++i) { if (i < 21) og[(i + 3) & 3] = FR(po[(i + 3) >> 3], ((i + 3) & 7) * 1024); SBAR(); acc = MFMA_BF(wf[8 + i], og[i & 3], acc); SBAR(); }
        LAS char* sc = lds + LB + 40960 + w * 2304;
#pragma unroll
        for (int g = 0; g < 4; ++g) *(LAS u32x2*)(sc + r32 * 72 + 16 * g + 8 * hi) = (u32x2){pk_bf16(acc[4 * g], acc[4 * g + 1]), pk_bf16(acc[4 * g + 2], acc[4 * g + 3])};
        LAS const char* sr = sc + (lane >> 2) * 72 + (lane & 3) * 16;
        const u32x2 a0 = *(LAS const u32x2*)sr, a1 = *(LAS const u32x2*)(sr + 8), b0 = *(LAS const u32x2*)(sr + 16 * 72), b1 = *(LAS const u32x2*)(sr + 16 * 72 + 8);
        const unsigned oo = (unsigned)((32 * qt + (lane >> 2)) * 2 * D + 32 * ct) * 2 + (lane & 3) * 16;
        __builtin_amdgcn_raw_buffer_store_b128((u32x4){a0[0], a0[1], a1[0], a1[1]}, ro, oo, 0, 16);
        __builtin_amdgcn_raw_buffer_store_b128((u32x4){b0[0], b0[1], b1[0], b1[1]}, ro, oo + 16 * 2 * D * 2, 0, 16);
    }
    if (b == 0 && w == 0) put_flag(ws, 0, tag);
}

__global__ __launch_bounds__(512, 2) void fused(const uint16_t* __restrict__ x, const uint16_t* __restrict__ wqkv, const uint16_t* __restrict__ wo,
                                               const uint16_t* __restrict__ u, const uint16_t* __restrict__ lar, const uint16_t* __restrict__ aim,
                                               const uint16_t* __restrict__ cre, const uint16_t* __restrict__ cim, const uint16_t* __restrict__ ldt,
                                               const uint16_t* __restrict__ dskip, uint16_t* __restrict__ out, char* __restrict__ ws) {
    extern __shared__ __attribute__((aligned(16))) char smem[];
    LAS char* lds = (LAS char*)smem;
    const int lane = threadIdx.x & 63, w = __builtin_amdgcn_readfirstlane(threadIdx.x >> 6);
    u64 code0; asm volatile("s_getpc_b64 %0" : "=s"(code0)); code0 &= ~255ull;
    if (w == 4) ((LAS unsigned*)(lds + LSY))[lane] = 0;
    asm volatile("" :: "s"(x), "s"(wqkv), "s"(wo), "s"(u), "s"(lar), "s"(aim), "s"(cre), "s"(cim), "s"(ldt), "s"(dskip), "s"(out), "s"(ws));
    asm volatile("s_waitcnt lgkmcnt(0)\n\ts_barrier" ::: "memory");
    unsigned pl = 0, pa = 0, pr = 0, pi = 0, pf = 0; u64 e0 = 0;
    if (w >= 4) { __builtin_amdgcn_s_setprio(3); head_b(wo, u, lar, aim, cre, cim, ws, lds, w - 4, lane, code0, pl, pa, pr, pi, e0, pf); }
    else head_a(x, wqkv, lds, w, lane);
    int wr = w; asm volatile("" : "+s"(wr));
    if (wr < 4) role_a(x, wqkv, out, ws, lds, wr, lane);
    else role_b(wo, u, lar, aim, cre, cim, ldt, dskip, out, ws, lds, wr - 4, lane, code0, pl, pa, pr, pi, e0, pf);
}
}

extern "C" void kernel_launch(void* const* d_in, const int* in_sizes, int n_in, void* d_out, int out_size, void* d_ws, size_t ws_size, hipStream_t stream) {
    using namespace k;
    (void)hipFuncSetAttribute((const void*)fused, hipFuncAttributeMaxDynamicSharedMemorySize, LDS_BYTES);
    const uint16_t* const* in = (const uint16_t* const*)d_in;
    fused<<<256, 512, LDS_BYTES, stream>>>(in[0], in[1], in[2], in[3], in[4], in[5], in[6], in[7], in[8], in[9], (uint16_t*)d_out, (char*)d_ws);
}
```

```cpp
#include <hip/hip_runtime.h>
#include <hip/hip_bf16.h>
#include <stdint.h>

typedef short bf16x8 __attribute__((ext_vector_type(8)));
typedef _Float16 f16x8 __attribute__((ext_vector_type(8)));
typedef _Float16 f16x2 __attribute__((ext_vector_type(2)));
typedef __bf16 bf16x2 __attribute__((ext_vector_type(2)));
typedef short s16x4 __attribute__((ext_vector_type(4)));
typedef float f32x16 __attribute__((ext_vector_type(16)));
typedef float f32x4 __attribute__((ext_vector_type(4)));
typedef float f32x2 __attribute__((ext_vector_type(2)));
typedef unsigned u32x4 __attribute__((ext_vector_type(4)));
typedef unsigned u32x2 __attribute__((ext_vector_type(2)));
typedef unsigned long long u64;
#define LAS __attribute__((address_space(3)))
#define GAS __attribute__((address_space(1)))
#define DI __device__ __forceinline__
#define MFMA_BF(a, b, c) __builtin_amdgcn_mfma_f32_32x32x16_bf16((a), (b), (c), 0, 0, 0)
#define MFMA_F16(a, b, c) __builtin_amdgcn_mfma_f32_32x32x16_f16((a), (b), (c), 0, 0, 0)
#define SBAR() __builtin_amdgcn_sched_barrier(0)
#define CFENCE() asm volatile("" ::: "memory")
#define PIN(x) asm volatile("" : "+v"(x))
#define BC8(x) __builtin_bit_cast(bf16x8, (x))
#define EX(x) __builtin_amdgcn_exp2f(x)

namespace k {
constexpr int S = 2048, D = 448, DH = 28, NS = 256;
constexpr unsigned FS = 128;
constexpr unsigned FL_KV = FS, FL_O = FL_KV + 1024 * FS, FL_UT = FL_O + 1024 * FS, FL_BT = FL_UT + 896 * FS, FL_WO = FL_BT + 896 * FS;
constexpr unsigned WS_KV = 512u << 10;
constexpr unsigned WS_O = WS_KV + (4u << 20);
constexpr unsigned WS_WO = WS_O + (2u << 20);
constexpr unsigned WS_UT = WS_WO + (448u << 10);
constexpr unsigned WS_BT = WS_UT + D * S * 2;
constexpr unsigned WS_END = WS_BT + D * S * 2;
constexpr int WSL = 8192, LWQ = 4 * WSL, LXR = LWQ + 16384, XSL = 16384, LB = LXR + 3 * XSL;
constexpr int LQ = LXR, LX = 0;
constexpr int TROW = 272, TABW = 33 * TROW, CPY = 4160, UOF = 2 * TABW, KOF = UOF + 7680, CBW = KOF + 1312;
constexpr int LT1 = LB + 2 * CBW, LSY = LT1 + 8192, LDS_BYTES = LSY + 256;
static_assert(LDS_BYTES <= 163840 && (LSY & 15) == 0 && 4 * CPY <= UOF && LB + 40960 + 4 * 2304 <= LSY, "lds");
enum { SY_F = 2, SY_P = 10, SY_WO = 20, SY_BG = 22, SY_B2 = 23, SY_Q = 24, SY_X = 25, SY_G = 26, SY_T2 = 27 };
constexpr int SPIN = 1 << 18;
constexpr unsigned CODE_LINES = 216;

struct IdxTab { uint16_t v[256][12]; };
constexpr IdxTab make_idx() {
    IdxTab t = {};
    for (int y = 0; y < 256; ++y) for (int e = 0; e < 12; ++e) {
        const int j = 2047 - 8 * y - e; int ix = 644;
        if (j >= 0) { const int sh = j >= 1024 ? 3 : (j >= 512 ? 2 : (j >= 256 ? 1 : 0));
            ix = sh ? 128 * sh + ((j + (1 << (sh - 1)) - 1 + ((j >> sh) & 1)) >> sh) : j; }
        t.v[y][e] = (uint16_t)(2 * ix);
    }
    return t;
}
__device__ const IdxTab g_idx __attribute__((aligned(16))) = make_idx();

DI unsigned pk_bf16(float a, float b) { f32x2 v = {a, b}; return __builtin_bit_cast(unsigned, __builtin_convertvector(v, bf16x2)); }
DI unsigned pk_f16(f32x2 v) { return __builtin_bit_cast(unsigned, __builtin_convertvector(v, f16x2)); }
DI float bf_lo(unsigned w) { return __uint_as_float(w << 16); }
DI float bf_hi(unsigned w) { return __uint_as_float(w & 0xffff0000u); }
DI float rbf(float x) { return bf_lo(pk_bf16(x, x)); }
#define GL0 "s_mov_b32 %0, m0\n\ts_mov_b32 m0, %2\n\ts_nop 0\n\t"
#define GL1(V, O, C) "global_load_lds_dwordx4 " V ", %1 offset:" #O C "\n\t"
#define GLE "s_mov_b32 m0, %0"
DI void glds4(const void* sb, unsigned v0, unsigned v1, unsigned v2, unsigned v3, unsigned lb) { unsigned sv;
    asm volatile(GL0 GL1("%3", 0, "") GL1("%4", 1024, "") GL1("%5", 2048, "") GL1("%6", 3072, "") GLE : "=&s"(sv) : "s"(sb), "s"(lb), "v"(v0), "v"(v1), "v"(v2), "v"(v3) : "memory"); }
DI void glds2(const void* sb, unsigned v0, unsigned v1, unsigned lb) { unsigned sv;
    asm volatile(GL0 GL1("%3", 0, "") GL1("%4", 1024, "") GLE : "=&s"(sv) : "s"(sb), "s"(lb), "v"(v0), "v"(v1) : "memory"); }
DI void glds1(const void* sb, unsigned v0, unsigned lb) { unsigned sv;
    asm volatile(GL0 GL1("%3", 0, "") GLE : "=&s"(sv) : "s"(sb), "s"(lb), "v"(v0) : "memory"); }
DI float wave_max(float v) {
#define DPPMAX(ctrl, rm) v = fmaxf(v, __uint_as_float(__builtin_amdgcn_update_dpp(0, __float_as_uint(v), ctrl, rm, 0xf, true)))
    DPPMAX(0x111, 0xf); DPPMAX(0x112, 0xf); DPPMAX(0x114, 0xf); DPPMAX(0x118, 0xf); DPPMAX(0x142, 0xa); DPPMAX(0x143, 0xc);
    return __uint_as_float(__builtin_amdgcn_readlane(__float_as_uint(v), 63));
}
DI s16x4 trread(LAS const char* p) { return __builtin_bit_cast(s16x4, __builtin_amdgcn_ds_read_tr16_b64_v4i16((LAS s16x4*)p)); }
DI bf16x8 cat8(s16x4 a, s16x4 b) { return __builtin_shufflevector(a, b, 0, 1, 2, 3, 4, 5, 6, 7); }
DI f32x2 pfma(f32x2 a, f32x2 b, f32x2 c) { return __builtin_elementwise_fma(a, b, c); }
DI f32x2 cmul(f32x2 a, f32x2 b, f32x2 bs) { return pfma(a.yy, bs, a.xx * b); }
DI f16x8 cmul4h(u32x4 p, u32x4 q) {
    u32x4 t;
#define PM(i, a, b) "v_pk_mul_f16 %" #i ", %" #a ", %" #b " op_sel:[0,0] op_sel_hi:[0,1]\n\t"
#define PF(i, a, b) "v_pk_fma_f16 %" #i ", %" #a ", %" #b ", %" #i " op_sel:[1,1,0] op_sel_hi:[1,0,1] neg_lo:[1,0,0]\n\t"
    asm(PM(0, 4, 8) PM(1, 5, 9) PM(2, 6, 10) PM(3, 7, 11) PF(0, 4, 8) PF(1, 5, 9) PF(2, 6, 10) PF(3, 7, 11) "s_nop 1"
        : "=&v"(t[0]), "=&v"(t[1]), "=&v"(t[2]), "=&v"(t[3]) : "v"(p[0]), "v"(p[1]), "v"(p[2]), "v"(p[3]), "v"(q[0]), "v"(q[1]), "v"(q[2]), "v"(q[3]));
    return __builtin_bit_cast(f16x8, t);
}
DI f32x2 rot(f32x2 b) { return (f32x2){-b.y, b.x}; }
DI f32x2 anchor(float a2, float rev, float s) {
    const float mag = EX(a2 * s);
    float r = rev * s; r -= __builtin_rintf(r);
    return (f32x2){mag * __builtin_amdgcn_cosf(r), mag * __builtin_amdgcn_sinf(r)};
}
DI void arrive(LAS unsigned* c) { asm volatile("s_mov_b64 exec, 1\n\tds_add_u32 %0, %1\n\ts_mov_b64 exec, -1" :: "v"((unsigned)(uintptr_t)c), "v"(1u) : "memory"); }
DI void await(LAS unsigned* c, unsigned n) {
    unsigned t, i = SPIN;
    asm volatile("1:\n\tds_read_b32 %0, %2\n\ts_waitcnt lgkmcnt(0)\n\tv_cmp_gt_u32 vcc, %3, %0\n\ts_cbranch_vccz 2f\n\ts_sleep 1\n\ts_sub_u32 %1, %1, 1\n\ts_cbranch_scc0 1b\n2:"
                 : "=&v"(t), "+s"(i) : "v"((unsigned)(uintptr_t)c), "s"(n) : "vcc", "scc", "memory");
}
DI void await_slow(LAS unsigned* c, unsigned n) {
    unsigned t, i = SPIN;
    asm volatile("1:\n\tds_read_b32 %0, %2\n\ts_waitcnt lgkmcnt(0)\n\tv_cmp_gt_u32 vcc, %3, %0\n\ts_cbranch_vccz 2f\n\ts_sleep 3\n\ts_sub_u32 %1, %1, 1\n\ts_cbranch_scc0 1b\n2:"
                 : "=&v"(t), "+s"(i) : "v"((unsigned)(uintptr_t)c), "s"(n) : "vcc", "scc", "memory");
}
DI u64 get_tag(LAS unsigned* sy) {
    await(sy, 1);
    const u64 v = __hip_atomic_load((LAS u64*)sy, __ATOMIC_RELAXED, __HIP_MEMORY_SCOPE_WORKGROUP);
    return ((u64)(unsigned)__builtin_amdgcn_readfirstlane((unsigned)(v >> 32)) << 32) | (unsigned)__builtin_amdgcn_readfirstlane((unsigned)v);
}
DI void set_tag(LAS unsigned* sy, u64 tag) { asm volatile("s_mov_b64 exec, 1\n\tds_write_b64 %0, %1\n\ts_mov_b64 exec, -1" :: "v"((unsigned)(uintptr_t)sy), "v"(tag) : "memory"); }
DI void drain() { asm volatile("s_waitcnt vmcnt(0)" ::: "memory"); }
DI void put_flag(char* ws, unsigned off, u64 tag) {
    asm volatile("s_mov_b64 exec, 1\n\tglobal_store_dwordx2 %0, %1, %2 sc1\n\ts_mov_b64 exec, -1" :: "v"(off), "v"(tag), "s"(ws) : "memory");
}
template <int SLP> DI u64 wait_flags(char* ws, unsigned off, u64 tag, u64 pend, u64 need) {
    u64 t, left; unsigned i = SPIN;
    asm volatile("s_mov_b64 exec, %5\n1:\n\tglobal_load_dwordx2 %0, %3, %4 sc1\n\ts_waitcnt vmcnt(0)\n\tv_cmpx_ne_u64 vcc, %6, %0\n\ts_and_b64 %2, exec, %7\n\t"
                 "s_cbranch_scc0 2f\n\ts_sleep %8\n\ts_sub_u32 %1, %1, 1\n\ts_cbranch_scc0 1b\n2:\n\ts_mov_b64 %2, exec\n\ts_mov_b64 exec, -1"
                 : "=&v"(t), "+s"(i), "=&s"(left) : "v"(off), "s"(ws), "s"(pend), "s"(tag), "s"(need), "i"(SLP) : "vcc", "scc", "memory");
    return left;
}
#define BST(v, off) __builtin_amdgcn_raw_buffer_store_b128((v), rs, (off), 0, 16)
#define BLD(off) __builtin_amdgcn_raw_buffer_load_b128(rs, (off), 0, 16)
#define FR(p, off) (*(LAS const bf16x8*)((p) + (off)))
DI float swapmax(float m) { const auto r = __builtin_amdgcn_permlane32_swap(__float_as_uint(m), __float_as_uint(m), false, false); return fmaxf(__uint_as_float(r[0]), __uint_as_float(r[1])); }
DI u32x4 put_ref(u32x4 q1, float m, int hi) {
    const float m1 = rbf(m), m2 = rbf(m - m1), m3 = m - m1 - m2;
    if (hi) { q1[2] = pk_bf16(-m1, -m2); q1[3] = pk_bf16(-m3, 0.f); }
    return q1;
}
DI u64 mix(u64 v) {
    const u64 e = ((u64)(unsigned)__builtin_amdgcn_readfirstlane((unsigned)(v >> 32)) << 32) | (unsigned)__builtin_amdgcn_readfirstlane((unsigned)v);
    u64 z = e + 0x9E3779B97F4A7C15ull; z = (z ^ (z >> 30)) * 0xBF58476D1CE4E5B9ull; z = (z ^ (z >> 27)) * 0x94D049BB133111EBull; return (z ^ (z >> 31)) | 1ull; }
DI void transpose_tile(LAS const char* stage, LAS char* scratch, int w, int lane, u32x4& o0, u32x4& o1) {
    LAS const char* p = stage + (lane >> 4) * 2048 + ((lane & 15) >> 2) * 128 + w * 32 + (lane & 3) * 8;
    const bf16x8 t0 = cat8(trread(p), trread(p + 512)), t1 = cat8(trread(p + 1024), trread(p + 1536));
    LAS char* sc = scratch + (lane & 15) * 144 + (lane >> 4) * 32;
    *(LAS bf16x8*)sc = t0; *(LAS bf16x8*)(sc + 16) = t1;
    LAS const char* sr = scratch + (lane >> 3) * 144 + (lane & 7) * 16;
    o0 = *(LAS const u32x4*)sr; o1 = *(LAS const u32x4*)(sr + 8 * 144);
}

DI void role_a(const uint16_t* __restrict__ x, const uint16_t* __restrict__ wqkv, uint16_t* __restrict__ out, char* __restrict__ ws, LAS char* lds, int w, int lane) {
    const int head = 2 * (blockIdx.x & 7) + ((blockIdx.x >> 3) & 1), grp = blockIdx.x >> 4;
    const unsigned lds0 = (unsigned)(uintptr_t)lds;
    const int r32 = lane & 31, hi = lane >> 5;
    const int wsh = (head & 7) == 7 ? 4 : 0;
    const unsigned wv = (unsigned)((lane >> 2) * 3 * D + (lane & 3) * 8) * 2;
    const char* wb = (const char*)(wqkv + DH * head - wsh + (16 * w) * 3 * D);
    const unsigned xv0 = (unsigned)((lane >> 3) * D + (((lane & 7) ^ (lane >> 4)) * 8)) * 2, xv1 = xv0 ^ 64;
    const char* xb = (const char*)(x + (128 * grp + 32 * w) * D);
    const int rho = ((blockIdx.x >> 3) + ((blockIdx.x >> 1) & 3)) % 7;
#define SP(st) ((st) + rho >= 7 ? (st) + rho - 7 : (st) + rho)
#define WQO(st) ((st) < 4 ? LWQ + (st) * 4096 : ((st) < 6 ? 3 * WSL + ((st) - 4) * 4096 : 0))
#define LOADW(st) glds2(wb + SP(st) * (64 * 3 * D * 2), wv + 2 * D, wv + 4 * D - 1024, lds0 + ((st) & 3) * WSL + w * 2048)
#define LOADQ(st) glds1(wb + SP(st) * (64 * 3 * D * 2), wv, lds0 + WQO(st) + w * 1024)
#define LOADX(st) glds4(xb + SP(st) * 128, xv0, xv1 + 16 * D - 1024, xv0 + 32 * D - 2048, xv1 + 48 * D - 3072, lds0 + LXR + ((st) % 3) * XSL + w * 4096)
    LOADX(0); LOADW(0);
    LOADX(1); LOADW(1);
    LAS unsigned* sy = (LAS unsigned*)(lds + LSY);
    const __amdgpu_buffer_rsrc_t rs = __builtin_amdgcn_make_buffer_rsrc(ws, 0, WS_END, 0x00020000);
    const int xs = (r32 >> 1) & 7;
    LAS const char* xp[4];
#pragma unroll
    for (int ks = 0; ks < 4; ++ks) xp[ks] = lds + LXR + w * 4096 + r32 * 128 + (((2 * ks + hi) ^ xs) * 16);
    LAS const char* wp = lds + (8 * hi + ((lane & 15) >> 2)) * 64 + ((lane >> 4) & 1) * 32 + (lane & 3) * 8 + wsh * 2;
    f32x16 aq = {}, ak = {}, av = {};
    bf16x8 xf[7][4]; s16x4 fr[4][4];
#define RX(st, ks) xf[st][ks] = FR(xp[ks], ((st) % 3) * XSL)
#define RW(st, ks, j) fr[ks][j] = trread(wp + ((st) & 3) * WSL + (ks) * 2048 + ((j) >> 1) * 1024 + ((j) & 1) * 256)
#define MK(st, ks) ak = MFMA_BF(cat8(fr[ks][0], fr[ks][1]), xf[st][ks], ak); SBAR()
#define MV(st, ks) av = MFMA_BF(xf[st][ks], cat8(fr[ks][2], fr[ks][3]), av); SBAR()
#define QSTEP(st, ks) MK(st, ks); RW(st, ks + 1, 0); RW(st, ks + 1, 1); SBAR(); MV(st, ks); RW(st, ks + 1, 2); RW(st, ks + 1, 3); SBAR()
    u64 tag = 0;
    asm volatile("s_waitcnt vmcnt(6)" ::: "memory");
    arrive(sy + SY_F); LOADW(2); LOADX(2); await(sy + SY_F, 4);
    RX(0, 0); RW(0, 0, 0); RW(0, 0, 1); RW(0, 0, 2); RW(0, 0, 3); RX(0, 1); RX(0, 2); RX(0, 3); SBAR();
#pragma unroll
    for (int st = 0; st < 7; ++st) {
        QSTEP(st, 0); if (st < 4) LOADW(st + 3); if (st == 4) { LOADQ(4); LOADQ(5); } if (st == 5) LOADQ(6);
        QSTEP(st, 1); if (st < 4) LOADX(st + 3);
        QSTEP(st, 2); if (st == 3) { LOADQ(0); LOADQ(1); LOADQ(2); LOADQ(3); }
        if (st < 6) {
            if (st == 3) asm volatile("s_waitcnt vmcnt(16)" ::: "memory"); else if (st == 5) asm volatile("s_waitcnt vmcnt(7)" ::: "memory"); else asm volatile("s_waitcnt vmcnt(12)" ::: "memory");
            arrive(sy + SY_F + 1 + st); SBAR();
        }
        MK(st, 3);
        if (st < 6) { await(sy + SY_F + 1 + st, 4); RX(st + 1, 0); RW(st + 1, 0, 0); RW(st + 1, 0, 1); RW(st + 1, 0, 2); RW(st + 1, 0, 3); } SBAR();
        MV(st, 3); if (st < 6) { RX(st + 1, 1); RX(st + 1, 2); RX(st + 1, 3); } SBAR();
        if (st == 2) tag = get_tag(sy);
    }
    const float cq = 0.18898223650461362f * 1.4426950408889634f;
    const unsigned voff = lane * 16;
    u32x4 q0, q1, k0, k1, v0, v1;
    const unsigned tb = WS_KV + (unsigned)(head * 64 + 4 * grp + w) * 4096 + voff;
#pragma unroll
    for (int i = 0; i < 4; ++i) { k0[i] = pk_bf16(ak[2 * i], ak[2 * i + 1]); k1[i] = pk_bf16(ak[8 + 2 * i], ak[9 + 2 * i]); }
    if (hi) { k1[2] = 0x3f803f80u; k1[3] = 0x00003f80u; }
    BST(k0, tb); BST(k1, tb + 1024);
#pragma unroll
    for (int i = 0; i < 4; ++i) {
        v0[i] = pk_bf16(av[2 * i], av[2 * i + 1]); v1[i] = pk_bf16(av[8 + 2 * i], av[9 + 2 * i]);
        if (r32 >= DH) { v0[i] = v1[i] = (r32 == DH ? 0x3f803f80u : 0u); }
    }
    BST(v0, tb + 2048); BST(v1, tb + 3072);
    asm volatile("s_waitcnt vmcnt(4)" ::: "memory");
    arrive(sy + SY_F + 7); await(sy + SY_F + 7, 4);
    {
        s16x4 fq[4][2];
#define RQ(j) do { fq[(j) & 3][0] = trread(wp + WQO((j) >> 2) + ((j) & 3) * 1024); fq[(j) & 3][1] = trread(wp + WQO((j) >> 2) + ((j) & 3) * 1024 + 256); } while (0)
        RQ(0); RQ(1); RQ(2); SBAR();
#pragma unroll
        for (int j = 0; j < 28; ++j) {
            aq = MFMA_BF(cat8(fq[j & 3][0], fq[j & 3][1]), xf[j >> 2][j & 3], aq); SBAR(); if (j < 25) RQ(j + 3); SBAR();
            if (j == 13) { drain(); put_flag(ws, FL_KV + (unsigned)((head * 4 + w) * 16 + grp) * FS, tag); }
        }
    }
#pragma unroll
    for (int i = 0; i < 4; ++i) { q0[i] = pk_bf16(aq[2 * i] * cq, aq[2 * i + 1] * cq); q1[i] = pk_bf16(aq[8 + 2 * i] * cq, aq[9 + 2 * i] * cq); }
    if (hi) { q1[2] = 0; q1[3] = 0; }
    const unsigned kvf = FL_KV + (unsigned)((head * 4 + w) * 16 + ((grp + lane) & 15)) * FS;
    const f32x16 zero = {};
    {
        f32x16 s = MFMA_BF(BC8(k0), BC8(q0), zero); s = MFMA_BF(BC8(k1), BC8(q1), s);
        float m = s[0];
#pragma unroll
        for (int i = 1; i < 16; ++i) m = fmaxf(m, s[i]);
        q1 = put_ref(q1, swapmax(m), hi);
    }
    { LAS u32x4* qo = (LAS u32x4*)(lds + LQ + w * 2048) + lane; qo[0] = q0; qo[64] = q1; }
    arrive(sy + SY_Q);
    const u64 kv1 = __hip_atomic_load((GAS u64*)(ws + kvf), __ATOMIC_RELAXED, __HIP_MEMORY_SCOPE_AGENT); SBAR();
    f32x16 O[4] = {}; PIN(O[0]); PIN(O[1]); PIN(O[2]); PIN(O[3]);
    await(sy + SY_Q, 4);
    bf16x8 Q[4][2];
    Q[0][0] = BC8(q0); Q[0][1] = BC8(q1);
#pragma unroll
    for (int i = 1; i < 4; ++i) { LAS const char* p = lds + LQ + ((w + i) & 3) * 2048 + voff; Q[i][0] = FR(p, 0); Q[i][1] = FR(p, 1024); }
    u32x4 kv[4][4] = {{k0, k1, v0, v1}, {}, {}, {}};
    f32x16 sa, sb;
    u32x4 p0, p1 = {};
#define GRP(SC, i, P, j) do { float e0 = EX(SC[i]); SBAR(); float e1 = EX(SC[i + 1]); SBAR(); float e2 = EX(SC[i + 2]); SBAR(); float e3 = EX(SC[i + 3]); SBAR(); \
        P[j] = pk_bf16(e0, e1); SBAR(); P[j + 1] = pk_bf16(e2, e3); PIN(P); SBAR(); } while (0)
#define SLOT(SC, SN, cb, i, QK, PV1, LD) do { constexpr int nb_ = (i) < 3 ? (cb) : ((cb) + 1) % 4, pb_ = (i) > 0 ? (cb) : ((cb) + 3) % 4; \
        if (QK) SN = MFMA_BF(BC8(kv[nb_][0]), Q[((i) + 1) & 3][0], zero); SBAR(); GRP(SC, 0, p0, 0); \
        if (PV1) O[((i) + 3) & 3] = MFMA_BF(BC8(kv[pb_][3]), BC8(p1), O[((i) + 3) & 3]); SBAR(); \
        if (LD) { kv[((cb) + 3) % 4][i] = __builtin_amdgcn_raw_buffer_load_b128(rs, voff + (i) * 1024, kvo, 16); SBAR(); } GRP(SC, 4, p0, 2); \
        if (QK) SN = MFMA_BF(BC8(kv[nb_][1]), Q[((i) + 1) & 3][1], SN); SBAR(); GRP(SC, 8, p1, 0); \
        O[i] = MFMA_BF(BC8(kv[cb][2]), BC8(p0), O[i]); SBAR(); GRP(SC, 12, p1, 2); } while (0)
    const unsigned kvb = WS_KV + (unsigned)(head * 64 + w) * 4096;
    unsigned kvo = 0;
    u64 miss = __ballot(kv1 != tag) & 0xfffe;
    if (miss & 6) miss = wait_flags<2>(ws, kvf, tag, miss, 6);
    CFENCE();
#pragma unroll
    for (int j = 1; j < 3; ++j)
#pragma unroll
        for (int f = 0; f < 4; ++f) kv[j][f] = __builtin_amdgcn_raw_buffer_load_b128(rs, voff + f * 1024, kvb + ((grp + j) & 15) * 16384, 16);
    arrive(sy + SY_G);
    sa = MFMA_BF(BC8(k0), Q[0][0], zero); sa = MFMA_BF(BC8(k1), Q[0][1], sa);
    int nx = grp + 3;
#define TILE(cb) do { kvo = kvb + (nx & 15) * 16384; ++nx; SBAR(); \
        SLOT(sa, sb, cb, 0, true, true, true); SLOT(sb, sa, cb, 1, true, true, true); SLOT(sa, sb, cb, 2, true, true, true); SLOT(sb, sa, cb, 3, true, true, true); } while (0)
#pragma unroll 1
    for (unsigned msk = 0x78; msk < 0x80000; msk <<= 4) { if (__builtin_expect((miss & msk) != 0, 0)) miss = wait_flags<2>(ws, kvf, tag, miss, msk); TILE(0); TILE(1); TILE(2); TILE(3); }
    O[3] = MFMA_BF(BC8(kv[3][3]), BC8(p1), O[3]);
    f32x16 o = O[0];
    {
#pragma unroll
        for (int i = 1; i < 4; ++i) {
            LAS char* ex = lds + LX + (((w + i) & 3) * 3 + i - 1) * 4096 + voff;
#pragma unroll
            for (int g = 0; g < 4; ++g) *(LAS f32x4*)(ex + g * 1024) = (f32x4){O[i][4 * g], O[i][4 * g + 1], O[i][4 * g + 2], O[i][4 * g + 3]};
        }
        arrive(sy + SY_X); await(sy + SY_X, 4);
        LAS const char* er = lds + LX + w * 3 * 4096 + voff;
#pragma unroll
        for (int i = 0; i < 3; ++i)
#pragma unroll
            for (int g = 0; g < 4; ++g) { const f32x4 r = *(LAS const f32x4*)(er + i * 4096 + g * 1024);
#pragma unroll
                for (int e = 0; e < 4; ++e) o[4 * g + e] += r[e]; }
        f32x2 chk = {0.f, 0.f};
#pragma unroll
        for (int i = 0; i < 8; ++i) chk = pfma((f32x2){o[2 * i], o[2 * i + 1]}, (f32x2){0.f, 0.f}, chk);
        if (__builtin_expect(__any(!(chk[0] + chk[1] == 0.f) || !(o[12] < 1e30f)), 0)) {
            const unsigned hb = WS_KV + (unsigned)head * 64 * 4096 + voff;
            u32x4 qr = __builtin_bit_cast(u32x4, Q[0][1]); if (hi) { qr[2] = 0; qr[3] = 0; }
            float mm = -INFINITY;
            wait_flags<4>(ws, FL_KV + (unsigned)(head * 64 + lane) * FS, tag, ~0ull, ~0ull);
#pragma unroll 1
            for (int t = 0; t < 64; ++t) {
                f32x16 s = MFMA_BF(BC8(BLD(hb + t * 4096)), Q[0][0], zero); s = MFMA_BF(BC8(BLD(hb + t * 4096 + 1024)), BC8(qr), s);
#pragma unroll
                for (int i = 0; i < 16; ++i) mm = fmaxf(mm, s[i]);
            }
            qr = put_ref(qr, swapmax(mm), hi);
            o = zero;
#pragma unroll 1
            for (int t = 0; t < 64; ++t) {
                f32x16 s = MFMA_BF(BC8(BLD(hb + t * 4096)), Q[0][0], zero); s = MFMA_BF(BC8(BLD(hb + t * 4096 + 1024)), BC8(qr), s);
#pragma unroll
                for (int i = 0; i < 4; ++i) { p0[i] = pk_bf16(EX(s[2 * i]), EX(s[2 * i + 1])); p1[i] = pk_bf16(EX(s[8 + 2 * i]), EX(s[9 + 2 * i])); }
                o = MFMA_BF(BC8(BLD(hb + t * 4096 + 2048)), BC8(p0), o); o = MFMA_BF(BC8(BLD(hb + t * 4096 + 3072)), BC8(p1), o);
            }
        }
    }
    {
        const auto r = __builtin_amdgcn_permlane32_swap(__float_as_uint(o[12]), __float_as_uint(o[12]), false, false);
        const float rl = __builtin_amdgcn_rcpf(__uint_as_float(r[1]));
        u32x4 o0, o1;
#pragma unroll
        for (int i = 0; i < 4; ++i) { o0[i] = pk_bf16(o[2 * i] * rl, o[2 * i + 1] * rl); o1[i] = pk_bf16(o[8 + 2 * i] * rl, o[9 + 2 * i] * rl); }
        if (hi) { o1[2] = 0; o1[3] = 0; }
        const unsigned fi = (unsigned)((4 * grp + w) * 16 + head);
        BST(o0, WS_O + fi * 2048 + voff); BST(o1, WS_O + fi * 2048 + 1024 + voff);
        drain();
        put_flag(ws, FL_O + fi * FS, tag);
    }
    const int xcd = blockIdx.x & 7, bi = blockIdx.x >> 3;
    if (xcd < 7) {
        wait_flags<2>(ws, FL_BT + (unsigned)(xcd * 128 + w * 32 + r32) * FS, tag, 0xffffffffull, 0xffffffffull);
        const unsigned bo = WS_BT + (unsigned)((64 * xcd + 16 * w + (lane >> 3)) * S + 64 * bi + (lane & 7) * 8) * 2;
        const u32x4 b0 = BLD(bo), b1 = BLD(bo + 8 * S * 2);
        *(LAS u32x4*)(lds + 65536 + w * 2048 + voff) = b0; *(LAS u32x4*)(lds + 65536 + w * 2048 + 1024 + voff) = b1;
        arrive(sy + SY_T2); await(sy + SY_T2, 4);
        u32x4 o0, o1; transpose_tile(lds + 65536, lds + 73728 + w * 2304, w, lane, o0, o1);
        const __amdgpu_buffer_rsrc_t ro = __builtin_amdgcn_make_buffer_rsrc(out, 0, S * 2 * D * 2, 0x00020000);
        const unsigned oo = (unsigned)((64 * bi + 16 * w + (lane >> 3)) * 2 * D + D + 64 * xcd) * 2 + (lane & 7) * 16;
        __builtin_amdgcn_raw_buffer_store_b128(o0, ro, oo, 0, 16); __builtin_amdgcn_raw_buffer_store_b128(o1, ro, oo + 8 * 2 * D * 2, 0, 16);
    }
}

DI void role_b(const uint16_t* __restrict__ wo, const uint16_t* __restrict__ u, const uint16_t* __restrict__ lar, const uint16_t* __restrict__ aim,
               const uint16_t* __restrict__ cre, const uint16_t* __restrict__ cim, const uint16_t* __restrict__ ldt, const uint16_t* __restrict__ dskip,
               uint16_t* __restrict__ out, char* __restrict__ ws, LAS char* lds, int w, int lane, u64 code0) {
    u64 tag;
    const unsigned lds0 = (unsigned)(uintptr_t)lds;
    LAS unsigned* sy = (LAS unsigned*)(lds + LSY);
    const __amdgpu_buffer_rsrc_t rs = __builtin_amdgcn_make_buffer_rsrc(ws, 0, WS_END, 0x00020000);
    const int b = blockIdx.x, xcd = b & 7, bi = b >> 3, r32 = lane & 31, hi = lane >> 5;
    const unsigned voff = lane * 16;
    LAS char* tab = lds + LB + (w >> 1) * CBW + (w & 1) * TABW;
    unsigned pf = 0;
#define CODE_PF() asm volatile("global_load_dword %0, %1, %2" : "+v"(pf) : "v"(min((unsigned)((bi * 4 + w) * 3 + (lane < 3 ? lane : 0)), CODE_LINES - 1) * 128), "s"(code0) : "memory")
    const int qt = 8 * xcd + (bi >> 2), ct = 4 * (bi & 3) + w;
    bf16x8 wf[32];
    const unsigned wvo = ct < 14 ? voff : 0x80000000u;
    unsigned wgb[4];
#pragma unroll
    for (int g = 0; g < 4; ++g) wgb[g] = WS_WO + (unsigned)(((w + g) & 3) * 8 * 14 + ct) * 1024;
#define LOADWF(i) wf[i] = BC8(__builtin_amdgcn_raw_buffer_load_b128(rs, wvo, wgb[(i) >> 3] + ((i) & 7) * 14336, 16))
    CODE_PF();
    if (xcd < 7) {
        const int cc = w >> 1, hh = w & 1, pair = 32 * xcd + bi, ch = 2 * pair + cc;
        LAS char* cb = lds + LB + cc * CBW;
        const unsigned po = (unsigned)(ch * NS + 128 * hh) / 2 + lane;
        const unsigned pl = ((const unsigned*)lar)[po], pa = ((const unsigned*)aim)[po], pr = ((const unsigned*)cre)[po], pi = ((const unsigned*)cim)[po];
        const unsigned uo = (unsigned)((64 * bi + 16 * w + (lane >> 3)) * D + 64 * xcd + (lane & 7) * 8) * 2;
        glds2(u, uo, uo + 8 * D * 2 - 1024, lds0 + LT1 + w * 2048);
        const u64 e0 = __hip_atomic_load((GAS u64*)ws, __ATOMIC_RELAXED, __HIP_MEMORY_SCOPE_AGENT);
        u32x2 gi[2][3];
        const unsigned lw = ((const unsigned*)ldt)[pair], dw = ((const unsigned*)dskip)[pair];
        LAS char* ui = cb + UOF;
        const u32x4 z4 = {};
        *(LAS u32x4*)(ui + (64 * hh + lane) * 16) = z4; *(LAS u32x4*)(ui + (96 + lane) * 16) = z4;
        const float dt = rbf(expf(cc ? bf_hi(lw) : bf_lo(lw)));
        drain(); asm volatile("" :: "v"(pf));
        arrive(sy + SY_WO);
        tag = mix(e0);
        const float cm = wave_max(fmaxf(fmaxf(fabsf(bf_lo(pr)), fabsf(bf_hi(pr))), fmaxf(fabsf(bf_lo(pi)), fabsf(bf_hi(pi)))));
        unsigned ce = __float_as_uint(cm) & 0x7f800000u; ce = ce < 0x01000000u ? 0x01000000u : (ce > 0x7e800000u ? 0x7e800000u : ce);
        const float cscale = __uint_as_float(0x7f000000u - ce), oscale = 2.0f * dt * __uint_as_float(ce);
        LAS unsigned* tw = (LAS unsigned*)(tab + lane * 4);
        LAS const char* trd = tab + r32 * TROW + hi * 16;
        f32x16 dacc = {};
        f32x2 z1, z8, z256, x0;
#define ANCH(vl, va, vr, vi) do { const float a2 = (-rbf(expf(vl)) * dt) * 1.4426950408889634f, rev = ((va) * dt) * 0.15915494309189535f; \
            z1 = anchor(a2, -rev, 1.f); z8 = anchor(a2, rev, 8.f); z256 = anchor(a2, -rev, 256.f); x0 = (f32x2){(vr) * cscale, -(vi) * cscale}; \
            PIN(z1); PIN(z8); PIN(z256); SBAR(); } while (0)
#define TW(row) tw[(row) * (TROW / 4)]
#define CHAIN() do { \
            const f32x2 z1s = rot(z1), zs = rot(z256), z8s = rot(z8); \
            f32x2 X[8]; const f32x2 z16 = cmul(z8, z8, z8s); X[0] = x0; X[1] = cmul(x0, z1, z1s); f32x2 a = cmul(x0, z256, zs); \
            const f32x2 z24 = cmul(z16, z8, z8s), z32 = cmul(z16, z16, rot(z16)), z32s = rot(z32); X[2] = cmul(X[1], z1, z1s); f32x2 y = z32; \
            f32x2 c4 = cmul(X[2], z256, zs); \
            unsigned c[2][4], d0 = pk_f16(X[0]), d1 = pk_f16(X[1]), d2 = pk_f16(z8), d3 = pk_f16(z16), d4 = pk_f16(z24), one = 0x3c00u; \
            c[0][0] = pk_f16(y); c[0][1] = pk_f16(X[2]); c[0][2] = pk_f16(a); c[0][3] = pk_f16(c4); \
            TW(0) = d0; TW(1) = d1; TW(30) = d2; TW(31) = d3; TW(32) = d4; TW(21) = one; TW(29) = one; SBAR(); \
            _Pragma("unroll") for (int i = 1; i < 9; ++i) { \
                f32x2 t0, t4, t5, t6; const int j = i - 1; unsigned* pc = c[j & 1]; unsigned* nc = c[i & 1]; \
                if (i < 8) { if (i < 7) t0 = y.xx * z32; if (i < 6) t4 = X[i + 1].xx * z1; t5 = a.xx * z256; \
                    if (i == 3) t6 = X[4].xx * z256; if (i == 4 || i == 5) t6 = c4.xx * z256; if (i == 6) t6 = X[6].xx * z256; } SBAR(); \
                if (j < 7) TW(22 + j) = pc[0]; if (j < 6) TW(j + 2) = pc[1]; TW(j == 0 ? 8 : (j == 1 ? 12 : (j == 2 ? 14 : 13 + j))) = pc[2]; \
                if (j == 0) TW(9) = pc[3]; if (j == 3) TW(10) = pc[3]; if (j == 4) TW(13) = pc[3]; if (j == 5) TW(15) = pc[3]; if (j == 6) TW(11) = pc[3]; SBAR(); \
                if (i < 8) { if (i < 7) y = pfma(y.yy, z32s, t0); if (i < 6) X[i + 2] = pfma(X[i + 1].yy, z1s, t4); a = pfma(a.yy, zs, t5); \
                    if (i == 3) c4 = pfma(X[4].yy, zs, t6); if (i == 4 || i == 5) c4 = pfma(c4.yy, zs, t6); if (i == 6) c4 = pfma(X[6].yy, zs, t6); SBAR(); \
                    if (i < 7) nc[0] = pk_f16(y); if (i < 6) nc[1] = pk_f16(X[i + 2]); nc[2] = pk_f16(a); if (i >= 3 && i <= 6) nc[3] = pk_f16(c4); \
                    asm volatile("" :: "v"(pc[0]), "v"(pc[1]), "v"(pc[2]), "v"(pc[3])); SBAR(); } \
            } } while (0)
        f16x8 fa[8]; u32x4 fp[8], fq[8];
        LAS const char* trp = tab + (21 + (r32 >> 2)) * TROW + hi * 16;
        LAS const char* trq = tab + (29 + (r32 & 3)) * TROW + hi * 16;
#define RDT() do { _Pragma("unroll") for (int kk = 0; kk < 8; ++kk) { fp[kk] = *(LAS const u32x4*)(trp + kk * 32); fq[kk] = *(LAS const u32x4*)(trq + kk * 32); fa[kk] = *(LAS const f16x8*)(trd + kk * 32); } SBAR(); } while (0)
#define MMT() do { _Pragma("unroll") for (int kk = 0; kk < 8; ++kk) dacc = MFMA_F16(fa[kk], cmul4h(fp[kk], fq[kk]), dacc); } while (0)
        ANCH(bf_lo(pl), bf_lo(pa), bf_lo(pr), bf_lo(pi));
        await(sy + SY_WO, 4);
        set_tag(sy, tag);
        {
            u32x4 o0, o1; transpose_tile(lds + LT1, tab, w, lane, o0, o1);
            const unsigned to = WS_UT + (unsigned)((64 * xcd + 16 * w + (lane >> 3)) * S + 64 * bi) * 2 + (lane & 7) * 16;
            __builtin_amdgcn_s_setprio(3); BST(o0, to); BST(o1, to + 8 * S * 2);
#pragma unroll
        for (int ps = 0; ps < 2; ++ps)
#pragma unroll
            for (int i = 0; i < 3; ++i) gi[ps][i] = ((const u32x2*)g_idx.v[128 * hh + 64 * ps + lane])[i];
            __builtin_amdgcn_s_setprio(0);
        }
        CHAIN(); RDT();
        drain(); __builtin_amdgcn_s_setprio(3); put_flag(ws, FL_UT + (unsigned)(xcd * 128 + w * 32 + bi) * FS, tag); __builtin_amdgcn_s_setprio(0);
        ANCH(bf_hi(pl), bf_hi(pa), bf_hi(pr), bf_hi(pi));
        MMT(); SBAR();
        CHAIN(); RDT();
        const unsigned utf = hi ? FL_WO + (unsigned)(w * 32 + r32) * FS : FL_UT + (unsigned)(xcd * 128 + (bi >> 3) * 32 + r32) * FS;
        __builtin_amdgcn_s_setprio(3);
        const u64 utv = __hip_atomic_load((GAS u64*)(ws + utf), __ATOMIC_RELAXED, __HIP_MEMORY_SCOPE_AGENT); SBAR();
        __builtin_amdgcn_s_setprio(1);
        MMT();
        {
            LAS float* pt = (LAS float*)tab;
            *(LAS f32x4*)(pt + 8 * r32 + 4 * hi) = (f32x4){dacc[0] * oscale, dacc[1] * oscale, dacc[2] * oscale, dacc[3] * oscale};
            *(LAS f32x2*)(pt + (hi ? 384 + 2 * r32 : 256 + 4 * r32)) = (f32x2){dacc[4] * oscale, dacc[5] * oscale};
            *(LAS f32x2*)(pt + (hi ? 448 + 2 * r32 : 258 + 4 * r32)) = (f32x2){dacc[6] * oscale, dacc[7] * oscale};
            pt[hi ? 640 + r32 : 512 + r32] = dacc[8] * oscale;
            pt[hi ? 672 + r32 : 544 + r32] = dacc[9] * oscale;
            pt[hi ? 672 + r32 : 576 + r32] = dacc[10] * oscale;
            pt[hi ? 672 + r32 : 608 + r32] = dacc[11] * oscale;
        }
        LAS unsigned* ps_ = sy + SY_P + 5 * cc;
        arrive(ps_ + 0);
        LAS char* kt = cb + KOF;
        await(ps_ + 0, 2);
#pragma unroll
        for (int ps = 0; ps < 2; ++ps) {
            const int v = 81 * hh + 64 * ps + lane;
            if (ps == 0 || lane < 17) {
                const f32x4 p0 = *(LAS const f32x4*)(cb + v * 16), p1 = *(LAS const f32x4*)(cb + TABW + v * 16);
                u32x2 r = {pk_bf16(p0[0] + p1[0], p0[1] + p1[1]), pk_bf16(p0[2] + p1[2], p0[3] + p1[3])};
                if (v == 161) r = (u32x2){0u, 0u};
                *(LAS u32x2*)(kt + v * 8) = r;
            }
        }
        arrive(ps_ + 1);
        { const u64 ms = __ballot(utv != tag); if (ms) wait_flags<8>(ws, utf, tag, ms, ms); }
        CFENCE();
        const unsigned ub = WS_UT + (unsigned)(ch * S + 1024 * hh) * 2 + voff;
        const u32x4 u0 = BLD(ub), u1 = BLD(ub + 1024); SBAR();
        await(ps_ + 1, 2);
        __builtin_amdgcn_s_setprio(1);
        {
            unsigned W[2][6];
#pragma unroll
            for (int ps = 0; ps < 2; ++ps)
#pragma unroll
                for (int i = 0; i < 6; ++i) { const unsigned gv = gi[ps][i >> 1][i & 1];
                    W[ps][i] = *(LAS const uint16_t*)(kt + (gv & 0xffffu));
                    if (i < 5) W[ps][i] |= (unsigned)*(LAS const uint16_t*)(kt + (gv >> 16)) << 16; }
            if (lane < 8) *(LAS u32x4*)(cb + (2 * hh + (lane >> 2)) * CPY + (256 + (lane & 3)) * 16) = z4;
#pragma unroll
            for (int ps = 0; ps < 2; ++ps) {
                unsigned V[5];
#pragma unroll
                for (int i = 0; i < 5; ++i) V[i] = __builtin_amdgcn_alignbit(W[ps][i + 1], W[ps][i], 16);
                LAS char* cp = cb + (128 * hh + 64 * ps + lane) * 16;
                *(LAS u32x4*)(cp) = (u32x4){W[ps][0], W[ps][1], W[ps][2], W[ps][3]};
                *(LAS u32x4*)(cp + CPY) = (u32x4){V[0], V[1], V[2], V[3]};
                *(LAS u32x4*)(cp + 2 * CPY) = (u32x4){W[ps][1], W[ps][2], W[ps][3], W[ps][4]};
                *(LAS u32x4*)(cp + 3 * CPY) = (u32x4){V[1], V[2], V[3], V[4]};
            }
        }
        *(LAS u32x4*)(ui + (32 + 32 * hh + (lane >> 2)) * 80 + (lane & 3) * 16) = u0;
        *(LAS u32x4*)(ui + (48 + 32 * hh + (lane >> 2)) * 80 + (lane & 3) * 16) = u1;
        arrive(ps_ + 2); await(ps_ + 2, 2);
        __builtin_amdgcn_s_setprio(0);
        f32x16 a0 = {}, a1 = {};
        {
            const int sb = 7 - (r32 & 7);
            LAS const char* pa = cb + (sb & 3) * CPY + (255 - (r32 >> 3) + hi) * 16 + (sb >> 2) * 8 - 64 * (16 * hh + 15) - 2048;
            LAS const char* pa2 = pa + 8; asm volatile("" : "+v"(pa2));
            LAS const char* pb = ui + (32 + r32 - 16 * hh - 15) * 80 + 16 * hi;
            s16x4 ta[2][2], tb[2][2]; bf16x8 b0[2], b1[2];
#define T4(q, off) (*(LAS const s16x4*)((q) + (off)))
#define CSTEP(i, j, RD) do { const int o_ = 64 * (15 - ((j) >> 1)) + 32 * ((j) & 1), u_ = 80 * (15 - ((j) >> 1)) + 32 * ((j) & 1); \
                a1 = MFMA_BF(cat8(ta[i][0], ta[i][1]), b1[i], a1); SBAR(); if (RD) { ta[1 - i][0] = T4(pa, o_ + 2048); ta[1 - i][1] = T4(pa2, o_ + 2048); } SBAR(); \
                a0 = MFMA_BF(cat8(ta[i][0], ta[i][1]), b0[i], a0); SBAR(); if (RD) { b1[1 - i] = FR(pb, u_ + 2560); b0[1 - i] = FR(pb, u_); } SBAR(); \
                a1 = MFMA_BF(cat8(tb[i][0], tb[i][1]), b0[i], a1); SBAR(); if (RD) { tb[1 - i][0] = T4(pa, o_); tb[1 - i][1] = T4(pa2, o_); } SBAR(); } while (0)
            ta[0][0] = T4(pa, 960 + 2048); ta[0][1] = T4(pa2, 960 + 2048); b1[0] = FR(pb, 1200 + 2560); b0[0] = FR(pb, 1200); tb[0][0] = T4(pa, 960); tb[0][1] = T4(pa2, 960);
#pragma unroll
            for (int j = 0; j < 32; ++j) {
                if (j == 31) arrive(ps_ + 3);
                CSTEP(j & 1, j + 1, j < 31);
                LOADWF(j);
                SBAR();
            }
        }
        __builtin_amdgcn_s_setprio(1);
        {
            LAS char* ex = cb + hh * 9216 + r32 * 144 + hi * 16;
            await(ps_ + 3, 2);
#pragma unroll
            for (int g = 0; g < 4; ++g) {
                *(LAS f32x4*)(ex + g * 32) = (f32x4){a0[4 * g], a0[4 * g + 1], a0[4 * g + 2], a0[4 * g + 3]};
                *(LAS f32x4*)(ex + 4608 + g * 32) = (f32x4){a1[4 * g], a1[4 * g + 1], a1[4 * g + 2], a1[4 * g + 3]};
            }
            arrive(ps_ + 4);
            const float dsk = cc ? bf_hi(dw) : bf_lo(dw);
            u32x4 sk[2];
#pragma unroll
            for (int e = 0; e < 4; ++e) { sk[0][e] = pk_bf16(bf_lo(u0[e]) * dsk, bf_hi(u0[e]) * dsk); sk[1][e] = pk_bf16(bf_lo(u1[e]) * dsk, bf_hi(u1[e]) * dsk); }
            PIN(sk[0]); PIN(sk[1]);
            await(ps_ + 4, 2);
            LAS const char* er = cb + (32 * hh + (lane >> 2)) * 144 + (lane & 3) * 32;
            const unsigned bo = WS_BT + (unsigned)(ch * S + 1024 * hh) * 2 + voff;
#pragma unroll
            for (int i = 0; i < 2; ++i) {
                const f32x4 y0 = *(LAS const f32x4*)(er + i * 2304) + *(LAS const f32x4*)(er + i * 2304 + 9216);
                const f32x4 y1 = *(LAS const f32x4*)(er + i * 2304 + 16) + *(LAS const f32x4*)(er + i * 2304 + 9216 + 16);
                const u32x4 yb = {pk_bf16(y0[0], y0[1]), pk_bf16(y0[2], y0[3]), pk_bf16(y1[0], y1[1]), pk_bf16(y1[2], y1[3])};
                u32x4 r;
#pragma unroll
                for (int e = 0; e < 4; ++e) r[e] = pk_bf16(bf_lo(yb[e]) + bf_lo(sk[i][e]), bf_hi(yb[e]) + bf_hi(sk[i][e]));
                BST(r, bo + i * 1024);
            }
        }
        arrive(sy + SY_BG);
        drain();
        put_flag(ws, FL_BT + (unsigned)(xcd * 128 + bi * 4 + w) * FS, tag);
    } else {
        {
            const unsigned nb = (bi & 1) ? 12 * 2 * D : 16 * 2 * D;
            unsigned v4[4];
#pragma unroll
            for (int i = 0; i < 4; ++i) { const unsigned o = (unsigned)(w * 4096 + i * 1024) + voff; v4[i] = (o < nb ? o : 0u) + 4096 - i * 1024; }
            glds4((const char*)(wo + (DH * (bi >> 1) + 16 * (bi & 1)) * D) - 4096, v4[0], v4[1], v4[2], v4[3], lds0 + LB + w * 4096);
        }
        const u64 e0 = __hip_atomic_load((GAS u64*)ws, __ATOMIC_RELAXED, __HIP_MEMORY_SCOPE_AGENT);
        drain(); asm volatile("" :: "v"(pf));
        tag = mix(e0);
        arrive(sy + SY_WO); await(sy + SY_WO, 4);
        set_tag(sy, tag);
        LAS const char* p = lds + LB + (4 * hi + ((lane & 15) >> 2)) * (2 * D) + ((lane >> 4) & 1) * 32 + (lane & 3) * 8;
#pragma unroll
        for (int i = 0; i < 4; ++i) {
            const int ct = w + 4 * i;
            if (ct < 14) {
                u32x4 o = __builtin_bit_cast(u32x4, cat8(trread(p + ct * 64), trread(p + 8 * 2 * D + ct * 64)));
                if (hi && (bi & 1)) { o[2] = 0; o[3] = 0; }
                BST(o, WS_WO + (unsigned)(bi * 14 + ct) * 1024 + voff);
            }
        }
        drain();
        put_flag(ws, FL_WO + (unsigned)(w * 32 + bi) * FS, tag);
        __builtin_amdgcn_s_setprio(0);
        arrive(sy + SY_BG);
        await_slow(sy + SY_G, 4);
        wait_flags<8>(ws, FL_WO + (unsigned)(w * 32 + r32) * FS, tag, 0xffffffffull, 0xffffffffull);
        if (ct < 14) {
#pragma unroll
        for (int i = 0; i < 32; ++i) { LOADWF(i); __builtin_amdgcn_s_sleep(2); SBAR(); } }
    }
    const __amdgpu_buffer_rsrc_t ro = __builtin_amdgcn_make_buffer_rsrc(out, 0, S * 2 * D * 2, 0x00020000);
    await(sy + SY_BG, 4);
    CODE_PF();
    await_slow(sy + SY_X, 4);
    wait_flags<2>(ws, FL_O + (unsigned)(qt * 16 + 4 * w + (lane & 3)) * FS, tag, 15, 15);
    asm volatile("" :: "v"(pf));
    u32x4 of[8];
#pragma unroll
    for (int i = 0; i < 8; ++i) of[i] = BLD(WS_O + (unsigned)(qt * 32 + 8 * w + i) * 1024 + voff);
#pragma unroll
    for (int i = 0; i < 8; ++i) *(LAS u32x4*)(lds + LB + (8 * w + i) * 1024 + voff) = of[i];
    arrive(sy + SY_B2);
    f32x16 acc = {};
    if (ct < 14) {
#pragma unroll
        for (int i = 0; i < 8; ++i) acc = MFMA_BF(wf[i], BC8(of[i]), acc);
    }
    SBAR();
    await(sy + SY_B2, 4);
    if (ct < 14) {
        LAS const char* po[3];
#pragma unroll
        for (int j = 0; j < 3; ++j) po[j] = lds + LB + ((w + 1 + j) & 3) * 8192 + voff;
        bf16x8 og[4];
#pragma unroll
        for (int i = 0; i < 3; ++i) og[i] = FR(po[0], i * 1024);
#pragma unroll
        for (int i = 0; i < 24; ++i) { if (i < 21) og[(i + 3) & 3] = FR(po[(i + 3) >> 3], ((i + 3) & 7) * 1024); SBAR(); acc = MFMA_BF(wf[8 + i], og[i & 3], acc); SBAR(); }
        LAS char* sc = lds + LB + 40960 + w * 2304;
#pragma unroll
        for (int g = 0; g < 4; ++g) *(LAS u32x2*)(sc + r32 * 72 + 16 * g + 8 * hi) = (u32x2){pk_bf16(acc[4 * g], acc[4 * g + 1]), pk_bf16(acc[4 * g + 2], acc[4 * g + 3])};
        LAS const char* sr = sc + (lane >> 2) * 72 + (lane & 3) * 16;
        const u32x2 a0 = *(LAS const u32x2*)sr, a1 = *(LAS const u32x2*)(sr + 8), b0 = *(LAS const u32x2*)(sr + 16 * 72), b1 = *(LAS const u32x2*)(sr + 16 * 72 + 8);
        const unsigned oo = (unsigned)((32 * qt + (lane >> 2)) * 2 * D + 32 * ct) * 2 + (lane & 3) * 16;
        __builtin_amdgcn_raw_buffer_store_b128((u32x4){a0[0], a0[1], a1[0], a1[1]}, ro, oo, 0, 16);
        __builtin_amdgcn_raw_buffer_store_b128((u32x4){b0[0], b0[1], b1[0], b1[1]}, ro, oo + 16 * 2 * D * 2, 0, 16);
    }
    if (b == 0 && w == 0) put_flag(ws, 0, tag);
}

__global__ __launch_bounds__(512, 2) void fused(const uint16_t* __restrict__ x, const uint16_t* __restrict__ wqkv, const uint16_t* __restrict__ wo,
                                               const uint16_t* __restrict__ u, const uint16_t* __restrict__ lar, const uint16_t* __restrict__ aim,
                                               const uint16_t* __restrict__ cre, const uint16_t* __restrict__ cim, const uint16_t* __restrict__ ldt,
                                               const uint16_t* __restrict__ dskip, uint16_t* __restrict__ out, char* __restrict__ ws) {
    extern __shared__ __attribute__((aligned(16))) char smem[];
    LAS char* lds = (LAS char*)smem;
    const int lane = threadIdx.x & 63, w = __builtin_amdgcn_readfirstlane(threadIdx.x >> 6);
    u64 code0; asm volatile("s_getpc_b64 %0" : "=s"(code0)); code0 &= ~255ull;
    if (w >= 4) __builtin_amdgcn_s_setprio(3);
    if (w == 4) ((LAS unsigned*)(lds + LSY))[lane] = 0;
    asm volatile("" :: "s"(x), "s"(wqkv), "s"(wo), "s"(u), "s"(lar), "s"(aim), "s"(cre), "s"(cim), "s"(ldt), "s"(dskip), "s"(out), "s"(ws));
    asm volatile("s_waitcnt lgkmcnt(0)\n\ts_barrier" ::: "memory");
    if (w < 4) role_a(x, wqkv, out, ws, lds, w, lane);
    else role_b(wo, u, lar, aim, cre, cim, ldt, dskip, out, ws, lds, w - 4, lane, code0);
}
}

extern "C" void kernel_launch(void* const* d_in, const int* in_sizes, int n_in, void* d_out, int out_size, void* d_ws, size_t ws_size, hipStream_t stream) {
    using namespace k;
    (void)hipFuncSetAttribute((const void*)fused, hipFuncAttributeMaxDynamicSharedMemorySize, LDS_BYTES);
    const uint16_t* const* in = (const uint16_t* const*)d_in;
    fused<<<256, 512, LDS_BYTES, stream>>>(in[0], in[1], in[2], in[3], in[4], in[5], in[6], in[7], in[8], in[9], (uint16_t*)d_out, (char*)d_ws);
}
```

```cpp
#include <hip/hip_runtime.h>
#include <hip/hip_bf16.h>
#include <stdint.h>

typedef short bf16x8 __attribute__((ext_vector_type(8)));
typedef _Float16 f16x8 __attribute__((ext_vector_type(8)));
typedef _Float16 f16x2 __attribute__((ext_vector_type(2)));
typedef __bf16 bf16x2 __attribute__((ext_vector_type(2)));
typedef short s16x4 __attribute__((ext_vector_type(4)));
typedef float f32x16 __attribute__((ext_vector_type(16)));
typedef float f32x4 __attribute__((ext_vector_type(4)));
typedef float f32x2 __attribute__((ext_vector_type(2)));
typedef unsigned u32x4 __attribute__((ext_vector_type(4)));
typedef unsigned u32x2 __attribute__((ext_vector_type(2)));
typedef unsigned long long u64;
#define LAS __attribute__((address_space(3)))
#define GAS __attribute__((address_space(1)))
#define DI __device__ __forceinline__
#define MFMA_BF(a, b, c) __builtin_amdgcn_mfma_f32_32x32x16_bf16((a), (b), (c), 0, 0, 0)
#define MFMA_F16(a, b, c) __builtin_amdgcn_mfma_f32_32x32x16_f16((a), (b), (c), 0, 0, 0)
#define SBAR() __builtin_amdgcn_sched_barrier(0)
#define CFENCE() asm volatile("" ::: "memory")
#define PIN(x) asm volatile("" : "+v"(x))
#define BC8(x) __builtin_bit_cast(bf16x8, (x))
#define EX(x) __builtin_amdgcn_exp2f(x)

namespace k {
constexpr int S = 2048, D = 448, DH = 28, NS = 256;
constexpr unsigned FS = 128;
constexpr unsigned FL_KV = FS, FL_O = FL_KV + 1024 * FS, FL_UT = FL_O + 1024 * FS, FL_BT = FL_UT + 896 * FS, FL_WO = FL_BT + 896 * FS;
constexpr unsigned WS_KV = 512u << 10;
constexpr unsigned WS_O = WS_KV + (4u << 20);
constexpr unsigned WS_WO = WS_O + (2u << 20);
constexpr unsigned WS_UT = WS_WO + (448u << 10);
constexpr unsigned WS_BT = WS_UT + D * S * 2;
constexpr unsigned WS_END = WS_BT + D * S * 2;
constexpr int WSL = 8192, LWQ = 4 * WSL, LXR = LWQ + 16384, XSL = 16384, LB = LXR + 3 * XSL;
constexpr int LQ = LXR, LX = 0;
constexpr int TROW = 272, TABW = 33 * TROW, CPY = 4160, UOF = 2 * TABW, KOF = UOF + 7680, CBW = KOF + 1312;
constexpr int LT1 = LB + 2 * CBW, LSY = LT1 + 8192, LDS_BYTES = LSY + 256;
static_assert(LDS_BYTES <= 163840 && (LSY & 15) == 0 && 4 * CPY <= UOF && LB + 40960 + 4 * 2304 <= LSY, "lds");
enum { SY_F = 2, SY_P = 10, SY_WO = 20, SY_BG = 22, SY_B2 = 23, SY_Q = 24, SY_X = 25, SY_G = 26, SY_T2 = 27 };
constexpr int SPIN = 1 << 18;
constexpr unsigned CODE_LINES = 216;

struct IdxTab { uint16_t v[256][12]; };
constexpr IdxTab make_idx() {
    IdxTab t = {};
    for (int y = 0; y < 256; ++y) for (int e = 0; e < 12; ++e) {
        const int j = 2047 - 8 * y - e; int ix = 644;
        if (j >= 0) { const int sh = j >= 1024 ? 3 : (j >= 512 ? 2 : (j >= 256 ? 1 : 0));
            ix = sh ? 128 * sh + ((j + (1 << (sh - 1)) - 1 + ((j >> sh) & 1)) >> sh) : j; }
        t.v[y][e] = (uint16_t)(2 * ix);
    }
    return t;
}
__device__ const IdxTab g_idx __attribute__((aligned(16))) = make_idx();

DI unsigned pk_bf16(float a, float b) { f32x2 v = {a, b}; return __builtin_bit_cast(unsigned, __builtin_convertvector(v, bf16x2)); }
DI unsigned pk_f16(f32x2 v) { return __builtin_bit_cast(unsigned, __builtin_convertvector(v, f16x2)); }
DI float bf_lo(unsigned w) { return __uint_as_float(w << 16); }
DI float bf_hi(unsigned w) { return __uint_as_float(w & 0xffff0000u); }
DI float rbf(float x) { return bf_lo(pk_bf16(x, x)); }
#define GL0 "s_mov_b32 %0, m0\n\ts_mov_b32 m0, %2\n\ts_nop 0\n\t"
#define GL1(V, O, C) "global_load_lds_dwordx4 " V ", %1 offset:" #O C "\n\t"
#define GLE "s_mov_b32 m0, %0"
DI void glds4(const void* sb, unsigned v0, unsigned v1, unsigned v2, unsigned v3, unsigned lb) { unsigned sv;
    asm volatile(GL0 GL1("%3", 0, "") GL1("%4", 1024, "") GL1("%5", 2048, "") GL1("%6", 3072, "") GLE : "=&s"(sv) : "s"(sb), "s"(lb), "v"(v0), "v"(v1), "v"(v2), "v"(v3) : "memory"); }
DI void glds2(const void* sb, unsigned v0, unsigned v1, unsigned lb) { unsigned sv;
    asm volatile(GL0 GL1("%3", 0, "") GL1("%4", 1024, "") GLE : "=&s"(sv) : "s"(sb), "s"(lb), "v"(v0), "v"(v1) : "memory"); }
DI void glds1(const void* sb, unsigned v0, unsigned lb) { unsigned sv;
    asm volatile(GL0 GL1("%3", 0, "") GLE : "=&s"(sv) : "s"(sb), "s"(lb), "v"(v0) : "memory"); }
DI float wave_max(float v) {
#define DPPMAX(ctrl, rm) v = fmaxf(v, __uint_as_float(__builtin_amdgcn_update_dpp(0, __float_as_uint(v), ctrl, rm, 0xf, true)))
    DPPMAX(0x111, 0xf); DPPMAX(0x112, 0xf); DPPMAX(0x114, 0xf); DPPMAX(0x118, 0xf); DPPMAX(0x142, 0xa); DPPMAX(0x143, 0xc);
    return __uint_as_float(__builtin_amdgcn_readlane(__float_as_uint(v), 63));
}
DI s16x4 trread(LAS const char* p) { return __builtin_bit_cast(s16x4, __builtin_amdgcn_ds_read_tr16_b64_v4i16((LAS s16x4*)p)); }
DI bf16x8 cat8(s16x4 a, s16x4 b) { return __builtin_shufflevector(a, b, 0, 1, 2, 3, 4, 5, 6, 7); }
DI f32x2 pfma(f32x2 a, f32x2 b, f32x2 c) { return __builtin_elementwise_fma(a, b, c); }
DI f32x2 cmul(f32x2 a, f32x2 b, f32x2 bs) { return pfma(a.yy, bs, a.xx * b); }
DI f16x8 cmul4h(u32x4 p, u32x4 q) {
    u32x4 t;
#define PM(i, a, b) "v_pk_mul_f16 %" #i ", %" #a ", %" #b " op_sel:[0,0] op_sel_hi:[0,1]\n\t"
#define PF(i, a, b) "v_pk_fma_f16 %" #i ", %" #a ", %" #b ", %" #i " op_sel:[1,1,0] op_sel_hi:[1,0,1] neg_lo:[1,0,0]\n\t"
    asm(PM(0, 4, 8) PM(1, 5, 9) PM(2, 6, 10) PM(3, 7, 11) PF(0, 4, 8) PF(1, 5, 9) PF(2, 6, 10) PF(3, 7, 11) "s_nop 1"
        : "=&v"(t[0]), "=&v"(t[1]), "=&v"(t[2]), "=&v"(t[3]) : "v"(p[0]), "v"(p[1]), "v"(p[2]), "v"(p[3]), "v"(q[0]), "v"(q[1]), "v"(q[2]), "v"(q[3]));
    return __builtin_bit_cast(f16x8, t);
}
DI f32x2 rot(f32x2 b) { return (f32x2){-b.y, b.x}; }
DI f32x2 anchor(float a2, float rev, float s) {
    const float mag = EX(a2 * s);
    float r = rev * s; r -= __builtin_rintf(r);
    return (f32x2){mag * __builtin_amdgcn_cosf(r), mag * __builtin_amdgcn_sinf(r)};
}
DI void arrive(LAS unsigned* c) { asm volatile("s_mov_b64 exec, 1\n\tds_add_u32 %0, %1\n\ts_mov_b64 exec, -1" :: "v"((unsigned)(uintptr_t)c), "v"(1u) : "memory"); }
DI void await(LAS unsigned* c, unsigned n) {
    unsigned t, i = SPIN;
    asm volatile("1:\n\tds_read_b32 %0, %2\n\ts_waitcnt lgkmcnt(0)\n\tv_cmp_gt_u32 vcc, %3, %0\n\ts_cbranch_vccz 2f\n\ts_sleep 1\n\ts_sub_u32 %1, %1, 1\n\ts_cbranch_scc0 1b\n2:"
                 : "=&v"(t), "+s"(i) : "v"((unsigned)(uintptr_t)c), "s"(n) : "vcc", "scc", "memory");
}
DI void await_slow(LAS unsigned* c, unsigned n) {
    unsigned t, i = SPIN;
    asm volatile("1:\n\tds_read_b32 %0, %2\n\ts_waitcnt lgkmcnt(0)\n\tv_cmp_gt_u32 vcc, %3, %0\n\ts_cbranch_vccz 2f\n\ts_sleep 3\n\ts_sub_u32 %1, %1, 1\n\ts_cbranch_scc0 1b\n2:"
                 : "=&v"(t), "+s"(i) : "v"((unsigned)(uintptr_t)c), "s"(n) : "vcc", "scc", "memory");
}
DI u64 get_tag(LAS unsigned* sy) {
    await(sy, 1);
    const u64 v = __hip_atomic_load((LAS u64*)sy, __ATOMIC_RELAXED, __HIP_MEMORY_SCOPE_WORKGROUP);
    return ((u64)(unsigned)__builtin_amdgcn_readfirstlane((unsigned)(v >> 32)) << 32) | (unsigned)__builtin_amdgcn_readfirstlane((unsigned)v);
}
DI void set_tag(LAS unsigned* sy, u64 tag) { asm volatile("s_mov_b64 exec, 1\n\tds_write_b64 %0, %1\n\ts_mov_b64 exec, -1" :: "v"((unsigned)(uintptr_t)sy), "v"(tag) : "memory"); }
DI void drain() { asm volatile("s_waitcnt vmcnt(0)" ::: "memory"); }
DI void put_flag(char* ws, unsigned off, u64 tag) {
    asm volatile("s_mov_b64 exec, 1\n\tglobal_store_dwordx2 %0, %1, %2 sc1\n\ts_mov_b64 exec, -1" :: "v"(off), "v"(tag), "s"(ws) : "memory");
}
template <int SLP> DI u64 wait_flags(char* ws, unsigned off, u64 tag, u64 pend, u64 need) {
    u64 t, left; unsigned i = SPIN;
    asm volatile("s_mov_b64 exec, %5\n1:\n\tglobal_load_dwordx2 %0, %3, %4 sc1\n\ts_waitcnt vmcnt(0)\n\tv_cmpx_ne_u64 vcc, %6, %0\n\ts_and_b64 %2, exec, %7\n\t"
                 "s_cbranch_scc0 2f\n\ts_sleep %8\n\ts_sub_u32 %1, %1, 1\n\ts_cbranch_scc0 1b\n2:\n\ts_mov_b64 %2, exec\n\ts_mov_b64 exec, -1"
                 : "=&v"(t), "+s"(i), "=&s"(left) : "v"(off), "s"(ws), "s"(pend), "s"(tag), "s"(need), "i"(SLP) : "vcc", "scc", "memory");
    return left;
}
#define BST(v, off) __builtin_amdgcn_raw_buffer_store_b128((v), rs, (off), 0, 16)
#define BLD(off) __builtin_amdgcn_raw_buffer_load_b128(rs, (off), 0, 16)
#define FR(p, off) (*(LAS const bf16x8*)((p) + (off)))
DI float swapmax(float m) { const auto r = __builtin_amdgcn_permlane32_swap(__float_as_uint(m), __float_as_uint(m), false, false); return fmaxf(__uint_as_float(r[0]), __uint_as_float(r[1])); }
DI u32x4 put_ref(u32x4 q1, float m, int hi) {
    const float m1 = rbf(m), m2 = rbf(m - m1), m3 = m - m1 - m2;
    if (hi) { q1[2] = pk_bf16(-m1, -m2); q1[3] = pk_bf16(-m3, 0.f); }
    return q1;
}
DI u64 mix(u64 v) {
    const u64 e = ((u64)(unsigned)__builtin_amdgcn_readfirstlane((unsigned)(v >> 32)) << 32) | (unsigned)__builtin_amdgcn_readfirstlane((unsigned)v);
    u64 z = e + 0x9E3779B97F4A7C15ull; z = (z ^ (z >> 30)) * 0xBF58476D1CE4E5B9ull; z = (z ^ (z >> 27)) * 0x94D049BB133111EBull; return (z ^ (z >> 31)) | 1ull; }
DI void transpose_tile(LAS const char* stage, LAS char* scratch, int w, int lane, u32x4& o0, u32x4& o1) {
    LAS const char* p = stage + (lane >> 4) * 2048 + ((lane & 15) >> 2) * 128 + w * 32 + (lane & 3) * 8;
    const bf16x8 t0 = cat8(trread(p), trread(p + 512)), t1 = cat8(trread(p + 1024), trread(p + 1536));
    LAS char* sc = scratch + (lane & 15) * 144 + (lane >> 4) * 32;
    *(LAS bf16x8*)sc = t0; *(LAS bf16x8*)(sc + 16) = t1;
    LAS const char* sr = scratch + (lane >> 3) * 144 + (lane & 7) * 16;
    o0 = *(LAS const u32x4*)sr; o1 = *(LAS const u32x4*)(sr + 8 * 144);
}

DI void role_a(const uint16_t* __restrict__ x, const uint16_t* __restrict__ wqkv, uint16_t* __restrict__ out, char* __restrict__ ws, LAS char* lds, int w, int lane) {
    const int head = 2 * (blockIdx.x & 7) + ((blockIdx.x >> 3) & 1), grp = blockIdx.x >> 4;
    const unsigned lds0 = (unsigned)(uintptr_t)lds;
    const int r32 = lane & 31, hi = lane >> 5;
    const int wsh = (head & 7) == 7 ? 4 : 0;
    const unsigned wv = (unsigned)((lane >> 2) * 3 * D + (lane & 3) * 8) * 2;
    const char* wb = (const char*)(wqkv + DH * head - wsh + (16 * w) * 3 * D);
    const unsigned xv0 = (unsigned)((lane >> 3) * D + (((lane & 7) ^ (lane >> 4)) * 8)) * 2, xv1 = xv0 ^ 64;
    const char* xb = (const char*)(x + (128 * grp + 32 * w) * D);
    const int rho = ((blockIdx.x >> 4) + (blockIdx.x & 7)) % 7;
#define SP(st) ((st) + rho >= 7 ? (st) + rho - 7 : (st) + rho)
#define WQO(st) ((st) < 4 ? LWQ + (st) * 4096 : ((st) < 6 ? 3 * WSL + ((st) - 4) * 4096 : 0))
#define LOADW(st) glds2(wb + SP(st) * (64 * 3 * D * 2), wv + 2 * D, wv + 4 * D - 1024, lds0 + ((st) & 3) * WSL + w * 2048)
#define LOADQ(st) glds1(wb + SP(st) * (64 * 3 * D * 2), wv, lds0 + WQO(st) + w * 1024)
#define LOADX(st) glds4(xb + SP(st) * 128, xv0, xv1 + 16 * D - 1024, xv0 + 32 * D - 2048, xv1 + 48 * D - 3072, lds0 + LXR + ((st) % 3) * XSL + w * 4096)
    LOADX(0); LOADW(0);
    LOADX(1); LOADW(1);
    LAS unsigned* sy = (LAS unsigned*)(lds + LSY);
    const __amdgpu_buffer_rsrc_t rs = __builtin_amdgcn_make_buffer_rsrc(ws, 0, WS_END, 0x00020000);
    const int xs = (r32 >> 1) & 7;
    LAS const char* xp[4];
#pragma unroll
    for (int ks = 0; ks < 4; ++ks) xp[ks] = lds + LXR + w * 4096 + r32 * 128 + (((2 * ks + hi) ^ xs) * 16);
    LAS const char* wp = lds + (8 * hi + ((lane & 15) >> 2)) * 64 + ((lane >> 4) & 1) * 32 + (lane & 3) * 8 + wsh * 2;
    f32x16 aq = {}, ak = {}, av = {};
    bf16x8 xf[7][4]; s16x4 fr[4][4];
#define RX(st, ks) xf[st][ks] = FR(xp[ks], ((st) % 3) * XSL)
#define RW(st, ks, j) fr[ks][j] = trread(wp + ((st) & 3) * WSL + (ks) * 2048 + ((j) >> 1) * 1024 + ((j) & 1) * 256)
#define MK(st, ks) ak = MFMA_BF(cat8(fr[ks][0], fr[ks][1]), xf[st][ks], ak); SBAR()
#define MV(st, ks) av = MFMA_BF(xf[st][ks], cat8(fr[ks][2], fr[ks][3]), av); SBAR()
#define QSTEP(st, ks) MK(st, ks); RW(st, ks + 1, 0); RW(st, ks + 1, 1); SBAR(); MV(st, ks); RW(st, ks + 1, 2); RW(st, ks + 1, 3); SBAR()
    u64 tag = 0;
    asm volatile("s_waitcnt vmcnt(6)" ::: "memory");
    arrive(sy + SY_F); LOADW(2); LOADX(2); await(sy + SY_F, 4);
    RX(0, 0); RW(0, 0, 0); RW(0, 0, 1); RW(0, 0, 2); RW(0, 0, 3); RX(0, 1); RX(0, 2); RX(0, 3); SBAR();
#pragma unroll
    for (int st = 0; st < 7; ++st) {
        QSTEP(st, 0); if (st < 4) LOADW(st + 3); if (st == 4) { LOADQ(4); LOADQ(5); } if (st == 5) LOADQ(6);
        QSTEP(st, 1); if (st < 4) LOADX(st + 3);
        QSTEP(st, 2); if (st == 3) { LOADQ(0); LOADQ(1); LOADQ(2); LOADQ(3); }
        if (st < 6) {
            if (st == 3) asm volatile("s_waitcnt vmcnt(16)" ::: "memory"); else if (st == 5) asm volatile("s_waitcnt vmcnt(7)" ::: "memory"); else asm volatile("s_waitcnt vmcnt(12)" ::: "memory");
            arrive(sy + SY_F + 1 + st); SBAR();
        }
        MK(st, 3);
        if (st < 6) { await(sy + SY_F + 1 + st, 4); RX(st + 1, 0); RW(st + 1, 0, 0); RW(st + 1, 0, 1); RW(st + 1, 0, 2); RW(st + 1, 0, 3); } SBAR();
        MV(st, 3); if (st < 6) { RX(st + 1, 1); RX(st + 1, 2); RX(st + 1, 3); } SBAR();
        if (st == 2) tag = get_tag(sy);
    }
    const float cq = 0.18898223650461362f * 1.4426950408889634f;
    const unsigned voff = lane * 16;
    u32x4 q0, q1, k0, k1, v0, v1;
    const unsigned tb = WS_KV + (unsigned)(head * 64 + 4 * grp + w) * 4096 + voff;
#pragma unroll
    for (int i = 0; i < 4; ++i) { k0[i] = pk_bf16(ak[2 * i], ak[2 * i + 1]); k1[i] = pk_bf16(ak[8 + 2 * i], ak[9 + 2 * i]); }
    if (hi) { k1[2] = 0x3f803f80u; k1[3] = 0x00003f80u; }
    BST(k0, tb); BST(k1, tb + 1024);
#pragma unroll
    for (int i = 0; i < 4; ++i) {
        v0[i] = pk_bf16(av[2 * i], av[2 * i + 1]); v1[i] = pk_bf16(av[8 + 2 * i], av[9 + 2 * i]);
        if (r32 >= DH) { v0[i] = v1[i] = (r32 == DH ? 0x3f803f80u : 0u); }
    }
    BST(v0, tb + 2048); BST(v1, tb + 3072);
    asm volatile("s_waitcnt vmcnt(4)" ::: "memory");
    arrive(sy + SY_F + 7); await(sy + SY_F + 7, 4);
    {
        s16x4 fq[4][2];
#define RQ(j) do { fq[(j) & 3][0] = trread(wp + WQO((j) >> 2) + ((j) & 3) * 1024); fq[(j) & 3][1] = trread(wp + WQO((j) >> 2) + ((j) & 3) * 1024 + 256); } while (0)
        RQ(0); RQ(1); RQ(2); SBAR();
#pragma unroll
        for (int j = 0; j < 28; ++j) {
            aq = MFMA_BF(cat8(fq[j & 3][0], fq[j & 3][1]), xf[j >> 2][j & 3], aq); SBAR(); if (j < 25) RQ(j + 3); SBAR();
            if (j == 13) { drain(); put_flag(ws, FL_KV + (unsigned)((head * 4 + w) * 16 + grp) * FS, tag); }
        }
    }
#pragma unroll
    for (int i = 0; i < 4; ++i) { q0[i] = pk_bf16(aq[2 * i] * cq, aq[2 * i + 1] * cq); q1[i] = pk_bf16(aq[8 + 2 * i] * cq, aq[9 + 2 * i] * cq); }
    if (hi) { q1[2] = 0; q1[3] = 0; }
    const unsigned kvf = FL_KV + (unsigned)((head * 4 + w) * 16 + ((grp + lane) & 15)) * FS;
    const f32x16 zero = {};
    {
        f32x16 s = MFMA_BF(BC8(k0), BC8(q0), zero); s = MFMA_BF(BC8(k1), BC8(q1), s);
        float m = s[0];
#pragma unroll
        for (int i = 1; i < 16; ++i) m = fmaxf(m, s[i]);
        q1 = put_ref(q1, swapmax(m), hi);
    }
    { LAS u32x4* qo = (LAS u32x4*)(lds + LQ + w * 2048) + lane; qo[0] = q0; qo[64] = q1; }
    arrive(sy + SY_Q);
    const u64 kv1 = __hip_atomic_load((GAS u64*)(ws + kvf), __ATOMIC_RELAXED, __HIP_MEMORY_SCOPE_AGENT); SBAR();
    f32x16 O[4] = {}; PIN(O[0]); PIN(O[1]); PIN(O[2]); PIN(O[3]);
    await(sy + SY_Q, 4);
    bf16x8 Q[4][2];
    Q[0][0] = BC8(q0); Q[0][1] = BC8(q1);
#pragma unroll
    for (int i = 1; i < 4; ++i) { LAS const char* p = lds + LQ + ((w + i) & 3) * 2048 + voff; Q[i][0] = FR(p, 0); Q[i][1] = FR(p, 1024); }
    u32x4 kv[4][4] = {{k0, k1, v0, v1}, {}, {}, {}};
    f32x16 sa, sb;
    u32x4 p0, p1 = {};
#define GRP(SC, i, P, j) do { float e0 = EX(SC[i]); SBAR(); float e1 = EX(SC[i + 1]); SBAR(); float e2 = EX(SC[i + 2]); SBAR(); float e3 = EX(SC[i + 3]); SBAR(); \
        P[j] = pk_bf16(e0, e1); SBAR(); P[j + 1] = pk_bf16(e2, e3); PIN(P); SBAR(); } while (0)
#define SLOT(SC, SN, cb, i, QK, PV1, LD) do { constexpr int nb_ = (i) < 3 ? (cb) : ((cb) + 1) % 4, pb_ = (i) > 0 ? (cb) : ((cb) + 3) % 4; \
        if (QK) SN = MFMA_BF(BC8(kv[nb_][0]), Q[((i) + 1) & 3][0], zero); SBAR(); GRP(SC, 0, p0, 0); \
        if (PV1) O[((i) + 3) & 3] = MFMA_BF(BC8(kv[pb_][3]), BC8(p1), O[((i) + 3) & 3]); SBAR(); \
        if (LD) { kv[((cb) + 3) % 4][i] = __builtin_amdgcn_raw_buffer_load_b128(rs, voff + (i) * 1024, kvo, 16); SBAR(); } GRP(SC, 4, p0, 2); \
        if (QK) SN = MFMA_BF(BC8(kv[nb_][1]), Q[((i) + 1) & 3][1], SN); SBAR(); GRP(SC, 8, p1, 0); \
        O[i] = MFMA_BF(BC8(kv[cb][2]), BC8(p0), O[i]); SBAR(); GRP(SC, 12, p1, 2); } while (0)
    const unsigned kvb = WS_KV + (unsigned)(head * 64 + w) * 4096;
    unsigned kvo = 0;
    u64 miss = __ballot(kv1 != tag) & 0xfffe;
    if (miss & 6) miss = wait_flags<2>(ws, kvf, tag, miss, 6);
    CFENCE();
#pragma unroll
    for (int j = 1; j < 3; ++j)
#pragma unroll
        for (int f = 0; f < 4; ++f) kv[j][f] = __builtin_amdgcn_raw_buffer_load_b128(rs, voff + f * 1024, kvb + ((grp + j) & 15) * 16384, 16);
    arrive(sy + SY_G);
    sa = MFMA_BF(BC8(k0), Q[0][0], zero); sa = MFMA_BF(BC8(k1), Q[0][1], sa);
    int nx = grp + 3;
#define TILE(cb) do { kvo = kvb + (nx & 15) * 16384; ++nx; SBAR(); \
        SLOT(sa, sb, cb, 0, true, true, true); SLOT(sb, sa, cb, 1, true, true, true); SLOT(sa, sb, cb, 2, true, true, true); SLOT(sb, sa, cb, 3, true, true, true); } while (0)
#pragma unroll 1
    for (unsigned msk = 0x78; msk < 0x80000; msk <<= 4) { if (__builtin_expect((miss & msk) != 0, 0)) miss = wait_flags<2>(ws, kvf, tag, miss, msk); TILE(0); TILE(1); TILE(2); TILE(3); }
    O[3] = MFMA_BF(BC8(kv[3][3]), BC8(p1), O[3]);
    f32x16 o = O[0];
    {
#pragma unroll
        for (int i = 1; i < 4; ++i) {
            LAS char* ex = lds + LX + (((w + i) & 3) * 3 + i - 1) * 4096 + voff;
#pragma unroll
            for (int g = 0; g < 4; ++g) *(LAS f32x4*)(ex + g * 1024) = (f32x4){O[i][4 * g], O[i][4 * g + 1], O[i][4 * g + 2], O[i][4 * g + 3]};
        }
        arrive(sy + SY_X); await(sy + SY_X, 4);
        LAS const char* er = lds + LX + w * 3 * 4096 + voff;
#pragma unroll
        for (int i = 0; i < 3; ++i)
#pragma unroll
            for (int g = 0; g < 4; ++g) { const f32x4 r = *(LAS const f32x4*)(er + i * 4096 + g * 1024);
#pragma unroll
                for (int e = 0; e < 4; ++e) o[4 * g + e] += r[e]; }
        f32x2 chk = {0.f, 0.f};
#pragma unroll
        for (int i = 0; i < 8; ++i) chk = pfma((f32x2){o[2 * i], o[2 * i + 1]}, (f32x2){0.f, 0.f}, chk);
        if (__builtin_expect(__any(!(chk[0] + chk[1] == 0.f) || !(o[12] < 1e30f)), 0)) {
            const unsigned hb = WS_KV + (unsigned)head * 64 * 4096 + voff;
            u32x4 qr = __builtin_bit_cast(u32x4, Q[0][1]); if (hi) { qr[2] = 0; qr[3] = 0; }
            float mm = -INFINITY;
            wait_flags<4>(ws, FL_KV + (unsigned)(head * 64 + lane) * FS, tag, ~0ull, ~0ull);
#pragma unroll 1
            for (int t = 0; t < 64; ++t) {
                f32x16 s = MFMA_BF(BC8(BLD(hb + t * 4096)), Q[0][0], zero); s = MFMA_BF(BC8(BLD(hb + t * 4096 + 1024)), BC8(qr), s);
#pragma unroll
                for (int i = 0; i < 16; ++i) mm = fmaxf(mm, s[i]);
            }
            qr = put_ref(qr, swapmax(mm), hi);
            o = zero;
#pragma unroll 1
            for (int t = 0; t < 64; ++t) {
                f32x16 s = MFMA_BF(BC8(BLD(hb + t * 4096)), Q[0][0], zero); s = MFMA_BF(BC8(BLD(hb + t * 4096 + 1024)), BC8(qr), s);
#pragma unroll
                for (int i = 0; i < 4; ++i) { p0[i] = pk_bf16(EX(s[2 * i]), EX(s[2 * i + 1])); p1[i] = pk_bf16(EX(s[8 + 2 * i]), EX(s[9 + 2 * i])); }
                o = MFMA_BF(BC8(BLD(hb + t * 4096 + 2048)), BC8(p0), o); o = MFMA_BF(BC8(BLD(hb + t * 4096 + 3072)), BC8(p1), o);
            }
        }
    }
    {
        const auto r = __builtin_amdgcn_permlane32_swap(__float_as_uint(o[12]), __float_as_uint(o[12]), false, false);
        const float rl = __builtin_amdgcn_rcpf(__uint_as_float(r[1]));
        u32x4 o0, o1;
#pragma unroll
        for (int i = 0; i < 4; ++i) { o0[i] = pk_bf16(o[2 * i] * rl, o[2 * i + 1] * rl); o1[i] = pk_bf16(o[8 + 2 * i] * rl, o[9 + 2 * i] * rl); }
        if (hi) { o1[2] = 0; o1[3] = 0; }
        const unsigned fi = (unsigned)((4 * grp + w) * 16 + head);
        BST(o0, WS_O + fi * 2048 + voff); BST(o1, WS_O + fi * 2048 + 1024 + voff);
        drain();
        put_flag(ws, FL_O + fi * FS, tag);
    }
    const int xcd = blockIdx.x & 7, bi = blockIdx.x >> 3;
    if (xcd < 7) {
        wait_flags<2>(ws, FL_BT + (unsigned)(xcd * 128 + w * 32 + r32) * FS, tag, 0xffffffffull, 0xffffffffull);
        const unsigned bo = WS_BT + (unsigned)((64 * xcd + 16 * w + (lane >> 3)) * S + 64 * bi + (lane & 7) * 8) * 2;
        const u32x4 b0 = BLD(bo), b1 = BLD(bo + 8 * S * 2);
        *(LAS u32x4*)(lds + 65536 + w * 2048 + voff) = b0; *(LAS u32x4*)(lds + 65536 + w * 2048 + 1024 + voff) = b1;
        arrive(sy + SY_T2); await(sy + SY_T2, 4);
        u32x4 o0, o1; transpose_tile(lds + 65536, lds + 73728 + w * 2304, w, lane, o0, o1);
        const __amdgpu_buffer_rsrc_t ro = __builtin_amdgcn_make_buffer_rsrc(out, 0, S * 2 * D * 2, 0x00020000);
        const unsigned oo = (unsigned)((64 * bi + 16 * w + (lane >> 3)) * 2 * D + D + 64 * xcd) * 2 + (lane & 7) * 16;
        __builtin_amdgcn_raw_buffer_store_b128(o0, ro, oo, 0, 16); __builtin_amdgcn_raw_buffer_store_b128(o1, ro, oo + 8 * 2 * D * 2, 0, 16);
    }
}

DI void role_b(const uint16_t* __restrict__ wo, const uint16_t* __restrict__ u, const uint16_t* __restrict__ lar, const uint16_t* __restrict__ aim,
               const uint16_t* __restrict__ cre, const uint16_t* __restrict__ cim, const uint16_t* __restrict__ ldt, const uint16_t* __restrict__ dskip,
               uint16_t* __restrict__ out, char* __restrict__ ws, LAS char* lds, int w, int lane, u64 code0) {
    u64 tag;
    const unsigned lds0 = (unsigned)(uintptr_t)lds;
    LAS unsigned* sy = (LAS unsigned*)(lds + LSY);
    const __amdgpu_buffer_rsrc_t rs = __builtin_amdgcn_make_buffer_rsrc(ws, 0, WS_END, 0x00020000);
    const int b = blockIdx.x, xcd = b & 7, bi = b >> 3, r32 = lane & 31, hi = lane >> 5;
    const unsigned voff = lane * 16;
    LAS char* tab = lds + LB + (w >> 1) * CBW + (w & 1) * TABW;
    unsigned pf = 0;
#define CODE_PF() asm volatile("global_load_dword %0, %1, %2" : "+v"(pf) : "v"(min((unsigned)((bi * 4 + w) * 3 + (lane < 3 ? lane : 0)), CODE_LINES - 1) * 128), "s"(code0) : "memory")
    const int qt = 8 * xcd + (bi >> 2), ct = 4 * (bi & 3) + w;
    bf16x8 wf[32];
    const unsigned wvo = ct < 14 ? voff : 0x80000000u;
    unsigned wgb[4];
#pragma unroll
    for (int g = 0; g < 4; ++g) wgb[g] = WS_WO + (unsigned)(((w + g) & 3) * 8 * 14 + ct) * 1024;
#define LOADWF(i) wf[i] = BC8(__builtin_amdgcn_raw_buffer_load_b128(rs, wvo, wgb[(i) >> 3] + ((i) & 7) * 14336, 16))
    if (xcd < 7) {
        const int cc = w >> 1, hh = w & 1, pair = 32 * xcd + bi, ch = 2 * pair + cc;
        LAS char* cb = lds + LB + cc * CBW;
        const unsigned po = (unsigned)(ch * NS + 128 * hh) / 2 + lane;
        const unsigned pl = ((const unsigned*)lar)[po], pa = ((const unsigned*)aim)[po], pr = ((const unsigned*)cre)[po], pi = ((const unsigned*)cim)[po];
        const unsigned uo = (unsigned)((64 * bi + 16 * w + (lane >> 3)) * D + 64 * xcd + (lane & 7) * 8) * 2;
        glds2(u, uo, uo + 8 * D * 2 - 1024, lds0 + LT1 + w * 2048);
        const u64 e0 = __hip_atomic_load((GAS u64*)ws, __ATOMIC_RELAXED, __HIP_MEMORY_SCOPE_AGENT);
        u32x2 gi[2][3];
        CODE_PF();
        const unsigned lw = ((const unsigned*)ldt)[pair], dw = ((const unsigned*)dskip)[pair];
        LAS char* ui = cb + UOF;
        const u32x4 z4 = {};
        *(LAS u32x4*)(ui + (64 * hh + lane) * 16) = z4; *(LAS u32x4*)(ui + (96 + lane) * 16) = z4;
        const float dt = rbf(expf(cc ? bf_hi(lw) : bf_lo(lw)));
        drain(); asm volatile("" :: "v"(pf));
        arrive(sy + SY_WO);
        tag = mix(e0);
        const float cm = wave_max(fmaxf(fmaxf(fabsf(bf_lo(pr)), fabsf(bf_hi(pr))), fmaxf(fabsf(bf_lo(pi)), fabsf(bf_hi(pi)))));
        unsigned ce = __float_as_uint(cm) & 0x7f800000u; ce = ce < 0x01000000u ? 0x01000000u : (ce > 0x7e800000u ? 0x7e800000u : ce);
        const float cscale = __uint_as_float(0x7f000000u - ce), oscale = 2.0f * dt * __uint_as_float(ce);
        LAS unsigned* tw = (LAS unsigned*)(tab + lane * 4);
        LAS const char* trd = tab + r32 * TROW + hi * 16;
        f32x16 dacc = {};
        f32x2 z1, z8, z256, x0;
#define ANCH(vl, va, vr, vi) do { const float a2 = (-rbf(expf(vl)) * dt) * 1.4426950408889634f, rev = ((va) * dt) * 0.15915494309189535f; \
            z1 = anchor(a2, -rev, 1.f); z8 = anchor(a2, rev, 8.f); z256 = anchor(a2, -rev, 256.f); x0 = (f32x2){(vr) * cscale, -(vi) * cscale}; \
            PIN(z1); PIN(z8); PIN(z256); SBAR(); } while (0)
#define TW(row) tw[(row) * (TROW / 4)]
#define CHAIN() do { \
            const f32x2 z1s = rot(z1), zs = rot(z256), z8s = rot(z8); \
            f32x2 X[8]; const f32x2 z16 = cmul(z8, z8, z8s); X[0] = x0; X[1] = cmul(x0, z1, z1s); f32x2 a = cmul(x0, z256, zs); \
            const f32x2 z24 = cmul(z16, z8, z8s), z32 = cmul(z16, z16, rot(z16)), z32s = rot(z32); X[2] = cmul(X[1], z1, z1s); f32x2 y = z32; \
            f32x2 c4 = cmul(X[2], z256, zs); \
            unsigned c[2][4], d0 = pk_f16(X[0]), d1 = pk_f16(X[1]), d2 = pk_f16(z8), d3 = pk_f16(z16), d4 = pk_f16(z24), one = 0x3c00u; \
            c[0][0] = pk_f16(y); c[0][1] = pk_f16(X[2]); c[0][2] = pk_f16(a); c[0][3] = pk_f16(c4); \
            TW(0) = d0; TW(1) = d1; TW(30) = d2; TW(31) = d3; TW(32) = d4; TW(21) = one; TW(29) = one; SBAR(); \
            _Pragma("unroll") for (int i = 1; i < 9; ++i) { \
                f32x2 t0, t4, t5, t6; const int j = i - 1; unsigned* pc = c[j & 1]; unsigned* nc = c[i & 1]; \
                if (i < 8) { if (i < 7) t0 = y.xx * z32; if (i < 6) t4 = X[i + 1].xx * z1; t5 = a.xx * z256; \
                    if (i == 3) t6 = X[4].xx * z256; if (i == 4 || i == 5) t6 = c4.xx * z256; if (i == 6) t6 = X[6].xx * z256; } SBAR(); \
                if (j < 7) TW(22 + j) = pc[0]; if (j < 6) TW(j + 2) = pc[1]; TW(j == 0 ? 8 : (j == 1 ? 12 : (j == 2 ? 14 : 13 + j))) = pc[2]; \
                if (j == 0) TW(9) = pc[3]; if (j == 3) TW(10) = pc[3]; if (j == 4) TW(13) = pc[3]; if (j == 5) TW(15) = pc[3]; if (j == 6) TW(11) = pc[3]; SBAR(); \
                if (i < 8) { if (i < 7) y = pfma(y.yy, z32s, t0); if (i < 6) X[i + 2] = pfma(X[i + 1].yy, z1s, t4); a = pfma(a.yy, zs, t5); \
                    if (i == 3) c4 = pfma(X[4].yy, zs, t6); if (i == 4 || i == 5) c4 = pfma(c4.yy, zs, t6); if (i == 6) c4 = pfma(X[6].yy, zs, t6); SBAR(); \
                    if (i < 7) nc[0] = pk_f16(y); if (i < 6) nc[1] = pk_f16(X[i + 2]); nc[2] = pk_f16(a); if (i >= 3 && i <= 6) nc[3] = pk_f16(c4); \
                    asm volatile("" :: "v"(pc[0]), "v"(pc[1]), "v"(pc[2]), "v"(pc[3])); SBAR(); } \
            } } while (0)
        f16x8 fa[8]; u32x4 fp[8], fq[8];
        LAS const char* trp = tab + (21 + (r32 >> 2)) * TROW + hi * 16;
        LAS const char* trq = tab + (29 + (r32 & 3)) * TROW + hi * 16;
#define RDT() do { _Pragma("unroll") for (int kk = 0; kk < 8; ++kk) { fp[kk] = *(LAS const u32x4*)(trp + kk * 32); fq[kk] = *(LAS const u32x4*)(trq + kk * 32); fa[kk] = *(LAS const f16x8*)(trd + kk * 32); } SBAR(); } while (0)
#define MMT() do { _Pragma("unroll") for (int kk = 0; kk < 8; ++kk) dacc = MFMA_F16(fa[kk], cmul4h(fp[kk], fq[kk]), dacc); } while (0)
        ANCH(bf_lo(pl), bf_lo(pa), bf_lo(pr), bf_lo(pi));
        await(sy + SY_WO, 4);
        set_tag(sy, tag);
        {
            u32x4 o0, o1; transpose_tile(lds + LT1, tab, w, lane, o0, o1);
            const unsigned to = WS_UT + (unsigned)((64 * xcd + 16 * w + (lane >> 3)) * S + 64 * bi) * 2 + (lane & 7) * 16;
            __builtin_amdgcn_s_setprio(3); BST(o0, to); BST(o1, to + 8 * S * 2);
#pragma unroll
            for (int ps = 0; ps < 2; ++ps)
#pragma unroll
                for (int i = 0; i < 3; ++i) gi[ps][i] = ((const u32x2*)g_idx.v[128 * hh + 64 * ps + lane])[i];
            __builtin_amdgcn_s_setprio(0);
        }
        CHAIN(); RDT();
        drain(); __builtin_amdgcn_s_setprio(3); put_flag(ws, FL_UT + (unsigned)(xcd * 128 + w * 32 + bi) * FS, tag); __builtin_amdgcn_s_setprio(0);
        ANCH(bf_hi(pl), bf_hi(pa), bf_hi(pr), bf_hi(pi));
        MMT(); SBAR();
        CHAIN(); RDT();
        const unsigned utf = hi ? FL_WO + (unsigned)(w * 32 + r32) * FS : FL_UT + (unsigned)(xcd * 128 + (bi >> 3) * 32 + r32) * FS;
        __builtin_amdgcn_s_setprio(3);
        const u64 utv = __hip_atomic_load((GAS u64*)(ws + utf), __ATOMIC_RELAXED, __HIP_MEMORY_SCOPE_AGENT); SBAR();
        __builtin_amdgcn_s_setprio(1);
        MMT();
        {
            LAS float* pt = (LAS float*)tab;
            *(LAS f32x4*)(pt + 8 * r32 + 4 * hi) = (f32x4){dacc[0] * oscale, dacc[1] * oscale, dacc[2] * oscale, dacc[3] * oscale};
            *(LAS f32x2*)(pt + (hi ? 384 + 2 * r32 : 256 + 4 * r32)) = (f32x2){dacc[4] * oscale, dacc[5] * oscale};
            *(LAS f32x2*)(pt + (hi ? 448 + 2 * r32 : 258 + 4 * r32)) = (f32x2){dacc[6] * oscale, dacc[7] * oscale};
            pt[hi ? 640 + r32 : 512 + r32] = dacc[8] * oscale;
            pt[hi ? 672 + r32 : 544 + r32] = dacc[9] * oscale;
            pt[hi ? 672 + r32 : 576 + r32] = dacc[10] * oscale;
            pt[hi ? 672 + r32 : 608 + r32] = dacc[11] * oscale;
        }
        LAS unsigned* ps_ = sy + SY_P + 5 * cc;
        arrive(ps_ + 0);
        LAS char* kt = cb + KOF;
        await(ps_ + 0, 2);
#pragma unroll
        for (int ps = 0; ps < 2; ++ps) {
            const int v = 81 * hh + 64 * ps + lane;
            if (ps == 0 || lane < 17) {
                const f32x4 p0 = *(LAS const f32x4*)(cb + v * 16), p1 = *(LAS const f32x4*)(cb + TABW + v * 16);
                u32x2 r = {pk_bf16(p0[0] + p1[0], p0[1] + p1[1]), pk_bf16(p0[2] + p1[2], p0[3] + p1[3])};
                if (v == 161) r = (u32x2){0u, 0u};
                *(LAS u32x2*)(kt + v * 8) = r;
            }
        }
        arrive(ps_ + 1);
        { const u64 ms = __ballot(utv != tag); if (ms) wait_flags<8>(ws, utf, tag, ms, ms); }
        CFENCE();
        const unsigned ub = WS_UT + (unsigned)(ch * S + 1024 * hh) * 2 + voff;
        const u32x4 u0 = BLD(ub), u1 = BLD(ub + 1024); SBAR();
        await(ps_ + 1, 2);
        __builtin_amdgcn_s_setprio(1);
        {
            unsigned W[2][6];
#pragma unroll
            for (int ps = 0; ps < 2; ++ps)
#pragma unroll
                for (int i = 0; i < 6; ++i) { const unsigned gv = gi[ps][i >> 1][i & 1];
                    W[ps][i] = *(LAS const uint16_t*)(kt + (gv & 0xffffu));
                    if (i < 5) W[ps][i] |= (unsigned)*(LAS const uint16_t*)(kt + (gv >> 16)) << 16; }
            if (lane < 8) *(LAS u32x4*)(cb + (2 * hh + (lane >> 2)) * CPY + (256 + (lane & 3)) * 16) = z4;
#pragma unroll
            for (int ps = 0; ps < 2; ++ps) {
                unsigned V[5];
#pragma unroll
                for (int i = 0; i < 5; ++i) V[i] = __builtin_amdgcn_alignbit(W[ps][i + 1], W[ps][i], 16);
                LAS char* cp = cb + (128 * hh + 64 * ps + lane) * 16;
                *(LAS u32x4*)(cp) = (u32x4){W[ps][0], W[ps][1], W[ps][2], W[ps][3]};
                *(LAS u32x4*)(cp + CPY) = (u32x4){V[0], V[1], V[2], V[3]};
                *(LAS u32x4*)(cp + 2 * CPY) = (u32x4){W[ps][1], W[ps][2], W[ps][3], W[ps][4]};
                *(LAS u32x4*)(cp + 3 * CPY) = (u32x4){V[1], V[2], V[3], V[4]};
            }
        }
        *(LAS u32x4*)(ui + (32 + 32 * hh + (lane >> 2)) * 80 + (lane & 3) * 16) = u0;
        *(LAS u32x4*)(ui + (48 + 32 * hh + (lane >> 2)) * 80 + (lane & 3) * 16) = u1;
        arrive(ps_ + 2); await(ps_ + 2, 2);
        __builtin_amdgcn_s_setprio(0);
        f32x16 a0 = {}, a1 = {};
        {
            const int sb = 7 - (r32 & 7);
            LAS const char* pa = cb + (sb & 3) * CPY + (255 - (r32 >> 3) + hi) * 16 + (sb >> 2) * 8 - 64 * (16 * hh + 15) - 2048;
            LAS const char* pa2 = pa + 8; asm volatile("" : "+v"(pa2));
            LAS const char* pb = ui + (32 + r32 - 16 * hh - 15) * 80 + 16 * hi;
            s16x4 ta[2][2], tb[2][2]; bf16x8 b0[2], b1[2];
#define T4(q, off) (*(LAS const s16x4*)((q) + (off)))
#define CSTEP(i, j, RD) do { const int o_ = 64 * (15 - ((j) >> 1)) + 32 * ((j) & 1), u_ = 80 * (15 - ((j) >> 1)) + 32 * ((j) & 1); \
                a1 = MFMA_BF(cat8(ta[i][0], ta[i][1]), b1[i], a1); SBAR(); if (RD) { ta[1 - i][0] = T4(pa, o_ + 2048); ta[1 - i][1] = T4(pa2, o_ + 2048); } SBAR(); \
                a0 = MFMA_BF(cat8(ta[i][0], ta[i][1]), b0[i], a0); SBAR(); if (RD) { b1[1 - i] = FR(pb, u_ + 2560); b0[1 - i] = FR(pb, u_); } SBAR(); \
                a1 = MFMA_BF(cat8(tb[i][0], tb[i][1]), b0[i], a1); SBAR(); if (RD) { tb[1 - i][0] = T4(pa, o_); tb[1 - i][1] = T4(pa2, o_); } SBAR(); } while (0)
            ta[0][0] = T4(pa, 960 + 2048); ta[0][1] = T4(pa2, 960 + 2048); b1[0] = FR(pb, 1200 + 2560); b0[0] = FR(pb, 1200); tb[0][0] = T4(pa, 960); tb[0][1] = T4(pa2, 960);
#pragma unroll
            for (int j = 0; j < 32; ++j) {
                if (j == 31) arrive(ps_ + 3);
                CSTEP(j & 1, j + 1, j < 31);
                LOADWF(j);
                SBAR();
            }
        }
        __builtin_amdgcn_s_setprio(1);
        {
            LAS char* ex = cb + hh * 9216 + r32 * 144 + hi * 16;
            await(ps_ + 3, 2);
#pragma unroll
            for (int g = 0; g < 4; ++g) {
                *(LAS f32x4*)(ex + g * 32) = (f32x4){a0[4 * g], a0[4 * g + 1], a0[4 * g + 2], a0[4 * g + 3]};
                *(LAS f32x4*)(ex + 4608 + g * 32) = (f32x4){a1[4 * g], a1[4 * g + 1], a1[4 * g + 2], a1[4 * g + 3]};
            }
            arrive(ps_ + 4);
            const float dsk = cc ? bf_hi(dw) : bf_lo(dw);
            u32x4 sk[2];
#pragma unroll
            for (int e = 0; e < 4; ++e) { sk[0][e] = pk_bf16(bf_lo(u0[e]) * dsk, bf_hi(u0[e]) * dsk); sk[1][e] = pk_bf16(bf_lo(u1[e]) * dsk, bf_hi(u1[e]) * dsk); }
            PIN(sk[0]); PIN(sk[1]);
            await(ps_ + 4, 2);
            LAS const char* er = cb + (32 * hh + (lane >> 2)) * 144 + (lane & 3) * 32;
            const unsigned bo = WS_BT + (unsigned)(ch * S + 1024 * hh) * 2 + voff;
#pragma unroll
            for (int i = 0; i < 2; ++i) {
                const f32x4 y0 = *(LAS const f32x4*)(er + i * 2304) + *(LAS const f32x4*)(er + i * 2304 + 9216);
                const f32x4 y1 = *(LAS const f32x4*)(er + i * 2304 + 16) + *(LAS const f32x4*)(er + i * 2304 + 9216 + 16);
                const u32x4 yb = {pk_bf16(y0[0], y0[1]), pk_bf16(y0[2], y0[3]), pk_bf16(y1[0], y1[1]), pk_bf16(y1[2], y1[3])};
                u32x4 r;
#pragma unroll
                for (int e = 0; e < 4; ++e) r[e] = pk_bf16(bf_lo(yb[e]) + bf_lo(sk[i][e]), bf_hi(yb[e]) + bf_hi(sk[i][e]));
                BST(r, bo + i * 1024);
            }
        }
        arrive(sy + SY_BG);
        drain();
        put_flag(ws, FL_BT + (unsigned)(xcd * 128 + bi * 4 + w) * FS, tag);
    } else {
        {
            const unsigned nb = (bi & 1) ? 12 * 2 * D : 16 * 2 * D;
            unsigned v4[4];
#pragma unroll
            for (int i = 0; i < 4; ++i) { const unsigned o = (unsigned)(w * 4096 + i * 1024) + voff; v4[i] = (o < nb ? o : 0u) + 4096 - i * 1024; }
            glds4((const char*)(wo + (DH * (bi >> 1) + 16 * (bi & 1)) * D) - 4096, v4[0], v4[1], v4[2], v4[3], lds0 + LB + w * 4096);
        }
        const u64 e0 = __hip_atomic_load((GAS u64*)ws, __ATOMIC_RELAXED, __HIP_MEMORY_SCOPE_AGENT);
        CODE_PF();
        drain(); asm volatile("" :: "v"(pf));
        tag = mix(e0);
        arrive(sy + SY_WO); await(sy + SY_WO, 4);
        set_tag(sy, tag);
        LAS const char* p = lds + LB + (4 * hi + ((lane & 15) >> 2)) * (2 * D) + ((lane >> 4) & 1) * 32 + (lane & 3) * 8;
#pragma unroll
        for (int i = 0; i < 4; ++i) {
            const int ct = w + 4 * i;
            if (ct < 14) {
                u32x4 o = __builtin_bit_cast(u32x4, cat8(trread(p + ct * 64), trread(p + 8 * 2 * D + ct * 64)));
                if (hi && (bi & 1)) { o[2] = 0; o[3] = 0; }
                BST(o, WS_WO + (unsigned)(bi * 14 + ct) * 1024 + voff);
            }
        }
        drain();
        put_flag(ws, FL_WO + (unsigned)(w * 32 + bi) * FS, tag);
        __builtin_amdgcn_s_setprio(0);
        arrive(sy + SY_BG);
        await_slow(sy + SY_G, 4);
        wait_flags<8>(ws, FL_WO + (unsigned)(w * 32 + r32) * FS, tag, 0xffffffffull, 0xffffffffull);
        if (ct < 14) {
#pragma unroll
            for (int i = 0; i < 32; ++i) { LOADWF(i); __builtin_amdgcn_s_sleep(2); SBAR(); }
        }
    }
    const __amdgpu_buffer_rsrc_t ro = __builtin_amdgcn_make_buffer_rsrc(out, 0, S * 2 * D * 2, 0x00020000);
    await(sy + SY_BG, 4);
    CODE_PF();
    await_slow(sy + SY_X, 4);
    wait_flags<2>(ws, FL_O + (unsigned)(qt * 16 + 4 * w + (lane & 3)) * FS, tag, 15, 15);
    asm volatile("" :: "v"(pf));
    u32x4 of[8];
#pragma unroll
    for (int i = 0; i < 8; ++i) of[i] = BLD(WS_O + (unsigned)(qt * 32 + 8 * w + i) * 1024 + voff);
#pragma unroll
    for (int i = 0; i < 8; ++i) *(LAS u32x4*)(lds + LB + (8 * w + i) * 1024 + voff) = of[i];
    arrive(sy + SY_B2);
    f32x16 acc = {};
    if (ct < 14) {
#pragma unroll
        for (int i = 0; i < 8; ++i) acc = MFMA_BF(wf[i], BC8(of[i]), acc);
    }
    SBAR();
    await(sy + SY_B2, 4);
    if (ct < 14) {
        LAS const char* po[3];
#pragma unroll
        for (int j = 0; j < 3; ++j) po[j] = lds + LB + ((w + 1 + j) & 3) * 8192 + voff;
        bf16x8 og[4];
#pragma unroll
        for (int i = 0; i < 3; ++i) og[i] = FR(po[0], i * 1024);
#pragma unroll
        for (int i = 0; i < 24; ++i) { if (i < 21) og[(i + 3) & 3] = FR(po[(i + 3) >> 3], ((i + 3) & 7) * 1024); SBAR(); acc = MFMA_BF(wf[8 + i], og[i & 3], acc); SBAR(); }
        LAS char* sc = lds + LB + 40960 + w * 2304;
#pragma unroll
        for (int g = 0; g < 4; ++g) *(LAS u32x2*)(sc + r32 * 72 + 16 * g + 8 * hi) = (u32x2){pk_bf16(acc[4 * g], acc[4 * g + 1]), pk_bf16(acc[4 * g + 2], acc[4 * g + 3])};
        LAS const char* sr = sc + (lane >> 2) * 72 + (lane & 3) * 16;
        const u32x2 a0 = *(LAS const u32x2*)sr, a1 = *(LAS const u32x2*)(sr + 8), b0 = *(LAS const u32x2*)(sr + 16 * 72), b1 = *(LAS const u32x2*)(sr + 16 * 72 + 8);
        const unsigned oo = (unsigned)((32 * qt + (lane >> 2)) * 2 * D + 32 * ct) * 2 + (lane & 3) * 16;
        __builtin_amdgcn_raw_buffer_store_b128((u32x4){a0[0], a0[1], a1[0], a1[1]}, ro, oo, 0, 16);
        __builtin_amdgcn_raw_buffer_store_b128((u32x4){b0[0], b0[1], b1[0], b1[1]}, ro, oo + 16 * 2 * D * 2, 0, 16);
    }
    if (b == 0 && w == 0) put_flag(ws, 0, tag);
}

__global__ __launch_bounds__(512, 2) void fused(const uint16_t* __restrict__ x, const uint16_t* __restrict__ wqkv, const uint16_t* __restrict__ wo,
                                               const uint16_t* __restrict__ u, const uint16_t* __restrict__ lar, const uint16_t* __restrict__ aim,
                                               const uint16_t* __restrict__ cre, const uint16_t* __restrict__ cim, const uint16_t* __restrict__ ldt,
                                               const uint16_t* __restrict__ dskip, uint16_t* __restrict__ out, char* __restrict__ ws) {
    extern __shared__ __attribute__((aligned(16))) char smem[];
    LAS char* lds = (LAS char*)smem;
    const int lane = threadIdx.x & 63, w = __builtin_amdgcn_readfirstlane(threadIdx.x >> 6);
    u64 code0; asm volatile("s_getpc_b64 %0" : "=s"(code0)); code0 &= ~255ull;
    if (w >= 4) __builtin_amdgcn_s_setprio(3);
    if (w == 4) ((LAS unsigned*)(lds + LSY))[lane] = 0;
    asm volatile("" :: "s"(x), "s"(wqkv), "s"(wo), "s"(u), "s"(lar), "s"(aim), "s"(cre), "s"(cim), "s"(ldt), "s"(dskip), "s"(out), "s"(ws));
    asm volatile("s_waitcnt lgkmcnt(0)\n\ts_barrier" ::: "memory");
    if (w < 4) role_a(x, wqkv, out, ws, lds, w, lane);
    else role_b(wo, u, lar, aim, cre, cim, ldt, dskip, out, ws, lds, w - 4, lane, code0);
}
}

extern "C" void kernel_launch(void* const* d_in, const int* in_sizes, int n_in, void* d_out, int out_size, void* d_ws, size_t ws_size, hipStream_t stream) {
    using namespace k;
    (void)hipFuncSetAttribute((const void*)fused, hipFuncAttributeMaxDynamicSharedMemorySize, LDS_BYTES);
    const uint16_t* const* in = (const uint16_t* const*)d_in;
    fused<<<256, 512, LDS_BYTES, stream>>>(in[0], in[1], in[2], in[3], in[4], in[5], in[6], in[7], in[8], in[9], (uint16_t*)d_out, (char*)d_ws);
}
```
